```python
import torch

def _softmax(x, axis=-1):
    return torch.softmax(x, dim=axis)
def _gelu(x):
    return torch.nn.functional.gelu(x, approximate="tanh")
def _silu(x):
    return torch.nn.functional.silu(x)
def _sigmoid(x):
    return torch.sigmoid(x)
def _layernorm(x, axis=-1, eps=1e-5):
    m = x.mean(dim=axis, keepdim=True)
    v = x.var(dim=axis, keepdim=True, unbiased=False)
    return (x - m) / torch.sqrt(v + eps)
def _scatter_add(tgt, idx, src, axis=0):
    return tgt.clone().index_add_(axis, idx, src)
def _segment_sum(src, seg, n):
    out = torch.zeros((n,) + src.shape[1:], dtype=src.dtype)
    return out.index_add_(0, seg, src)
def _pad(x, pw):
    flat = tuple(v for pair in reversed(pw) for v in pair)
    return torch.nn.functional.pad(x, flat)

kernel_name = 'concat_heads__mha_block_cached__gqa_block_cached__near_ridge'

def setup_inputs():

    _w = (lambda a: (lambda t: t.to(torch.bfloat16) if t.is_floating_point() else t)(torch.as_tensor(a)))
    def _A_setup():
        B, Sq, D = 16, 160, 640
        H, Sk, Dh = 32, 512, 192
        return {
            "x": _w((lambda *s: torch.randn(*s, dtype=torch.bfloat16) * 0.8)(B, Sq, D)),
            "k_cache": _w((lambda *s: torch.randn(*s, dtype=torch.bfloat16) * 0.8)(B, H, Sk, Dh)),
            "v_cache": _w((lambda *s: torch.randn(*s, dtype=torch.bfloat16) * 0.05)(B, H, Sk, Dh)),
            "wq": _w((lambda *s: torch.randn(*s, dtype=torch.bfloat16) * 0.8)(D, H * Dh) * (D ** -0.5)),
            "wo": _w((lambda *s: torch.randn(*s, dtype=torch.bfloat16) * 0.05)(H * Dh, D) * ((H * Dh) ** -0.5)),
        }
    _A_in = _A_setup()

    def _B_setup():
        B, Sq, D = 16, 160, 640
        Hq, Hkv, Sk, Dh = 32, 32, 512, 192
        assert Hq % Hkv == 0, (Hq, Hkv)
        return {
            "x": _w((lambda *s: torch.randn(*s, dtype=torch.bfloat16) * 0.8)(B, Sq, D)),
            "k_cache": _w((lambda *s: torch.randn(*s, dtype=torch.bfloat16) * 0.8)(B, Hkv, Sk, Dh)),
            "v_cache": _w((lambda *s: torch.randn(*s, dtype=torch.bfloat16) * 0.05)(B, Hkv, Sk, Dh)),
            "wq": _w((lambda *s: torch.randn(*s, dtype=torch.bfloat16) * 0.8)(D, Hq * Dh) * (D ** -0.5)),
            "wo": _w((lambda *s: torch.randn(*s, dtype=torch.bfloat16) * 0.05)(Hq * Dh, D) * ((Hq * Dh) ** -0.5)),
        }
    _B_in = _B_setup()

    return {
        "A_x": _A_in["x"],
        "A_k_cache": _A_in["k_cache"],
        "A_v_cache": _A_in["v_cache"],
        "A_wq": _A_in["wq"],
        "A_wo": _A_in["wo"],
        "B_x": _B_in["x"],
        "B_k_cache": _B_in["k_cache"],
        "B_v_cache": _B_in["v_cache"],
        "B_wq": _B_in["wq"],
        "B_wo": _B_in["wo"],
    }

def reference(A_x, A_k_cache, A_v_cache, A_wq, A_wo, B_x, B_k_cache, B_v_cache, B_wq, B_wo):
    def _A_ref(x, k_cache, v_cache, wq, wo):
        B, Sq, D = x.shape
        H, Dh = 32, 192
        q = (lambda x, *d: x.permute(*d))((x @ wq).reshape(B, Sq, H, Dh), 0, 2, 1, 3)
        scores = torch.einsum("bhqd,bhkd->bhqk", q, k_cache) * (Dh ** -0.5)
        p = _softmax(scores, axis=-1)
        o = torch.einsum("bhqk,bhkd->bhqd", p, v_cache)
        return (lambda x, *d: x.permute(*d))(o, 0, 2, 1, 3).reshape(B, Sq, H * Dh) @ wo

    def _B_ref(x, k_cache, v_cache, wq, wo):
        B, Sq, D = x.shape
        Hq, Dh = 32, 192
        Hkv = k_cache.shape[1]
        rep = Hq // Hkv
        q = (lambda x, *d: x.permute(*d))((x @ wq).reshape(B, Sq, Hq, Dh), 0, 2, 1, 3)
        k = (lambda x, n, axis: torch.repeat_interleave(x, n, dim=axis))(k_cache, rep, axis=1)
        v = (lambda x, n, axis: torch.repeat_interleave(x, n, dim=axis))(v_cache, rep, axis=1)
        scores = torch.einsum("bhqd,bhkd->bhqk", q, k) * (Dh ** -0.5)
        p = _softmax(scores, axis=-1)
        o = torch.einsum("bhqk,bhkd->bhqd", p, v)
        return (lambda x, *d: x.permute(*d))(o, 0, 2, 1, 3).reshape(B, Sq, Hq * Dh) @ wo

    a = _A_ref(A_x, A_k_cache, A_v_cache, A_wq, A_wo)
    b = _B_ref(B_x, B_k_cache, B_v_cache, B_wq, B_wo)
    return torch.concatenate([a, b], axis=-1)
```

```cpp
#include <hip/hip_runtime.h>
#include <hip/hip_bf16.h>
typedef __hip_bfloat16 bf16;
__global__ __launch_bounds__(256) void gemm_naive(const bf16* A, const bf16* B, bf16* C, int M, int N, int K, int ldc) {
  __shared__ float As[16][65], Bs[16][65];
  int tx = threadIdx.x & 15, ty = threadIdx.x >> 4;
  int m0 = blockIdx.y * 64, n0 = blockIdx.x * 64;
  float acc[4][4] = {};
  for (int k0 = 0; k0 < K; k0 += 16) {
    for (int i = threadIdx.x; i < 1024; i += 256) {
      int r = i >> 4, c = i & 15; As[c][r] = __bfloat162float(A[(size_t)(m0 + r) * K + k0 + c]);
      int kr = i >> 6, nc = i & 63; Bs[kr][nc] = __bfloat162float(B[(size_t)(k0 + kr) * N + n0 + nc]);
    }
    __syncthreads();
    for (int k = 0; k < 16; ++k)
      for (int i = 0; i < 4; ++i) for (int j = 0; j < 4; ++j) acc[i][j] += As[k][ty * 4 + i] * Bs[k][tx * 4 + j];
    __syncthreads();
  }
  for (int i = 0; i < 4; ++i) for (int j = 0; j < 4; ++j) C[(size_t)(m0 + ty * 4 + i) * ldc + n0 + tx * 4 + j] = __float2bfloat16(acc[i][j]);
}
__global__ __launch_bounds__(256) void attn_naive(const bf16* Q, const bf16* K, const bf16* V, bf16* O) {
  __shared__ float q[192], s[512], red[256];
  int row = blockIdx.x, h = blockIdx.y, b = blockIdx.z, t = threadIdx.x;
  if (t < 192) q[t] = __bfloat162float(Q[((size_t)(b * 160 + row)) * 6144 + h * 192 + t]);
  __syncthreads();
  const bf16* Kh = K + (size_t)(b * 32 + h) * 512 * 192; const bf16* Vh = V + (size_t)(b * 32 + h) * 512 * 192;
  float mx = -1e30f;
  for (int k = t; k < 512; k += 256) { float a = 0; for (int d = 0; d < 192; ++d) a += q[d] * __bfloat162float(Kh[k * 192 + d]); a *= 0.07216878364870322f; s[k] = a; mx = fmaxf(mx, a); }
  red[t] = mx; __syncthreads();
  for (int o = 128; o; o >>= 1) { if (t < o) red[t] = fmaxf(red[t], red[t + o]); __syncthreads(); }
  mx = red[0]; __syncthreads();
  float sm = 0; for (int k = t; k < 512; k += 256) { float e = __expf(s[k] - mx); s[k] = e; sm += e; }
  red[t] = sm; __syncthreads();
  for (int o = 128; o; o >>= 1) { if (t < o) red[t] += red[t + o]; __syncthreads(); }
  sm = red[0];
  if (t < 192) { float a = 0; for (int k = 0; k < 512; ++k) a += s[k] * __bfloat162float(Vh[k * 192 + t]); O[((size_t)(b * 160 + row)) * 6144 + h * 192 + t] = __float2bfloat16(a / sm); }
}
extern "C" void kernel_launch(void* const* d_in, const int* in_sizes, int n_in, void* d_out, int out_size, void* d_ws, size_t ws_size, hipStream_t stream) {
  bf16* Q = (bf16*)d_ws; bf16* O = Q + (size_t)2560 * 6144;
  for (int p = 0; p < 2; ++p) {
    const bf16* x = (const bf16*)d_in[5 * p], *k = (const bf16*)d_in[5 * p + 1], *v = (const bf16*)d_in[5 * p + 2], *wq = (const bf16*)d_in[5 * p + 3], *wo = (const bf16*)d_in[5 * p + 4];
    gemm_naive<<<dim3(96, 40), 256, 0, stream>>>(x, wq, Q, 2560, 6144, 640, 6144);
    attn_naive<<<dim3(160, 32, 16), 256, 0, stream>>>(Q, k, v, O);
    gemm_naive<<<dim3(10, 40), 256, 0, stream>>>(O, wo, (bf16*)d_out + 640 * p, 2560, 640, 6144, 1280);
  }
}
```

```cpp
#include <hip/hip_runtime.h>
#include <hip/hip_bf16.h>

typedef __hip_bfloat16 bf16;

extern "C" __global__ __launch_bounds__(256, 1)
void fused_block(const bf16* x0, const bf16* k0, const bf16* v0, const bf16* wq0, const bf16* wo0,
                 const bf16* x1, const bf16* k1, const bf16* v1, const bf16* wq1, const bf16* wo1, char* ows, bf16* out) {
  __shared__ __attribute__((aligned(16))) char lds[163840];
  unsigned tid = threadIdx.x;
  asm volatile(
    "s_mov_b64 s[76:77], %1\n\t"
    "s_mov_b32 s78, %2\n\t"
    "v_lshrrev_b32 v1, 6, v0\n\t"
    "v_and_b32 v2, 63, v0\n\t"
    "v_readfirstlane_b32 s7, v1\n\t"
    "s_and_b32 s40, s78, 7\n\t"
    "s_lshr_b32 s41, s78, 3\n\t"
    "s_lshr_b32 s6, s40, 2\n\t"
    "s_bfe_u32 s70, s40, 0x10001\n\t"
    "s_lshl_b32 s70, s70, 3\n\t"
    "s_and_b32 s42, s41, 7\n\t"
    "s_add_u32 s70, s70, s42\n\t"
    "s_and_b32 s51, s7, 1\n\t"
    "s_lshr_b32 s72, s7, 1\n\t"
    "s_lshr_b32 s42, s41, 3\n\t"
    "s_lshl_b32 s73, s42, 1\n\t"
    "s_add_u32 s73, s73, s72\n\t"
    "s_lshr_b32 s74, s42, 1\n\t"
    "s_and_b32 s71, s40, 1\n\t"
    "s_lshl_b32 s71, s71, 4\n\t"
    "s_add_u32 s71, s71, s73\n\t"
    "s_mul_i32 s72, s72, 81920\n\t"
    "s_mul_i32 s42, s6, 40\n\t"
    "s_add_u32 s42, s76, s42\n\t"
    "s_addc_u32 s43, s77, 0\n\t"
    "s_load_dwordx8 s[60:67], s[42:43], 0x0\n\t"
    "s_load_dwordx2 s[68:69], s[76:77], 80\n\t"
    "s_load_dwordx2 s[4:5], s[76:77], 88\n\t"
    "s_load_dwordx2 s[94:95], s[42:43], 32\n\t"
    "s_mul_i32 s34, s51, 6144\n\t"
    "s_add_u32 s34, s34, s72\n\t"
    "s_lshl_b32 s35, s7, 10\n\t"
    "s_add_u32 s35, s35, 12288\n\t"
    "s_mul_i32 s76, s51, 196608\n\t"
    "s_mul_i32 s77, s7, 20480\n\t"
    "s_mov_b32 s48, 0x3dd53b95\n\t"
    "s_mov_b32 s49, 0x3dd53b95\n\t"
    "s_mov_b32 s50, 0x47800000\n\t"
    "s_mov_b32 s11, 0x00020000\n\t"
    "s_mov_b32 s15, 0x00020000\n\t"
    "s_mov_b32 s19, 0x00020000\n\t"
    "s_mov_b32 s23, 0x00020000\n\t"
    "s_mov_b32 s27, 0x00020000\n\t"
    "s_mov_b32 s10, 196608\n\t"
    "s_mov_b32 s14, 196608\n\t"
    "s_mov_b32 s22, 204800\n\t"
    "s_mov_b32 s26, 61440\n\t"
    "v_lshrrev_b32 v3, 4, v2\n\t"
    "v_and_b32 v4, 15, v2\n\t"
    "v_bfe_u32 v5, v2, 2, 2\n\t"
    "v_and_b32 v6, 3, v2\n\t"
    "v_add_u32 v8, 0, v2\n\t"
    "v_mul_u32_u24 v9, 2731, v8\n\t"
    "v_lshrrev_b32 v9, 16, v9\n\t"
    "v_bfe_u32 v10, v9, 1, 2\n\t"
    "v_lshlrev_b32 v10, 1, v10\n\t"
    "v_xor_b32 v10, v2, v10\n\t"
    "v_lshlrev_b32 v242, 4, v10\n\t"
    "v_mul_u32_u24 v10, 24, v9\n\t"
    "v_sub_u32 v10, v8, v10\n\t"
    "v_bfe_u32 v11, v9, 1, 1\n\t"
    "v_lshlrev_b32 v11, 2, v11\n\t"
    "v_xor_b32 v10, v10, v11\n\t"
    "v_mul_u32_u24 v11, 12288, v9\n\t"
    "v_lshl_add_u32 v10, v10, 4, v11\n\t"
    "v_subrev_u32 v245, 0, v10\n\t"
    "v_add_u32 v8, 64, v2\n\t"
    "v_mul_u32_u24 v9, 2731, v8\n\t"
    "v_lshrrev_b32 v9, 16, v9\n\t"
    "v_bfe_u32 v10, v9, 1, 2\n\t"
    "v_lshlrev_b32 v10, 1, v10\n\t"
    "v_xor_b32 v10, v2, v10\n\t"
    "v_lshlrev_b32 v243, 4, v10\n\t"
    "v_mul_u32_u24 v10, 24, v9\n\t"
    "v_sub_u32 v10, v8, v10\n\t"
    "v_bfe_u32 v11, v9, 1, 1\n\t"
    "v_lshlrev_b32 v11, 2, v11\n\t"
    "v_xor_b32 v10, v10, v11\n\t"
    "v_mul_u32_u24 v11, 12288, v9\n\t"
    "v_lshl_add_u32 v10, v10, 4, v11\n\t"
    "v_subrev_u32 v246, 1024, v10\n\t"
    "v_add_u32 v8, 128, v2\n\t"
    "v_mul_u32_u24 v9, 2731, v8\n\t"
    "v_lshrrev_b32 v9, 16, v9\n\t"
    "v_bfe_u32 v10, v9, 1, 2\n\t"
    "v_lshlrev_b32 v10, 1, v10\n\t"
    "v_xor_b32 v10, v2, v10\n\t"
    "v_lshlrev_b32 v244, 4, v10\n\t"
    "v_mul_u32_u24 v10, 24, v9\n\t"
    "v_sub_u32 v10, v8, v10\n\t"
    "v_bfe_u32 v11, v9, 1, 1\n\t"
    "v_lshlrev_b32 v11, 2, v11\n\t"
    "v_xor_b32 v10, v10, v11\n\t"
    "v_mul_u32_u24 v11, 12288, v9\n\t"
    "v_lshl_add_u32 v10, v10, 4, v11\n\t"
    "v_subrev_u32 v247, 2048, v10\n\t"
    "v_lshrrev_b32 v8, 2, v2\n\t"
    "v_mul_u32_u24 v8, 1280, v8\n\t"
    "v_lshrrev_b32 v9, 5, v2\n\t"
    "v_lshlrev_b32 v9, 1, v9\n\t"
    "v_xor_b32 v9, v6, v9\n\t"
    "v_lshl_add_u32 v248, v9, 4, v8\n\t"
    "s_waitcnt lgkmcnt(0)\n\t"
    "s_mul_i32 s42, s70, 204800\n\t"
    "s_add_u32 s20, s60, s42\n\t"
    "s_addc_u32 s21, s61, 0\n\t"
    "s_sub_u32 s60, s68, 1048576\n\t"
    "s_subb_u32 s61, s69, 0\n\t"
    "s_and_b32 s40, s78, 63\n\t"
    "s_lshl_b32 s40, s40, 7\n\t"
    "v_and_b32 v8, 3, v2\n\t"
    "v_lshlrev_b32 v8, 13, v8\n\t"
    "v_add_u32 v8, s40, v8\n\t"
    "v_and_b32 v9, 4, v2\n\t"
    "v_lshl_add_u32 v254, v9, 4, v8\n\t"
    "s_lshl_b32 s40, s78, 7\n\t"
    "s_xor_b32 s41, s40, 128\n\t"
    "s_add_u32 s42, s41, 96\n\t"
    "v_mov_b32 v8, s42\n\t"
    "v_cmp_le_u32 vcc, 8, v2\n\t"
    "s_nop 1\n\t"
    "v_cndmask_b32 v254, v254, v8, vcc\n\t"
    "s_add_u32 s42, s41, 0\n\t"
    "v_mov_b32 v8, s42\n\t"
    "v_cmp_le_u32 vcc, 9, v2\n\t"
    "s_nop 1\n\t"
    "v_cndmask_b32 v254, v254, v8, vcc\n\t"
    "s_add_u32 s42, s40, 96\n\t"
    "v_mov_b32 v8, s42\n\t"
    "v_cmp_le_u32 vcc, 10, v2\n\t"
    "s_nop 1\n\t"
    "v_cndmask_b32 v254, v254, v8, vcc\n\t"
    "global_load_dword v255, v254, s[60:61] sc1\n\t"
    "s_add_u32 s79, s71, 8\n\t"
    "s_mul_i32 s42, s71, 384\n\t"
    "s_add_u32 s16, s66, s42\n\t"
    "s_addc_u32 s17, s67, 0\n\t"
    "s_sub_u32 s18, 7864320, s42\n\t"
    "s_mov_b32 s38, s76\n\t"
    "s_mov_b32 s39, s77\n\t"
    "s_bitcmp0_b32 s78, 7\n\t"
    "s_cbranch_scc1 .Lk1_nostag\n\t"
    "s_mul_i32 s40, s70, 204800\n\t"
    "s_and_b32 s42, s78, 3\n\t"
    "s_lshl_b32 s42, s42, 4\n\t"
    "s_bfe_u32 s43, s78, 0x40003\n\t"
    "s_add_u32 s42, s42, s43\n\t"
    "s_lshl_b32 s42, s42, 2\n\t"
    "s_add_u32 s42, s42, s7\n\t"
    "s_mov_b32 s8, s66\n\t"
    "s_mov_b32 s9, s67\n\t"
    "s_mov_b32 s10, 7864320\n\t"
    "v_min_u32 v12, 59, v2\n\t"
    "v_lshlrev_b32 v12, 8, v12\n\t"
    "v_add_u32 v12, s42, v12\n\t"
    "v_mul_u32_u24 v13, 43691, v12\n\t"
    "v_lshrrev_b32 v13, 19, v13\n\t"
    "v_mul_u32_u24 v14, 12, v13\n\t"
    "v_sub_u32 v14, v12, v14\n\t"
    "v_mul_u32_u24 v13, 6144, v13\n\t"
    "v_lshl_add_u32 v13, v14, 7, v13\n\t"
    "buffer_load_dword v15, v13, s[8:11], 0 offen\n\t"
    "v_mov_b32 v16, v13\n\t"
    "s_sub_u32 s12, s20, s40\n\t"
    "s_subb_u32 s13, s21, 0\n\t"
    "s_mov_b32 s14, 3276800\n\t"
    "s_mul_i32 s43, s42, 12800\n\t"
    "v_lshlrev_b32 v13, 7, v2\n\t"
    "buffer_load_dword v15, v13, s[12:15], s43 offen\n\t"
    "v_min_u32 v12, 35, v2\n\t"
    "v_lshlrev_b32 v13, 7, v12\n\t"
    "s_add_u32 s43, s43, 8192\n\t"
    "buffer_load_dword v15, v13, s[12:15], s43 offen\n\t"
    "s_mov_b32 s14, 196608\n\t"
    "buffer_load_dword v15, v16, s[8:11], 0 offen offset:1536\n\t"
    "s_mul_i32 s43, s42, 120\n\t"
    "v_add_u32 v12, 0, v2\n\t"
    "v_min_u32 v12, 119, v12\n\t"
    "v_add_u32 v12, s43, v12\n\t"
    "v_mul_u32_u24 v13, 43691, v12\n\t"
    "v_lshrrev_b32 v13, 20, v13\n\t"
    "v_mul_u32_u24 v14, 24, v13\n\t"
    "v_sub_u32 v14, v12, v14\n\t"
    "v_mul_u32_u24 v13, 6144, v13\n\t"
    "v_lshl_add_u32 v13, v14, 7, v13\n\t"
    "v_add_u32 v13, 3072, v13\n\t"
    "buffer_load_dword v15, v13, s[8:11], 0 offen\n\t"
    "v_add_u32 v12, 64, v2\n\t"
    "v_min_u32 v12, 119, v12\n\t"
    "v_add_u32 v12, s43, v12\n\t"
    "v_mul_u32_u24 v13, 43691, v12\n\t"
    "v_lshrrev_b32 v13, 20, v13\n\t"
    "v_mul_u32_u24 v14, 24, v13\n\t"
    "v_sub_u32 v14, v12, v14\n\t"
    "v_mul_u32_u24 v13, 6144, v13\n\t"
    "v_lshl_add_u32 v13, v14, 7, v13\n\t"
    "v_add_u32 v13, 3072, v13\n\t"
    "buffer_load_dword v15, v13, s[8:11], 0 offen\n\t"
    "s_mul_i32 s43, s42, 30720\n\t"
    "s_add_u32 s8, s94, s43\n\t"
    "s_addc_u32 s9, s95, 0\n\t"
    "s_mov_b32 s10, 30720\n\t"
    "v_lshlrev_b32 v13, 7, v2\n\t"
    "s_mov_b32 s43, 0\n\t"
    "buffer_load_dword v15, v13, s[8:11], s43 offen\n\t"
    "s_mov_b32 s43, 8192\n\t"
    "buffer_load_dword v15, v13, s[8:11], s43 offen\n\t"
    "s_mov_b32 s43, 16384\n\t"
    "buffer_load_dword v15, v13, s[8:11], s43 offen\n\t"
    "s_mov_b32 s43, 24576\n\t"
    "buffer_load_dword v15, v13, s[8:11], s43 offen\n\t"
    "s_mov_b32 s10, 196608\n\t"
    "s_sleep 127\n\t"
    "s_sleep 96\n\t"
    "s_waitcnt vmcnt(0)\n\t"
    ".Lk1_nostag:\n\t"
    "s_add_u32 s58, s34, 0\n\t"
    "s_add_u32 s59, s35, 0\n\t"
    "s_add_u32 m0, s58, 0\n\t"
    "s_nop 0\n\t"
    "buffer_load_dwordx4 v245, s[16:19], s38 offen lds\n\t"
    "buffer_load_dwordx4 v246, s[16:19], s38 offen offset:1024 lds\n\t"
    "buffer_load_dwordx4 v247, s[16:19], s38 offen offset:2048 lds\n\t"
    "s_add_u32 m0, s58, 3072\n\t"
    "s_add_u32 s40, s38, 98304\n\t"
    "buffer_load_dwordx4 v245, s[16:19], s40 offen lds\n\t"
    "buffer_load_dwordx4 v246, s[16:19], s40 offen offset:1024 lds\n\t"
    "buffer_load_dwordx4 v247, s[16:19], s40 offen offset:2048 lds\n\t"
    "s_add_u32 m0, s59, 0\n\t"
    "s_nop 0\n\t"
    "buffer_load_dwordx4 v248, s[20:23], s39 offen lds\n\t"
    "s_add_u32 m0, s59, 4096\n\t"
    "s_add_u32 s41, s39, 81920\n\t"
    "buffer_load_dwordx4 v248, s[20:23], s41 offen lds\n\t"
    "s_add_u32 m0, s59, 8192\n\t"
    "s_add_u32 s40, s39, 163840\n\t"
    "buffer_load_dwordx4 v248, s[20:23], s40 offen lds\n\t"
    "s_add_u32 s38, s38, 393216\n\t"
    "s_add_u32 s39, s39, 64\n\t"
    "s_add_u32 s58, s34, 24576\n\t"
    "s_add_u32 s59, s35, 24576\n\t"
    "s_add_u32 m0, s58, 0\n\t"
    "s_nop 0\n\t"
    "buffer_load_dwordx4 v245, s[16:19], s38 offen lds\n\t"
    "buffer_load_dwordx4 v246, s[16:19], s38 offen offset:1024 lds\n\t"
    "buffer_load_dwordx4 v247, s[16:19], s38 offen offset:2048 lds\n\t"
    "s_add_u32 m0, s58, 3072\n\t"
    "s_add_u32 s40, s38, 98304\n\t"
    "buffer_load_dwordx4 v245, s[16:19], s40 offen lds\n\t"
    "buffer_load_dwordx4 v246, s[16:19], s40 offen offset:1024 lds\n\t"
    "buffer_load_dwordx4 v247, s[16:19], s40 offen offset:2048 lds\n\t"
    "s_add_u32 m0, s59, 0\n\t"
    "s_nop 0\n\t"
    "buffer_load_dwordx4 v248, s[20:23], s39 offen lds\n\t"
    "s_add_u32 m0, s59, 4096\n\t"
    "s_add_u32 s41, s39, 81920\n\t"
    "buffer_load_dwordx4 v248, s[20:23], s41 offen lds\n\t"
    "s_add_u32 m0, s59, 8192\n\t"
    "s_add_u32 s40, s39, 163840\n\t"
    "buffer_load_dwordx4 v248, s[20:23], s40 offen lds\n\t"
    "s_add_u32 s38, s38, 393216\n\t"
    "s_add_u32 s39, s39, 64\n\t"
    "s_add_u32 s58, s34, 49152\n\t"
    "s_add_u32 s59, s35, 49152\n\t"
    "s_add_u32 m0, s58, 0\n\t"
    "s_nop 0\n\t"
    "buffer_load_dwordx4 v245, s[16:19], s38 offen lds\n\t"
    "buffer_load_dwordx4 v246, s[16:19], s38 offen offset:1024 lds\n\t"
    "buffer_load_dwordx4 v247, s[16:19], s38 offen offset:2048 lds\n\t"
    "s_add_u32 m0, s58, 3072\n\t"
    "s_add_u32 s40, s38, 98304\n\t"
    "buffer_load_dwordx4 v245, s[16:19], s40 offen lds\n\t"
    "buffer_load_dwordx4 v246, s[16:19], s40 offen offset:1024 lds\n\t"
    "buffer_load_dwordx4 v247, s[16:19], s40 offen offset:2048 lds\n\t"
    "s_add_u32 m0, s59, 0\n\t"
    "s_nop 0\n\t"
    "buffer_load_dwordx4 v248, s[20:23], s39 offen lds\n\t"
    "s_add_u32 m0, s59, 4096\n\t"
    "s_add_u32 s41, s39, 81920\n\t"
    "buffer_load_dwordx4 v248, s[20:23], s41 offen lds\n\t"
    "s_add_u32 m0, s59, 8192\n\t"
    "s_add_u32 s40, s39, 163840\n\t"
    "buffer_load_dwordx4 v248, s[20:23], s40 offen lds\n\t"
    "s_add_u32 s38, s38, 393216\n\t"
    "s_add_u32 s39, s39, 64\n\t"
    "s_getpc_b64 s[54:55]\n\t"
    ".Lk1_pc0:\n\t"
    "s_add_u32 s54, s54, .Lk1_slow0-.Lk1_pc0\n\t"
    "s_addc_u32 s55, s55, 0\n\t"
    "s_getpc_b64 s[56:57]\n\t"
    ".Lk1_pc1:\n\t"
    "s_add_u32 s56, s56, .Lk1_slow1-.Lk1_pc1\n\t"
    "s_addc_u32 s57, s57, 0\n\t"
    "v_bfe_u32 v7, v4, 1, 2\n\t"
    "v_lshlrev_b32 v7, 1, v7\n\t"
    "v_mul_u32_u24 v8, 384, v4\n\t"
    "v_xor_b32 v9, v3, v7\n\t"
    "v_lshl_add_u32 v236, v9, 4, v8\n\t"
    "v_xor_b32 v9, 4, v9\n\t"
    "v_lshl_add_u32 v237, v9, 4, v8\n\t"
    "v_lshl_add_u32 v8, v3, 2, v5\n\t"
    "v_bfe_u32 v7, v8, 1, 2\n\t"
    "v_mul_u32_u24 v8, 384, v8\n\t"
    "v_lshl_add_u32 v8, v6, 3, v8\n\t"
    "v_add_u32 v8, 12288, v8\n\t"
    "v_xor_b32 v9, 0, v7\n\t"
    "v_lshl_add_u32 v238, v9, 5, v8\n\t"
    "v_xor_b32 v9, 1, v7\n\t"
    "v_lshl_add_u32 v239, v9, 5, v8\n\t"
    "v_xor_b32 v9, 2, v7\n\t"
    "v_lshl_add_u32 v240, v9, 5, v8\n\t"
    "v_xor_b32 v9, 3, v7\n\t"
    "v_lshl_add_u32 v241, v9, 5, v8\n\t"
    "v_lshl_add_u32 v8, v3, 3, v5\n\t"
    "v_mul_u32_u24 v8, 384, v8\n\t"
    "v_lshl_add_u32 v8, v6, 4, v8\n\t"
    "v_lshrrev_b32 v9, 1, v5\n\t"
    "v_lshl_add_u32 v249, v9, 6, v8\n\t"
    "v_xor_b32 v9, 1, v9\n\t"
    "v_lshl_add_u32 v250, v9, 6, v8\n\t"
    "v_lshrrev_b32 v9, 3, v4\n\t"
    "v_lshlrev_b32 v9, 1, v9\n\t"
    "v_xor_b32 v9, v3, v9\n\t"
    "v_lshlrev_b32 v9, 4, v9\n\t"
    "v_lshl_add_u32 v9, v4, 6, v9\n\t"
    "s_mul_i32 s40, s51, 5120\n\t"
    "s_add_u32 s40, s40, 12288\n\t"
    "v_add_u32 v251, s40, v9\n\t"
    "v_add_u32 v236, s72, v236\n\t"
    "v_add_u32 v237, s72, v237\n\t"
    "v_add_u32 v238, s72, v238\n\t"
    "v_add_u32 v239, s72, v239\n\t"
    "v_add_u32 v240, s72, v240\n\t"
    "v_add_u32 v241, s72, v241\n\t"
    "v_add_u32 v249, s72, v249\n\t"
    "v_add_u32 v250, s72, v250\n\t"
    "s_mul_i32 s40, s51, 5120\n\t"
    "v_lshlrev_b32 v9, 4, v2\n\t"
    "v_add_u32 v252, s40, v9\n\t"
    "s_lshl_b32 s42, s6, 4\n\t"
    "s_add_u32 s42, s42, s70\n\t"
    "s_lshl_b32 s42, s42, 5\n\t"
    "s_add_u32 s42, s42, s71\n\t"
    "s_mul_i32 s42, s42, 61440\n\t"
    "s_add_u32 s24, s68, s42\n\t"
    "s_addc_u32 s25, s69, 0\n\t"
    "s_lshl_b32 s42, s70, 5\n\t"
    "s_add_u32 s42, s42, s71\n\t"
    "s_mul_i32 s42, s42, 196608\n\t"
    "s_add_u32 s8, s62, s42\n\t"
    "s_addc_u32 s9, s63, 0\n\t"
    "s_add_u32 s12, s64, s42\n\t"
    "s_addc_u32 s13, s65, 0\n\t"
    "s_mul_i32 s36, s51, 6144\n\t"
    "s_mov_b32 s37, s36\n\t"
    "s_mov_b32 s45, 0\n\t"
    "s_mov_b32 s46, 0\n\t"
    "s_mov_b32 s47, 0\n\t"
    "s_mov_b32 s28, 0\n\t"
    "s_waitcnt vmcnt(18)\n\t"
    "s_barrier\n\t"
    "v_add_u32 v255, 1, v255\n\t"
    "v_xor_b32 v8, 0x5a5a5a5a, v255\n\t"
    "v_min_u32 v9, 4, v2\n\t"
    "v_lshlrev_b32 v9, 2, v9\n\t"
    "s_lshl_b32 s40, s78, 7\n\t"
    "v_add_u32 v9, s40, v9\n\t"
    "s_mov_b64 exec, 0x20f\n\t"
    "global_store_dword v9, v8, s[60:61] offset:8 sc1\n\t"
    "s_mov_b64 exec, -1\n\t"
    ".Lk1_q:\n\t"
    "s_sub_u32 s31, s28, s47\n\t"
    "v_add_u32 v249, s31, v249\n\t"
    "v_add_u32 v250, s31, v250\n\t"
    "v_add_u32 v251, s31, v251\n\t"
    "ds_read_b64_tr_b16 v[204:205], v249 offset:0\n\t"
    "ds_read_b64_tr_b16 v[206:207], v249 offset:1536\n\t"
    "ds_read_b128 v[104:107], v251 offset:0\n\t"
    "ds_read_b128 v[108:111], v251 offset:1024\n\t"
    "ds_read_b128 v[112:115], v251 offset:2048\n\t"
    "ds_read_b128 v[116:119], v251 offset:3072\n\t"
    "ds_read_b128 v[120:123], v251 offset:4096\n\t"
    "ds_read_b64_tr_b16 v[208:209], v249 offset:8\n\t"
    "ds_read_b64_tr_b16 v[210:211], v249 offset:1544\n\t"
    "ds_read_b64_tr_b16 v[212:213], v250 offset:0\n\t"
    "ds_read_b64_tr_b16 v[214:215], v250 offset:1536\n\t"
    "ds_read_b64_tr_b16 v[216:217], v250 offset:8\n\t"
    "s_waitcnt lgkmcnt(9)\n\t"
    "v_mfma_f32_16x16x32_bf16 a[0:3], v[204:207], v[104:107], 0\n\t"
    "s_waitcnt lgkmcnt(8)\n\t"
    "ds_read_b64_tr_b16 v[218:219], v250 offset:1544\n\t"
    "v_mfma_f32_16x16x32_bf16 a[4:7], v[204:207], v[108:111], 0\n\t"
    "s_waitcnt lgkmcnt(8)\n\t"
    "v_mfma_f32_16x16x32_bf16 a[8:11], v[204:207], v[112:115], 0\n\t"
    "s_waitcnt lgkmcnt(7)\n\t"
    "s_add_u32 s30, s28, 24576\n\t"
    "v_mfma_f32_16x16x32_bf16 a[12:15], v[204:207], v[116:119], 0\n\t"
    "s_waitcnt lgkmcnt(6)\n\t"
    "s_cmp_eq_u32 s30, 73728\n\t"
    "s_cselect_b32 s30, 0, s30\n\t"
    "v_mfma_f32_16x16x32_bf16 a[16:19], v[204:207], v[120:123], 0\n\t"
    "s_waitcnt lgkmcnt(4)\n\t"
    "ds_read_b64_tr_b16 v[204:205], v249 offset:128\n\t"
    "v_mfma_f32_16x16x32_bf16 a[20:23], v[208:211], v[104:107], 0\n\t"
    "s_sub_u32 s31, s30, s28\n\t"
    "ds_read_b64_tr_b16 v[206:207], v249 offset:1664\n\t"
    "v_mfma_f32_16x16x32_bf16 a[24:27], v[208:211], v[108:111], 0\n\t"
    "v_mfma_f32_16x16x32_bf16 a[28:31], v[208:211], v[112:115], 0\n\t"
    "v_add_u32 v251, s31, v251\n\t"
    "v_mfma_f32_16x16x32_bf16 a[32:35], v[208:211], v[116:119], 0\n\t"
    "v_mfma_f32_16x16x32_bf16 a[36:39], v[208:211], v[120:123], 0\n\t"
    "s_waitcnt lgkmcnt(4)\n\t"
    "ds_read_b64_tr_b16 v[208:209], v249 offset:136\n\t"
    "v_mfma_f32_16x16x32_bf16 a[40:43], v[212:215], v[104:107], 0\n\t"
    "ds_read_b64_tr_b16 v[210:211], v249 offset:1672\n\t"
    "v_mfma_f32_16x16x32_bf16 a[44:47], v[212:215], v[108:111], 0\n\t"
    "v_mfma_f32_16x16x32_bf16 a[48:51], v[212:215], v[112:115], 0\n\t"
    "v_mfma_f32_16x16x32_bf16 a[52:55], v[212:215], v[116:119], 0\n\t"
    "v_mfma_f32_16x16x32_bf16 a[56:59], v[212:215], v[120:123], 0\n\t"
    "s_waitcnt lgkmcnt(4)\n\t"
    "ds_read_b64_tr_b16 v[212:213], v250 offset:128\n\t"
    "v_mfma_f32_16x16x32_bf16 a[60:63], v[216:219], v[104:107], 0\n\t"
    "ds_read_b64_tr_b16 v[214:215], v250 offset:1664\n\t"
    "v_mfma_f32_16x16x32_bf16 a[64:67], v[216:219], v[108:111], 0\n\t"
    "v_mfma_f32_16x16x32_bf16 a[68:71], v[216:219], v[112:115], 0\n\t"
    "v_mfma_f32_16x16x32_bf16 a[72:75], v[216:219], v[116:119], 0\n\t"
    "v_mfma_f32_16x16x32_bf16 a[76:79], v[216:219], v[120:123], 0\n\t"
    "s_waitcnt lgkmcnt(4)\n\t"
    "ds_read_b64_tr_b16 v[216:217], v250 offset:136\n\t"
    "v_mfma_f32_16x16x32_bf16 a[80:83], v[204:207], v[104:107], 0\n\t"
    "ds_read_b64_tr_b16 v[218:219], v250 offset:1672\n\t"
    "v_mfma_f32_16x16x32_bf16 a[84:87], v[204:207], v[108:111], 0\n\t"
    "v_mfma_f32_16x16x32_bf16 a[88:91], v[204:207], v[112:115], 0\n\t"
    "v_mfma_f32_16x16x32_bf16 a[92:95], v[204:207], v[116:119], 0\n\t"
    "v_mfma_f32_16x16x32_bf16 a[96:99], v[204:207], v[120:123], 0\n\t"
    "s_waitcnt lgkmcnt(4)\n\t"
    "ds_read_b64_tr_b16 v[204:205], v249 offset:256\n\t"
    "v_mfma_f32_16x16x32_bf16 a[100:103], v[208:211], v[104:107], 0\n\t"
    "ds_read_b64_tr_b16 v[206:207], v249 offset:1792\n\t"
    "v_mfma_f32_16x16x32_bf16 a[104:107], v[208:211], v[108:111], 0\n\t"
    "v_mfma_f32_16x16x32_bf16 a[108:111], v[208:211], v[112:115], 0\n\t"
    "v_mfma_f32_16x16x32_bf16 a[112:115], v[208:211], v[116:119], 0\n\t"
    "v_mfma_f32_16x16x32_bf16 a[116:119], v[208:211], v[120:123], 0\n\t"
    "s_waitcnt lgkmcnt(4)\n\t"
    "ds_read_b64_tr_b16 v[208:209], v249 offset:264\n\t"
    "v_mfma_f32_16x16x32_bf16 a[120:123], v[212:215], v[104:107], 0\n\t"
    "ds_read_b64_tr_b16 v[210:211], v249 offset:1800\n\t"
    "v_mfma_f32_16x16x32_bf16 a[124:127], v[212:215], v[108:111], 0\n\t"
    "v_mfma_f32_16x16x32_bf16 a[128:131], v[212:215], v[112:115], 0\n\t"
    "v_mfma_f32_16x16x32_bf16 a[132:135], v[212:215], v[116:119], 0\n\t"
    "v_mfma_f32_16x16x32_bf16 a[136:139], v[212:215], v[120:123], 0\n\t"
    "s_waitcnt lgkmcnt(4)\n\t"
    "ds_read_b64_tr_b16 v[212:213], v250 offset:256\n\t"
    "v_mfma_f32_16x16x32_bf16 a[140:143], v[216:219], v[104:107], 0\n\t"
    "ds_read_b64_tr_b16 v[214:215], v250 offset:1792\n\t"
    "v_mfma_f32_16x16x32_bf16 a[144:147], v[216:219], v[108:111], 0\n\t"
    "v_mfma_f32_16x16x32_bf16 a[148:151], v[216:219], v[112:115], 0\n\t"
    "v_mfma_f32_16x16x32_bf16 a[152:155], v[216:219], v[116:119], 0\n\t"
    "v_mfma_f32_16x16x32_bf16 a[156:159], v[216:219], v[120:123], 0\n\t"
    "s_waitcnt lgkmcnt(4)\n\t"
    "ds_read_b64_tr_b16 v[216:217], v250 offset:264\n\t"
    "v_mfma_f32_16x16x32_bf16 a[160:163], v[204:207], v[104:107], 0\n\t"
    "ds_read_b64_tr_b16 v[218:219], v250 offset:1800\n\t"
    "v_mfma_f32_16x16x32_bf16 a[164:167], v[204:207], v[108:111], 0\n\t"
    "v_mfma_f32_16x16x32_bf16 a[168:171], v[204:207], v[112:115], 0\n\t"
    "v_add_u32 v249, s31, v249\n\t"
    "v_add_u32 v250, s31, v250\n\t"
    "v_mfma_f32_16x16x32_bf16 a[172:175], v[204:207], v[116:119], 0\n\t"
    "v_mfma_f32_16x16x32_bf16 a[176:179], v[204:207], v[120:123], 0\n\t"
    "s_waitcnt lgkmcnt(4)\n\t"
    "v_mfma_f32_16x16x32_bf16 a[180:183], v[208:211], v[104:107], 0\n\t"
    "v_mfma_f32_16x16x32_bf16 a[184:187], v[208:211], v[108:111], 0\n\t"
    "s_cmp_eq_u32 s45, 0\n\t"
    "s_cbranch_scc0 .Lk1_w1k0\n\t"
    "s_waitcnt vmcnt(10)\n\t"
    ".Lk1_w1k0:\n\t"
    "s_waitcnt vmcnt(40) lgkmcnt(0)\n\t"
    "s_barrier\n\t"
    "v_mfma_f32_16x16x32_bf16 a[188:191], v[208:211], v[112:115], 0\n\t"
    "s_mov_b32 s29, s28\n\t"
    "ds_read_b64_tr_b16 v[204:205], v249 offset:0\n\t"
    "v_mfma_f32_16x16x32_bf16 a[192:195], v[208:211], v[116:119], 0\n\t"
    "s_mov_b32 s28, s30\n\t"
    "ds_read_b64_tr_b16 v[206:207], v249 offset:1536\n\t"
    "v_mfma_f32_16x16x32_bf16 a[196:199], v[208:211], v[120:123], 0\n\t"
    "s_add_u32 s58, s29, s34\n\t"
    "ds_read_b128 v[124:127], v251 offset:0\n\t"
    "v_mfma_f32_16x16x32_bf16 a[200:203], v[212:215], v[104:107], 0\n\t"
    "s_add_u32 s59, s29, s35\n\t"
    "ds_read_b128 v[128:131], v251 offset:1024\n\t"
    "v_mfma_f32_16x16x32_bf16 a[204:207], v[212:215], v[108:111], 0\n\t"
    "ds_read_b128 v[132:135], v251 offset:2048\n\t"
    "v_mfma_f32_16x16x32_bf16 a[208:211], v[212:215], v[112:115], 0\n\t"
    "ds_read_b128 v[136:139], v251 offset:3072\n\t"
    "v_mfma_f32_16x16x32_bf16 a[212:215], v[212:215], v[116:119], 0\n\t"
    "ds_read_b128 v[140:143], v251 offset:4096\n\t"
    "v_mfma_f32_16x16x32_bf16 a[216:219], v[212:215], v[120:123], 0\n\t"
    "ds_read_b64_tr_b16 v[208:209], v249 offset:8\n\t"
    "v_mfma_f32_16x16x32_bf16 a[220:223], v[216:219], v[104:107], 0\n\t"
    "ds_read_b64_tr_b16 v[210:211], v249 offset:1544\n\t"
    "v_mfma_f32_16x16x32_bf16 a[224:227], v[216:219], v[108:111], 0\n\t"
    "ds_read_b64_tr_b16 v[212:213], v250 offset:0\n\t"
    "v_mfma_f32_16x16x32_bf16 a[228:231], v[216:219], v[112:115], 0\n\t"
    "ds_read_b64_tr_b16 v[214:215], v250 offset:1536\n\t"
    "v_mfma_f32_16x16x32_bf16 a[232:235], v[216:219], v[116:119], 0\n\t"
    "v_mfma_f32_16x16x32_bf16 a[236:239], v[216:219], v[120:123], 0\n\t"
    "s_waitcnt lgkmcnt(8)\n\t"
    "ds_read_b64_tr_b16 v[216:217], v250 offset:8\n\t"
    "v_mfma_f32_16x16x32_bf16 a[0:3], v[204:207], v[124:127], a[0:3]\n\t"
    "s_waitcnt lgkmcnt(8)\n\t"
    "s_add_u32 m0, s58, 0\n\t"
    "ds_read_b64_tr_b16 v[218:219], v250 offset:1544\n\t"
    "v_mfma_f32_16x16x32_bf16 a[4:7], v[204:207], v[128:131], a[4:7]\n\t"
    "s_waitcnt lgkmcnt(8)\n\t"
    "buffer_load_dwordx4 v245, s[16:19], s38 offen lds\n\t"
    "v_mfma_f32_16x16x32_bf16 a[8:11], v[204:207], v[132:135], a[8:11]\n\t"
    "s_waitcnt lgkmcnt(7)\n\t"
    "s_add_u32 s30, s28, 24576\n\t"
    "v_mfma_f32_16x16x32_bf16 a[12:15], v[204:207], v[136:139], a[12:15]\n\t"
    "s_waitcnt lgkmcnt(6)\n\t"
    "s_cmp_eq_u32 s30, 73728\n\t"
    "s_cselect_b32 s30, 0, s30\n\t"
    "v_mfma_f32_16x16x32_bf16 a[16:19], v[204:207], v[140:143], a[16:19]\n\t"
    "s_waitcnt lgkmcnt(4)\n\t"
    "ds_read_b64_tr_b16 v[204:205], v249 offset:128\n\t"
    "v_mfma_f32_16x16x32_bf16 a[20:23], v[208:211], v[124:127], a[20:23]\n\t"
    "s_sub_u32 s31, s30, s28\n\t"
    "ds_read_b64_tr_b16 v[206:207], v249 offset:1664\n\t"
    "v_mfma_f32_16x16x32_bf16 a[24:27], v[208:211], v[128:131], a[24:27]\n\t"
    "buffer_load_dwordx4 v246, s[16:19], s38 offen offset:1024 lds\n\t"
    "v_mfma_f32_16x16x32_bf16 a[28:31], v[208:211], v[132:135], a[28:31]\n\t"
    "v_add_u32 v251, s31, v251\n\t"
    "v_mfma_f32_16x16x32_bf16 a[32:35], v[208:211], v[136:139], a[32:35]\n\t"
    "v_mfma_f32_16x16x32_bf16 a[36:39], v[208:211], v[140:143], a[36:39]\n\t"
    "s_waitcnt lgkmcnt(4)\n\t"
    "ds_read_b64_tr_b16 v[208:209], v249 offset:136\n\t"
    "v_mfma_f32_16x16x32_bf16 a[40:43], v[212:215], v[124:127], a[40:43]\n\t"
    "ds_read_b64_tr_b16 v[210:211], v249 offset:1672\n\t"
    "v_mfma_f32_16x16x32_bf16 a[44:47], v[212:215], v[128:131], a[44:47]\n\t"
    "buffer_load_dwordx4 v247, s[16:19], s38 offen offset:2048 lds\n\t"
    "v_mfma_f32_16x16x32_bf16 a[48:51], v[212:215], v[132:135], a[48:51]\n\t"
    "v_mfma_f32_16x16x32_bf16 a[52:55], v[212:215], v[136:139], a[52:55]\n\t"
    "v_mfma_f32_16x16x32_bf16 a[56:59], v[212:215], v[140:143], a[56:59]\n\t"
    "s_waitcnt lgkmcnt(4)\n\t"
    "ds_read_b64_tr_b16 v[212:213], v250 offset:128\n\t"
    "v_mfma_f32_16x16x32_bf16 a[60:63], v[216:219], v[124:127], a[60:63]\n\t"
    "s_add_u32 m0, s58, 3072\n\t"
    "ds_read_b64_tr_b16 v[214:215], v250 offset:1664\n\t"
    "v_mfma_f32_16x16x32_bf16 a[64:67], v[216:219], v[128:131], a[64:67]\n\t"
    "s_add_u32 s40, s38, 98304\n\t"
    "buffer_load_dwordx4 v245, s[16:19], s40 offen lds\n\t"
    "v_mfma_f32_16x16x32_bf16 a[68:71], v[216:219], v[132:135], a[68:71]\n\t"
    "v_mfma_f32_16x16x32_bf16 a[72:75], v[216:219], v[136:139], a[72:75]\n\t"
    "v_mfma_f32_16x16x32_bf16 a[76:79], v[216:219], v[140:143], a[76:79]\n\t"
    "s_waitcnt lgkmcnt(4)\n\t"
    "ds_read_b64_tr_b16 v[216:217], v250 offset:136\n\t"
    "v_mfma_f32_16x16x32_bf16 a[80:83], v[204:207], v[124:127], a[80:83]\n\t"
    "ds_read_b64_tr_b16 v[218:219], v250 offset:1672\n\t"
    "v_mfma_f32_16x16x32_bf16 a[84:87], v[204:207], v[128:131], a[84:87]\n\t"
    "buffer_load_dwordx4 v246, s[16:19], s40 offen offset:1024 lds\n\t"
    "v_mfma_f32_16x16x32_bf16 a[88:91], v[204:207], v[132:135], a[88:91]\n\t"
    "v_mfma_f32_16x16x32_bf16 a[92:95], v[204:207], v[136:139], a[92:95]\n\t"
    "v_mfma_f32_16x16x32_bf16 a[96:99], v[204:207], v[140:143], a[96:99]\n\t"
    "s_waitcnt lgkmcnt(4)\n\t"
    "ds_read_b64_tr_b16 v[204:205], v249 offset:256\n\t"
    "v_mfma_f32_16x16x32_bf16 a[100:103], v[208:211], v[124:127], a[100:103]\n\t"
    "ds_read_b64_tr_b16 v[206:207], v249 offset:1792\n\t"
    "v_mfma_f32_16x16x32_bf16 a[104:107], v[208:211], v[128:131], a[104:107]\n\t"
    "buffer_load_dwordx4 v247, s[16:19], s40 offen offset:2048 lds\n\t"
    "v_mfma_f32_16x16x32_bf16 a[108:111], v[208:211], v[132:135], a[108:111]\n\t"
    "v_mfma_f32_16x16x32_bf16 a[112:115], v[208:211], v[136:139], a[112:115]\n\t"
    "v_mfma_f32_16x16x32_bf16 a[116:119], v[208:211], v[140:143], a[116:119]\n\t"
    "s_waitcnt lgkmcnt(4)\n\t"
    "ds_read_b64_tr_b16 v[208:209], v249 offset:264\n\t"
    "v_mfma_f32_16x16x32_bf16 a[120:123], v[212:215], v[124:127], a[120:123]\n\t"
    "s_add_u32 m0, s59, 0\n\t"
    "ds_read_b64_tr_b16 v[210:211], v249 offset:1800\n\t"
    "v_mfma_f32_16x16x32_bf16 a[124:127], v[212:215], v[128:131], a[124:127]\n\t"
    "buffer_load_dwordx4 v248, s[20:23], s39 offen lds\n\t"
    "v_mfma_f32_16x16x32_bf16 a[128:131], v[212:215], v[132:135], a[128:131]\n\t"
    "v_mfma_f32_16x16x32_bf16 a[132:135], v[212:215], v[136:139], a[132:135]\n\t"
    "v_mfma_f32_16x16x32_bf16 a[136:139], v[212:215], v[140:143], a[136:139]\n\t"
    "s_waitcnt lgkmcnt(4)\n\t"
    "ds_read_b64_tr_b16 v[212:213], v250 offset:256\n\t"
    "v_mfma_f32_16x16x32_bf16 a[140:143], v[216:219], v[124:127], a[140:143]\n\t"
    "s_add_u32 m0, s59, 4096\n\t"
    "ds_read_b64_tr_b16 v[214:215], v250 offset:1792\n\t"
    "v_mfma_f32_16x16x32_bf16 a[144:147], v[216:219], v[128:131], a[144:147]\n\t"
    "s_add_u32 s41, s39, 81920\n\t"
    "buffer_load_dwordx4 v248, s[20:23], s41 offen lds\n\t"
    "v_mfma_f32_16x16x32_bf16 a[148:151], v[216:219], v[132:135], a[148:151]\n\t"
    "v_mfma_f32_16x16x32_bf16 a[152:155], v[216:219], v[136:139], a[152:155]\n\t"
    "v_mfma_f32_16x16x32_bf16 a[156:159], v[216:219], v[140:143], a[156:159]\n\t"
    "s_waitcnt lgkmcnt(4)\n\t"
    "ds_read_b64_tr_b16 v[216:217], v250 offset:264\n\t"
    "v_mfma_f32_16x16x32_bf16 a[160:163], v[204:207], v[124:127], a[160:163]\n\t"
    "s_add_u32 m0, s59, 8192\n\t"
    "ds_read_b64_tr_b16 v[218:219], v250 offset:1800\n\t"
    "v_mfma_f32_16x16x32_bf16 a[164:167], v[204:207], v[128:131], a[164:167]\n\t"
    "s_add_u32 s40, s39, 163840\n\t"
    "buffer_load_dwordx4 v248, s[20:23], s40 offen lds\n\t"
    "v_mfma_f32_16x16x32_bf16 a[168:171], v[204:207], v[132:135], a[168:171]\n\t"
    "s_add_u32 s38, s38, 393216\n\t"
    "v_add_u32 v249, s31, v249\n\t"
    "v_add_u32 v250, s31, v250\n\t"
    "v_mfma_f32_16x16x32_bf16 a[172:175], v[204:207], v[136:139], a[172:175]\n\t"
    "s_add_u32 s39, s39, 64\n\t"
    "v_mfma_f32_16x16x32_bf16 a[176:179], v[204:207], v[140:143], a[176:179]\n\t"
    "s_waitcnt lgkmcnt(4)\n\t"
    "v_mfma_f32_16x16x32_bf16 a[180:183], v[208:211], v[124:127], a[180:183]\n\t"
    "v_mfma_f32_16x16x32_bf16 a[184:187], v[208:211], v[128:131], a[184:187]\n\t"
    "s_cmp_eq_u32 s45, 0\n\t"
    "s_cbranch_scc0 .Lk1_w1k1\n\t"
    "s_waitcnt vmcnt(10)\n\t"
    ".Lk1_w1k1:\n\t"
    "s_waitcnt vmcnt(40) lgkmcnt(0)\n\t"
    "s_barrier\n\t"
    "v_mfma_f32_16x16x32_bf16 a[188:191], v[208:211], v[132:135], a[188:191]\n\t"
    "s_mov_b32 s29, s28\n\t"
    "ds_read_b64_tr_b16 v[204:205], v249 offset:0\n\t"
    "v_mfma_f32_16x16x32_bf16 a[192:195], v[208:211], v[136:139], a[192:195]\n\t"
    "s_mov_b32 s28, s30\n\t"
    "ds_read_b64_tr_b16 v[206:207], v249 offset:1536\n\t"
    "v_mfma_f32_16x16x32_bf16 a[196:199], v[208:211], v[140:143], a[196:199]\n\t"
    "s_add_u32 s58, s29, s34\n\t"
    "ds_read_b128 v[104:107], v251 offset:0\n\t"
    "v_mfma_f32_16x16x32_bf16 a[200:203], v[212:215], v[124:127], a[200:203]\n\t"
    "s_add_u32 s59, s29, s35\n\t"
    "ds_read_b128 v[108:111], v251 offset:1024\n\t"
    "v_mfma_f32_16x16x32_bf16 a[204:207], v[212:215], v[128:131], a[204:207]\n\t"
    "ds_read_b128 v[112:115], v251 offset:2048\n\t"
    "v_mfma_f32_16x16x32_bf16 a[208:211], v[212:215], v[132:135], a[208:211]\n\t"
    "ds_read_b128 v[116:119], v251 offset:3072\n\t"
    "v_mfma_f32_16x16x32_bf16 a[212:215], v[212:215], v[136:139], a[212:215]\n\t"
    "ds_read_b128 v[120:123], v251 offset:4096\n\t"
    "v_mfma_f32_16x16x32_bf16 a[216:219], v[212:215], v[140:143], a[216:219]\n\t"
    "ds_read_b64_tr_b16 v[208:209], v249 offset:8\n\t"
    "v_mfma_f32_16x16x32_bf16 a[220:223], v[216:219], v[124:127], a[220:223]\n\t"
    "ds_read_b64_tr_b16 v[210:211], v249 offset:1544\n\t"
    "v_mfma_f32_16x16x32_bf16 a[224:227], v[216:219], v[128:131], a[224:227]\n\t"
    "ds_read_b64_tr_b16 v[212:213], v250 offset:0\n\t"
    "v_mfma_f32_16x16x32_bf16 a[228:231], v[216:219], v[132:135], a[228:231]\n\t"
    "ds_read_b64_tr_b16 v[214:215], v250 offset:1536\n\t"
    "v_mfma_f32_16x16x32_bf16 a[232:235], v[216:219], v[136:139], a[232:235]\n\t"
    "v_mfma_f32_16x16x32_bf16 a[236:239], v[216:219], v[140:143], a[236:239]\n\t"
    "s_waitcnt lgkmcnt(8)\n\t"
    "ds_read_b64_tr_b16 v[216:217], v250 offset:8\n\t"
    "s_mov_b32 s44, 8\n\t"
    ".Lk1_qloop:\n\t"
    "v_mfma_f32_16x16x32_bf16 a[0:3], v[204:207], v[104:107], a[0:3]\n\t"
    "s_waitcnt lgkmcnt(8)\n\t"
    "s_add_u32 m0, s58, 0\n\t"
    "ds_read_b64_tr_b16 v[218:219], v250 offset:1544\n\t"
    "v_mfma_f32_16x16x32_bf16 a[4:7], v[204:207], v[108:111], a[4:7]\n\t"
    "s_waitcnt lgkmcnt(8)\n\t"
    "buffer_load_dwordx4 v245, s[16:19], s38 offen lds\n\t"
    "v_mfma_f32_16x16x32_bf16 a[8:11], v[204:207], v[112:115], a[8:11]\n\t"
    "s_waitcnt lgkmcnt(7)\n\t"
    "s_add_u32 s30, s28, 24576\n\t"
    "v_mfma_f32_16x16x32_bf16 a[12:15], v[204:207], v[116:119], a[12:15]\n\t"
    "s_waitcnt lgkmcnt(6)\n\t"
    "s_cmp_eq_u32 s30, 73728\n\t"
    "s_cselect_b32 s30, 0, s30\n\t"
    "v_mfma_f32_16x16x32_bf16 a[16:19], v[204:207], v[120:123], a[16:19]\n\t"
    "s_waitcnt lgkmcnt(4)\n\t"
    "ds_read_b64_tr_b16 v[204:205], v249 offset:128\n\t"
    "v_mfma_f32_16x16x32_bf16 a[20:23], v[208:211], v[104:107], a[20:23]\n\t"
    "s_sub_u32 s31, s30, s28\n\t"
    "ds_read_b64_tr_b16 v[206:207], v249 offset:1664\n\t"
    "v_mfma_f32_16x16x32_bf16 a[24:27], v[208:211], v[108:111], a[24:27]\n\t"
    "buffer_load_dwordx4 v246, s[16:19], s38 offen offset:1024 lds\n\t"
    "v_mfma_f32_16x16x32_bf16 a[28:31], v[208:211], v[112:115], a[28:31]\n\t"
    "v_add_u32 v251, s31, v251\n\t"
    "v_mfma_f32_16x16x32_bf16 a[32:35], v[208:211], v[116:119], a[32:35]\n\t"
    "v_mfma_f32_16x16x32_bf16 a[36:39], v[208:211], v[120:123], a[36:39]\n\t"
    "s_waitcnt lgkmcnt(4)\n\t"
    "ds_read_b64_tr_b16 v[208:209], v249 offset:136\n\t"
    "v_mfma_f32_16x16x32_bf16 a[40:43], v[212:215], v[104:107], a[40:43]\n\t"
    "ds_read_b64_tr_b16 v[210:211], v249 offset:1672\n\t"
    "v_mfma_f32_16x16x32_bf16 a[44:47], v[212:215], v[108:111], a[44:47]\n\t"
    "buffer_load_dwordx4 v247, s[16:19], s38 offen offset:2048 lds\n\t"
    "v_mfma_f32_16x16x32_bf16 a[48:51], v[212:215], v[112:115], a[48:51]\n\t"
    "v_mfma_f32_16x16x32_bf16 a[52:55], v[212:215], v[116:119], a[52:55]\n\t"
    "v_mfma_f32_16x16x32_bf16 a[56:59], v[212:215], v[120:123], a[56:59]\n\t"
    "s_waitcnt lgkmcnt(4)\n\t"
    "ds_read_b64_tr_b16 v[212:213], v250 offset:128\n\t"
    "v_mfma_f32_16x16x32_bf16 a[60:63], v[216:219], v[104:107], a[60:63]\n\t"
    "s_add_u32 m0, s58, 3072\n\t"
    "ds_read_b64_tr_b16 v[214:215], v250 offset:1664\n\t"
    "v_mfma_f32_16x16x32_bf16 a[64:67], v[216:219], v[108:111], a[64:67]\n\t"
    "s_add_u32 s40, s38, 98304\n\t"
    "buffer_load_dwordx4 v245, s[16:19], s40 offen lds\n\t"
    "v_mfma_f32_16x16x32_bf16 a[68:71], v[216:219], v[112:115], a[68:71]\n\t"
    "v_mfma_f32_16x16x32_bf16 a[72:75], v[216:219], v[116:119], a[72:75]\n\t"
    "v_mfma_f32_16x16x32_bf16 a[76:79], v[216:219], v[120:123], a[76:79]\n\t"
    "s_waitcnt lgkmcnt(4)\n\t"
    "ds_read_b64_tr_b16 v[216:217], v250 offset:136\n\t"
    "v_mfma_f32_16x16x32_bf16 a[80:83], v[204:207], v[104:107], a[80:83]\n\t"
    "ds_read_b64_tr_b16 v[218:219], v250 offset:1672\n\t"
    "v_mfma_f32_16x16x32_bf16 a[84:87], v[204:207], v[108:111], a[84:87]\n\t"
    "buffer_load_dwordx4 v246, s[16:19], s40 offen offset:1024 lds\n\t"
    "v_mfma_f32_16x16x32_bf16 a[88:91], v[204:207], v[112:115], a[88:91]\n\t"
    "v_mfma_f32_16x16x32_bf16 a[92:95], v[204:207], v[116:119], a[92:95]\n\t"
    "v_mfma_f32_16x16x32_bf16 a[96:99], v[204:207], v[120:123], a[96:99]\n\t"
    "s_waitcnt lgkmcnt(4)\n\t"
    "ds_read_b64_tr_b16 v[204:205], v249 offset:256\n\t"
    "v_mfma_f32_16x16x32_bf16 a[100:103], v[208:211], v[104:107], a[100:103]\n\t"
    "ds_read_b64_tr_b16 v[206:207], v249 offset:1792\n\t"
    "v_mfma_f32_16x16x32_bf16 a[104:107], v[208:211], v[108:111], a[104:107]\n\t"
    "buffer_load_dwordx4 v247, s[16:19], s40 offen offset:2048 lds\n\t"
    "v_mfma_f32_16x16x32_bf16 a[108:111], v[208:211], v[112:115], a[108:111]\n\t"
    "v_mfma_f32_16x16x32_bf16 a[112:115], v[208:211], v[116:119], a[112:115]\n\t"
    "v_mfma_f32_16x16x32_bf16 a[116:119], v[208:211], v[120:123], a[116:119]\n\t"
    "s_waitcnt lgkmcnt(4)\n\t"
    "ds_read_b64_tr_b16 v[208:209], v249 offset:264\n\t"
    "v_mfma_f32_16x16x32_bf16 a[120:123], v[212:215], v[104:107], a[120:123]\n\t"
    "s_add_u32 m0, s59, 0\n\t"
    "ds_read_b64_tr_b16 v[210:211], v249 offset:1800\n\t"
    "v_mfma_f32_16x16x32_bf16 a[124:127], v[212:215], v[108:111], a[124:127]\n\t"
    "buffer_load_dwordx4 v248, s[20:23], s39 offen lds\n\t"
    "v_mfma_f32_16x16x32_bf16 a[128:131], v[212:215], v[112:115], a[128:131]\n\t"
    "v_mfma_f32_16x16x32_bf16 a[132:135], v[212:215], v[116:119], a[132:135]\n\t"
    "v_mfma_f32_16x16x32_bf16 a[136:139], v[212:215], v[120:123], a[136:139]\n\t"
    "s_waitcnt lgkmcnt(4)\n\t"
    "ds_read_b64_tr_b16 v[212:213], v250 offset:256\n\t"
    "v_mfma_f32_16x16x32_bf16 a[140:143], v[216:219], v[104:107], a[140:143]\n\t"
    "s_add_u32 m0, s59, 4096\n\t"
    "ds_read_b64_tr_b16 v[214:215], v250 offset:1792\n\t"
    "v_mfma_f32_16x16x32_bf16 a[144:147], v[216:219], v[108:111], a[144:147]\n\t"
    "s_add_u32 s41, s39, 81920\n\t"
    "buffer_load_dwordx4 v248, s[20:23], s41 offen lds\n\t"
    "v_mfma_f32_16x16x32_bf16 a[148:151], v[216:219], v[112:115], a[148:151]\n\t"
    "v_mfma_f32_16x16x32_bf16 a[152:155], v[216:219], v[116:119], a[152:155]\n\t"
    "v_mfma_f32_16x16x32_bf16 a[156:159], v[216:219], v[120:123], a[156:159]\n\t"
    "s_waitcnt lgkmcnt(4)\n\t"
    "ds_read_b64_tr_b16 v[216:217], v250 offset:264\n\t"
    "v_mfma_f32_16x16x32_bf16 a[160:163], v[204:207], v[104:107], a[160:163]\n\t"
    "s_add_u32 m0, s59, 8192\n\t"
    "ds_read_b64_tr_b16 v[218:219], v250 offset:1800\n\t"
    "v_mfma_f32_16x16x32_bf16 a[164:167], v[204:207], v[108:111], a[164:167]\n\t"
    "s_add_u32 s40, s39, 163840\n\t"
    "buffer_load_dwordx4 v248, s[20:23], s40 offen lds\n\t"
    "v_mfma_f32_16x16x32_bf16 a[168:171], v[204:207], v[112:115], a[168:171]\n\t"
    "s_add_u32 s38, s38, 393216\n\t"
    "v_add_u32 v249, s31, v249\n\t"
    "v_add_u32 v250, s31, v250\n\t"
    "v_mfma_f32_16x16x32_bf16 a[172:175], v[204:207], v[116:119], a[172:175]\n\t"
    "s_add_u32 s39, s39, 64\n\t"
    "v_mfma_f32_16x16x32_bf16 a[176:179], v[204:207], v[120:123], a[176:179]\n\t"
    "s_waitcnt lgkmcnt(4)\n\t"
    "v_mfma_f32_16x16x32_bf16 a[180:183], v[208:211], v[104:107], a[180:183]\n\t"
    "v_mfma_f32_16x16x32_bf16 a[184:187], v[208:211], v[108:111], a[184:187]\n\t"
    "s_waitcnt vmcnt(9) lgkmcnt(0)\n\t"
    "s_barrier\n\t"
    "v_mfma_f32_16x16x32_bf16 a[188:191], v[208:211], v[112:115], a[188:191]\n\t"
    "s_mov_b32 s29, s28\n\t"
    "ds_read_b64_tr_b16 v[204:205], v249 offset:0\n\t"
    "v_mfma_f32_16x16x32_bf16 a[192:195], v[208:211], v[116:119], a[192:195]\n\t"
    "s_mov_b32 s28, s30\n\t"
    "ds_read_b64_tr_b16 v[206:207], v249 offset:1536\n\t"
    "v_mfma_f32_16x16x32_bf16 a[196:199], v[208:211], v[120:123], a[196:199]\n\t"
    "s_add_u32 s58, s29, s34\n\t"
    "ds_read_b128 v[124:127], v251 offset:0\n\t"
    "v_mfma_f32_16x16x32_bf16 a[200:203], v[212:215], v[104:107], a[200:203]\n\t"
    "s_add_u32 s59, s29, s35\n\t"
    "ds_read_b128 v[128:131], v251 offset:1024\n\t"
    "v_mfma_f32_16x16x32_bf16 a[204:207], v[212:215], v[108:111], a[204:207]\n\t"
    "ds_read_b128 v[132:135], v251 offset:2048\n\t"
    "v_mfma_f32_16x16x32_bf16 a[208:211], v[212:215], v[112:115], a[208:211]\n\t"
    "ds_read_b128 v[136:139], v251 offset:3072\n\t"
    "v_mfma_f32_16x16x32_bf16 a[212:215], v[212:215], v[116:119], a[212:215]\n\t"
    "ds_read_b128 v[140:143], v251 offset:4096\n\t"
    "v_mfma_f32_16x16x32_bf16 a[216:219], v[212:215], v[120:123], a[216:219]\n\t"
    "ds_read_b64_tr_b16 v[208:209], v249 offset:8\n\t"
    "v_mfma_f32_16x16x32_bf16 a[220:223], v[216:219], v[104:107], a[220:223]\n\t"
    "ds_read_b64_tr_b16 v[210:211], v249 offset:1544\n\t"
    "v_mfma_f32_16x16x32_bf16 a[224:227], v[216:219], v[108:111], a[224:227]\n\t"
    "ds_read_b64_tr_b16 v[212:213], v250 offset:0\n\t"
    "v_mfma_f32_16x16x32_bf16 a[228:231], v[216:219], v[112:115], a[228:231]\n\t"
    "ds_read_b64_tr_b16 v[214:215], v250 offset:1536\n\t"
    "v_mfma_f32_16x16x32_bf16 a[232:235], v[216:219], v[116:119], a[232:235]\n\t"
    "v_mfma_f32_16x16x32_bf16 a[236:239], v[216:219], v[120:123], a[236:239]\n\t"
    "s_waitcnt lgkmcnt(8)\n\t"
    "ds_read_b64_tr_b16 v[216:217], v250 offset:8\n\t"
    "v_mfma_f32_16x16x32_bf16 a[0:3], v[204:207], v[124:127], a[0:3]\n\t"
    "s_waitcnt lgkmcnt(8)\n\t"
    "s_add_u32 m0, s58, 0\n\t"
    "ds_read_b64_tr_b16 v[218:219], v250 offset:1544\n\t"
    "v_mfma_f32_16x16x32_bf16 a[4:7], v[204:207], v[128:131], a[4:7]\n\t"
    "s_waitcnt lgkmcnt(8)\n\t"
    "buffer_load_dwordx4 v245, s[16:19], s38 offen lds\n\t"
    "v_mfma_f32_16x16x32_bf16 a[8:11], v[204:207], v[132:135], a[8:11]\n\t"
    "s_waitcnt lgkmcnt(7)\n\t"
    "s_add_u32 s30, s28, 24576\n\t"
    "v_mfma_f32_16x16x32_bf16 a[12:15], v[204:207], v[136:139], a[12:15]\n\t"
    "s_waitcnt lgkmcnt(6)\n\t"
    "s_cmp_eq_u32 s30, 73728\n\t"
    "s_cselect_b32 s30, 0, s30\n\t"
    "v_mfma_f32_16x16x32_bf16 a[16:19], v[204:207], v[140:143], a[16:19]\n\t"
    "s_waitcnt lgkmcnt(4)\n\t"
    "ds_read_b64_tr_b16 v[204:205], v249 offset:128\n\t"
    "v_mfma_f32_16x16x32_bf16 a[20:23], v[208:211], v[124:127], a[20:23]\n\t"
    "s_sub_u32 s31, s30, s28\n\t"
    "ds_read_b64_tr_b16 v[206:207], v249 offset:1664\n\t"
    "v_mfma_f32_16x16x32_bf16 a[24:27], v[208:211], v[128:131], a[24:27]\n\t"
    "buffer_load_dwordx4 v246, s[16:19], s38 offen offset:1024 lds\n\t"
    "v_mfma_f32_16x16x32_bf16 a[28:31], v[208:211], v[132:135], a[28:31]\n\t"
    "v_add_u32 v251, s31, v251\n\t"
    "v_mfma_f32_16x16x32_bf16 a[32:35], v[208:211], v[136:139], a[32:35]\n\t"
    "v_mfma_f32_16x16x32_bf16 a[36:39], v[208:211], v[140:143], a[36:39]\n\t"
    "s_waitcnt lgkmcnt(4)\n\t"
    "ds_read_b64_tr_b16 v[208:209], v249 offset:136\n\t"
    "v_mfma_f32_16x16x32_bf16 a[40:43], v[212:215], v[124:127], a[40:43]\n\t"
    "ds_read_b64_tr_b16 v[210:211], v249 offset:1672\n\t"
    "v_mfma_f32_16x16x32_bf16 a[44:47], v[212:215], v[128:131], a[44:47]\n\t"
    "buffer_load_dwordx4 v247, s[16:19], s38 offen offset:2048 lds\n\t"
    "v_mfma_f32_16x16x32_bf16 a[48:51], v[212:215], v[132:135], a[48:51]\n\t"
    "v_mfma_f32_16x16x32_bf16 a[52:55], v[212:215], v[136:139], a[52:55]\n\t"
    "v_mfma_f32_16x16x32_bf16 a[56:59], v[212:215], v[140:143], a[56:59]\n\t"
    "s_waitcnt lgkmcnt(4)\n\t"
    "ds_read_b64_tr_b16 v[212:213], v250 offset:128\n\t"
    "v_mfma_f32_16x16x32_bf16 a[60:63], v[216:219], v[124:127], a[60:63]\n\t"
    "s_add_u32 m0, s58, 3072\n\t"
    "ds_read_b64_tr_b16 v[214:215], v250 offset:1664\n\t"
    "v_mfma_f32_16x16x32_bf16 a[64:67], v[216:219], v[128:131], a[64:67]\n\t"
    "s_add_u32 s40, s38, 98304\n\t"
    "buffer_load_dwordx4 v245, s[16:19], s40 offen lds\n\t"
    "v_mfma_f32_16x16x32_bf16 a[68:71], v[216:219], v[132:135], a[68:71]\n\t"
    "v_mfma_f32_16x16x32_bf16 a[72:75], v[216:219], v[136:139], a[72:75]\n\t"
    "v_mfma_f32_16x16x32_bf16 a[76:79], v[216:219], v[140:143], a[76:79]\n\t"
    "s_waitcnt lgkmcnt(4)\n\t"
    "ds_read_b64_tr_b16 v[216:217], v250 offset:136\n\t"
    "v_mfma_f32_16x16x32_bf16 a[80:83], v[204:207], v[124:127], a[80:83]\n\t"
    "ds_read_b64_tr_b16 v[218:219], v250 offset:1672\n\t"
    "v_mfma_f32_16x16x32_bf16 a[84:87], v[204:207], v[128:131], a[84:87]\n\t"
    "buffer_load_dwordx4 v246, s[16:19], s40 offen offset:1024 lds\n\t"
    "v_mfma_f32_16x16x32_bf16 a[88:91], v[204:207], v[132:135], a[88:91]\n\t"
    "v_mfma_f32_16x16x32_bf16 a[92:95], v[204:207], v[136:139], a[92:95]\n\t"
    "v_mfma_f32_16x16x32_bf16 a[96:99], v[204:207], v[140:143], a[96:99]\n\t"
    "s_waitcnt lgkmcnt(4)\n\t"
    "ds_read_b64_tr_b16 v[204:205], v249 offset:256\n\t"
    "v_mfma_f32_16x16x32_bf16 a[100:103], v[208:211], v[124:127], a[100:103]\n\t"
    "ds_read_b64_tr_b16 v[206:207], v249 offset:1792\n\t"
    "v_mfma_f32_16x16x32_bf16 a[104:107], v[208:211], v[128:131], a[104:107]\n\t"
    "buffer_load_dwordx4 v247, s[16:19], s40 offen offset:2048 lds\n\t"
    "v_mfma_f32_16x16x32_bf16 a[108:111], v[208:211], v[132:135], a[108:111]\n\t"
    "v_mfma_f32_16x16x32_bf16 a[112:115], v[208:211], v[136:139], a[112:115]\n\t"
    "v_mfma_f32_16x16x32_bf16 a[116:119], v[208:211], v[140:143], a[116:119]\n\t"
    "s_waitcnt lgkmcnt(4)\n\t"
    "ds_read_b64_tr_b16 v[208:209], v249 offset:264\n\t"
    "v_mfma_f32_16x16x32_bf16 a[120:123], v[212:215], v[124:127], a[120:123]\n\t"
    "s_add_u32 m0, s59, 0\n\t"
    "ds_read_b64_tr_b16 v[210:211], v249 offset:1800\n\t"
    "v_mfma_f32_16x16x32_bf16 a[124:127], v[212:215], v[128:131], a[124:127]\n\t"
    "buffer_load_dwordx4 v248, s[20:23], s39 offen lds\n\t"
    "v_mfma_f32_16x16x32_bf16 a[128:131], v[212:215], v[132:135], a[128:131]\n\t"
    "v_mfma_f32_16x16x32_bf16 a[132:135], v[212:215], v[136:139], a[132:135]\n\t"
    "v_mfma_f32_16x16x32_bf16 a[136:139], v[212:215], v[140:143], a[136:139]\n\t"
    "s_waitcnt lgkmcnt(4)\n\t"
    "ds_read_b64_tr_b16 v[212:213], v250 offset:256\n\t"
    "v_mfma_f32_16x16x32_bf16 a[140:143], v[216:219], v[124:127], a[140:143]\n\t"
    "s_add_u32 m0, s59, 4096\n\t"
    "ds_read_b64_tr_b16 v[214:215], v250 offset:1792\n\t"
    "v_mfma_f32_16x16x32_bf16 a[144:147], v[216:219], v[128:131], a[144:147]\n\t"
    "s_add_u32 s41, s39, 81920\n\t"
    "buffer_load_dwordx4 v248, s[20:23], s41 offen lds\n\t"
    "v_mfma_f32_16x16x32_bf16 a[148:151], v[216:219], v[132:135], a[148:151]\n\t"
    "v_mfma_f32_16x16x32_bf16 a[152:155], v[216:219], v[136:139], a[152:155]\n\t"
    "v_mfma_f32_16x16x32_bf16 a[156:159], v[216:219], v[140:143], a[156:159]\n\t"
    "s_waitcnt lgkmcnt(4)\n\t"
    "ds_read_b64_tr_b16 v[216:217], v250 offset:264\n\t"
    "v_mfma_f32_16x16x32_bf16 a[160:163], v[204:207], v[124:127], a[160:163]\n\t"
    "s_add_u32 m0, s59, 8192\n\t"
    "ds_read_b64_tr_b16 v[218:219], v250 offset:1800\n\t"
    "v_mfma_f32_16x16x32_bf16 a[164:167], v[204:207], v[128:131], a[164:167]\n\t"
    "s_add_u32 s40, s39, 163840\n\t"
    "buffer_load_dwordx4 v248, s[20:23], s40 offen lds\n\t"
    "v_mfma_f32_16x16x32_bf16 a[168:171], v[204:207], v[132:135], a[168:171]\n\t"
    "s_add_u32 s38, s38, 393216\n\t"
    "v_add_u32 v249, s31, v249\n\t"
    "v_add_u32 v250, s31, v250\n\t"
    "v_mfma_f32_16x16x32_bf16 a[172:175], v[204:207], v[136:139], a[172:175]\n\t"
    "s_add_u32 s39, s39, 64\n\t"
    "v_mfma_f32_16x16x32_bf16 a[176:179], v[204:207], v[140:143], a[176:179]\n\t"
    "s_waitcnt lgkmcnt(4)\n\t"
    "v_mfma_f32_16x16x32_bf16 a[180:183], v[208:211], v[124:127], a[180:183]\n\t"
    "v_mfma_f32_16x16x32_bf16 a[184:187], v[208:211], v[128:131], a[184:187]\n\t"
    "s_waitcnt vmcnt(9) lgkmcnt(0)\n\t"
    "s_barrier\n\t"
    "v_mfma_f32_16x16x32_bf16 a[188:191], v[208:211], v[132:135], a[188:191]\n\t"
    "s_mov_b32 s29, s28\n\t"
    "ds_read_b64_tr_b16 v[204:205], v249 offset:0\n\t"
    "v_mfma_f32_16x16x32_bf16 a[192:195], v[208:211], v[136:139], a[192:195]\n\t"
    "s_mov_b32 s28, s30\n\t"
    "ds_read_b64_tr_b16 v[206:207], v249 offset:1536\n\t"
    "v_mfma_f32_16x16x32_bf16 a[196:199], v[208:211], v[140:143], a[196:199]\n\t"
    "s_add_u32 s58, s29, s34\n\t"
    "ds_read_b128 v[104:107], v251 offset:0\n\t"
    "v_mfma_f32_16x16x32_bf16 a[200:203], v[212:215], v[124:127], a[200:203]\n\t"
    "s_add_u32 s59, s29, s35\n\t"
    "ds_read_b128 v[108:111], v251 offset:1024\n\t"
    "v_mfma_f32_16x16x32_bf16 a[204:207], v[212:215], v[128:131], a[204:207]\n\t"
    "ds_read_b128 v[112:115], v251 offset:2048\n\t"
    "v_mfma_f32_16x16x32_bf16 a[208:211], v[212:215], v[132:135], a[208:211]\n\t"
    "ds_read_b128 v[116:119], v251 offset:3072\n\t"
    "v_mfma_f32_16x16x32_bf16 a[212:215], v[212:215], v[136:139], a[212:215]\n\t"
    "ds_read_b128 v[120:123], v251 offset:4096\n\t"
    "v_mfma_f32_16x16x32_bf16 a[216:219], v[212:215], v[140:143], a[216:219]\n\t"
    "ds_read_b64_tr_b16 v[208:209], v249 offset:8\n\t"
    "v_mfma_f32_16x16x32_bf16 a[220:223], v[216:219], v[124:127], a[220:223]\n\t"
    "ds_read_b64_tr_b16 v[210:211], v249 offset:1544\n\t"
    "v_mfma_f32_16x16x32_bf16 a[224:227], v[216:219], v[128:131], a[224:227]\n\t"
    "ds_read_b64_tr_b16 v[212:213], v250 offset:0\n\t"
    "v_mfma_f32_16x16x32_bf16 a[228:231], v[216:219], v[132:135], a[228:231]\n\t"
    "ds_read_b64_tr_b16 v[214:215], v250 offset:1536\n\t"
    "v_mfma_f32_16x16x32_bf16 a[232:235], v[216:219], v[136:139], a[232:235]\n\t"
    "v_mfma_f32_16x16x32_bf16 a[236:239], v[216:219], v[140:143], a[236:239]\n\t"
    "s_waitcnt lgkmcnt(8)\n\t"
    "ds_read_b64_tr_b16 v[216:217], v250 offset:8\n\t"
    "s_sub_u32 s44, s44, 1\n\t"
    "s_cmp_lg_u32 s44, 0\n\t"
    "s_cbranch_scc1 .Lk1_qloop\n\t"
    "s_cmp_eq_u32 s45, 0\n\t"
    "s_cbranch_scc1 .Lk1_noflag\n\t"
    "s_lshr_b32 s42, s78, 6\n\t"
    "v_readlane_b32 s43, v255, s42\n\t"
    "s_xor_b32 s43, s43, 0x5a5a5a5a\n\t"
    "s_nop 1\n\t"
    "s_mov_b32 s41, 32768\n\t"
    ".Lo_pollack:\n\t"
    "v_cmp_ne_u32 vcc, s43, v1\n\t"
    "s_and_b32 s40, vcc_lo, 0x1f\n\t"
    "s_cbranch_scc0 .Lo_okack ; scc-ok\n\t"
    "s_sleep 1\n\t"
    "global_load_dword v1, v0, s[60:61] sc1\n\t"
    "s_sub_u32 s41, s41, 1\n\t"
    "s_waitcnt vmcnt(0)\n\t"
    "s_cbranch_scc0 .Lo_pollack ; scc-ok\n\t"
    ".Lo_okack:\n\t"
    "s_lshr_b32 s40, s78, 6\n\t"
    "s_add_u32 s40, s40, 0\n\t"
    "s_lshl_b64 exec, 1, s40\n\t"
    "global_store_dword v254, v255, s[60:61] sc1\n\t"
    "s_mov_b64 exec, -1\n\t"
    ".Lk1_noflag:\n\t"
    "v_mfma_f32_16x16x32_bf16 a[0:3], v[204:207], v[104:107], a[0:3]\n\t"
    "s_waitcnt lgkmcnt(8)\n\t"
    "s_add_u32 m0, s58, 0\n\t"
    "ds_read_b64_tr_b16 v[218:219], v250 offset:1544\n\t"
    "v_mfma_f32_16x16x32_bf16 a[4:7], v[204:207], v[108:111], a[4:7]\n\t"
    "s_waitcnt lgkmcnt(8)\n\t"
    "buffer_load_dwordx4 v242, s[8:11], s36 offen nt lds\n\t"
    "v_mfma_f32_16x16x32_bf16 a[8:11], v[204:207], v[112:115], a[8:11]\n\t"
    "s_waitcnt lgkmcnt(7)\n\t"
    "s_add_u32 s30, s28, 24576\n\t"
    "v_mfma_f32_16x16x32_bf16 a[12:15], v[204:207], v[116:119], a[12:15]\n\t"
    "s_waitcnt lgkmcnt(6)\n\t"
    "s_cmp_eq_u32 s30, 73728\n\t"
    "s_cselect_b32 s30, 0, s30\n\t"
    "v_mfma_f32_16x16x32_bf16 a[16:19], v[204:207], v[120:123], a[16:19]\n\t"
    "s_waitcnt lgkmcnt(4)\n\t"
    "ds_read_b64_tr_b16 v[204:205], v249 offset:128\n\t"
    "v_mfma_f32_16x16x32_bf16 a[20:23], v[208:211], v[104:107], a[20:23]\n\t"
    "s_sub_u32 s31, s30, s28\n\t"
    "ds_read_b64_tr_b16 v[206:207], v249 offset:1664\n\t"
    "v_mfma_f32_16x16x32_bf16 a[24:27], v[208:211], v[108:111], a[24:27]\n\t"
    "buffer_load_dwordx4 v243, s[8:11], s36 offen offset:1024 nt lds\n\t"
    "v_mfma_f32_16x16x32_bf16 a[28:31], v[208:211], v[112:115], a[28:31]\n\t"
    "v_add_u32 v251, s31, v251\n\t"
    "v_mfma_f32_16x16x32_bf16 a[32:35], v[208:211], v[116:119], a[32:35]\n\t"
    "buffer_load_dwordx4 v244, s[8:11], s36 offen offset:2048 nt lds\n\t"
    "v_mfma_f32_16x16x32_bf16 a[36:39], v[208:211], v[120:123], a[36:39]\n\t"
    "s_waitcnt lgkmcnt(4)\n\t"
    "ds_read_b64_tr_b16 v[208:209], v249 offset:136\n\t"
    "v_mfma_f32_16x16x32_bf16 a[40:43], v[212:215], v[104:107], a[40:43]\n\t"
    "ds_read_b64_tr_b16 v[210:211], v249 offset:1672\n\t"
    "v_mfma_f32_16x16x32_bf16 a[44:47], v[212:215], v[108:111], a[44:47]\n\t"
    "s_add_u32 m0, s58, 3072\n\t"
    "v_mfma_f32_16x16x32_bf16 a[48:51], v[212:215], v[112:115], a[48:51]\n\t"
    "s_add_u32 s40, s36, 3072\n\t"
    "buffer_load_dwordx4 v242, s[8:11], s40 offen nt lds\n\t"
    "v_mfma_f32_16x16x32_bf16 a[52:55], v[212:215], v[116:119], a[52:55]\n\t"
    "v_mfma_f32_16x16x32_bf16 a[56:59], v[212:215], v[120:123], a[56:59]\n\t"
    "s_waitcnt lgkmcnt(4)\n\t"
    "ds_read_b64_tr_b16 v[212:213], v250 offset:128\n\t"
    "v_mfma_f32_16x16x32_bf16 a[60:63], v[216:219], v[104:107], a[60:63]\n\t"
    "ds_read_b64_tr_b16 v[214:215], v250 offset:1664\n\t"
    "v_mfma_f32_16x16x32_bf16 a[64:67], v[216:219], v[108:111], a[64:67]\n\t"
    "buffer_load_dwordx4 v243, s[8:11], s40 offen offset:1024 nt lds\n\t"
    "v_mfma_f32_16x16x32_bf16 a[68:71], v[216:219], v[112:115], a[68:71]\n\t"
    "v_mfma_f32_16x16x32_bf16 a[72:75], v[216:219], v[116:119], a[72:75]\n\t"
    "v_mfma_f32_16x16x32_bf16 a[76:79], v[216:219], v[120:123], a[76:79]\n\t"
    "s_waitcnt lgkmcnt(4)\n\t"
    "ds_read_b64_tr_b16 v[216:217], v250 offset:136\n\t"
    "v_mfma_f32_16x16x32_bf16 a[80:83], v[204:207], v[104:107], a[80:83]\n\t"
    "ds_read_b64_tr_b16 v[218:219], v250 offset:1672\n\t"
    "v_mfma_f32_16x16x32_bf16 a[84:87], v[204:207], v[108:111], a[84:87]\n\t"
    "buffer_load_dwordx4 v244, s[8:11], s40 offen offset:2048 nt lds\n\t"
    "v_mfma_f32_16x16x32_bf16 a[88:91], v[204:207], v[112:115], a[88:91]\n\t"
    "s_add_u32 s36, s36, 12288\n\t"
    "v_mfma_f32_16x16x32_bf16 a[92:95], v[204:207], v[116:119], a[92:95]\n\t"
    "v_mfma_f32_16x16x32_bf16 a[96:99], v[204:207], v[120:123], a[96:99]\n\t"
    "s_waitcnt lgkmcnt(4)\n\t"
    "ds_read_b64_tr_b16 v[204:205], v249 offset:256\n\t"
    "v_mfma_f32_16x16x32_bf16 a[100:103], v[208:211], v[104:107], a[100:103]\n\t"
    "ds_read_b64_tr_b16 v[206:207], v249 offset:1792\n\t"
    "v_mfma_f32_16x16x32_bf16 a[104:107], v[208:211], v[108:111], a[104:107]\n\t"
    "v_mfma_f32_16x16x32_bf16 a[108:111], v[208:211], v[112:115], a[108:111]\n\t"
    "v_mfma_f32_16x16x32_bf16 a[112:115], v[208:211], v[116:119], a[112:115]\n\t"
    "v_mfma_f32_16x16x32_bf16 a[116:119], v[208:211], v[120:123], a[116:119]\n\t"
    "s_waitcnt lgkmcnt(4)\n\t"
    "ds_read_b64_tr_b16 v[208:209], v249 offset:264\n\t"
    "v_mfma_f32_16x16x32_bf16 a[120:123], v[212:215], v[104:107], a[120:123]\n\t"
    "ds_read_b64_tr_b16 v[210:211], v249 offset:1800\n\t"
    "v_mfma_f32_16x16x32_bf16 a[124:127], v[212:215], v[108:111], a[124:127]\n\t"
    "v_mfma_f32_16x16x32_bf16 a[128:131], v[212:215], v[112:115], a[128:131]\n\t"
    "v_mfma_f32_16x16x32_bf16 a[132:135], v[212:215], v[116:119], a[132:135]\n\t"
    "v_mfma_f32_16x16x32_bf16 a[136:139], v[212:215], v[120:123], a[136:139]\n\t"
    "s_waitcnt lgkmcnt(4)\n\t"
    "ds_read_b64_tr_b16 v[212:213], v250 offset:256\n\t"
    "v_mfma_f32_16x16x32_bf16 a[140:143], v[216:219], v[104:107], a[140:143]\n\t"
    "ds_read_b64_tr_b16 v[214:215], v250 offset:1792\n\t"
    "v_mfma_f32_16x16x32_bf16 a[144:147], v[216:219], v[108:111], a[144:147]\n\t"
    "v_mfma_f32_16x16x32_bf16 a[148:151], v[216:219], v[112:115], a[148:151]\n\t"
    "v_mfma_f32_16x16x32_bf16 a[152:155], v[216:219], v[116:119], a[152:155]\n\t"
    "v_mfma_f32_16x16x32_bf16 a[156:159], v[216:219], v[120:123], a[156:159]\n\t"
    "s_waitcnt lgkmcnt(4)\n\t"
    "ds_read_b64_tr_b16 v[216:217], v250 offset:264\n\t"
    "v_mfma_f32_16x16x32_bf16 a[160:163], v[204:207], v[104:107], a[160:163]\n\t"
    "ds_read_b64_tr_b16 v[218:219], v250 offset:1800\n\t"
    "v_mfma_f32_16x16x32_bf16 a[164:167], v[204:207], v[108:111], a[164:167]\n\t"
    "v_mfma_f32_16x16x32_bf16 a[168:171], v[204:207], v[112:115], a[168:171]\n\t"
    "v_add_u32 v249, s31, v249\n\t"
    "v_add_u32 v250, s31, v250\n\t"
    "v_mfma_f32_16x16x32_bf16 a[172:175], v[204:207], v[116:119], a[172:175]\n\t"
    "v_mfma_f32_16x16x32_bf16 a[176:179], v[204:207], v[120:123], a[176:179]\n\t"
    "s_waitcnt lgkmcnt(4)\n\t"
    "v_mfma_f32_16x16x32_bf16 a[180:183], v[208:211], v[104:107], a[180:183]\n\t"
    "v_mfma_f32_16x16x32_bf16 a[184:187], v[208:211], v[108:111], a[184:187]\n\t"
    "s_waitcnt vmcnt(6) lgkmcnt(0)\n\t"
    "s_barrier\n\t"
    "v_mfma_f32_16x16x32_bf16 a[188:191], v[208:211], v[112:115], a[188:191]\n\t"
    "s_mov_b32 s29, s28\n\t"
    "ds_read_b64_tr_b16 v[204:205], v249 offset:0\n\t"
    "v_mfma_f32_16x16x32_bf16 a[192:195], v[208:211], v[116:119], a[192:195]\n\t"
    "s_mov_b32 s28, s30\n\t"
    "ds_read_b64_tr_b16 v[206:207], v249 offset:1536\n\t"
    "v_mfma_f32_16x16x32_bf16 a[196:199], v[208:211], v[120:123], a[196:199]\n\t"
    "s_add_u32 s58, s29, s34\n\t"
    "ds_read_b128 v[124:127], v251 offset:0\n\t"
    "v_mfma_f32_16x16x32_bf16 a[200:203], v[212:215], v[104:107], a[200:203]\n\t"
    "s_add_u32 s59, s29, s35\n\t"
    "ds_read_b128 v[128:131], v251 offset:1024\n\t"
    "v_mfma_f32_16x16x32_bf16 a[204:207], v[212:215], v[108:111], a[204:207]\n\t"
    "ds_read_b128 v[132:135], v251 offset:2048\n\t"
    "v_mfma_f32_16x16x32_bf16 a[208:211], v[212:215], v[112:115], a[208:211]\n\t"
    "ds_read_b128 v[136:139], v251 offset:3072\n\t"
    "v_mfma_f32_16x16x32_bf16 a[212:215], v[212:215], v[116:119], a[212:215]\n\t"
    "ds_read_b128 v[140:143], v251 offset:4096\n\t"
    "v_mfma_f32_16x16x32_bf16 a[216:219], v[212:215], v[120:123], a[216:219]\n\t"
    "ds_read_b64_tr_b16 v[208:209], v249 offset:8\n\t"
    "v_mfma_f32_16x16x32_bf16 a[220:223], v[216:219], v[104:107], a[220:223]\n\t"
    "ds_read_b64_tr_b16 v[210:211], v249 offset:1544\n\t"
    "v_mfma_f32_16x16x32_bf16 a[224:227], v[216:219], v[108:111], a[224:227]\n\t"
    "ds_read_b64_tr_b16 v[212:213], v250 offset:0\n\t"
    "v_mfma_f32_16x16x32_bf16 a[228:231], v[216:219], v[112:115], a[228:231]\n\t"
    "ds_read_b64_tr_b16 v[214:215], v250 offset:1536\n\t"
    "v_mfma_f32_16x16x32_bf16 a[232:235], v[216:219], v[116:119], a[232:235]\n\t"
    "v_mfma_f32_16x16x32_bf16 a[236:239], v[216:219], v[120:123], a[236:239]\n\t"
    "s_waitcnt lgkmcnt(8)\n\t"
    "ds_read_b64_tr_b16 v[216:217], v250 offset:8\n\t"
    "v_mfma_f32_16x16x32_bf16 a[0:3], v[204:207], v[124:127], a[0:3]\n\t"
    "s_waitcnt lgkmcnt(8)\n\t"
    "s_add_u32 m0, s58, 0\n\t"
    "ds_read_b64_tr_b16 v[218:219], v250 offset:1544\n\t"
    "v_mfma_f32_16x16x32_bf16 a[4:7], v[204:207], v[128:131], a[4:7]\n\t"
    "s_waitcnt lgkmcnt(8)\n\t"
    "buffer_load_dwordx4 v242, s[8:11], s36 offen nt lds\n\t"
    "v_mfma_f32_16x16x32_bf16 a[8:11], v[204:207], v[132:135], a[8:11]\n\t"
    "s_waitcnt lgkmcnt(7)\n\t"
    "s_add_u32 s30, s28, 24576\n\t"
    "v_mfma_f32_16x16x32_bf16 a[12:15], v[204:207], v[136:139], a[12:15]\n\t"
    "s_waitcnt lgkmcnt(6)\n\t"
    "s_cmp_eq_u32 s30, 73728\n\t"
    "s_cselect_b32 s30, 0, s30\n\t"
    "v_mfma_f32_16x16x32_bf16 a[16:19], v[204:207], v[140:143], a[16:19]\n\t"
    "s_waitcnt lgkmcnt(4)\n\t"
    "ds_read_b64_tr_b16 v[204:205], v249 offset:128\n\t"
    "v_mfma_f32_16x16x32_bf16 a[20:23], v[208:211], v[124:127], a[20:23]\n\t"
    "s_sub_u32 s31, s30, s28\n\t"
    "ds_read_b64_tr_b16 v[206:207], v249 offset:1664\n\t"
    "v_mfma_f32_16x16x32_bf16 a[24:27], v[208:211], v[128:131], a[24:27]\n\t"
    "buffer_load_dwordx4 v243, s[8:11], s36 offen offset:1024 nt lds\n\t"
    "v_mfma_f32_16x16x32_bf16 a[28:31], v[208:211], v[132:135], a[28:31]\n\t"
    "v_mfma_f32_16x16x32_bf16 a[32:35], v[208:211], v[136:139], a[32:35]\n\t"
    "buffer_load_dwordx4 v244, s[8:11], s36 offen offset:2048 nt lds\n\t"
    "v_mfma_f32_16x16x32_bf16 a[36:39], v[208:211], v[140:143], a[36:39]\n\t"
    "s_waitcnt lgkmcnt(4)\n\t"
    "ds_read_b64_tr_b16 v[208:209], v249 offset:136\n\t"
    "v_mfma_f32_16x16x32_bf16 a[40:43], v[212:215], v[124:127], a[40:43]\n\t"
    "ds_read_b64_tr_b16 v[210:211], v249 offset:1672\n\t"
    "v_mfma_f32_16x16x32_bf16 a[44:47], v[212:215], v[128:131], a[44:47]\n\t"
    "s_add_u32 m0, s58, 3072\n\t"
    "v_mfma_f32_16x16x32_bf16 a[48:51], v[212:215], v[132:135], a[48:51]\n\t"
    "s_add_u32 s40, s36, 3072\n\t"
    "buffer_load_dwordx4 v242, s[8:11], s40 offen nt lds\n\t"
    "v_mfma_f32_16x16x32_bf16 a[52:55], v[212:215], v[136:139], a[52:55]\n\t"
    "v_mfma_f32_16x16x32_bf16 a[56:59], v[212:215], v[140:143], a[56:59]\n\t"
    "s_waitcnt lgkmcnt(4)\n\t"
    "ds_read_b64_tr_b16 v[212:213], v250 offset:128\n\t"
    "v_mfma_f32_16x16x32_bf16 a[60:63], v[216:219], v[124:127], a[60:63]\n\t"
    "ds_read_b64_tr_b16 v[214:215], v250 offset:1664\n\t"
    "v_mfma_f32_16x16x32_bf16 a[64:67], v[216:219], v[128:131], a[64:67]\n\t"
    "buffer_load_dwordx4 v243, s[8:11], s40 offen offset:1024 nt lds\n\t"
    "v_mfma_f32_16x16x32_bf16 a[68:71], v[216:219], v[132:135], a[68:71]\n\t"
    "v_mfma_f32_16x16x32_bf16 a[72:75], v[216:219], v[136:139], a[72:75]\n\t"
    "v_mfma_f32_16x16x32_bf16 a[76:79], v[216:219], v[140:143], a[76:79]\n\t"
    "s_waitcnt lgkmcnt(4)\n\t"
    "ds_read_b64_tr_b16 v[216:217], v250 offset:136\n\t"
    "v_mfma_f32_16x16x32_bf16 a[80:83], v[204:207], v[124:127], a[80:83]\n\t"
    "ds_read_b64_tr_b16 v[218:219], v250 offset:1672\n\t"
    "v_mfma_f32_16x16x32_bf16 a[84:87], v[204:207], v[128:131], a[84:87]\n\t"
    "buffer_load_dwordx4 v244, s[8:11], s40 offen offset:2048 nt lds\n\t"
    "v_mfma_f32_16x16x32_bf16 a[88:91], v[204:207], v[132:135], a[88:91]\n\t"
    "s_add_u32 m0, s58, 12288\n\t"
    "v_mfma_f32_16x16x32_bf16 a[92:95], v[204:207], v[136:139], a[92:95]\n\t"
    "buffer_load_dwordx4 v242, s[12:15], s37 offen nt lds\n\t"
    "v_mfma_f32_16x16x32_bf16 a[96:99], v[204:207], v[140:143], a[96:99]\n\t"
    "s_waitcnt lgkmcnt(4)\n\t"
    "ds_read_b64_tr_b16 v[204:205], v249 offset:256\n\t"
    "v_mfma_f32_16x16x32_bf16 a[100:103], v[208:211], v[124:127], a[100:103]\n\t"
    "ds_read_b64_tr_b16 v[206:207], v249 offset:1792\n\t"
    "v_mfma_f32_16x16x32_bf16 a[104:107], v[208:211], v[128:131], a[104:107]\n\t"
    "v_mfma_f32_16x16x32_bf16 a[108:111], v[208:211], v[132:135], a[108:111]\n\t"
    "buffer_load_dwordx4 v243, s[12:15], s37 offen offset:1024 nt lds\n\t"
    "v_mfma_f32_16x16x32_bf16 a[112:115], v[208:211], v[136:139], a[112:115]\n\t"
    "v_mfma_f32_16x16x32_bf16 a[116:119], v[208:211], v[140:143], a[116:119]\n\t"
    "s_waitcnt lgkmcnt(4)\n\t"
    "ds_read_b64_tr_b16 v[208:209], v249 offset:264\n\t"
    "v_mfma_f32_16x16x32_bf16 a[120:123], v[212:215], v[124:127], a[120:123]\n\t"
    "ds_read_b64_tr_b16 v[210:211], v249 offset:1800\n\t"
    "v_mfma_f32_16x16x32_bf16 a[124:127], v[212:215], v[128:131], a[124:127]\n\t"
    "buffer_load_dwordx4 v244, s[12:15], s37 offen offset:2048 nt lds\n\t"
    "v_mfma_f32_16x16x32_bf16 a[128:131], v[212:215], v[132:135], a[128:131]\n\t"
    "v_mfma_f32_16x16x32_bf16 a[132:135], v[212:215], v[136:139], a[132:135]\n\t"
    "v_mfma_f32_16x16x32_bf16 a[136:139], v[212:215], v[140:143], a[136:139]\n\t"
    "s_waitcnt lgkmcnt(4)\n\t"
    "ds_read_b64_tr_b16 v[212:213], v250 offset:256\n\t"
    "v_mfma_f32_16x16x32_bf16 a[140:143], v[216:219], v[124:127], a[140:143]\n\t"
    "s_add_u32 m0, s58, 15360\n\t"
    "ds_read_b64_tr_b16 v[214:215], v250 offset:1792\n\t"
    "v_mfma_f32_16x16x32_bf16 a[144:147], v[216:219], v[128:131], a[144:147]\n\t"
    "s_add_u32 s41, s37, 3072\n\t"
    "buffer_load_dwordx4 v242, s[12:15], s41 offen nt lds\n\t"
    "v_mfma_f32_16x16x32_bf16 a[148:151], v[216:219], v[132:135], a[148:151]\n\t"
    "v_mfma_f32_16x16x32_bf16 a[152:155], v[216:219], v[136:139], a[152:155]\n\t"
    "buffer_load_dwordx4 v243, s[12:15], s41 offen offset:1024 nt lds\n\t"
    "v_mfma_f32_16x16x32_bf16 a[156:159], v[216:219], v[140:143], a[156:159]\n\t"
    "s_waitcnt lgkmcnt(4)\n\t"
    "ds_read_b64_tr_b16 v[216:217], v250 offset:264\n\t"
    "v_mfma_f32_16x16x32_bf16 a[160:163], v[204:207], v[124:127], a[160:163]\n\t"
    "ds_read_b64_tr_b16 v[218:219], v250 offset:1800\n\t"
    "v_mfma_f32_16x16x32_bf16 a[164:167], v[204:207], v[128:131], a[164:167]\n\t"
    "v_mfma_f32_16x16x32_bf16 a[168:171], v[204:207], v[132:135], a[168:171]\n\t"
    "buffer_load_dwordx4 v244, s[12:15], s41 offen offset:2048 nt lds\n\t"
    "v_mfma_f32_16x16x32_bf16 a[172:175], v[204:207], v[136:139], a[172:175]\n\t"
    "s_add_u32 s36, s36, 12288\n\t"
    "v_mfma_f32_16x16x32_bf16 a[176:179], v[204:207], v[140:143], a[176:179]\n\t"
    "s_waitcnt lgkmcnt(4)\n\t"
    "s_add_u32 s37, s37, 12288\n\t"
    "v_mfma_f32_16x16x32_bf16 a[180:183], v[208:211], v[124:127], a[180:183]\n\t"
    "s_mov_b32 s47, s28\n\t"
    "v_mfma_f32_16x16x32_bf16 a[184:187], v[208:211], v[128:131], a[184:187]\n\t"
    "s_waitcnt vmcnt(12) lgkmcnt(0)\n\t"
    "s_barrier\n\t"
    "v_mfma_f32_16x16x32_bf16 a[188:191], v[208:211], v[132:135], a[188:191]\n\t"
    "s_mov_b32 s29, s28\n\t"
    "v_mfma_f32_16x16x32_bf16 a[192:195], v[208:211], v[136:139], a[192:195]\n\t"
    "s_mov_b32 s28, s30\n\t"
    "v_mfma_f32_16x16x32_bf16 a[196:199], v[208:211], v[140:143], a[196:199]\n\t"
    "s_add_u32 s58, s29, s34\n\t"
    "v_mfma_f32_16x16x32_bf16 a[200:203], v[212:215], v[124:127], a[200:203]\n\t"
    "s_add_u32 s59, s29, s35\n\t"
    "v_mfma_f32_16x16x32_bf16 a[204:207], v[212:215], v[128:131], a[204:207]\n\t"
    "v_mfma_f32_16x16x32_bf16 a[208:211], v[212:215], v[132:135], a[208:211]\n\t"
    "v_mfma_f32_16x16x32_bf16 a[212:215], v[212:215], v[136:139], a[212:215]\n\t"
    "v_mfma_f32_16x16x32_bf16 a[216:219], v[212:215], v[140:143], a[216:219]\n\t"
    "v_mfma_f32_16x16x32_bf16 a[220:223], v[216:219], v[124:127], a[220:223]\n\t"
    "v_mfma_f32_16x16x32_bf16 a[224:227], v[216:219], v[128:131], a[224:227]\n\t"
    "v_mfma_f32_16x16x32_bf16 a[228:231], v[216:219], v[132:135], a[228:231]\n\t"
    "v_mfma_f32_16x16x32_bf16 a[232:235], v[216:219], v[136:139], a[232:235]\n\t"
    "v_mfma_f32_16x16x32_bf16 a[236:239], v[216:219], v[140:143], a[236:239]\n\t"
    "s_mul_i32 s42, s79, 384\n\t"
    "s_add_u32 s16, s66, s42\n\t"
    "s_addc_u32 s17, s67, 0\n\t"
    "s_sub_u32 s18, 7864320, s42\n\t"
    "s_mov_b32 s38, s76\n\t"
    "s_mov_b32 s39, s77\n\t"
    "s_sub_u32 s31, s28, s46\n\t"
    "v_add_u32 v236, s31, v236\n\t"
    "v_add_u32 v237, s31, v237\n\t"
    "v_add_u32 v238, s31, v238\n\t"
    "v_add_u32 v239, s31, v239\n\t"
    "v_add_u32 v240, s31, v240\n\t"
    "v_add_u32 v241, s31, v241\n\t"
    "v_accvgpr_read_b32 v164, a0\n\t"
    "v_accvgpr_read_b32 v165, a1\n\t"
    "v_accvgpr_read_b32 v166, a2\n\t"
    "v_accvgpr_read_b32 v167, a3\n\t"
    "v_accvgpr_read_b32 v168, a20\n\t"
    "v_accvgpr_read_b32 v169, a21\n\t"
    "v_accvgpr_read_b32 v170, a22\n\t"
    "v_accvgpr_read_b32 v171, a23\n\t"
    "v_pk_mul_f32 v[164:165], v[164:165], s[48:49]\n\t"
    "v_pk_mul_f32 v[166:167], v[166:167], s[48:49]\n\t"
    "v_pk_mul_f32 v[168:169], v[168:169], s[48:49]\n\t"
    "v_pk_mul_f32 v[170:171], v[170:171], s[48:49]\n\t"
    "v_cvt_pk_bf16_f32 v0, v164, v165\n\t"
    "v_cvt_pk_bf16_f32 v1, v166, v167\n\t"
    "v_cvt_pk_bf16_f32 v2, v168, v169\n\t"
    "v_cvt_pk_bf16_f32 v3, v170, v171\n\t"
    "s_add_u32 m0, s58, 0\n\t"
    "s_nop 0\n\t"
    "buffer_load_dwordx4 v242, s[8:11], s36 offen nt lds\n\t"
    "v_accvgpr_read_b32 v172, a40\n\t"
    "v_accvgpr_read_b32 v173, a41\n\t"
    "v_accvgpr_read_b32 v174, a42\n\t"
    "v_accvgpr_read_b32 v175, a43\n\t"
    "v_accvgpr_read_b32 v176, a60\n\t"
    "v_accvgpr_read_b32 v177, a61\n\t"
    "v_accvgpr_read_b32 v178, a62\n\t"
    "v_accvgpr_read_b32 v179, a63\n\t"
    "v_pk_mul_f32 v[172:173], v[172:173], s[48:49]\n\t"
    "v_pk_mul_f32 v[174:175], v[174:175], s[48:49]\n\t"
    "v_pk_mul_f32 v[176:177], v[176:177], s[48:49]\n\t"
    "v_pk_mul_f32 v[178:179], v[178:179], s[48:49]\n\t"
    "v_cvt_pk_bf16_f32 v4, v172, v173\n\t"
    "v_cvt_pk_bf16_f32 v5, v174, v175\n\t"
    "v_cvt_pk_bf16_f32 v6, v176, v177\n\t"
    "v_cvt_pk_bf16_f32 v7, v178, v179\n\t"
    "buffer_load_dwordx4 v243, s[8:11], s36 offen offset:1024 nt lds\n\t"
    "v_accvgpr_read_b32 v180, a80\n\t"
    "v_accvgpr_read_b32 v181, a81\n\t"
    "v_accvgpr_read_b32 v182, a82\n\t"
    "v_accvgpr_read_b32 v183, a83\n\t"
    "v_accvgpr_read_b32 v184, a100\n\t"
    "v_accvgpr_read_b32 v185, a101\n\t"
    "v_accvgpr_read_b32 v186, a102\n\t"
    "v_accvgpr_read_b32 v187, a103\n\t"
    "v_pk_mul_f32 v[180:181], v[180:181], s[48:49]\n\t"
    "v_pk_mul_f32 v[182:183], v[182:183], s[48:49]\n\t"
    "v_pk_mul_f32 v[184:185], v[184:185], s[48:49]\n\t"
    "v_pk_mul_f32 v[186:187], v[186:187], s[48:49]\n\t"
    "v_cvt_pk_bf16_f32 v8, v180, v181\n\t"
    "v_cvt_pk_bf16_f32 v9, v182, v183\n\t"
    "v_cvt_pk_bf16_f32 v10, v184, v185\n\t"
    "v_cvt_pk_bf16_f32 v11, v186, v187\n\t"
    "buffer_load_dwordx4 v244, s[8:11], s36 offen offset:2048 nt lds\n\t"
    "v_accvgpr_read_b32 v188, a120\n\t"
    "v_accvgpr_read_b32 v189, a121\n\t"
    "v_accvgpr_read_b32 v190, a122\n\t"
    "v_accvgpr_read_b32 v191, a123\n\t"
    "v_accvgpr_read_b32 v192, a140\n\t"
    "v_accvgpr_read_b32 v193, a141\n\t"
    "v_accvgpr_read_b32 v194, a142\n\t"
    "v_accvgpr_read_b32 v195, a143\n\t"
    "v_pk_mul_f32 v[188:189], v[188:189], s[48:49]\n\t"
    "v_pk_mul_f32 v[190:191], v[190:191], s[48:49]\n\t"
    "v_pk_mul_f32 v[192:193], v[192:193], s[48:49]\n\t"
    "v_pk_mul_f32 v[194:195], v[194:195], s[48:49]\n\t"
    "v_cvt_pk_bf16_f32 v12, v188, v189\n\t"
    "v_cvt_pk_bf16_f32 v13, v190, v191\n\t"
    "v_cvt_pk_bf16_f32 v14, v192, v193\n\t"
    "v_cvt_pk_bf16_f32 v15, v194, v195\n\t"
    "s_add_u32 m0, s58, 3072\n\t"
    "s_add_u32 s40, s36, 3072\n\t"
    "buffer_load_dwordx4 v242, s[8:11], s40 offen nt lds\n\t"
    "v_accvgpr_read_b32 v164, a160\n\t"
    "v_accvgpr_read_b32 v165, a161\n\t"
    "v_accvgpr_read_b32 v166, a162\n\t"
    "v_accvgpr_read_b32 v167, a163\n\t"
    "v_accvgpr_read_b32 v168, a180\n\t"
    "v_accvgpr_read_b32 v169, a181\n\t"
    "v_accvgpr_read_b32 v170, a182\n\t"
    "v_accvgpr_read_b32 v171, a183\n\t"
    "v_pk_mul_f32 v[164:165], v[164:165], s[48:49]\n\t"
    "v_pk_mul_f32 v[166:167], v[166:167], s[48:49]\n\t"
    "v_pk_mul_f32 v[168:169], v[168:169], s[48:49]\n\t"
    "v_pk_mul_f32 v[170:171], v[170:171], s[48:49]\n\t"
    "v_cvt_pk_bf16_f32 v16, v164, v165\n\t"
    "v_cvt_pk_bf16_f32 v17, v166, v167\n\t"
    "v_cvt_pk_bf16_f32 v18, v168, v169\n\t"
    "v_cvt_pk_bf16_f32 v19, v170, v171\n\t"
    "buffer_load_dwordx4 v243, s[8:11], s40 offen offset:1024 nt lds\n\t"
    "v_accvgpr_read_b32 v172, a200\n\t"
    "v_accvgpr_read_b32 v173, a201\n\t"
    "v_accvgpr_read_b32 v174, a202\n\t"
    "v_accvgpr_read_b32 v175, a203\n\t"
    "v_accvgpr_read_b32 v176, a220\n\t"
    "v_accvgpr_read_b32 v177, a221\n\t"
    "v_accvgpr_read_b32 v178, a222\n\t"
    "v_accvgpr_read_b32 v179, a223\n\t"
    "v_pk_mul_f32 v[172:173], v[172:173], s[48:49]\n\t"
    "v_pk_mul_f32 v[174:175], v[174:175], s[48:49]\n\t"
    "v_pk_mul_f32 v[176:177], v[176:177], s[48:49]\n\t"
    "v_pk_mul_f32 v[178:179], v[178:179], s[48:49]\n\t"
    "v_cvt_pk_bf16_f32 v20, v172, v173\n\t"
    "v_cvt_pk_bf16_f32 v21, v174, v175\n\t"
    "v_cvt_pk_bf16_f32 v22, v176, v177\n\t"
    "v_cvt_pk_bf16_f32 v23, v178, v179\n\t"
    "buffer_load_dwordx4 v244, s[8:11], s40 offen offset:2048 nt lds\n\t"
    "v_accvgpr_read_b32 v180, a4\n\t"
    "v_accvgpr_read_b32 v181, a5\n\t"
    "v_accvgpr_read_b32 v182, a6\n\t"
    "v_accvgpr_read_b32 v183, a7\n\t"
    "v_accvgpr_read_b32 v184, a24\n\t"
    "v_accvgpr_read_b32 v185, a25\n\t"
    "v_accvgpr_read_b32 v186, a26\n\t"
    "v_accvgpr_read_b32 v187, a27\n\t"
    "v_pk_mul_f32 v[180:181], v[180:181], s[48:49]\n\t"
    "v_pk_mul_f32 v[182:183], v[182:183], s[48:49]\n\t"
    "v_pk_mul_f32 v[184:185], v[184:185], s[48:49]\n\t"
    "v_pk_mul_f32 v[186:187], v[186:187], s[48:49]\n\t"
    "v_cvt_pk_bf16_f32 v24, v180, v181\n\t"
    "v_cvt_pk_bf16_f32 v25, v182, v183\n\t"
    "v_cvt_pk_bf16_f32 v26, v184, v185\n\t"
    "v_cvt_pk_bf16_f32 v27, v186, v187\n\t"
    "s_add_u32 m0, s58, 12288\n\t"
    "s_nop 0\n\t"
    "buffer_load_dwordx4 v242, s[12:15], s37 offen nt lds\n\t"
    "v_accvgpr_read_b32 v188, a44\n\t"
    "v_accvgpr_read_b32 v189, a45\n\t"
    "v_accvgpr_read_b32 v190, a46\n\t"
    "v_accvgpr_read_b32 v191, a47\n\t"
    "v_accvgpr_read_b32 v192, a64\n\t"
    "v_accvgpr_read_b32 v193, a65\n\t"
    "v_accvgpr_read_b32 v194, a66\n\t"
    "v_accvgpr_read_b32 v195, a67\n\t"
    "v_pk_mul_f32 v[188:189], v[188:189], s[48:49]\n\t"
    "v_pk_mul_f32 v[190:191], v[190:191], s[48:49]\n\t"
    "v_pk_mul_f32 v[192:193], v[192:193], s[48:49]\n\t"
    "v_pk_mul_f32 v[194:195], v[194:195], s[48:49]\n\t"
    "v_cvt_pk_bf16_f32 v28, v188, v189\n\t"
    "v_cvt_pk_bf16_f32 v29, v190, v191\n\t"
    "v_cvt_pk_bf16_f32 v30, v192, v193\n\t"
    "v_cvt_pk_bf16_f32 v31, v194, v195\n\t"
    "buffer_load_dwordx4 v243, s[12:15], s37 offen offset:1024 nt lds\n\t"
    "v_accvgpr_read_b32 v164, a84\n\t"
    "v_accvgpr_read_b32 v165, a85\n\t"
    "v_accvgpr_read_b32 v166, a86\n\t"
    "v_accvgpr_read_b32 v167, a87\n\t"
    "v_accvgpr_read_b32 v168, a104\n\t"
    "v_accvgpr_read_b32 v169, a105\n\t"
    "v_accvgpr_read_b32 v170, a106\n\t"
    "v_accvgpr_read_b32 v171, a107\n\t"
    "v_pk_mul_f32 v[164:165], v[164:165], s[48:49]\n\t"
    "v_pk_mul_f32 v[166:167], v[166:167], s[48:49]\n\t"
    "v_pk_mul_f32 v[168:169], v[168:169], s[48:49]\n\t"
    "v_pk_mul_f32 v[170:171], v[170:171], s[48:49]\n\t"
    "v_cvt_pk_bf16_f32 v32, v164, v165\n\t"
    "v_cvt_pk_bf16_f32 v33, v166, v167\n\t"
    "v_cvt_pk_bf16_f32 v34, v168, v169\n\t"
    "v_cvt_pk_bf16_f32 v35, v170, v171\n\t"
    "buffer_load_dwordx4 v244, s[12:15], s37 offen offset:2048 nt lds\n\t"
    "v_accvgpr_read_b32 v172, a124\n\t"
    "v_accvgpr_read_b32 v173, a125\n\t"
    "v_accvgpr_read_b32 v174, a126\n\t"
    "v_accvgpr_read_b32 v175, a127\n\t"
    "v_accvgpr_read_b32 v176, a144\n\t"
    "v_accvgpr_read_b32 v177, a145\n\t"
    "v_accvgpr_read_b32 v178, a146\n\t"
    "v_accvgpr_read_b32 v179, a147\n\t"
    "v_pk_mul_f32 v[172:173], v[172:173], s[48:49]\n\t"
    "v_pk_mul_f32 v[174:175], v[174:175], s[48:49]\n\t"
    "v_pk_mul_f32 v[176:177], v[176:177], s[48:49]\n\t"
    "v_pk_mul_f32 v[178:179], v[178:179], s[48:49]\n\t"
    "v_cvt_pk_bf16_f32 v36, v172, v173\n\t"
    "v_cvt_pk_bf16_f32 v37, v174, v175\n\t"
    "v_cvt_pk_bf16_f32 v38, v176, v177\n\t"
    "v_cvt_pk_bf16_f32 v39, v178, v179\n\t"
    "s_add_u32 m0, s58, 15360\n\t"
    "s_add_u32 s41, s37, 3072\n\t"
    "buffer_load_dwordx4 v242, s[12:15], s41 offen nt lds\n\t"
    "v_accvgpr_read_b32 v180, a164\n\t"
    "v_accvgpr_read_b32 v181, a165\n\t"
    "v_accvgpr_read_b32 v182, a166\n\t"
    "v_accvgpr_read_b32 v183, a167\n\t"
    "v_accvgpr_read_b32 v184, a184\n\t"
    "v_accvgpr_read_b32 v185, a185\n\t"
    "v_accvgpr_read_b32 v186, a186\n\t"
    "v_accvgpr_read_b32 v187, a187\n\t"
    "v_pk_mul_f32 v[180:181], v[180:181], s[48:49]\n\t"
    "v_pk_mul_f32 v[182:183], v[182:183], s[48:49]\n\t"
    "v_pk_mul_f32 v[184:185], v[184:185], s[48:49]\n\t"
    "v_pk_mul_f32 v[186:187], v[186:187], s[48:49]\n\t"
    "v_cvt_pk_bf16_f32 v40, v180, v181\n\t"
    "v_cvt_pk_bf16_f32 v41, v182, v183\n\t"
    "v_cvt_pk_bf16_f32 v42, v184, v185\n\t"
    "v_cvt_pk_bf16_f32 v43, v186, v187\n\t"
    "buffer_load_dwordx4 v243, s[12:15], s41 offen offset:1024 nt lds\n\t"
    "v_accvgpr_read_b32 v188, a204\n\t"
    "v_accvgpr_read_b32 v189, a205\n\t"
    "v_accvgpr_read_b32 v190, a206\n\t"
    "v_accvgpr_read_b32 v191, a207\n\t"
    "v_accvgpr_read_b32 v192, a224\n\t"
    "v_accvgpr_read_b32 v193, a225\n\t"
    "v_accvgpr_read_b32 v194, a226\n\t"
    "v_accvgpr_read_b32 v195, a227\n\t"
    "v_pk_mul_f32 v[188:189], v[188:189], s[48:49]\n\t"
    "v_pk_mul_f32 v[190:191], v[190:191], s[48:49]\n\t"
    "v_pk_mul_f32 v[192:193], v[192:193], s[48:49]\n\t"
    "v_pk_mul_f32 v[194:195], v[194:195], s[48:49]\n\t"
    "v_cvt_pk_bf16_f32 v44, v188, v189\n\t"
    "v_cvt_pk_bf16_f32 v45, v190, v191\n\t"
    "v_cvt_pk_bf16_f32 v46, v192, v193\n\t"
    "v_cvt_pk_bf16_f32 v47, v194, v195\n\t"
    "buffer_load_dwordx4 v244, s[12:15], s41 offen offset:2048 nt lds\n\t"
    "s_add_u32 s36, s36, 12288\n\t"
    "s_add_u32 s37, s37, 12288\n\t"
    "v_accvgpr_read_b32 v164, a8\n\t"
    "v_accvgpr_read_b32 v165, a9\n\t"
    "v_accvgpr_read_b32 v166, a10\n\t"
    "v_accvgpr_read_b32 v167, a11\n\t"
    "v_accvgpr_read_b32 v168, a28\n\t"
    "v_accvgpr_read_b32 v169, a29\n\t"
    "v_accvgpr_read_b32 v170, a30\n\t"
    "v_accvgpr_read_b32 v171, a31\n\t"
    "v_pk_mul_f32 v[164:165], v[164:165], s[48:49]\n\t"
    "v_pk_mul_f32 v[166:167], v[166:167], s[48:49]\n\t"
    "v_pk_mul_f32 v[168:169], v[168:169], s[48:49]\n\t"
    "v_pk_mul_f32 v[170:171], v[170:171], s[48:49]\n\t"
    "v_cvt_pk_bf16_f32 v48, v164, v165\n\t"
    "v_cvt_pk_bf16_f32 v49, v166, v167\n\t"
    "v_cvt_pk_bf16_f32 v50, v168, v169\n\t"
    "v_cvt_pk_bf16_f32 v51, v170, v171\n\t"
    "v_accvgpr_read_b32 v172, a48\n\t"
    "v_accvgpr_read_b32 v173, a49\n\t"
    "v_accvgpr_read_b32 v174, a50\n\t"
    "v_accvgpr_read_b32 v175, a51\n\t"
    "v_accvgpr_read_b32 v176, a68\n\t"
    "v_accvgpr_read_b32 v177, a69\n\t"
    "v_accvgpr_read_b32 v178, a70\n\t"
    "v_accvgpr_read_b32 v179, a71\n\t"
    "v_pk_mul_f32 v[172:173], v[172:173], s[48:49]\n\t"
    "v_pk_mul_f32 v[174:175], v[174:175], s[48:49]\n\t"
    "v_pk_mul_f32 v[176:177], v[176:177], s[48:49]\n\t"
    "v_pk_mul_f32 v[178:179], v[178:179], s[48:49]\n\t"
    "v_cvt_pk_bf16_f32 v52, v172, v173\n\t"
    "v_cvt_pk_bf16_f32 v53, v174, v175\n\t"
    "v_cvt_pk_bf16_f32 v54, v176, v177\n\t"
    "v_cvt_pk_bf16_f32 v55, v178, v179\n\t"
    "v_accvgpr_read_b32 v180, a88\n\t"
    "v_accvgpr_read_b32 v181, a89\n\t"
    "v_accvgpr_read_b32 v182, a90\n\t"
    "v_accvgpr_read_b32 v183, a91\n\t"
    "v_accvgpr_read_b32 v184, a108\n\t"
    "v_accvgpr_read_b32 v185, a109\n\t"
    "v_accvgpr_read_b32 v186, a110\n\t"
    "v_accvgpr_read_b32 v187, a111\n\t"
    "v_pk_mul_f32 v[180:181], v[180:181], s[48:49]\n\t"
    "v_pk_mul_f32 v[182:183], v[182:183], s[48:49]\n\t"
    "v_pk_mul_f32 v[184:185], v[184:185], s[48:49]\n\t"
    "v_pk_mul_f32 v[186:187], v[186:187], s[48:49]\n\t"
    "v_cvt_pk_bf16_f32 v56, v180, v181\n\t"
    "v_cvt_pk_bf16_f32 v57, v182, v183\n\t"
    "v_cvt_pk_bf16_f32 v58, v184, v185\n\t"
    "v_cvt_pk_bf16_f32 v59, v186, v187\n\t"
    "v_accvgpr_read_b32 v188, a128\n\t"
    "v_accvgpr_read_b32 v189, a129\n\t"
    "v_accvgpr_read_b32 v190, a130\n\t"
    "v_accvgpr_read_b32 v191, a131\n\t"
    "v_accvgpr_read_b32 v192, a148\n\t"
    "v_accvgpr_read_b32 v193, a149\n\t"
    "v_accvgpr_read_b32 v194, a150\n\t"
    "v_accvgpr_read_b32 v195, a151\n\t"
    "v_pk_mul_f32 v[188:189], v[188:189], s[48:49]\n\t"
    "v_pk_mul_f32 v[190:191], v[190:191], s[48:49]\n\t"
    "v_pk_mul_f32 v[192:193], v[192:193], s[48:49]\n\t"
    "v_pk_mul_f32 v[194:195], v[194:195], s[48:49]\n\t"
    "v_cvt_pk_bf16_f32 v60, v188, v189\n\t"
    "v_cvt_pk_bf16_f32 v61, v190, v191\n\t"
    "v_cvt_pk_bf16_f32 v62, v192, v193\n\t"
    "v_cvt_pk_bf16_f32 v63, v194, v195\n\t"
    "v_accvgpr_read_b32 v164, a168\n\t"
    "v_accvgpr_read_b32 v165, a169\n\t"
    "v_accvgpr_read_b32 v166, a170\n\t"
    "v_accvgpr_read_b32 v167, a171\n\t"
    "v_accvgpr_read_b32 v168, a188\n\t"
    "v_accvgpr_read_b32 v169, a189\n\t"
    "v_accvgpr_read_b32 v170, a190\n\t"
    "v_accvgpr_read_b32 v171, a191\n\t"
    "v_pk_mul_f32 v[164:165], v[164:165], s[48:49]\n\t"
    "v_pk_mul_f32 v[166:167], v[166:167], s[48:49]\n\t"
    "v_pk_mul_f32 v[168:169], v[168:169], s[48:49]\n\t"
    "v_pk_mul_f32 v[170:171], v[170:171], s[48:49]\n\t"
    "v_cvt_pk_bf16_f32 v64, v164, v165\n\t"
    "v_cvt_pk_bf16_f32 v65, v166, v167\n\t"
    "v_cvt_pk_bf16_f32 v66, v168, v169\n\t"
    "v_cvt_pk_bf16_f32 v67, v170, v171\n\t"
    "v_accvgpr_read_b32 v172, a208\n\t"
    "v_accvgpr_read_b32 v173, a209\n\t"
    "v_accvgpr_read_b32 v174, a210\n\t"
    "v_accvgpr_read_b32 v175, a211\n\t"
    "v_accvgpr_read_b32 v176, a228\n\t"
    "v_accvgpr_read_b32 v177, a229\n\t"
    "v_accvgpr_read_b32 v178, a230\n\t"
    "v_accvgpr_read_b32 v179, a231\n\t"
    "v_pk_mul_f32 v[172:173], v[172:173], s[48:49]\n\t"
    "v_pk_mul_f32 v[174:175], v[174:175], s[48:49]\n\t"
    "v_pk_mul_f32 v[176:177], v[176:177], s[48:49]\n\t"
    "v_pk_mul_f32 v[178:179], v[178:179], s[48:49]\n\t"
    "v_cvt_pk_bf16_f32 v68, v172, v173\n\t"
    "v_cvt_pk_bf16_f32 v69, v174, v175\n\t"
    "v_cvt_pk_bf16_f32 v70, v176, v177\n\t"
    "v_cvt_pk_bf16_f32 v71, v178, v179\n\t"
    "v_accvgpr_read_b32 v180, a12\n\t"
    "v_accvgpr_read_b32 v181, a13\n\t"
    "v_accvgpr_read_b32 v182, a14\n\t"
    "v_accvgpr_read_b32 v183, a15\n\t"
    "v_accvgpr_read_b32 v184, a32\n\t"
    "v_accvgpr_read_b32 v185, a33\n\t"
    "v_accvgpr_read_b32 v186, a34\n\t"
    "v_accvgpr_read_b32 v187, a35\n\t"
    "v_pk_mul_f32 v[180:181], v[180:181], s[48:49]\n\t"
    "v_pk_mul_f32 v[182:183], v[182:183], s[48:49]\n\t"
    "v_pk_mul_f32 v[184:185], v[184:185], s[48:49]\n\t"
    "v_pk_mul_f32 v[186:187], v[186:187], s[48:49]\n\t"
    "v_cvt_pk_bf16_f32 v72, v180, v181\n\t"
    "v_cvt_pk_bf16_f32 v73, v182, v183\n\t"
    "v_cvt_pk_bf16_f32 v74, v184, v185\n\t"
    "v_cvt_pk_bf16_f32 v75, v186, v187\n\t"
    "v_accvgpr_read_b32 v188, a52\n\t"
    "v_accvgpr_read_b32 v189, a53\n\t"
    "v_accvgpr_read_b32 v190, a54\n\t"
    "v_accvgpr_read_b32 v191, a55\n\t"
    "v_accvgpr_read_b32 v192, a72\n\t"
    "v_accvgpr_read_b32 v193, a73\n\t"
    "v_accvgpr_read_b32 v194, a74\n\t"
    "v_accvgpr_read_b32 v195, a75\n\t"
    "v_pk_mul_f32 v[188:189], v[188:189], s[48:49]\n\t"
    "v_pk_mul_f32 v[190:191], v[190:191], s[48:49]\n\t"
    "v_pk_mul_f32 v[192:193], v[192:193], s[48:49]\n\t"
    "v_pk_mul_f32 v[194:195], v[194:195], s[48:49]\n\t"
    "v_cvt_pk_bf16_f32 v76, v188, v189\n\t"
    "v_cvt_pk_bf16_f32 v77, v190, v191\n\t"
    "v_cvt_pk_bf16_f32 v78, v192, v193\n\t"
    "v_cvt_pk_bf16_f32 v79, v194, v195\n\t"
    "v_accvgpr_read_b32 v164, a92\n\t"
    "v_accvgpr_read_b32 v165, a93\n\t"
    "v_accvgpr_read_b32 v166, a94\n\t"
    "v_accvgpr_read_b32 v167, a95\n\t"
    "v_accvgpr_read_b32 v168, a112\n\t"
    "v_accvgpr_read_b32 v169, a113\n\t"
    "v_accvgpr_read_b32 v170, a114\n\t"
    "v_accvgpr_read_b32 v171, a115\n\t"
    "v_pk_mul_f32 v[164:165], v[164:165], s[48:49]\n\t"
    "v_pk_mul_f32 v[166:167], v[166:167], s[48:49]\n\t"
    "v_pk_mul_f32 v[168:169], v[168:169], s[48:49]\n\t"
    "v_pk_mul_f32 v[170:171], v[170:171], s[48:49]\n\t"
    "v_cvt_pk_bf16_f32 v80, v164, v165\n\t"
    "v_cvt_pk_bf16_f32 v81, v166, v167\n\t"
    "v_cvt_pk_bf16_f32 v82, v168, v169\n\t"
    "v_cvt_pk_bf16_f32 v83, v170, v171\n\t"
    "v_accvgpr_read_b32 v172, a132\n\t"
    "v_accvgpr_read_b32 v173, a133\n\t"
    "v_accvgpr_read_b32 v174, a134\n\t"
    "v_accvgpr_read_b32 v175, a135\n\t"
    "v_accvgpr_read_b32 v176, a152\n\t"
    "v_accvgpr_read_b32 v177, a153\n\t"
    "v_accvgpr_read_b32 v178, a154\n\t"
    "v_accvgpr_read_b32 v179, a155\n\t"
    "v_pk_mul_f32 v[172:173], v[172:173], s[48:49]\n\t"
    "v_pk_mul_f32 v[174:175], v[174:175], s[48:49]\n\t"
    "v_pk_mul_f32 v[176:177], v[176:177], s[48:49]\n\t"
    "v_pk_mul_f32 v[178:179], v[178:179], s[48:49]\n\t"
    "v_cvt_pk_bf16_f32 v84, v172, v173\n\t"
    "v_cvt_pk_bf16_f32 v85, v174, v175\n\t"
    "v_cvt_pk_bf16_f32 v86, v176, v177\n\t"
    "v_cvt_pk_bf16_f32 v87, v178, v179\n\t"
    "v_accvgpr_read_b32 v180, a172\n\t"
    "v_accvgpr_read_b32 v181, a173\n\t"
    "v_accvgpr_read_b32 v182, a174\n\t"
    "v_accvgpr_read_b32 v183, a175\n\t"
    "v_accvgpr_read_b32 v184, a192\n\t"
    "v_accvgpr_read_b32 v185, a193\n\t"
    "v_accvgpr_read_b32 v186, a194\n\t"
    "v_accvgpr_read_b32 v187, a195\n\t"
    "v_pk_mul_f32 v[180:181], v[180:181], s[48:49]\n\t"
    "v_pk_mul_f32 v[182:183], v[182:183], s[48:49]\n\t"
    "v_pk_mul_f32 v[184:185], v[184:185], s[48:49]\n\t"
    "v_pk_mul_f32 v[186:187], v[186:187], s[48:49]\n\t"
    "v_cvt_pk_bf16_f32 v88, v180, v181\n\t"
    "v_cvt_pk_bf16_f32 v89, v182, v183\n\t"
    "v_cvt_pk_bf16_f32 v90, v184, v185\n\t"
    "v_cvt_pk_bf16_f32 v91, v186, v187\n\t"
    "v_accvgpr_read_b32 v188, a212\n\t"
    "v_accvgpr_read_b32 v189, a213\n\t"
    "v_accvgpr_read_b32 v190, a214\n\t"
    "v_accvgpr_read_b32 v191, a215\n\t"
    "v_accvgpr_read_b32 v192, a232\n\t"
    "v_accvgpr_read_b32 v193, a233\n\t"
    "v_accvgpr_read_b32 v194, a234\n\t"
    "v_accvgpr_read_b32 v195, a235\n\t"
    "v_pk_mul_f32 v[188:189], v[188:189], s[48:49]\n\t"
    "v_pk_mul_f32 v[190:191], v[190:191], s[48:49]\n\t"
    "v_pk_mul_f32 v[192:193], v[192:193], s[48:49]\n\t"
    "v_pk_mul_f32 v[194:195], v[194:195], s[48:49]\n\t"
    "v_cvt_pk_bf16_f32 v92, v188, v189\n\t"
    "v_cvt_pk_bf16_f32 v93, v190, v191\n\t"
    "v_cvt_pk_bf16_f32 v94, v192, v193\n\t"
    "v_cvt_pk_bf16_f32 v95, v194, v195\n\t"
    "v_accvgpr_read_b32 v164, a16\n\t"
    "v_accvgpr_read_b32 v165, a17\n\t"
    "v_accvgpr_read_b32 v166, a18\n\t"
    "v_accvgpr_read_b32 v167, a19\n\t"
    "v_accvgpr_read_b32 v168, a36\n\t"
    "v_accvgpr_read_b32 v169, a37\n\t"
    "v_accvgpr_read_b32 v170, a38\n\t"
    "v_accvgpr_read_b32 v171, a39\n\t"
    "v_pk_mul_f32 v[164:165], v[164:165], s[48:49]\n\t"
    "v_pk_mul_f32 v[166:167], v[166:167], s[48:49]\n\t"
    "v_pk_mul_f32 v[168:169], v[168:169], s[48:49]\n\t"
    "v_pk_mul_f32 v[170:171], v[170:171], s[48:49]\n\t"
    "v_cvt_pk_bf16_f32 v96, v164, v165\n\t"
    "v_cvt_pk_bf16_f32 v97, v166, v167\n\t"
    "v_cvt_pk_bf16_f32 v98, v168, v169\n\t"
    "v_cvt_pk_bf16_f32 v99, v170, v171\n\t"
    "v_accvgpr_read_b32 v172, a56\n\t"
    "v_accvgpr_read_b32 v173, a57\n\t"
    "v_accvgpr_read_b32 v174, a58\n\t"
    "v_accvgpr_read_b32 v175, a59\n\t"
    "v_accvgpr_read_b32 v176, a76\n\t"
    "v_accvgpr_read_b32 v177, a77\n\t"
    "v_accvgpr_read_b32 v178, a78\n\t"
    "v_accvgpr_read_b32 v179, a79\n\t"
    "v_pk_mul_f32 v[172:173], v[172:173], s[48:49]\n\t"
    "v_pk_mul_f32 v[174:175], v[174:175], s[48:49]\n\t"
    "v_pk_mul_f32 v[176:177], v[176:177], s[48:49]\n\t"
    "v_pk_mul_f32 v[178:179], v[178:179], s[48:49]\n\t"
    "v_cvt_pk_bf16_f32 v100, v172, v173\n\t"
    "v_cvt_pk_bf16_f32 v101, v174, v175\n\t"
    "v_cvt_pk_bf16_f32 v102, v176, v177\n\t"
    "v_cvt_pk_bf16_f32 v103, v178, v179\n\t"
    "v_accvgpr_read_b32 v180, a96\n\t"
    "v_accvgpr_read_b32 v181, a97\n\t"
    "v_accvgpr_read_b32 v182, a98\n\t"
    "v_accvgpr_read_b32 v183, a99\n\t"
    "v_accvgpr_read_b32 v184, a116\n\t"
    "v_accvgpr_read_b32 v185, a117\n\t"
    "v_accvgpr_read_b32 v186, a118\n\t"
    "v_accvgpr_read_b32 v187, a119\n\t"
    "v_pk_mul_f32 v[180:181], v[180:181], s[48:49]\n\t"
    "v_pk_mul_f32 v[182:183], v[182:183], s[48:49]\n\t"
    "v_pk_mul_f32 v[184:185], v[184:185], s[48:49]\n\t"
    "v_pk_mul_f32 v[186:187], v[186:187], s[48:49]\n\t"
    "v_cvt_pk_bf16_f32 v180, v180, v181\n\t"
    "v_cvt_pk_bf16_f32 v181, v182, v183\n\t"
    "v_cvt_pk_bf16_f32 v182, v184, v185\n\t"
    "v_cvt_pk_bf16_f32 v183, v186, v187\n\t"
    "v_accvgpr_write_b32 a240, v180\n\t"
    "v_accvgpr_write_b32 a241, v181\n\t"
    "v_accvgpr_write_b32 a242, v182\n\t"
    "v_accvgpr_write_b32 a243, v183\n\t"
    "v_accvgpr_read_b32 v188, a136\n\t"
    "v_accvgpr_read_b32 v189, a137\n\t"
    "v_accvgpr_read_b32 v190, a138\n\t"
    "v_accvgpr_read_b32 v191, a139\n\t"
    "v_accvgpr_read_b32 v192, a156\n\t"
    "v_accvgpr_read_b32 v193, a157\n\t"
    "v_accvgpr_read_b32 v194, a158\n\t"
    "v_accvgpr_read_b32 v195, a159\n\t"
    "v_pk_mul_f32 v[188:189], v[188:189], s[48:49]\n\t"
    "v_pk_mul_f32 v[190:191], v[190:191], s[48:49]\n\t"
    "v_pk_mul_f32 v[192:193], v[192:193], s[48:49]\n\t"
    "v_pk_mul_f32 v[194:195], v[194:195], s[48:49]\n\t"
    "v_cvt_pk_bf16_f32 v188, v188, v189\n\t"
    "v_cvt_pk_bf16_f32 v189, v190, v191\n\t"
    "v_cvt_pk_bf16_f32 v190, v192, v193\n\t"
    "v_cvt_pk_bf16_f32 v191, v194, v195\n\t"
    "v_accvgpr_write_b32 a244, v188\n\t"
    "v_accvgpr_write_b32 a245, v189\n\t"
    "v_accvgpr_write_b32 a246, v190\n\t"
    "v_accvgpr_write_b32 a247, v191\n\t"
    "ds_read_b128 v[204:207], v236 offset:0\n\t"
    "ds_read_b128 v[208:211], v237 offset:0\n\t"
    "ds_read_b128 v[212:215], v236 offset:128\n\t"
    "ds_read_b128 v[216:219], v237 offset:128\n\t"
    "v_accvgpr_read_b32 v164, a176\n\t"
    "v_accvgpr_read_b32 v165, a177\n\t"
    "v_accvgpr_read_b32 v166, a178\n\t"
    "v_accvgpr_read_b32 v167, a179\n\t"
    "v_accvgpr_read_b32 v168, a196\n\t"
    "v_accvgpr_read_b32 v169, a197\n\t"
    "v_accvgpr_read_b32 v170, a198\n\t"
    "v_accvgpr_read_b32 v171, a199\n\t"
    "v_pk_mul_f32 v[164:165], v[164:165], s[48:49]\n\t"
    "v_pk_mul_f32 v[166:167], v[166:167], s[48:49]\n\t"
    "v_pk_mul_f32 v[168:169], v[168:169], s[48:49]\n\t"
    "v_pk_mul_f32 v[170:171], v[170:171], s[48:49]\n\t"
    "v_cvt_pk_bf16_f32 v164, v164, v165\n\t"
    "v_cvt_pk_bf16_f32 v165, v166, v167\n\t"
    "v_cvt_pk_bf16_f32 v166, v168, v169\n\t"
    "v_cvt_pk_bf16_f32 v167, v170, v171\n\t"
    "v_accvgpr_write_b32 a248, v164\n\t"
    "v_accvgpr_write_b32 a249, v165\n\t"
    "v_accvgpr_write_b32 a250, v166\n\t"
    "v_accvgpr_write_b32 a251, v167\n\t"
    "v_accvgpr_read_b32 v172, a216\n\t"
    "v_accvgpr_read_b32 v173, a217\n\t"
    "v_accvgpr_read_b32 v174, a218\n\t"
    "v_accvgpr_read_b32 v175, a219\n\t"
    "v_accvgpr_read_b32 v176, a236\n\t"
    "v_accvgpr_read_b32 v177, a237\n\t"
    "v_accvgpr_read_b32 v178, a238\n\t"
    "v_accvgpr_read_b32 v179, a239\n\t"
    "v_pk_mul_f32 v[172:173], v[172:173], s[48:49]\n\t"
    "v_pk_mul_f32 v[174:175], v[174:175], s[48:49]\n\t"
    "v_pk_mul_f32 v[176:177], v[176:177], s[48:49]\n\t"
    "v_pk_mul_f32 v[178:179], v[178:179], s[48:49]\n\t"
    "v_cvt_pk_bf16_f32 v172, v172, v173\n\t"
    "v_cvt_pk_bf16_f32 v173, v174, v175\n\t"
    "v_cvt_pk_bf16_f32 v174, v176, v177\n\t"
    "v_cvt_pk_bf16_f32 v175, v178, v179\n\t"
    "v_accvgpr_write_b32 a252, v172\n\t"
    "v_accvgpr_write_b32 a253, v173\n\t"
    "v_accvgpr_write_b32 a254, v174\n\t"
    "v_accvgpr_write_b32 a255, v175\n\t"
    "s_nop 1\n\t"
    "s_waitcnt lgkmcnt(3)\n\t"
    "v_mfma_f32_16x16x32_bf16 v[104:107], v[204:207], v[0:3], 0\n\t"
    "v_mfma_f32_16x16x32_bf16 v[108:111], v[204:207], v[24:27], 0\n\t"
    "v_mfma_f32_16x16x32_bf16 v[112:115], v[204:207], v[48:51], 0\n\t"
    "v_mfma_f32_16x16x32_bf16 v[116:119], v[204:207], v[72:75], 0\n\t"
    "v_mfma_f32_16x16x32_bf16 v[120:123], v[204:207], v[96:99], 0\n\t"
    "s_waitcnt lgkmcnt(2)\n\t"
    "ds_read_b128 v[204:207], v236 offset:256\n\t"
    "v_mfma_f32_16x16x32_bf16 v[104:107], v[208:211], v[4:7], v[104:107]\n\t"
    "v_mfma_f32_16x16x32_bf16 v[108:111], v[208:211], v[28:31], v[108:111]\n\t"
    "v_mfma_f32_16x16x32_bf16 v[112:115], v[208:211], v[52:55], v[112:115]\n\t"
    "v_mfma_f32_16x16x32_bf16 v[116:119], v[208:211], v[76:79], v[116:119]\n\t"
    "v_mfma_f32_16x16x32_bf16 v[120:123], v[208:211], v[100:103], v[120:123]\n\t"
    "s_waitcnt lgkmcnt(2)\n\t"
    "ds_read_b128 v[208:211], v237 offset:256\n\t"
    "v_mfma_f32_16x16x32_bf16 v[104:107], v[212:215], v[8:11], v[104:107]\n\t"
    "v_mfma_f32_16x16x32_bf16 v[108:111], v[212:215], v[32:35], v[108:111]\n\t"
    "v_mfma_f32_16x16x32_bf16 v[112:115], v[212:215], v[56:59], v[112:115]\n\t"
    "v_mfma_f32_16x16x32_bf16 v[116:119], v[212:215], v[80:83], v[116:119]\n\t"
    "v_mfma_f32_16x16x32_bf16 v[120:123], v[212:215], a[240:243], v[120:123]\n\t"
    "s_waitcnt lgkmcnt(2)\n\t"
    "ds_read_b128 v[212:215], v236 offset:6144\n\t"
    "v_mfma_f32_16x16x32_bf16 v[104:107], v[216:219], v[12:15], v[104:107]\n\t"
    "v_mfma_f32_16x16x32_bf16 v[108:111], v[216:219], v[36:39], v[108:111]\n\t"
    "v_mfma_f32_16x16x32_bf16 v[112:115], v[216:219], v[60:63], v[112:115]\n\t"
    "v_mfma_f32_16x16x32_bf16 v[116:119], v[216:219], v[84:87], v[116:119]\n\t"
    "v_mfma_f32_16x16x32_bf16 v[120:123], v[216:219], a[244:247], v[120:123]\n\t"
    "s_waitcnt lgkmcnt(2)\n\t"
    "ds_read_b128 v[216:219], v237 offset:6144\n\t"
    "v_mfma_f32_16x16x32_bf16 v[104:107], v[204:207], v[16:19], v[104:107]\n\t"
    "v_mfma_f32_16x16x32_bf16 v[108:111], v[204:207], v[40:43], v[108:111]\n\t"
    "v_mfma_f32_16x16x32_bf16 v[112:115], v[204:207], v[64:67], v[112:115]\n\t"
    "v_mfma_f32_16x16x32_bf16 v[116:119], v[204:207], v[88:91], v[116:119]\n\t"
    "v_mfma_f32_16x16x32_bf16 v[120:123], v[204:207], a[248:251], v[120:123]\n\t"
    "s_waitcnt lgkmcnt(2)\n\t"
    "ds_read_b128 v[204:207], v236 offset:6272\n\t"
    "v_mfma_f32_16x16x32_bf16 v[104:107], v[208:211], v[20:23], v[104:107]\n\t"
    "v_mfma_f32_16x16x32_bf16 v[108:111], v[208:211], v[44:47], v[108:111]\n\t"
    "v_mfma_f32_16x16x32_bf16 v[112:115], v[208:211], v[68:71], v[112:115]\n\t"
    "v_mfma_f32_16x16x32_bf16 v[116:119], v[208:211], v[92:95], v[116:119]\n\t"
    "v_mfma_f32_16x16x32_bf16 v[120:123], v[208:211], a[252:255], v[120:123]\n\t"
    "s_waitcnt lgkmcnt(2)\n\t"
    "ds_read_b128 v[208:211], v237 offset:6272\n\t"
    "v_mfma_f32_16x16x32_bf16 v[124:127], v[212:215], v[0:3], 0\n\t"
    "s_add_u32 s30, s28, 24576\n\t"
    "v_mfma_f32_16x16x32_bf16 v[128:131], v[212:215], v[24:27], 0\n\t"
    "s_cmp_eq_u32 s30, 73728\n\t"
    "v_mfma_f32_16x16x32_bf16 v[132:135], v[212:215], v[48:51], 0\n\t"
    "s_cselect_b32 s30, 0, s30\n\t"
    "v_mfma_f32_16x16x32_bf16 v[136:139], v[212:215], v[72:75], 0\n\t"
    "s_sub_u32 s31, s30, s28\n\t"
    "v_mfma_f32_16x16x32_bf16 v[140:143], v[212:215], v[96:99], 0\n\t"
    "s_waitcnt lgkmcnt(2)\n\t"
    "ds_read_b128 v[212:215], v236 offset:6400\n\t"
    "v_mfma_f32_16x16x32_bf16 v[124:127], v[216:219], v[4:7], v[124:127]\n\t"
    "v_mfma_f32_16x16x32_bf16 v[128:131], v[216:219], v[28:31], v[128:131]\n\t"
    "v_mfma_f32_16x16x32_bf16 v[132:135], v[216:219], v[52:55], v[132:135]\n\t"
    "v_mfma_f32_16x16x32_bf16 v[136:139], v[216:219], v[76:79], v[136:139]\n\t"
    "v_mfma_f32_16x16x32_bf16 v[140:143], v[216:219], v[100:103], v[140:143]\n\t"
    "s_waitcnt lgkmcnt(2)\n\t"
    "ds_read_b128 v[216:219], v237 offset:6400\n\t"
    "v_mfma_f32_16x16x32_bf16 v[124:127], v[204:207], v[8:11], v[124:127]\n\t"
    "v_add_u32 v236, s31, v236\n\t"
    "v_add_u32 v237, s31, v237\n\t"
    "v_mfma_f32_16x16x32_bf16 v[128:131], v[204:207], v[32:35], v[128:131]\n\t"
    "s_add_u32 s75, s28, s34\n\t"
    "v_mfma_f32_16x16x32_bf16 v[132:135], v[204:207], v[56:59], v[132:135]\n\t"
    "v_mfma_f32_16x16x32_bf16 v[136:139], v[204:207], v[80:83], v[136:139]\n\t"
    "v_mfma_f32_16x16x32_bf16 v[140:143], v[204:207], a[240:243], v[140:143]\n\t"
    "s_waitcnt lgkmcnt(2)\n\t"
    "v_mfma_f32_16x16x32_bf16 v[124:127], v[208:211], v[12:15], v[124:127]\n\t"
    "v_mfma_f32_16x16x32_bf16 v[128:131], v[208:211], v[36:39], v[128:131]\n\t"
    "s_waitcnt vmcnt(18) lgkmcnt(0)\n\t"
    "s_barrier\n\t"
    "v_mfma_f32_16x16x32_bf16 v[132:135], v[208:211], v[60:63], v[132:135]\n\t"
    "s_mov_b32 s29, s28\n\t"
    "s_add_u32 m0, s75, 0\n\t"
    "ds_read_b128 v[204:207], v236 offset:0\n\t"
    "v_mfma_f32_16x16x32_bf16 v[136:139], v[208:211], v[84:87], v[136:139]\n\t"
    "s_mov_b32 s28, s30\n\t"
    "buffer_load_dwordx4 v242, s[8:11], s36 offen nt lds\n\t"
    "v_mfma_f32_16x16x32_bf16 v[140:143], v[208:211], a[244:247], v[140:143]\n\t"
    "s_add_u32 s58, s29, s34\n\t"
    "buffer_load_dwordx4 v243, s[8:11], s36 offen offset:1024 nt lds\n\t"
    "v_mfma_f32_16x16x32_bf16 v[124:127], v[212:215], v[16:19], v[124:127]\n\t"
    "s_add_u32 s59, s29, s35\n\t"
    "ds_read_b128 v[208:211], v237 offset:0\n\t"
    "v_mfma_f32_16x16x32_bf16 v[128:131], v[212:215], v[40:43], v[128:131]\n\t"
    "buffer_load_dwordx4 v244, s[8:11], s36 offen offset:2048 nt lds\n\t"
    "v_mfma_f32_16x16x32_bf16 v[132:135], v[212:215], v[64:67], v[132:135]\n\t"
    "s_add_u32 m0, s75, 3072\n\t"
    "v_mfma_f32_16x16x32_bf16 v[136:139], v[212:215], v[88:91], v[136:139]\n\t"
    "s_add_u32 s40, s36, 3072\n\t"
    "buffer_load_dwordx4 v242, s[8:11], s40 offen nt lds\n\t"
    "v_mfma_f32_16x16x32_bf16 v[140:143], v[212:215], a[248:251], v[140:143]\n\t"
    "buffer_load_dwordx4 v243, s[8:11], s40 offen offset:1024 nt lds\n\t"
    "v_mfma_f32_16x16x32_bf16 v[124:127], v[216:219], v[20:23], v[124:127]\n\t"
    "ds_read_b128 v[212:215], v236 offset:128\n\t"
    "v_mfma_f32_16x16x32_bf16 v[128:131], v[216:219], v[44:47], v[128:131]\n\t"
    "buffer_load_dwordx4 v244, s[8:11], s40 offen offset:2048 nt lds\n\t"
    "v_mfma_f32_16x16x32_bf16 v[132:135], v[216:219], v[68:71], v[132:135]\n\t"
    "s_add_u32 s36, s36, 12288\n\t"
    "v_mfma_f32_16x16x32_bf16 v[136:139], v[216:219], v[92:95], v[136:139]\n\t"
    "v_mfma_f32_16x16x32_bf16 v[140:143], v[216:219], a[252:255], v[140:143]\n\t"
    "ds_read_b128 v[216:219], v237 offset:128\n\t"
    "s_nop 7\n\t"
    "v_max3_f32 v225, v104, v105, v106\n\t"
    "v_max3_f32 v225, v225, v107, v124\n\t"
    "v_max3_f32 v225, v225, v125, v126\n\t"
    "v_max_f32 v225, v225, v127\n\t"
    "v_max3_f32 v226, v108, v109, v110\n\t"
    "v_max3_f32 v226, v226, v111, v128\n\t"
    "v_max3_f32 v226, v226, v129, v130\n\t"
    "v_max_f32 v226, v226, v131\n\t"
    "v_max3_f32 v227, v112, v113, v114\n\t"
    "v_max3_f32 v227, v227, v115, v132\n\t"
    "v_max3_f32 v227, v227, v133, v134\n\t"
    "v_max_f32 v227, v227, v135\n\t"
    "v_max3_f32 v228, v116, v117, v118\n\t"
    "v_max3_f32 v228, v228, v119, v136\n\t"
    "v_max3_f32 v228, v228, v137, v138\n\t"
    "v_max_f32 v228, v228, v139\n\t"
    "v_max3_f32 v229, v120, v121, v122\n\t"
    "v_max3_f32 v229, v229, v123, v140\n\t"
    "v_max3_f32 v229, v229, v141, v142\n\t"
    "v_max_f32 v229, v229, v143\n\t"
    "v_mov_b32 v230, v225\n\t"
    "v_mov_b32 v231, v226\n\t"
    "v_mov_b32 v232, v227\n\t"
    "v_mov_b32 v233, v228\n\t"
    "v_mov_b32 v234, v229\n\t"
    "s_nop 0\n\t"
    "v_permlane32_swap_b32 v225, v230\n\t"
    "v_permlane32_swap_b32 v226, v231\n\t"
    "v_permlane32_swap_b32 v227, v232\n\t"
    "v_permlane32_swap_b32 v228, v233\n\t"
    "v_permlane32_swap_b32 v229, v234\n\t"
    "v_max_f32 v225, v225, v230\n\t"
    "v_max_f32 v226, v226, v231\n\t"
    "v_max_f32 v227, v227, v232\n\t"
    "v_max_f32 v228, v228, v233\n\t"
    "v_max_f32 v229, v229, v234\n\t"
    "v_mov_b32 v230, v225\n\t"
    "v_mov_b32 v231, v226\n\t"
    "v_mov_b32 v232, v227\n\t"
    "v_mov_b32 v233, v228\n\t"
    "v_mov_b32 v234, v229\n\t"
    "s_nop 0\n\t"
    "v_permlane16_swap_b32 v225, v230\n\t"
    "v_permlane16_swap_b32 v226, v231\n\t"
    "v_permlane16_swap_b32 v227, v232\n\t"
    "v_permlane16_swap_b32 v228, v233\n\t"
    "v_permlane16_swap_b32 v229, v234\n\t"
    "v_max_f32 v225, v225, v230\n\t"
    "v_max_f32 v226, v226, v231\n\t"
    "v_max_f32 v227, v227, v232\n\t"
    "v_max_f32 v228, v228, v233\n\t"
    "v_max_f32 v229, v229, v234\n\t"
    "v_sub_f32 v144, 0, v225\n\t"
    "v_sub_f32 v145, 0, v225\n\t"
    "v_sub_f32 v146, 0, v225\n\t"
    "v_sub_f32 v147, 0, v225\n\t"
    "v_sub_f32 v104, v104, v225\n\t"
    "v_sub_f32 v105, v105, v225\n\t"
    "v_sub_f32 v106, v106, v225\n\t"
    "v_sub_f32 v107, v107, v225\n\t"
    "v_sub_f32 v124, v124, v225\n\t"
    "v_sub_f32 v125, v125, v225\n\t"
    "v_sub_f32 v126, v126, v225\n\t"
    "v_sub_f32 v127, v127, v225\n\t"
    "v_sub_f32 v148, 0, v226\n\t"
    "v_sub_f32 v149, 0, v226\n\t"
    "v_sub_f32 v150, 0, v226\n\t"
    "v_sub_f32 v151, 0, v226\n\t"
    "v_sub_f32 v108, v108, v226\n\t"
    "v_sub_f32 v109, v109, v226\n\t"
    "v_sub_f32 v110, v110, v226\n\t"
    "v_sub_f32 v111, v111, v226\n\t"
    "v_sub_f32 v128, v128, v226\n\t"
    "v_sub_f32 v129, v129, v226\n\t"
    "v_sub_f32 v130, v130, v226\n\t"
    "v_sub_f32 v131, v131, v226\n\t"
    "v_sub_f32 v152, 0, v227\n\t"
    "v_sub_f32 v153, 0, v227\n\t"
    "v_sub_f32 v154, 0, v227\n\t"
    "v_sub_f32 v155, 0, v227\n\t"
    "v_sub_f32 v112, v112, v227\n\t"
    "v_sub_f32 v113, v113, v227\n\t"
    "v_sub_f32 v114, v114, v227\n\t"
    "v_sub_f32 v115, v115, v227\n\t"
    "v_sub_f32 v132, v132, v227\n\t"
    "v_sub_f32 v133, v133, v227\n\t"
    "v_sub_f32 v134, v134, v227\n\t"
    "v_sub_f32 v135, v135, v227\n\t"
    "v_sub_f32 v156, 0, v228\n\t"
    "v_sub_f32 v157, 0, v228\n\t"
    "v_sub_f32 v158, 0, v228\n\t"
    "v_sub_f32 v159, 0, v228\n\t"
    "v_sub_f32 v116, v116, v228\n\t"
    "v_sub_f32 v117, v117, v228\n\t"
    "v_sub_f32 v118, v118, v228\n\t"
    "v_sub_f32 v119, v119, v228\n\t"
    "v_sub_f32 v136, v136, v228\n\t"
    "v_sub_f32 v137, v137, v228\n\t"
    "v_sub_f32 v138, v138, v228\n\t"
    "v_sub_f32 v139, v139, v228\n\t"
    "v_sub_f32 v160, 0, v229\n\t"
    "v_sub_f32 v161, 0, v229\n\t"
    "v_sub_f32 v162, 0, v229\n\t"
    "v_sub_f32 v163, 0, v229\n\t"
    "v_sub_f32 v120, v120, v229\n\t"
    "v_sub_f32 v121, v121, v229\n\t"
    "v_sub_f32 v122, v122, v229\n\t"
    "v_sub_f32 v123, v123, v229\n\t"
    "v_sub_f32 v140, v140, v229\n\t"
    "v_sub_f32 v141, v141, v229\n\t"
    "v_sub_f32 v142, v142, v229\n\t"
    "v_sub_f32 v143, v143, v229\n\t"
    "v_mov_b32 v220, 0\n\t"
    "v_mov_b32 v221, 0\n\t"
    "v_mov_b32 v222, 0\n\t"
    "v_mov_b32 v223, 0\n\t"
    "v_mov_b32 v224, 0\n\t"
    "v_exp_f32 v230, v104\n\t"
    "v_exp_f32 v231, v105\n\t"
    "v_exp_f32 v232, v106\n\t"
    "v_exp_f32 v233, v107\n\t"
    "v_add_f32 v225, v230, v231\n\t"
    "v_cvt_pk_bf16_f32 v164, v230, v231\n\t"
    "v_add_f32 v225, v232, v225\n\t"
    "v_add_f32 v225, v233, v225\n\t"
    "v_cvt_pk_bf16_f32 v165, v232, v233\n\t"
    "v_exp_f32 v230, v124\n\t"
    "v_exp_f32 v231, v125\n\t"
    "v_exp_f32 v232, v126\n\t"
    "v_exp_f32 v233, v127\n\t"
    "v_add_f32 v225, v230, v225\n\t"
    "v_add_f32 v225, v231, v225\n\t"
    "v_cvt_pk_bf16_f32 v166, v230, v231\n\t"
    "v_add_f32 v225, v232, v225\n\t"
    "v_add_f32 v225, v233, v225\n\t"
    "v_cvt_pk_bf16_f32 v167, v232, v233\n\t"
    "v_exp_f32 v230, v108\n\t"
    "v_exp_f32 v231, v109\n\t"
    "v_exp_f32 v232, v110\n\t"
    "v_exp_f32 v233, v111\n\t"
    "v_add_f32 v226, v230, v231\n\t"
    "v_cvt_pk_bf16_f32 v168, v230, v231\n\t"
    "v_add_f32 v226, v232, v226\n\t"
    "v_add_f32 v226, v233, v226\n\t"
    "v_cvt_pk_bf16_f32 v169, v232, v233\n\t"
    "v_exp_f32 v230, v128\n\t"
    "v_exp_f32 v231, v129\n\t"
    "v_exp_f32 v232, v130\n\t"
    "v_exp_f32 v233, v131\n\t"
    "v_add_f32 v226, v230, v226\n\t"
    "v_add_f32 v226, v231, v226\n\t"
    "v_cvt_pk_bf16_f32 v170, v230, v231\n\t"
    "v_add_f32 v226, v232, v226\n\t"
    "v_add_f32 v226, v233, v226\n\t"
    "v_cvt_pk_bf16_f32 v171, v232, v233\n\t"
    "v_exp_f32 v230, v112\n\t"
    "v_exp_f32 v231, v113\n\t"
    "v_exp_f32 v232, v114\n\t"
    "v_exp_f32 v233, v115\n\t"
    "v_add_f32 v227, v230, v231\n\t"
    "v_cvt_pk_bf16_f32 v172, v230, v231\n\t"
    "v_add_f32 v227, v232, v227\n\t"
    "v_add_f32 v227, v233, v227\n\t"
    "v_cvt_pk_bf16_f32 v173, v232, v233\n\t"
    "v_exp_f32 v230, v132\n\t"
    "v_exp_f32 v231, v133\n\t"
    "v_exp_f32 v232, v134\n\t"
    "v_exp_f32 v233, v135\n\t"
    "v_add_f32 v227, v230, v227\n\t"
    "v_add_f32 v227, v231, v227\n\t"
    "v_cvt_pk_bf16_f32 v174, v230, v231\n\t"
    "v_add_f32 v227, v232, v227\n\t"
    "v_add_f32 v227, v233, v227\n\t"
    "v_cvt_pk_bf16_f32 v175, v232, v233\n\t"
    "v_exp_f32 v230, v116\n\t"
    "v_exp_f32 v231, v117\n\t"
    "v_exp_f32 v232, v118\n\t"
    "v_exp_f32 v233, v119\n\t"
    "v_add_f32 v228, v230, v231\n\t"
    "v_cvt_pk_bf16_f32 v176, v230, v231\n\t"
    "v_add_f32 v228, v232, v228\n\t"
    "v_add_f32 v228, v233, v228\n\t"
    "v_cvt_pk_bf16_f32 v177, v232, v233\n\t"
    "v_exp_f32 v230, v136\n\t"
    "v_exp_f32 v231, v137\n\t"
    "v_exp_f32 v232, v138\n\t"
    "v_exp_f32 v233, v139\n\t"
    "v_add_f32 v228, v230, v228\n\t"
    "v_add_f32 v228, v231, v228\n\t"
    "v_cvt_pk_bf16_f32 v178, v230, v231\n\t"
    "v_add_f32 v228, v232, v228\n\t"
    "v_add_f32 v228, v233, v228\n\t"
    "v_cvt_pk_bf16_f32 v179, v232, v233\n\t"
    "v_exp_f32 v230, v120\n\t"
    "v_exp_f32 v231, v121\n\t"
    "v_exp_f32 v232, v122\n\t"
    "v_exp_f32 v233, v123\n\t"
    "v_add_f32 v229, v230, v231\n\t"
    "v_cvt_pk_bf16_f32 v180, v230, v231\n\t"
    "v_add_f32 v229, v232, v229\n\t"
    "v_add_f32 v229, v233, v229\n\t"
    "v_cvt_pk_bf16_f32 v181, v232, v233\n\t"
    "v_exp_f32 v230, v140\n\t"
    "v_exp_f32 v231, v141\n\t"
    "v_exp_f32 v232, v142\n\t"
    "v_exp_f32 v233, v143\n\t"
    "v_add_f32 v229, v230, v229\n\t"
    "v_add_f32 v229, v231, v229\n\t"
    "v_cvt_pk_bf16_f32 v182, v230, v231\n\t"
    "v_add_f32 v229, v232, v229\n\t"
    "v_add_f32 v229, v233, v229\n\t"
    "v_cvt_pk_bf16_f32 v183, v232, v233\n\t"
    "s_nop 1\n\t"
    "s_waitcnt lgkmcnt(3)\n\t"
    "v_mfma_f32_16x16x32_bf16 v[104:107], v[204:207], v[0:3], v[144:147]\n\t"
    "s_add_u32 m0, s58, 12288\n\t"
    "v_add_u32 v238, s31, v238\n\t"
    "v_add_u32 v239, s31, v239\n\t"
    "v_mfma_f32_16x16x32_bf16 v[108:111], v[204:207], v[24:27], v[148:151]\n\t"
    "buffer_load_dwordx4 v242, s[12:15], s37 offen nt lds\n\t"
    "v_mfma_f32_16x16x32_bf16 v[112:115], v[204:207], v[48:51], v[152:155]\n\t"
    "v_add_u32 v240, s31, v240\n\t"
    "v_add_u32 v241, s31, v241\n\t"
    "v_mfma_f32_16x16x32_bf16 v[116:119], v[204:207], v[72:75], v[156:159]\n\t"
    "v_add_f32 v220, v225, v220\n\t"
    "v_add_f32 v221, v226, v221\n\t"
    "v_mfma_f32_16x16x32_bf16 v[120:123], v[204:207], v[96:99], v[160:163]\n\t"
    "s_waitcnt lgkmcnt(2)\n\t"
    "ds_read_b128 v[204:207], v236 offset:256\n\t"
    "v_add_f32 v222, v227, v222\n\t"
    "v_mfma_f32_16x16x32_bf16 v[104:107], v[208:211], v[4:7], v[104:107]\n\t"
    "v_add_f32 v223, v228, v223\n\t"
    "v_add_f32 v224, v229, v224\n\t"
    "v_mfma_f32_16x16x32_bf16 v[108:111], v[208:211], v[28:31], v[108:111]\n\t"
    "buffer_load_dwordx4 v243, s[12:15], s37 offen offset:1024 nt lds\n\t"
    "v_mfma_f32_16x16x32_bf16 v[112:115], v[208:211], v[52:55], v[112:115]\n\t"
    "v_mfma_f32_16x16x32_bf16 v[116:119], v[208:211], v[76:79], v[116:119]\n\t"
    "v_mfma_f32_16x16x32_bf16 v[120:123], v[208:211], v[100:103], v[120:123]\n\t"
    "s_waitcnt lgkmcnt(2)\n\t"
    "ds_read_b128 v[208:211], v237 offset:256\n\t"
    "v_mfma_f32_16x16x32_bf16 v[104:107], v[212:215], v[8:11], v[104:107]\n\t"
    "v_mfma_f32_16x16x32_bf16 v[108:111], v[212:215], v[32:35], v[108:111]\n\t"
    "buffer_load_dwordx4 v244, s[12:15], s37 offen offset:2048 nt lds\n\t"
    "v_mfma_f32_16x16x32_bf16 v[112:115], v[212:215], v[56:59], v[112:115]\n\t"
    "v_mfma_f32_16x16x32_bf16 v[116:119], v[212:215], v[80:83], v[116:119]\n\t"
    "v_mfma_f32_16x16x32_bf16 v[120:123], v[212:215], a[240:243], v[120:123]\n\t"
    "s_waitcnt lgkmcnt(2)\n\t"
    "ds_read_b128 v[212:215], v236 offset:6144\n\t"
    "v_mfma_f32_16x16x32_bf16 v[104:107], v[216:219], v[12:15], v[104:107]\n\t"
    "s_add_u32 m0, s58, 15360\n\t"
    "v_mfma_f32_16x16x32_bf16 v[108:111], v[216:219], v[36:39], v[108:111]\n\t"
    "s_add_u32 s41, s37, 3072\n\t"
    "buffer_load_dwordx4 v242, s[12:15], s41 offen nt lds\n\t"
    "v_mfma_f32_16x16x32_bf16 v[112:115], v[216:219], v[60:63], v[112:115]\n\t"
    "v_mfma_f32_16x16x32_bf16 v[116:119], v[216:219], v[84:87], v[116:119]\n\t"
    "v_mfma_f32_16x16x32_bf16 v[120:123], v[216:219], a[244:247], v[120:123]\n\t"
    "s_waitcnt lgkmcnt(2)\n\t"
    "ds_read_b128 v[216:219], v237 offset:6144\n\t"
    "v_mfma_f32_16x16x32_bf16 v[104:107], v[204:207], v[16:19], v[104:107]\n\t"
    "v_mfma_f32_16x16x32_bf16 v[108:111], v[204:207], v[40:43], v[108:111]\n\t"
    "buffer_load_dwordx4 v243, s[12:15], s41 offen offset:1024 nt lds\n\t"
    "v_mfma_f32_16x16x32_bf16 v[112:115], v[204:207], v[64:67], v[112:115]\n\t"
    "v_mfma_f32_16x16x32_bf16 v[116:119], v[204:207], v[88:91], v[116:119]\n\t"
    "v_mfma_f32_16x16x32_bf16 v[120:123], v[204:207], a[248:251], v[120:123]\n\t"
    "s_waitcnt lgkmcnt(2)\n\t"
    "ds_read_b128 v[204:207], v236 offset:6272\n\t"
    "v_mfma_f32_16x16x32_bf16 v[104:107], v[208:211], v[20:23], v[104:107]\n\t"
    "v_mfma_f32_16x16x32_bf16 v[108:111], v[208:211], v[44:47], v[108:111]\n\t"
    "buffer_load_dwordx4 v244, s[12:15], s41 offen offset:2048 nt lds\n\t"
    "v_mfma_f32_16x16x32_bf16 v[112:115], v[208:211], v[68:71], v[112:115]\n\t"
    "s_add_u32 s37, s37, 12288\n\t"
    "v_mfma_f32_16x16x32_bf16 v[116:119], v[208:211], v[92:95], v[116:119]\n\t"
    "v_mfma_f32_16x16x32_bf16 v[120:123], v[208:211], a[252:255], v[120:123]\n\t"
    "s_waitcnt lgkmcnt(2)\n\t"
    "ds_read_b128 v[208:211], v237 offset:6272\n\t"
    "v_mfma_f32_16x16x32_bf16 v[124:127], v[212:215], v[0:3], v[144:147]\n\t"
    "v_mfma_f32_16x16x32_bf16 v[128:131], v[212:215], v[24:27], v[148:151]\n\t"
    "s_add_u32 s30, s28, 24576\n\t"
    "v_exp_f32 v230, v104\n\t"
    "v_mfma_f32_16x16x32_bf16 v[132:135], v[212:215], v[48:51], v[152:155]\n\t"
    "s_cmp_eq_u32 s30, 73728\n\t"
    "v_exp_f32 v231, v105\n\t"
    "v_mfma_f32_16x16x32_bf16 v[136:139], v[212:215], v[72:75], v[156:159]\n\t"
    "s_cselect_b32 s30, 0, s30\n\t"
    "v_exp_f32 v232, v106\n\t"
    "v_mfma_f32_16x16x32_bf16 v[140:143], v[212:215], v[96:99], v[160:163]\n\t"
    "s_waitcnt lgkmcnt(2)\n\t"
    "ds_read_b128 v[212:215], v236 offset:6400\n\t"
    "v_add_f32 v225, v230, v231\n\t"
    "v_mfma_f32_16x16x32_bf16 v[124:127], v[216:219], v[4:7], v[124:127]\n\t"
    "s_sub_u32 s31, s30, s28\n\t"
    "v_exp_f32 v233, v107\n\t"
    "v_mfma_f32_16x16x32_bf16 v[128:131], v[216:219], v[28:31], v[128:131]\n\t"
    "v_exp_f32 v234, v108\n\t"
    "v_mfma_f32_16x16x32_bf16 v[132:135], v[216:219], v[52:55], v[132:135]\n\t"
    "v_cvt_pk_bf16_f32 v184, v230, v231\n\t"
    "v_add_f32 v225, v232, v225\n\t"
    "v_mfma_f32_16x16x32_bf16 v[136:139], v[216:219], v[76:79], v[136:139]\n\t"
    "v_exp_f32 v235, v109\n\t"
    "v_mfma_f32_16x16x32_bf16 v[140:143], v[216:219], v[100:103], v[140:143]\n\t"
    "s_waitcnt lgkmcnt(2)\n\t"
    "ds_read_b128 v[216:219], v237 offset:6400\n\t"
    "v_add_f32 v225, v233, v225\n\t"
    "v_mfma_f32_16x16x32_bf16 v[124:127], v[204:207], v[8:11], v[124:127]\n\t"
    "v_add_u32 v236, s31, v236\n\t"
    "v_add_u32 v237, s31, v237\n\t"
    "v_mfma_f32_16x16x32_bf16 v[128:131], v[204:207], v[32:35], v[128:131]\n\t"
    "s_add_u32 s75, s28, s34\n\t"
    "v_exp_f32 v230, v110\n\t"
    "v_mfma_f32_16x16x32_bf16 v[132:135], v[204:207], v[56:59], v[132:135]\n\t"
    "v_cvt_pk_bf16_f32 v185, v232, v233\n\t"
    "v_add_f32 v226, v234, v235\n\t"
    "v_mfma_f32_16x16x32_bf16 v[136:139], v[204:207], v[80:83], v[136:139]\n\t"
    "v_exp_f32 v231, v111\n\t"
    "v_mfma_f32_16x16x32_bf16 v[140:143], v[204:207], a[240:243], v[140:143]\n\t"
    "s_waitcnt vmcnt(24) lgkmcnt(0)\n\t"
    "s_barrier\n\t"
    "ds_read_b64_tr_b16 v[204:205], v238 offset:0\n\t"
    "v_cvt_pk_bf16_f32 v188, v234, v235\n\t"
    "v_mfma_f32_16x16x32_bf16 v[124:127], v[208:211], v[12:15], v[124:127]\n\t"
    "ds_read_b64_tr_b16 v[206:207], v238 offset:6144\n\t"
    "v_add_f32 v226, v230, v226\n\t"
    "v_mfma_f32_16x16x32_bf16 v[128:131], v[208:211], v[36:39], v[128:131]\n\t"
    "s_add_u32 m0, s75, 0\n\t"
    "v_exp_f32 v232, v112\n\t"
    "v_mfma_f32_16x16x32_bf16 v[132:135], v[208:211], v[60:63], v[132:135]\n\t"
    "buffer_load_dwordx4 v242, s[8:11], s36 offen nt lds\n\t"
    "v_add_f32 v226, v231, v226\n\t"
    "v_mfma_f32_16x16x32_bf16 v[136:139], v[208:211], v[84:87], v[136:139]\n\t"
    "v_exp_f32 v233, v113\n\t"
    "v_mfma_f32_16x16x32_bf16 v[140:143], v[208:211], a[244:247], v[140:143]\n\t"
    "ds_read_b64_tr_b16 v[208:209], v239 offset:0\n\t"
    "v_cvt_pk_bf16_f32 v189, v230, v231\n\t"
    "v_mfma_f32_16x16x32_bf16 v[124:127], v[212:215], v[16:19], v[124:127]\n\t"
    "ds_read_b64_tr_b16 v[210:211], v239 offset:6144\n\t"
    "v_add_f32 v227, v232, v233\n\t"
    "v_mfma_f32_16x16x32_bf16 v[128:131], v[212:215], v[40:43], v[128:131]\n\t"
    "v_exp_f32 v234, v114\n\t"
    "v_mfma_f32_16x16x32_bf16 v[132:135], v[212:215], v[64:67], v[132:135]\n\t"
    "v_exp_f32 v235, v115\n\t"
    "v_mfma_f32_16x16x32_bf16 v[136:139], v[212:215], v[88:91], v[136:139]\n\t"
    "v_exp_f32 v230, v116\n\t"
    "v_mfma_f32_16x16x32_bf16 v[140:143], v[212:215], a[248:251], v[140:143]\n\t"
    "ds_read_b64_tr_b16 v[212:213], v240 offset:0\n\t"
    "v_cvt_pk_bf16_f32 v192, v232, v233\n\t"
    "v_mfma_f32_16x16x32_bf16 v[124:127], v[216:219], v[20:23], v[124:127]\n\t"
    "ds_read_b64_tr_b16 v[214:215], v240 offset:6144\n\t"
    "v_add_f32 v227, v234, v227\n\t"
    "v_mfma_f32_16x16x32_bf16 v[128:131], v[216:219], v[44:47], v[128:131]\n\t"
    "v_exp_f32 v231, v117\n\t"
    "v_mfma_f32_16x16x32_bf16 v[132:135], v[216:219], v[68:71], v[132:135]\n\t"
    "buffer_load_dwordx4 v243, s[8:11], s36 offen offset:1024 nt lds\n\t"
    "v_add_f32 v227, v235, v227\n\t"
    "v_mfma_f32_16x16x32_bf16 v[136:139], v[216:219], v[92:95], v[136:139]\n\t"
    "v_exp_f32 v232, v118\n\t"
    "v_mfma_f32_16x16x32_bf16 v[140:143], v[216:219], a[252:255], v[140:143]\n\t"
    "s_waitcnt lgkmcnt(4)\n\t"
    "ds_read_b64_tr_b16 v[216:217], v241 offset:0\n\t"
    "v_cvt_pk_bf16_f32 v193, v234, v235\n\t"
    "v_mfma_f32_16x16x32_bf16 a[0:3], v[204:207], v[164:167], 0\n\t"
    "ds_read_b64_tr_b16 v[218:219], v241 offset:6144\n\t"
    "v_add_f32 v228, v230, v231\n\t"
    "v_mfma_f32_16x16x32_bf16 a[4:7], v[204:207], v[168:171], 0\n\t"
    "v_exp_f32 v233, v119\n\t"
    "v_mfma_f32_16x16x32_bf16 a[8:11], v[204:207], v[172:175], 0\n\t"
    "v_exp_f32 v234, v120\n\t"
    "v_mfma_f32_16x16x32_bf16 a[12:15], v[204:207], v[176:179], 0\n\t"
    "v_cvt_pk_bf16_f32 v196, v230, v231\n\t"
    "v_add_f32 v228, v232, v228\n\t"
    "v_mfma_f32_16x16x32_bf16 a[16:19], v[204:207], v[180:183], 0\n\t"
    "s_waitcnt lgkmcnt(4)\n\t"
    "ds_read_b64_tr_b16 v[204:205], v238 offset:128\n\t"
    "v_add_f32 v228, v233, v228\n\t"
    "v_mfma_f32_16x16x32_bf16 a[20:23], v[208:211], v[164:167], 0\n\t"
    "ds_read_b64_tr_b16 v[206:207], v238 offset:6272\n\t"
    "v_cvt_pk_bf16_f32 v197, v232, v233\n\t"
    "v_mfma_f32_16x16x32_bf16 a[24:27], v[208:211], v[168:171], 0\n\t"
    "v_exp_f32 v235, v121\n\t"
    "v_mfma_f32_16x16x32_bf16 a[28:31], v[208:211], v[172:175], 0\n\t"
    "buffer_load_dwordx4 v244, s[8:11], s36 offen offset:2048 nt lds\n\t"
    "v_add_f32 v229, v234, v235\n\t"
    "v_mfma_f32_16x16x32_bf16 a[32:35], v[208:211], v[176:179], 0\n\t"
    "v_exp_f32 v230, v122\n\t"
    "v_mfma_f32_16x16x32_bf16 a[36:39], v[208:211], v[180:183], 0\n\t"
    "s_waitcnt lgkmcnt(4)\n\t"
    "ds_read_b64_tr_b16 v[208:209], v239 offset:128\n\t"
    "v_cvt_pk_bf16_f32 v200, v234, v235\n\t"
    "v_mfma_f32_16x16x32_bf16 a[40:43], v[212:215], v[164:167], 0\n\t"
    "ds_read_b64_tr_b16 v[210:211], v239 offset:6272\n\t"
    "v_add_f32 v229, v230, v229\n\t"
    "v_mfma_f32_16x16x32_bf16 a[44:47], v[212:215], v[168:171], 0\n\t"
    "v_exp_f32 v231, v123\n\t"
    "v_mfma_f32_16x16x32_bf16 a[48:51], v[212:215], v[172:175], 0\n\t"
    "v_exp_f32 v232, v124\n\t"
    "v_mfma_f32_16x16x32_bf16 a[52:55], v[212:215], v[176:179], 0\n\t"
    "v_exp_f32 v233, v125\n\t"
    "v_mfma_f32_16x16x32_bf16 a[56:59], v[212:215], v[180:183], 0\n\t"
    "s_waitcnt lgkmcnt(4)\n\t"
    "ds_read_b64_tr_b16 v[212:213], v240 offset:128\n\t"
    "v_add_f32 v229, v231, v229\n\t"
    "v_mfma_f32_16x16x32_bf16 a[60:63], v[216:219], v[164:167], 0\n\t"
    "ds_read_b64_tr_b16 v[214:215], v240 offset:6272\n\t"
    "v_cvt_pk_bf16_f32 v201, v230, v231\n\t"
    "v_mfma_f32_16x16x32_bf16 a[64:67], v[216:219], v[168:171], 0\n\t"
    "s_add_u32 m0, s75, 3072\n\t"
    "v_exp_f32 v234, v126\n\t"
    "v_mfma_f32_16x16x32_bf16 a[68:71], v[216:219], v[172:175], 0\n\t"
    "s_add_u32 s40, s36, 3072\n\t"
    "buffer_load_dwordx4 v242, s[8:11], s40 offen nt lds\n\t"
    "v_add_f32 v225, v232, v225\n\t"
    "v_mfma_f32_16x16x32_bf16 a[72:75], v[216:219], v[176:179], 0\n\t"
    "v_exp_f32 v235, v127\n\t"
    "v_mfma_f32_16x16x32_bf16 a[76:79], v[216:219], v[180:183], 0\n\t"
    "s_waitcnt lgkmcnt(4)\n\t"
    "ds_read_b64_tr_b16 v[216:217], v241 offset:128\n\t"
    "v_add_f32 v225, v233, v225\n\t"
    "v_mfma_f32_16x16x32_bf16 a[80:83], v[204:207], v[164:167], 0\n\t"
    "ds_read_b64_tr_b16 v[218:219], v241 offset:6272\n\t"
    "v_cvt_pk_bf16_f32 v186, v232, v233\n\t"
    "v_mfma_f32_16x16x32_bf16 a[84:87], v[204:207], v[168:171], 0\n\t"
    "v_exp_f32 v230, v128\n\t"
    "v_mfma_f32_16x16x32_bf16 a[88:91], v[204:207], v[172:175], 0\n\t"
    "v_exp_f32 v231, v129\n\t"
    "v_mfma_f32_16x16x32_bf16 a[92:95], v[204:207], v[176:179], 0\n\t"
    "v_exp_f32 v232, v130\n\t"
    "v_mfma_f32_16x16x32_bf16 a[96:99], v[204:207], v[180:183], 0\n\t"
    "s_waitcnt lgkmcnt(4)\n\t"
    "ds_read_b64_tr_b16 v[204:205], v238 offset:256\n\t"
    "v_add_f32 v225, v234, v225\n\t"
    "v_mfma_f32_16x16x32_bf16 a[100:103], v[208:211], v[164:167], 0\n\t"
    "ds_read_b64_tr_b16 v[206:207], v238 offset:6400\n\t"
    "v_add_f32 v225, v235, v225\n\t"
    "v_mfma_f32_16x16x32_bf16 a[104:107], v[208:211], v[168:171], 0\n\t"
    "v_cvt_pk_bf16_f32 v187, v234, v235\n\t"
    "v_add_f32 v226, v230, v226\n\t"
    "v_mfma_f32_16x16x32_bf16 a[108:111], v[208:211], v[172:175], 0\n\t"
    "buffer_load_dwordx4 v243, s[8:11], s40 offen offset:1024 nt lds\n\t"
    "v_add_f32 v226, v231, v226\n\t"
    "v_mfma_f32_16x16x32_bf16 a[112:115], v[208:211], v[176:179], 0\n\t"
    "v_exp_f32 v233, v131\n\t"
    "v_mfma_f32_16x16x32_bf16 a[116:119], v[208:211], v[180:183], 0\n\t"
    "s_waitcnt lgkmcnt(4)\n\t"
    "ds_read_b64_tr_b16 v[208:209], v239 offset:256\n\t"
    "v_cvt_pk_bf16_f32 v190, v230, v231\n\t"
    "v_mfma_f32_16x16x32_bf16 a[120:123], v[212:215], v[164:167], 0\n\t"
    "ds_read_b64_tr_b16 v[210:211], v239 offset:6400\n\t"
    "v_add_f32 v226, v232, v226\n\t"
    "v_mfma_f32_16x16x32_bf16 a[124:127], v[212:215], v[168:171], 0\n\t"
    "v_exp_f32 v234, v132\n\t"
    "v_mfma_f32_16x16x32_bf16 a[128:131], v[212:215], v[172:175], 0\n\t"
    "v_exp_f32 v235, v133\n\t"
    "v_mfma_f32_16x16x32_bf16 a[132:135], v[212:215], v[176:179], 0\n\t"
    "v_exp_f32 v230, v134\n\t"
    "v_mfma_f32_16x16x32_bf16 a[136:139], v[212:215], v[180:183], 0\n\t"
    "s_waitcnt lgkmcnt(4)\n\t"
    "ds_read_b64_tr_b16 v[212:213], v240 offset:256\n\t"
    "v_add_f32 v226, v233, v226\n\t"
    "v_mfma_f32_16x16x32_bf16 a[140:143], v[216:219], v[164:167], 0\n\t"
    "ds_read_b64_tr_b16 v[214:215], v240 offset:6400\n\t"
    "v_cvt_pk_bf16_f32 v191, v232, v233\n\t"
    "v_mfma_f32_16x16x32_bf16 a[144:147], v[216:219], v[168:171], 0\n\t"
    "v_exp_f32 v231, v135\n\t"
    "v_mfma_f32_16x16x32_bf16 a[148:151], v[216:219], v[172:175], 0\n\t"
    "buffer_load_dwordx4 v244, s[8:11], s40 offen offset:2048 nt lds\n\t"
    "v_add_f32 v227, v234, v227\n\t"
    "v_mfma_f32_16x16x32_bf16 a[152:155], v[216:219], v[176:179], 0\n\t"
    "s_add_u32 s36, s36, 12288\n\t"
    "v_exp_f32 v232, v136\n\t"
    "v_mfma_f32_16x16x32_bf16 a[156:159], v[216:219], v[180:183], 0\n\t"
    "s_waitcnt lgkmcnt(4)\n\t"
    "ds_read_b64_tr_b16 v[216:217], v241 offset:256\n\t"
    "v_add_f32 v227, v235, v227\n\t"
    "v_mfma_f32_16x16x32_bf16 a[160:163], v[204:207], v[164:167], 0\n\t"
    "ds_read_b64_tr_b16 v[218:219], v241 offset:6400\n\t"
    "v_cvt_pk_bf16_f32 v194, v234, v235\n\t"
    "v_mfma_f32_16x16x32_bf16 a[164:167], v[204:207], v[168:171], 0\n\t"
    "v_exp_f32 v233, v137\n\t"
    "v_mfma_f32_16x16x32_bf16 a[168:171], v[204:207], v[172:175], 0\n\t"
    "v_exp_f32 v234, v138\n\t"
    "v_mfma_f32_16x16x32_bf16 a[172:175], v[204:207], v[176:179], 0\n\t"
    "v_add_f32 v227, v230, v227\n\t"
    "v_add_f32 v227, v231, v227\n\t"
    "v_mfma_f32_16x16x32_bf16 a[176:179], v[204:207], v[180:183], 0\n\t"
    "s_waitcnt lgkmcnt(4)\n\t"
    "v_cvt_pk_bf16_f32 v195, v230, v231\n\t"
    "v_add_f32 v228, v232, v228\n\t"
    "v_mfma_f32_16x16x32_bf16 a[180:183], v[208:211], v[164:167], 0\n\t"
    "v_exp_f32 v235, v139\n\t"
    "v_mfma_f32_16x16x32_bf16 a[184:187], v[208:211], v[168:171], 0\n\t"
    "v_exp_f32 v230, v140\n\t"
    "v_mfma_f32_16x16x32_bf16 a[188:191], v[208:211], v[172:175], 0\n\t"
    "v_add_f32 v228, v233, v228\n\t"
    "v_cvt_pk_bf16_f32 v198, v232, v233\n\t"
    "v_mfma_f32_16x16x32_bf16 a[192:195], v[208:211], v[176:179], 0\n\t"
    "s_waitcnt vmcnt(24) lgkmcnt(0)\n\t"
    "s_barrier\n\t"
    "ds_read_b128 v[204:207], v236 offset:0\n\t"
    "v_add_f32 v228, v234, v228\n\t"
    "v_mfma_f32_16x16x32_bf16 a[196:199], v[208:211], v[180:183], 0\n\t"
    "v_exp_f32 v231, v141\n\t"
    "v_mfma_f32_16x16x32_bf16 a[200:203], v[212:215], v[164:167], 0\n\t"
    "s_mov_b32 s29, s28\n\t"
    "ds_read_b128 v[208:211], v237 offset:0\n\t"
    "v_add_f32 v228, v235, v228\n\t"
    "v_mfma_f32_16x16x32_bf16 a[204:207], v[212:215], v[168:171], 0\n\t"
    "s_mov_b32 s28, s30\n\t"
    "v_exp_f32 v232, v142\n\t"
    "v_mfma_f32_16x16x32_bf16 a[208:211], v[212:215], v[172:175], 0\n\t"
    "s_add_u32 s58, s29, s34\n\t"
    "v_cvt_pk_bf16_f32 v199, v234, v235\n\t"
    "v_add_f32 v229, v230, v229\n\t"
    "v_add_f32 v229, v231, v229\n\t"
    "v_mfma_f32_16x16x32_bf16 a[212:215], v[212:215], v[176:179], 0\n\t"
    "s_add_u32 s59, s29, s35\n\t"
    "v_exp_f32 v233, v143\n\t"
    "v_mfma_f32_16x16x32_bf16 a[216:219], v[212:215], v[180:183], 0\n\t"
    "v_cvt_pk_bf16_f32 v202, v230, v231\n\t"
    "v_add_f32 v229, v232, v229\n\t"
    "v_add_f32 v229, v233, v229\n\t"
    "v_mfma_f32_16x16x32_bf16 a[220:223], v[216:219], v[164:167], 0\n\t"
    "ds_read_b128 v[212:215], v236 offset:128\n\t"
    "v_cvt_pk_bf16_f32 v203, v232, v233\n\t"
    "v_mfma_f32_16x16x32_bf16 a[224:227], v[216:219], v[168:171], 0\n\t"
    "v_max3_f32 v253, v225, v226, v227\n\t"
    "v_max3_f32 v253, v253, v228, v229\n\t"
    "v_mfma_f32_16x16x32_bf16 a[228:231], v[216:219], v[172:175], 0\n\t"
    "v_cmp_nge_f32 vcc, s50, v253\n\t"
    "v_mfma_f32_16x16x32_bf16 a[232:235], v[216:219], v[176:179], 0\n\t"
    "v_mfma_f32_16x16x32_bf16 a[236:239], v[216:219], v[180:183], 0\n\t"
    "s_waitcnt lgkmcnt(2)\n\t"
    "ds_read_b128 v[216:219], v237 offset:128\n\t"
    "s_cbranch_vccnz .Lk1_stub_s0\n\t"
    ".Lk1_cont_s0:\n\t"
    "s_mov_b32 s44, 5\n\t"
    ".Lk1_aloop:\n\t"
    "v_mfma_f32_16x16x32_bf16 v[104:107], v[204:207], v[0:3], v[144:147]\n\t"
    "s_add_u32 m0, s58, 12288\n\t"
    "v_add_u32 v238, s31, v238\n\t"
    "v_add_u32 v239, s31, v239\n\t"
    "v_mfma_f32_16x16x32_bf16 v[108:111], v[204:207], v[24:27], v[148:151]\n\t"
    "buffer_load_dwordx4 v242, s[12:15], s37 offen nt lds\n\t"
    "v_mfma_f32_16x16x32_bf16 v[112:115], v[204:207], v[48:51], v[152:155]\n\t"
    "v_add_u32 v240, s31, v240\n\t"
    "v_add_u32 v241, s31, v241\n\t"
    "v_mfma_f32_16x16x32_bf16 v[116:119], v[204:207], v[72:75], v[156:159]\n\t"
    "v_add_f32 v220, v225, v220\n\t"
    "v_add_f32 v221, v226, v221\n\t"
    "v_mfma_f32_16x16x32_bf16 v[120:123], v[204:207], v[96:99], v[160:163]\n\t"
    "s_waitcnt lgkmcnt(2)\n\t"
    "ds_read_b128 v[204:207], v236 offset:256\n\t"
    "v_add_f32 v222, v227, v222\n\t"
    "v_mfma_f32_16x16x32_bf16 v[104:107], v[208:211], v[4:7], v[104:107]\n\t"
    "v_add_f32 v223, v228, v223\n\t"
    "v_add_f32 v224, v229, v224\n\t"
    "v_mfma_f32_16x16x32_bf16 v[108:111], v[208:211], v[28:31], v[108:111]\n\t"
    "buffer_load_dwordx4 v243, s[12:15], s37 offen offset:1024 nt lds\n\t"
    "v_mfma_f32_16x16x32_bf16 v[112:115], v[208:211], v[52:55], v[112:115]\n\t"
    "v_mfma_f32_16x16x32_bf16 v[116:119], v[208:211], v[76:79], v[116:119]\n\t"
    "v_mfma_f32_16x16x32_bf16 v[120:123], v[208:211], v[100:103], v[120:123]\n\t"
    "s_waitcnt lgkmcnt(2)\n\t"
    "ds_read_b128 v[208:211], v237 offset:256\n\t"
    "v_mfma_f32_16x16x32_bf16 v[104:107], v[212:215], v[8:11], v[104:107]\n\t"
    "v_mfma_f32_16x16x32_bf16 v[108:111], v[212:215], v[32:35], v[108:111]\n\t"
    "buffer_load_dwordx4 v244, s[12:15], s37 offen offset:2048 nt lds\n\t"
    "v_mfma_f32_16x16x32_bf16 v[112:115], v[212:215], v[56:59], v[112:115]\n\t"
    "v_mfma_f32_16x16x32_bf16 v[116:119], v[212:215], v[80:83], v[116:119]\n\t"
    "v_mfma_f32_16x16x32_bf16 v[120:123], v[212:215], a[240:243], v[120:123]\n\t"
    "s_waitcnt lgkmcnt(2)\n\t"
    "ds_read_b128 v[212:215], v236 offset:6144\n\t"
    "v_mfma_f32_16x16x32_bf16 v[104:107], v[216:219], v[12:15], v[104:107]\n\t"
    "s_add_u32 m0, s58, 15360\n\t"
    "v_mfma_f32_16x16x32_bf16 v[108:111], v[216:219], v[36:39], v[108:111]\n\t"
    "s_add_u32 s41, s37, 3072\n\t"
    "buffer_load_dwordx4 v242, s[12:15], s41 offen nt lds\n\t"
    "v_mfma_f32_16x16x32_bf16 v[112:115], v[216:219], v[60:63], v[112:115]\n\t"
    "v_mfma_f32_16x16x32_bf16 v[116:119], v[216:219], v[84:87], v[116:119]\n\t"
    "v_mfma_f32_16x16x32_bf16 v[120:123], v[216:219], a[244:247], v[120:123]\n\t"
    "s_waitcnt lgkmcnt(2)\n\t"
    "ds_read_b128 v[216:219], v237 offset:6144\n\t"
    "v_mfma_f32_16x16x32_bf16 v[104:107], v[204:207], v[16:19], v[104:107]\n\t"
    "v_mfma_f32_16x16x32_bf16 v[108:111], v[204:207], v[40:43], v[108:111]\n\t"
    "buffer_load_dwordx4 v243, s[12:15], s41 offen offset:1024 nt lds\n\t"
    "v_mfma_f32_16x16x32_bf16 v[112:115], v[204:207], v[64:67], v[112:115]\n\t"
    "v_mfma_f32_16x16x32_bf16 v[116:119], v[204:207], v[88:91], v[116:119]\n\t"
    "v_mfma_f32_16x16x32_bf16 v[120:123], v[204:207], a[248:251], v[120:123]\n\t"
    "s_waitcnt lgkmcnt(2)\n\t"
    "ds_read_b128 v[204:207], v236 offset:6272\n\t"
    "v_mfma_f32_16x16x32_bf16 v[104:107], v[208:211], v[20:23], v[104:107]\n\t"
    "v_mfma_f32_16x16x32_bf16 v[108:111], v[208:211], v[44:47], v[108:111]\n\t"
    "buffer_load_dwordx4 v244, s[12:15], s41 offen offset:2048 nt lds\n\t"
    "v_mfma_f32_16x16x32_bf16 v[112:115], v[208:211], v[68:71], v[112:115]\n\t"
    "s_add_u32 s37, s37, 12288\n\t"
    "v_mfma_f32_16x16x32_bf16 v[116:119], v[208:211], v[92:95], v[116:119]\n\t"
    "v_mfma_f32_16x16x32_bf16 v[120:123], v[208:211], a[252:255], v[120:123]\n\t"
    "s_waitcnt lgkmcnt(2)\n\t"
    "ds_read_b128 v[208:211], v237 offset:6272\n\t"
    "v_mfma_f32_16x16x32_bf16 v[124:127], v[212:215], v[0:3], v[144:147]\n\t"
    "v_mfma_f32_16x16x32_bf16 v[128:131], v[212:215], v[24:27], v[148:151]\n\t"
    "s_add_u32 s30, s28, 24576\n\t"
    "v_exp_f32 v230, v104\n\t"
    "v_mfma_f32_16x16x32_bf16 v[132:135], v[212:215], v[48:51], v[152:155]\n\t"
    "s_cmp_eq_u32 s30, 73728\n\t"
    "v_exp_f32 v231, v105\n\t"
    "v_mfma_f32_16x16x32_bf16 v[136:139], v[212:215], v[72:75], v[156:159]\n\t"
    "s_cselect_b32 s30, 0, s30\n\t"
    "v_exp_f32 v232, v106\n\t"
    "v_mfma_f32_16x16x32_bf16 v[140:143], v[212:215], v[96:99], v[160:163]\n\t"
    "s_waitcnt lgkmcnt(2)\n\t"
    "ds_read_b128 v[212:215], v236 offset:6400\n\t"
    "v_add_f32 v225, v230, v231\n\t"
    "v_mfma_f32_16x16x32_bf16 v[124:127], v[216:219], v[4:7], v[124:127]\n\t"
    "s_sub_u32 s31, s30, s28\n\t"
    "v_exp_f32 v233, v107\n\t"
    "v_mfma_f32_16x16x32_bf16 v[128:131], v[216:219], v[28:31], v[128:131]\n\t"
    "v_exp_f32 v234, v108\n\t"
    "v_mfma_f32_16x16x32_bf16 v[132:135], v[216:219], v[52:55], v[132:135]\n\t"
    "v_cvt_pk_bf16_f32 v164, v230, v231\n\t"
    "v_add_f32 v225, v232, v225\n\t"
    "v_mfma_f32_16x16x32_bf16 v[136:139], v[216:219], v[76:79], v[136:139]\n\t"
    "v_exp_f32 v235, v109\n\t"
    "v_mfma_f32_16x16x32_bf16 v[140:143], v[216:219], v[100:103], v[140:143]\n\t"
    "s_waitcnt lgkmcnt(2)\n\t"
    "ds_read_b128 v[216:219], v237 offset:6400\n\t"
    "v_add_f32 v225, v233, v225\n\t"
    "v_mfma_f32_16x16x32_bf16 v[124:127], v[204:207], v[8:11], v[124:127]\n\t"
    "v_add_u32 v236, s31, v236\n\t"
    "v_add_u32 v237, s31, v237\n\t"
    "v_mfma_f32_16x16x32_bf16 v[128:131], v[204:207], v[32:35], v[128:131]\n\t"
    "s_add_u32 s75, s28, s34\n\t"
    "v_exp_f32 v230, v110\n\t"
    "v_mfma_f32_16x16x32_bf16 v[132:135], v[204:207], v[56:59], v[132:135]\n\t"
    "v_cvt_pk_bf16_f32 v165, v232, v233\n\t"
    "v_add_f32 v226, v234, v235\n\t"
    "v_mfma_f32_16x16x32_bf16 v[136:139], v[204:207], v[80:83], v[136:139]\n\t"
    "v_exp_f32 v231, v111\n\t"
    "v_mfma_f32_16x16x32_bf16 v[140:143], v[204:207], a[240:243], v[140:143]\n\t"
    "s_waitcnt vmcnt(24) lgkmcnt(0)\n\t"
    "s_barrier\n\t"
    "ds_read_b64_tr_b16 v[204:205], v238 offset:0\n\t"
    "v_cvt_pk_bf16_f32 v168, v234, v235\n\t"
    "v_mfma_f32_16x16x32_bf16 v[124:127], v[208:211], v[12:15], v[124:127]\n\t"
    "ds_read_b64_tr_b16 v[206:207], v238 offset:6144\n\t"
    "v_add_f32 v226, v230, v226\n\t"
    "v_mfma_f32_16x16x32_bf16 v[128:131], v[208:211], v[36:39], v[128:131]\n\t"
    "s_add_u32 m0, s75, 0\n\t"
    "v_exp_f32 v232, v112\n\t"
    "v_mfma_f32_16x16x32_bf16 v[132:135], v[208:211], v[60:63], v[132:135]\n\t"
    "buffer_load_dwordx4 v242, s[8:11], s36 offen nt lds\n\t"
    "v_add_f32 v226, v231, v226\n\t"
    "v_mfma_f32_16x16x32_bf16 v[136:139], v[208:211], v[84:87], v[136:139]\n\t"
    "v_exp_f32 v233, v113\n\t"
    "v_mfma_f32_16x16x32_bf16 v[140:143], v[208:211], a[244:247], v[140:143]\n\t"
    "ds_read_b64_tr_b16 v[208:209], v239 offset:0\n\t"
    "v_cvt_pk_bf16_f32 v169, v230, v231\n\t"
    "v_mfma_f32_16x16x32_bf16 v[124:127], v[212:215], v[16:19], v[124:127]\n\t"
    "ds_read_b64_tr_b16 v[210:211], v239 offset:6144\n\t"
    "v_add_f32 v227, v232, v233\n\t"
    "v_mfma_f32_16x16x32_bf16 v[128:131], v[212:215], v[40:43], v[128:131]\n\t"
    "v_exp_f32 v234, v114\n\t"
    "v_mfma_f32_16x16x32_bf16 v[132:135], v[212:215], v[64:67], v[132:135]\n\t"
    "v_exp_f32 v235, v115\n\t"
    "v_mfma_f32_16x16x32_bf16 v[136:139], v[212:215], v[88:91], v[136:139]\n\t"
    "v_exp_f32 v230, v116\n\t"
    "v_mfma_f32_16x16x32_bf16 v[140:143], v[212:215], a[248:251], v[140:143]\n\t"
    "ds_read_b64_tr_b16 v[212:213], v240 offset:0\n\t"
    "v_cvt_pk_bf16_f32 v172, v232, v233\n\t"
    "v_mfma_f32_16x16x32_bf16 v[124:127], v[216:219], v[20:23], v[124:127]\n\t"
    "ds_read_b64_tr_b16 v[214:215], v240 offset:6144\n\t"
    "v_add_f32 v227, v234, v227\n\t"
    "v_mfma_f32_16x16x32_bf16 v[128:131], v[216:219], v[44:47], v[128:131]\n\t"
    "v_exp_f32 v231, v117\n\t"
    "v_mfma_f32_16x16x32_bf16 v[132:135], v[216:219], v[68:71], v[132:135]\n\t"
    "buffer_load_dwordx4 v243, s[8:11], s36 offen offset:1024 nt lds\n\t"
    "v_add_f32 v227, v235, v227\n\t"
    "v_mfma_f32_16x16x32_bf16 v[136:139], v[216:219], v[92:95], v[136:139]\n\t"
    "v_exp_f32 v232, v118\n\t"
    "v_mfma_f32_16x16x32_bf16 v[140:143], v[216:219], a[252:255], v[140:143]\n\t"
    "s_waitcnt lgkmcnt(4)\n\t"
    "ds_read_b64_tr_b16 v[216:217], v241 offset:0\n\t"
    "v_cvt_pk_bf16_f32 v173, v234, v235\n\t"
    "v_mfma_f32_16x16x32_bf16 a[0:3], v[204:207], v[184:187], a[0:3]\n\t"
    "ds_read_b64_tr_b16 v[218:219], v241 offset:6144\n\t"
    "v_add_f32 v228, v230, v231\n\t"
    "v_mfma_f32_16x16x32_bf16 a[4:7], v[204:207], v[188:191], a[4:7]\n\t"
    "v_exp_f32 v233, v119\n\t"
    "v_mfma_f32_16x16x32_bf16 a[8:11], v[204:207], v[192:195], a[8:11]\n\t"
    "v_exp_f32 v234, v120\n\t"
    "v_mfma_f32_16x16x32_bf16 a[12:15], v[204:207], v[196:199], a[12:15]\n\t"
    "v_cvt_pk_bf16_f32 v176, v230, v231\n\t"
    "v_add_f32 v228, v232, v228\n\t"
    "v_mfma_f32_16x16x32_bf16 a[16:19], v[204:207], v[200:203], a[16:19]\n\t"
    "s_waitcnt lgkmcnt(4)\n\t"
    "ds_read_b64_tr_b16 v[204:205], v238 offset:128\n\t"
    "v_add_f32 v228, v233, v228\n\t"
    "v_mfma_f32_16x16x32_bf16 a[20:23], v[208:211], v[184:187], a[20:23]\n\t"
    "ds_read_b64_tr_b16 v[206:207], v238 offset:6272\n\t"
    "v_cvt_pk_bf16_f32 v177, v232, v233\n\t"
    "v_mfma_f32_16x16x32_bf16 a[24:27], v[208:211], v[188:191], a[24:27]\n\t"
    "v_exp_f32 v235, v121\n\t"
    "v_mfma_f32_16x16x32_bf16 a[28:31], v[208:211], v[192:195], a[28:31]\n\t"
    "buffer_load_dwordx4 v244, s[8:11], s36 offen offset:2048 nt lds\n\t"
    "v_add_f32 v229, v234, v235\n\t"
    "v_mfma_f32_16x16x32_bf16 a[32:35], v[208:211], v[196:199], a[32:35]\n\t"
    "v_exp_f32 v230, v122\n\t"
    "v_mfma_f32_16x16x32_bf16 a[36:39], v[208:211], v[200:203], a[36:39]\n\t"
    "s_waitcnt lgkmcnt(4)\n\t"
    "ds_read_b64_tr_b16 v[208:209], v239 offset:128\n\t"
    "v_cvt_pk_bf16_f32 v180, v234, v235\n\t"
    "v_mfma_f32_16x16x32_bf16 a[40:43], v[212:215], v[184:187], a[40:43]\n\t"
    "ds_read_b64_tr_b16 v[210:211], v239 offset:6272\n\t"
    "v_add_f32 v229, v230, v229\n\t"
    "v_mfma_f32_16x16x32_bf16 a[44:47], v[212:215], v[188:191], a[44:47]\n\t"
    "v_exp_f32 v231, v123\n\t"
    "v_mfma_f32_16x16x32_bf16 a[48:51], v[212:215], v[192:195], a[48:51]\n\t"
    "v_exp_f32 v232, v124\n\t"
    "v_mfma_f32_16x16x32_bf16 a[52:55], v[212:215], v[196:199], a[52:55]\n\t"
    "v_exp_f32 v233, v125\n\t"
    "v_mfma_f32_16x16x32_bf16 a[56:59], v[212:215], v[200:203], a[56:59]\n\t"
    "s_waitcnt lgkmcnt(4)\n\t"
    "ds_read_b64_tr_b16 v[212:213], v240 offset:128\n\t"
    "v_add_f32 v229, v231, v229\n\t"
    "v_mfma_f32_16x16x32_bf16 a[60:63], v[216:219], v[184:187], a[60:63]\n\t"
    "ds_read_b64_tr_b16 v[214:215], v240 offset:6272\n\t"
    "v_cvt_pk_bf16_f32 v181, v230, v231\n\t"
    "v_mfma_f32_16x16x32_bf16 a[64:67], v[216:219], v[188:191], a[64:67]\n\t"
    "s_add_u32 m0, s75, 3072\n\t"
    "v_exp_f32 v234, v126\n\t"
    "v_mfma_f32_16x16x32_bf16 a[68:71], v[216:219], v[192:195], a[68:71]\n\t"
    "s_add_u32 s40, s36, 3072\n\t"
    "buffer_load_dwordx4 v242, s[8:11], s40 offen nt lds\n\t"
    "v_add_f32 v225, v232, v225\n\t"
    "v_mfma_f32_16x16x32_bf16 a[72:75], v[216:219], v[196:199], a[72:75]\n\t"
    "v_exp_f32 v235, v127\n\t"
    "v_mfma_f32_16x16x32_bf16 a[76:79], v[216:219], v[200:203], a[76:79]\n\t"
    "s_waitcnt lgkmcnt(4)\n\t"
    "ds_read_b64_tr_b16 v[216:217], v241 offset:128\n\t"
    "v_add_f32 v225, v233, v225\n\t"
    "v_mfma_f32_16x16x32_bf16 a[80:83], v[204:207], v[184:187], a[80:83]\n\t"
    "ds_read_b64_tr_b16 v[218:219], v241 offset:6272\n\t"
    "v_cvt_pk_bf16_f32 v166, v232, v233\n\t"
    "v_mfma_f32_16x16x32_bf16 a[84:87], v[204:207], v[188:191], a[84:87]\n\t"
    "v_exp_f32 v230, v128\n\t"
    "v_mfma_f32_16x16x32_bf16 a[88:91], v[204:207], v[192:195], a[88:91]\n\t"
    "v_exp_f32 v231, v129\n\t"
    "v_mfma_f32_16x16x32_bf16 a[92:95], v[204:207], v[196:199], a[92:95]\n\t"
    "v_exp_f32 v232, v130\n\t"
    "v_mfma_f32_16x16x32_bf16 a[96:99], v[204:207], v[200:203], a[96:99]\n\t"
    "s_waitcnt lgkmcnt(4)\n\t"
    "ds_read_b64_tr_b16 v[204:205], v238 offset:256\n\t"
    "v_add_f32 v225, v234, v225\n\t"
    "v_mfma_f32_16x16x32_bf16 a[100:103], v[208:211], v[184:187], a[100:103]\n\t"
    "ds_read_b64_tr_b16 v[206:207], v238 offset:6400\n\t"
    "v_add_f32 v225, v235, v225\n\t"
    "v_mfma_f32_16x16x32_bf16 a[104:107], v[208:211], v[188:191], a[104:107]\n\t"
    "v_cvt_pk_bf16_f32 v167, v234, v235\n\t"
    "v_add_f32 v226, v230, v226\n\t"
    "v_mfma_f32_16x16x32_bf16 a[108:111], v[208:211], v[192:195], a[108:111]\n\t"
    "buffer_load_dwordx4 v243, s[8:11], s40 offen offset:1024 nt lds\n\t"
    "v_add_f32 v226, v231, v226\n\t"
    "v_mfma_f32_16x16x32_bf16 a[112:115], v[208:211], v[196:199], a[112:115]\n\t"
    "v_exp_f32 v233, v131\n\t"
    "v_mfma_f32_16x16x32_bf16 a[116:119], v[208:211], v[200:203], a[116:119]\n\t"
    "s_waitcnt lgkmcnt(4)\n\t"
    "ds_read_b64_tr_b16 v[208:209], v239 offset:256\n\t"
    "v_cvt_pk_bf16_f32 v170, v230, v231\n\t"
    "v_mfma_f32_16x16x32_bf16 a[120:123], v[212:215], v[184:187], a[120:123]\n\t"
    "ds_read_b64_tr_b16 v[210:211], v239 offset:6400\n\t"
    "v_add_f32 v226, v232, v226\n\t"
    "v_mfma_f32_16x16x32_bf16 a[124:127], v[212:215], v[188:191], a[124:127]\n\t"
    "v_exp_f32 v234, v132\n\t"
    "v_mfma_f32_16x16x32_bf16 a[128:131], v[212:215], v[192:195], a[128:131]\n\t"
    "v_exp_f32 v235, v133\n\t"
    "v_mfma_f32_16x16x32_bf16 a[132:135], v[212:215], v[196:199], a[132:135]\n\t"
    "v_exp_f32 v230, v134\n\t"
    "v_mfma_f32_16x16x32_bf16 a[136:139], v[212:215], v[200:203], a[136:139]\n\t"
    "s_waitcnt lgkmcnt(4)\n\t"
    "ds_read_b64_tr_b16 v[212:213], v240 offset:256\n\t"
    "v_add_f32 v226, v233, v226\n\t"
    "v_mfma_f32_16x16x32_bf16 a[140:143], v[216:219], v[184:187], a[140:143]\n\t"
    "ds_read_b64_tr_b16 v[214:215], v240 offset:6400\n\t"
    "v_cvt_pk_bf16_f32 v171, v232, v233\n\t"
    "v_mfma_f32_16x16x32_bf16 a[144:147], v[216:219], v[188:191], a[144:147]\n\t"
    "v_exp_f32 v231, v135\n\t"
    "v_mfma_f32_16x16x32_bf16 a[148:151], v[216:219], v[192:195], a[148:151]\n\t"
    "buffer_load_dwordx4 v244, s[8:11], s40 offen offset:2048 nt lds\n\t"
    "v_add_f32 v227, v234, v227\n\t"
    "v_mfma_f32_16x16x32_bf16 a[152:155], v[216:219], v[196:199], a[152:155]\n\t"
    "s_add_u32 s36, s36, 12288\n\t"
    "v_exp_f32 v232, v136\n\t"
    "v_mfma_f32_16x16x32_bf16 a[156:159], v[216:219], v[200:203], a[156:159]\n\t"
    "s_waitcnt lgkmcnt(4)\n\t"
    "ds_read_b64_tr_b16 v[216:217], v241 offset:256\n\t"
    "v_add_f32 v227, v235, v227\n\t"
    "v_mfma_f32_16x16x32_bf16 a[160:163], v[204:207], v[184:187], a[160:163]\n\t"
    "ds_read_b64_tr_b16 v[218:219], v241 offset:6400\n\t"
    "v_cvt_pk_bf16_f32 v174, v234, v235\n\t"
    "v_mfma_f32_16x16x32_bf16 a[164:167], v[204:207], v[188:191], a[164:167]\n\t"
    "v_exp_f32 v233, v137\n\t"
    "v_mfma_f32_16x16x32_bf16 a[168:171], v[204:207], v[192:195], a[168:171]\n\t"
    "v_exp_f32 v234, v138\n\t"
    "v_mfma_f32_16x16x32_bf16 a[172:175], v[204:207], v[196:199], a[172:175]\n\t"
    "v_add_f32 v227, v230, v227\n\t"
    "v_add_f32 v227, v231, v227\n\t"
    "v_mfma_f32_16x16x32_bf16 a[176:179], v[204:207], v[200:203], a[176:179]\n\t"
    "s_waitcnt lgkmcnt(4)\n\t"
    "v_cvt_pk_bf16_f32 v175, v230, v231\n\t"
    "v_add_f32 v228, v232, v228\n\t"
    "v_mfma_f32_16x16x32_bf16 a[180:183], v[208:211], v[184:187], a[180:183]\n\t"
    "v_exp_f32 v235, v139\n\t"
    "v_mfma_f32_16x16x32_bf16 a[184:187], v[208:211], v[188:191], a[184:187]\n\t"
    "v_exp_f32 v230, v140\n\t"
    "v_mfma_f32_16x16x32_bf16 a[188:191], v[208:211], v[192:195], a[188:191]\n\t"
    "v_add_f32 v228, v233, v228\n\t"
    "v_cvt_pk_bf16_f32 v178, v232, v233\n\t"
    "v_mfma_f32_16x16x32_bf16 a[192:195], v[208:211], v[196:199], a[192:195]\n\t"
    "s_waitcnt vmcnt(24) lgkmcnt(0)\n\t"
    "s_barrier\n\t"
    "ds_read_b128 v[204:207], v236 offset:0\n\t"
    "v_add_f32 v228, v234, v228\n\t"
    "v_mfma_f32_16x16x32_bf16 a[196:199], v[208:211], v[200:203], a[196:199]\n\t"
    "v_exp_f32 v231, v141\n\t"
    "v_mfma_f32_16x16x32_bf16 a[200:203], v[212:215], v[184:187], a[200:203]\n\t"
    "s_mov_b32 s29, s28\n\t"
    "ds_read_b128 v[208:211], v237 offset:0\n\t"
    "v_add_f32 v228, v235, v228\n\t"
    "v_mfma_f32_16x16x32_bf16 a[204:207], v[212:215], v[188:191], a[204:207]\n\t"
    "s_mov_b32 s28, s30\n\t"
    "v_exp_f32 v232, v142\n\t"
    "v_mfma_f32_16x16x32_bf16 a[208:211], v[212:215], v[192:195], a[208:211]\n\t"
    "s_add_u32 s58, s29, s34\n\t"
    "v_cvt_pk_bf16_f32 v179, v234, v235\n\t"
    "v_add_f32 v229, v230, v229\n\t"
    "v_add_f32 v229, v231, v229\n\t"
    "v_mfma_f32_16x16x32_bf16 a[212:215], v[212:215], v[196:199], a[212:215]\n\t"
    "s_add_u32 s59, s29, s35\n\t"
    "v_exp_f32 v233, v143\n\t"
    "v_mfma_f32_16x16x32_bf16 a[216:219], v[212:215], v[200:203], a[216:219]\n\t"
    "v_cvt_pk_bf16_f32 v182, v230, v231\n\t"
    "v_add_f32 v229, v232, v229\n\t"
    "v_add_f32 v229, v233, v229\n\t"
    "v_mfma_f32_16x16x32_bf16 a[220:223], v[216:219], v[184:187], a[220:223]\n\t"
    "ds_read_b128 v[212:215], v236 offset:128\n\t"
    "v_cvt_pk_bf16_f32 v183, v232, v233\n\t"
    "v_mfma_f32_16x16x32_bf16 a[224:227], v[216:219], v[188:191], a[224:227]\n\t"
    "v_max3_f32 v253, v225, v226, v227\n\t"
    "v_max3_f32 v253, v253, v228, v229\n\t"
    "v_mfma_f32_16x16x32_bf16 a[228:231], v[216:219], v[192:195], a[228:231]\n\t"
    "v_cmp_nge_f32 vcc, s50, v253\n\t"
    "v_mfma_f32_16x16x32_bf16 a[232:235], v[216:219], v[196:199], a[232:235]\n\t"
    "v_mfma_f32_16x16x32_bf16 a[236:239], v[216:219], v[200:203], a[236:239]\n\t"
    "s_waitcnt lgkmcnt(2)\n\t"
    "ds_read_b128 v[216:219], v237 offset:128\n\t"
    "s_cbranch_vccnz .Lk1_stub_sa\n\t"
    ".Lk1_cont_sa:\n\t"
    "v_mfma_f32_16x16x32_bf16 v[104:107], v[204:207], v[0:3], v[144:147]\n\t"
    "s_add_u32 m0, s58, 12288\n\t"
    "v_add_u32 v238, s31, v238\n\t"
    "v_add_u32 v239, s31, v239\n\t"
    "v_mfma_f32_16x16x32_bf16 v[108:111], v[204:207], v[24:27], v[148:151]\n\t"
    "buffer_load_dwordx4 v242, s[12:15], s37 offen nt lds\n\t"
    "v_mfma_f32_16x16x32_bf16 v[112:115], v[204:207], v[48:51], v[152:155]\n\t"
    "v_add_u32 v240, s31, v240\n\t"
    "v_add_u32 v241, s31, v241\n\t"
    "v_mfma_f32_16x16x32_bf16 v[116:119], v[204:207], v[72:75], v[156:159]\n\t"
    "v_add_f32 v220, v225, v220\n\t"
    "v_add_f32 v221, v226, v221\n\t"
    "v_mfma_f32_16x16x32_bf16 v[120:123], v[204:207], v[96:99], v[160:163]\n\t"
    "s_waitcnt lgkmcnt(2)\n\t"
    "ds_read_b128 v[204:207], v236 offset:256\n\t"
    "v_add_f32 v222, v227, v222\n\t"
    "v_mfma_f32_16x16x32_bf16 v[104:107], v[208:211], v[4:7], v[104:107]\n\t"
    "v_add_f32 v223, v228, v223\n\t"
    "v_add_f32 v224, v229, v224\n\t"
    "v_mfma_f32_16x16x32_bf16 v[108:111], v[208:211], v[28:31], v[108:111]\n\t"
    "buffer_load_dwordx4 v243, s[12:15], s37 offen offset:1024 nt lds\n\t"
    "v_mfma_f32_16x16x32_bf16 v[112:115], v[208:211], v[52:55], v[112:115]\n\t"
    "v_mfma_f32_16x16x32_bf16 v[116:119], v[208:211], v[76:79], v[116:119]\n\t"
    "v_mfma_f32_16x16x32_bf16 v[120:123], v[208:211], v[100:103], v[120:123]\n\t"
    "s_waitcnt lgkmcnt(2)\n\t"
    "ds_read_b128 v[208:211], v237 offset:256\n\t"
    "v_mfma_f32_16x16x32_bf16 v[104:107], v[212:215], v[8:11], v[104:107]\n\t"
    "v_mfma_f32_16x16x32_bf16 v[108:111], v[212:215], v[32:35], v[108:111]\n\t"
    "buffer_load_dwordx4 v244, s[12:15], s37 offen offset:2048 nt lds\n\t"
    "v_mfma_f32_16x16x32_bf16 v[112:115], v[212:215], v[56:59], v[112:115]\n\t"
    "v_mfma_f32_16x16x32_bf16 v[116:119], v[212:215], v[80:83], v[116:119]\n\t"
    "v_mfma_f32_16x16x32_bf16 v[120:123], v[212:215], a[240:243], v[120:123]\n\t"
    "s_waitcnt lgkmcnt(2)\n\t"
    "ds_read_b128 v[212:215], v236 offset:6144\n\t"
    "v_mfma_f32_16x16x32_bf16 v[104:107], v[216:219], v[12:15], v[104:107]\n\t"
    "s_add_u32 m0, s58, 15360\n\t"
    "v_mfma_f32_16x16x32_bf16 v[108:111], v[216:219], v[36:39], v[108:111]\n\t"
    "s_add_u32 s41, s37, 3072\n\t"
    "buffer_load_dwordx4 v242, s[12:15], s41 offen nt lds\n\t"
    "v_mfma_f32_16x16x32_bf16 v[112:115], v[216:219], v[60:63], v[112:115]\n\t"
    "v_mfma_f32_16x16x32_bf16 v[116:119], v[216:219], v[84:87], v[116:119]\n\t"
    "v_mfma_f32_16x16x32_bf16 v[120:123], v[216:219], a[244:247], v[120:123]\n\t"
    "s_waitcnt lgkmcnt(2)\n\t"
    "ds_read_b128 v[216:219], v237 offset:6144\n\t"
    "v_mfma_f32_16x16x32_bf16 v[104:107], v[204:207], v[16:19], v[104:107]\n\t"
    "v_mfma_f32_16x16x32_bf16 v[108:111], v[204:207], v[40:43], v[108:111]\n\t"
    "buffer_load_dwordx4 v243, s[12:15], s41 offen offset:1024 nt lds\n\t"
    "v_mfma_f32_16x16x32_bf16 v[112:115], v[204:207], v[64:67], v[112:115]\n\t"
    "v_mfma_f32_16x16x32_bf16 v[116:119], v[204:207], v[88:91], v[116:119]\n\t"
    "v_mfma_f32_16x16x32_bf16 v[120:123], v[204:207], a[248:251], v[120:123]\n\t"
    "s_waitcnt lgkmcnt(2)\n\t"
    "ds_read_b128 v[204:207], v236 offset:6272\n\t"
    "v_mfma_f32_16x16x32_bf16 v[104:107], v[208:211], v[20:23], v[104:107]\n\t"
    "v_mfma_f32_16x16x32_bf16 v[108:111], v[208:211], v[44:47], v[108:111]\n\t"
    "buffer_load_dwordx4 v244, s[12:15], s41 offen offset:2048 nt lds\n\t"
    "v_mfma_f32_16x16x32_bf16 v[112:115], v[208:211], v[68:71], v[112:115]\n\t"
    "s_add_u32 s37, s37, 12288\n\t"
    "v_mfma_f32_16x16x32_bf16 v[116:119], v[208:211], v[92:95], v[116:119]\n\t"
    "v_mfma_f32_16x16x32_bf16 v[120:123], v[208:211], a[252:255], v[120:123]\n\t"
    "s_waitcnt lgkmcnt(2)\n\t"
    "ds_read_b128 v[208:211], v237 offset:6272\n\t"
    "v_mfma_f32_16x16x32_bf16 v[124:127], v[212:215], v[0:3], v[144:147]\n\t"
    "v_mfma_f32_16x16x32_bf16 v[128:131], v[212:215], v[24:27], v[148:151]\n\t"
    "s_add_u32 s30, s28, 24576\n\t"
    "v_exp_f32 v230, v104\n\t"
    "v_mfma_f32_16x16x32_bf16 v[132:135], v[212:215], v[48:51], v[152:155]\n\t"
    "s_cmp_eq_u32 s30, 73728\n\t"
    "v_exp_f32 v231, v105\n\t"
    "v_mfma_f32_16x16x32_bf16 v[136:139], v[212:215], v[72:75], v[156:159]\n\t"
    "s_cselect_b32 s30, 0, s30\n\t"
    "v_exp_f32 v232, v106\n\t"
    "v_mfma_f32_16x16x32_bf16 v[140:143], v[212:215], v[96:99], v[160:163]\n\t"
    "s_waitcnt lgkmcnt(2)\n\t"
    "ds_read_b128 v[212:215], v236 offset:6400\n\t"
    "v_add_f32 v225, v230, v231\n\t"
    "v_mfma_f32_16x16x32_bf16 v[124:127], v[216:219], v[4:7], v[124:127]\n\t"
    "s_sub_u32 s31, s30, s28\n\t"
    "v_exp_f32 v233, v107\n\t"
    "v_mfma_f32_16x16x32_bf16 v[128:131], v[216:219], v[28:31], v[128:131]\n\t"
    "v_exp_f32 v234, v108\n\t"
    "v_mfma_f32_16x16x32_bf16 v[132:135], v[216:219], v[52:55], v[132:135]\n\t"
    "v_cvt_pk_bf16_f32 v184, v230, v231\n\t"
    "v_add_f32 v225, v232, v225\n\t"
    "v_mfma_f32_16x16x32_bf16 v[136:139], v[216:219], v[76:79], v[136:139]\n\t"
    "v_exp_f32 v235, v109\n\t"
    "v_mfma_f32_16x16x32_bf16 v[140:143], v[216:219], v[100:103], v[140:143]\n\t"
    "s_waitcnt lgkmcnt(2)\n\t"
    "ds_read_b128 v[216:219], v237 offset:6400\n\t"
    "v_add_f32 v225, v233, v225\n\t"
    "v_mfma_f32_16x16x32_bf16 v[124:127], v[204:207], v[8:11], v[124:127]\n\t"
    "v_add_u32 v236, s31, v236\n\t"
    "v_add_u32 v237, s31, v237\n\t"
    "v_mfma_f32_16x16x32_bf16 v[128:131], v[204:207], v[32:35], v[128:131]\n\t"
    "s_add_u32 s75, s28, s34\n\t"
    "v_exp_f32 v230, v110\n\t"
    "v_mfma_f32_16x16x32_bf16 v[132:135], v[204:207], v[56:59], v[132:135]\n\t"
    "v_cvt_pk_bf16_f32 v185, v232, v233\n\t"
    "v_add_f32 v226, v234, v235\n\t"
    "v_mfma_f32_16x16x32_bf16 v[136:139], v[204:207], v[80:83], v[136:139]\n\t"
    "v_exp_f32 v231, v111\n\t"
    "v_mfma_f32_16x16x32_bf16 v[140:143], v[204:207], a[240:243], v[140:143]\n\t"
    "s_waitcnt vmcnt(24) lgkmcnt(0)\n\t"
    "s_barrier\n\t"
    "ds_read_b64_tr_b16 v[204:205], v238 offset:0\n\t"
    "v_cvt_pk_bf16_f32 v188, v234, v235\n\t"
    "v_mfma_f32_16x16x32_bf16 v[124:127], v[208:211], v[12:15], v[124:127]\n\t"
    "ds_read_b64_tr_b16 v[206:207], v238 offset:6144\n\t"
    "v_add_f32 v226, v230, v226\n\t"
    "v_mfma_f32_16x16x32_bf16 v[128:131], v[208:211], v[36:39], v[128:131]\n\t"
    "s_add_u32 m0, s75, 0\n\t"
    "v_exp_f32 v232, v112\n\t"
    "v_mfma_f32_16x16x32_bf16 v[132:135], v[208:211], v[60:63], v[132:135]\n\t"
    "buffer_load_dwordx4 v242, s[8:11], s36 offen nt lds\n\t"
    "v_add_f32 v226, v231, v226\n\t"
    "v_mfma_f32_16x16x32_bf16 v[136:139], v[208:211], v[84:87], v[136:139]\n\t"
    "v_exp_f32 v233, v113\n\t"
    "v_mfma_f32_16x16x32_bf16 v[140:143], v[208:211], a[244:247], v[140:143]\n\t"
    "ds_read_b64_tr_b16 v[208:209], v239 offset:0\n\t"
    "v_cvt_pk_bf16_f32 v189, v230, v231\n\t"
    "v_mfma_f32_16x16x32_bf16 v[124:127], v[212:215], v[16:19], v[124:127]\n\t"
    "ds_read_b64_tr_b16 v[210:211], v239 offset:6144\n\t"
    "v_add_f32 v227, v232, v233\n\t"
    "v_mfma_f32_16x16x32_bf16 v[128:131], v[212:215], v[40:43], v[128:131]\n\t"
    "v_exp_f32 v234, v114\n\t"
    "v_mfma_f32_16x16x32_bf16 v[132:135], v[212:215], v[64:67], v[132:135]\n\t"
    "v_exp_f32 v235, v115\n\t"
    "v_mfma_f32_16x16x32_bf16 v[136:139], v[212:215], v[88:91], v[136:139]\n\t"
    "v_exp_f32 v230, v116\n\t"
    "v_mfma_f32_16x16x32_bf16 v[140:143], v[212:215], a[248:251], v[140:143]\n\t"
    "ds_read_b64_tr_b16 v[212:213], v240 offset:0\n\t"
    "v_cvt_pk_bf16_f32 v192, v232, v233\n\t"
    "v_mfma_f32_16x16x32_bf16 v[124:127], v[216:219], v[20:23], v[124:127]\n\t"
    "ds_read_b64_tr_b16 v[214:215], v240 offset:6144\n\t"
    "v_add_f32 v227, v234, v227\n\t"
    "v_mfma_f32_16x16x32_bf16 v[128:131], v[216:219], v[44:47], v[128:131]\n\t"
    "v_exp_f32 v231, v117\n\t"
    "v_mfma_f32_16x16x32_bf16 v[132:135], v[216:219], v[68:71], v[132:135]\n\t"
    "buffer_load_dwordx4 v243, s[8:11], s36 offen offset:1024 nt lds\n\t"
    "v_add_f32 v227, v235, v227\n\t"
    "v_mfma_f32_16x16x32_bf16 v[136:139], v[216:219], v[92:95], v[136:139]\n\t"
    "v_exp_f32 v232, v118\n\t"
    "v_mfma_f32_16x16x32_bf16 v[140:143], v[216:219], a[252:255], v[140:143]\n\t"
    "s_waitcnt lgkmcnt(4)\n\t"
    "ds_read_b64_tr_b16 v[216:217], v241 offset:0\n\t"
    "v_cvt_pk_bf16_f32 v193, v234, v235\n\t"
    "v_mfma_f32_16x16x32_bf16 a[0:3], v[204:207], v[164:167], a[0:3]\n\t"
    "ds_read_b64_tr_b16 v[218:219], v241 offset:6144\n\t"
    "v_add_f32 v228, v230, v231\n\t"
    "v_mfma_f32_16x16x32_bf16 a[4:7], v[204:207], v[168:171], a[4:7]\n\t"
    "v_exp_f32 v233, v119\n\t"
    "v_mfma_f32_16x16x32_bf16 a[8:11], v[204:207], v[172:175], a[8:11]\n\t"
    "v_exp_f32 v234, v120\n\t"
    "v_mfma_f32_16x16x32_bf16 a[12:15], v[204:207], v[176:179], a[12:15]\n\t"
    "v_cvt_pk_bf16_f32 v196, v230, v231\n\t"
    "v_add_f32 v228, v232, v228\n\t"
    "v_mfma_f32_16x16x32_bf16 a[16:19], v[204:207], v[180:183], a[16:19]\n\t"
    "s_waitcnt lgkmcnt(4)\n\t"
    "ds_read_b64_tr_b16 v[204:205], v238 offset:128\n\t"
    "v_add_f32 v228, v233, v228\n\t"
    "v_mfma_f32_16x16x32_bf16 a[20:23], v[208:211], v[164:167], a[20:23]\n\t"
    "ds_read_b64_tr_b16 v[206:207], v238 offset:6272\n\t"
    "v_cvt_pk_bf16_f32 v197, v232, v233\n\t"
    "v_mfma_f32_16x16x32_bf16 a[24:27], v[208:211], v[168:171], a[24:27]\n\t"
    "v_exp_f32 v235, v121\n\t"
    "v_mfma_f32_16x16x32_bf16 a[28:31], v[208:211], v[172:175], a[28:31]\n\t"
    "buffer_load_dwordx4 v244, s[8:11], s36 offen offset:2048 nt lds\n\t"
    "v_add_f32 v229, v234, v235\n\t"
    "v_mfma_f32_16x16x32_bf16 a[32:35], v[208:211], v[176:179], a[32:35]\n\t"
    "v_exp_f32 v230, v122\n\t"
    "v_mfma_f32_16x16x32_bf16 a[36:39], v[208:211], v[180:183], a[36:39]\n\t"
    "s_waitcnt lgkmcnt(4)\n\t"
    "ds_read_b64_tr_b16 v[208:209], v239 offset:128\n\t"
    "v_cvt_pk_bf16_f32 v200, v234, v235\n\t"
    "v_mfma_f32_16x16x32_bf16 a[40:43], v[212:215], v[164:167], a[40:43]\n\t"
    "ds_read_b64_tr_b16 v[210:211], v239 offset:6272\n\t"
    "v_add_f32 v229, v230, v229\n\t"
    "v_mfma_f32_16x16x32_bf16 a[44:47], v[212:215], v[168:171], a[44:47]\n\t"
    "v_exp_f32 v231, v123\n\t"
    "v_mfma_f32_16x16x32_bf16 a[48:51], v[212:215], v[172:175], a[48:51]\n\t"
    "v_exp_f32 v232, v124\n\t"
    "v_mfma_f32_16x16x32_bf16 a[52:55], v[212:215], v[176:179], a[52:55]\n\t"
    "v_exp_f32 v233, v125\n\t"
    "v_mfma_f32_16x16x32_bf16 a[56:59], v[212:215], v[180:183], a[56:59]\n\t"
    "s_waitcnt lgkmcnt(4)\n\t"
    "ds_read_b64_tr_b16 v[212:213], v240 offset:128\n\t"
    "v_add_f32 v229, v231, v229\n\t"
    "v_mfma_f32_16x16x32_bf16 a[60:63], v[216:219], v[164:167], a[60:63]\n\t"
    "ds_read_b64_tr_b16 v[214:215], v240 offset:6272\n\t"
    "v_cvt_pk_bf16_f32 v201, v230, v231\n\t"
    "v_mfma_f32_16x16x32_bf16 a[64:67], v[216:219], v[168:171], a[64:67]\n\t"
    "s_add_u32 m0, s75, 3072\n\t"
    "v_exp_f32 v234, v126\n\t"
    "v_mfma_f32_16x16x32_bf16 a[68:71], v[216:219], v[172:175], a[68:71]\n\t"
    "s_add_u32 s40, s36, 3072\n\t"
    "buffer_load_dwordx4 v242, s[8:11], s40 offen nt lds\n\t"
    "v_add_f32 v225, v232, v225\n\t"
    "v_mfma_f32_16x16x32_bf16 a[72:75], v[216:219], v[176:179], a[72:75]\n\t"
    "v_exp_f32 v235, v127\n\t"
    "v_mfma_f32_16x16x32_bf16 a[76:79], v[216:219], v[180:183], a[76:79]\n\t"
    "s_waitcnt lgkmcnt(4)\n\t"
    "ds_read_b64_tr_b16 v[216:217], v241 offset:128\n\t"
    "v_add_f32 v225, v233, v225\n\t"
    "v_mfma_f32_16x16x32_bf16 a[80:83], v[204:207], v[164:167], a[80:83]\n\t"
    "ds_read_b64_tr_b16 v[218:219], v241 offset:6272\n\t"
    "v_cvt_pk_bf16_f32 v186, v232, v233\n\t"
    "v_mfma_f32_16x16x32_bf16 a[84:87], v[204:207], v[168:171], a[84:87]\n\t"
    "v_exp_f32 v230, v128\n\t"
    "v_mfma_f32_16x16x32_bf16 a[88:91], v[204:207], v[172:175], a[88:91]\n\t"
    "v_exp_f32 v231, v129\n\t"
    "v_mfma_f32_16x16x32_bf16 a[92:95], v[204:207], v[176:179], a[92:95]\n\t"
    "v_exp_f32 v232, v130\n\t"
    "v_mfma_f32_16x16x32_bf16 a[96:99], v[204:207], v[180:183], a[96:99]\n\t"
    "s_waitcnt lgkmcnt(4)\n\t"
    "ds_read_b64_tr_b16 v[204:205], v238 offset:256\n\t"
    "v_add_f32 v225, v234, v225\n\t"
    "v_mfma_f32_16x16x32_bf16 a[100:103], v[208:211], v[164:167], a[100:103]\n\t"
    "ds_read_b64_tr_b16 v[206:207], v238 offset:6400\n\t"
    "v_add_f32 v225, v235, v225\n\t"
    "v_mfma_f32_16x16x32_bf16 a[104:107], v[208:211], v[168:171], a[104:107]\n\t"
    "v_cvt_pk_bf16_f32 v187, v234, v235\n\t"
    "v_add_f32 v226, v230, v226\n\t"
    "v_mfma_f32_16x16x32_bf16 a[108:111], v[208:211], v[172:175], a[108:111]\n\t"
    "buffer_load_dwordx4 v243, s[8:11], s40 offen offset:1024 nt lds\n\t"
    "v_add_f32 v226, v231, v226\n\t"
    "v_mfma_f32_16x16x32_bf16 a[112:115], v[208:211], v[176:179], a[112:115]\n\t"
    "v_exp_f32 v233, v131\n\t"
    "v_mfma_f32_16x16x32_bf16 a[116:119], v[208:211], v[180:183], a[116:119]\n\t"
    "s_waitcnt lgkmcnt(4)\n\t"
    "ds_read_b64_tr_b16 v[208:209], v239 offset:256\n\t"
    "v_cvt_pk_bf16_f32 v190, v230, v231\n\t"
    "v_mfma_f32_16x16x32_bf16 a[120:123], v[212:215], v[164:167], a[120:123]\n\t"
    "ds_read_b64_tr_b16 v[210:211], v239 offset:6400\n\t"
    "v_add_f32 v226, v232, v226\n\t"
    "v_mfma_f32_16x16x32_bf16 a[124:127], v[212:215], v[168:171], a[124:127]\n\t"
    "v_exp_f32 v234, v132\n\t"
    "v_mfma_f32_16x16x32_bf16 a[128:131], v[212:215], v[172:175], a[128:131]\n\t"
    "v_exp_f32 v235, v133\n\t"
    "v_mfma_f32_16x16x32_bf16 a[132:135], v[212:215], v[176:179], a[132:135]\n\t"
    "v_exp_f32 v230, v134\n\t"
    "v_mfma_f32_16x16x32_bf16 a[136:139], v[212:215], v[180:183], a[136:139]\n\t"
    "s_waitcnt lgkmcnt(4)\n\t"
    "ds_read_b64_tr_b16 v[212:213], v240 offset:256\n\t"
    "v_add_f32 v226, v233, v226\n\t"
    "v_mfma_f32_16x16x32_bf16 a[140:143], v[216:219], v[164:167], a[140:143]\n\t"
    "ds_read_b64_tr_b16 v[214:215], v240 offset:6400\n\t"
    "v_cvt_pk_bf16_f32 v191, v232, v233\n\t"
    "v_mfma_f32_16x16x32_bf16 a[144:147], v[216:219], v[168:171], a[144:147]\n\t"
    "v_exp_f32 v231, v135\n\t"
    "v_mfma_f32_16x16x32_bf16 a[148:151], v[216:219], v[172:175], a[148:151]\n\t"
    "buffer_load_dwordx4 v244, s[8:11], s40 offen offset:2048 nt lds\n\t"
    "v_add_f32 v227, v234, v227\n\t"
    "v_mfma_f32_16x16x32_bf16 a[152:155], v[216:219], v[176:179], a[152:155]\n\t"
    "s_add_u32 s36, s36, 12288\n\t"
    "v_exp_f32 v232, v136\n\t"
    "v_mfma_f32_16x16x32_bf16 a[156:159], v[216:219], v[180:183], a[156:159]\n\t"
    "s_waitcnt lgkmcnt(4)\n\t"
    "ds_read_b64_tr_b16 v[216:217], v241 offset:256\n\t"
    "v_add_f32 v227, v235, v227\n\t"
    "v_mfma_f32_16x16x32_bf16 a[160:163], v[204:207], v[164:167], a[160:163]\n\t"
    "ds_read_b64_tr_b16 v[218:219], v241 offset:6400\n\t"
    "v_cvt_pk_bf16_f32 v194, v234, v235\n\t"
    "v_mfma_f32_16x16x32_bf16 a[164:167], v[204:207], v[168:171], a[164:167]\n\t"
    "v_exp_f32 v233, v137\n\t"
    "v_mfma_f32_16x16x32_bf16 a[168:171], v[204:207], v[172:175], a[168:171]\n\t"
    "v_exp_f32 v234, v138\n\t"
    "v_mfma_f32_16x16x32_bf16 a[172:175], v[204:207], v[176:179], a[172:175]\n\t"
    "v_add_f32 v227, v230, v227\n\t"
    "v_add_f32 v227, v231, v227\n\t"
    "v_mfma_f32_16x16x32_bf16 a[176:179], v[204:207], v[180:183], a[176:179]\n\t"
    "s_waitcnt lgkmcnt(4)\n\t"
    "v_cvt_pk_bf16_f32 v195, v230, v231\n\t"
    "v_add_f32 v228, v232, v228\n\t"
    "v_mfma_f32_16x16x32_bf16 a[180:183], v[208:211], v[164:167], a[180:183]\n\t"
    "v_exp_f32 v235, v139\n\t"
    "v_mfma_f32_16x16x32_bf16 a[184:187], v[208:211], v[168:171], a[184:187]\n\t"
    "v_exp_f32 v230, v140\n\t"
    "v_mfma_f32_16x16x32_bf16 a[188:191], v[208:211], v[172:175], a[188:191]\n\t"
    "v_add_f32 v228, v233, v228\n\t"
    "v_cvt_pk_bf16_f32 v198, v232, v233\n\t"
    "v_mfma_f32_16x16x32_bf16 a[192:195], v[208:211], v[176:179], a[192:195]\n\t"
    "s_waitcnt vmcnt(24) lgkmcnt(0)\n\t"
    "s_barrier\n\t"
    "ds_read_b128 v[204:207], v236 offset:0\n\t"
    "v_add_f32 v228, v234, v228\n\t"
    "v_mfma_f32_16x16x32_bf16 a[196:199], v[208:211], v[180:183], a[196:199]\n\t"
    "v_exp_f32 v231, v141\n\t"
    "v_mfma_f32_16x16x32_bf16 a[200:203], v[212:215], v[164:167], a[200:203]\n\t"
    "s_mov_b32 s29, s28\n\t"
    "ds_read_b128 v[208:211], v237 offset:0\n\t"
    "v_add_f32 v228, v235, v228\n\t"
    "v_mfma_f32_16x16x32_bf16 a[204:207], v[212:215], v[168:171], a[204:207]\n\t"
    "s_mov_b32 s28, s30\n\t"
    "v_exp_f32 v232, v142\n\t"
    "v_mfma_f32_16x16x32_bf16 a[208:211], v[212:215], v[172:175], a[208:211]\n\t"
    "s_add_u32 s58, s29, s34\n\t"
    "v_cvt_pk_bf16_f32 v199, v234, v235\n\t"
    "v_add_f32 v229, v230, v229\n\t"
    "v_add_f32 v229, v231, v229\n\t"
    "v_mfma_f32_16x16x32_bf16 a[212:215], v[212:215], v[176:179], a[212:215]\n\t"
    "s_add_u32 s59, s29, s35\n\t"
    "v_exp_f32 v233, v143\n\t"
    "v_mfma_f32_16x16x32_bf16 a[216:219], v[212:215], v[180:183], a[216:219]\n\t"
    "v_cvt_pk_bf16_f32 v202, v230, v231\n\t"
    "v_add_f32 v229, v232, v229\n\t"
    "v_add_f32 v229, v233, v229\n\t"
    "v_mfma_f32_16x16x32_bf16 a[220:223], v[216:219], v[164:167], a[220:223]\n\t"
    "ds_read_b128 v[212:215], v236 offset:128\n\t"
    "v_cvt_pk_bf16_f32 v203, v232, v233\n\t"
    "v_mfma_f32_16x16x32_bf16 a[224:227], v[216:219], v[168:171], a[224:227]\n\t"
    "v_max3_f32 v253, v225, v226, v227\n\t"
    "v_max3_f32 v253, v253, v228, v229\n\t"
    "v_mfma_f32_16x16x32_bf16 a[228:231], v[216:219], v[172:175], a[228:231]\n\t"
    "v_cmp_nge_f32 vcc, s50, v253\n\t"
    "v_mfma_f32_16x16x32_bf16 a[232:235], v[216:219], v[176:179], a[232:235]\n\t"
    "v_mfma_f32_16x16x32_bf16 a[236:239], v[216:219], v[180:183], a[236:239]\n\t"
    "s_waitcnt lgkmcnt(2)\n\t"
    "ds_read_b128 v[216:219], v237 offset:128\n\t"
    "s_cbranch_vccnz .Lk1_stub_sb\n\t"
    ".Lk1_cont_sb:\n\t"
    "s_sub_u32 s44, s44, 1\n\t"
    "s_cmp_lg_u32 s44, 0\n\t"
    "s_cbranch_scc1 .Lk1_aloop\n\t"
    "v_mfma_f32_16x16x32_bf16 v[104:107], v[204:207], v[0:3], v[144:147]\n\t"
    "s_add_u32 m0, s58, 12288\n\t"
    "v_add_u32 v238, s31, v238\n\t"
    "v_add_u32 v239, s31, v239\n\t"
    "v_mfma_f32_16x16x32_bf16 v[108:111], v[204:207], v[24:27], v[148:151]\n\t"
    "buffer_load_dwordx4 v242, s[12:15], s37 offen nt lds\n\t"
    "v_mfma_f32_16x16x32_bf16 v[112:115], v[204:207], v[48:51], v[152:155]\n\t"
    "v_add_u32 v240, s31, v240\n\t"
    "v_add_u32 v241, s31, v241\n\t"
    "v_mfma_f32_16x16x32_bf16 v[116:119], v[204:207], v[72:75], v[156:159]\n\t"
    "v_add_f32 v220, v225, v220\n\t"
    "v_add_f32 v221, v226, v221\n\t"
    "v_mfma_f32_16x16x32_bf16 v[120:123], v[204:207], v[96:99], v[160:163]\n\t"
    "s_waitcnt lgkmcnt(2)\n\t"
    "ds_read_b128 v[204:207], v236 offset:256\n\t"
    "v_add_f32 v222, v227, v222\n\t"
    "v_mfma_f32_16x16x32_bf16 v[104:107], v[208:211], v[4:7], v[104:107]\n\t"
    "v_add_f32 v223, v228, v223\n\t"
    "v_add_f32 v224, v229, v224\n\t"
    "v_mfma_f32_16x16x32_bf16 v[108:111], v[208:211], v[28:31], v[108:111]\n\t"
    "buffer_load_dwordx4 v243, s[12:15], s37 offen offset:1024 nt lds\n\t"
    "v_mfma_f32_16x16x32_bf16 v[112:115], v[208:211], v[52:55], v[112:115]\n\t"
    "v_mfma_f32_16x16x32_bf16 v[116:119], v[208:211], v[76:79], v[116:119]\n\t"
    "v_mfma_f32_16x16x32_bf16 v[120:123], v[208:211], v[100:103], v[120:123]\n\t"
    "s_waitcnt lgkmcnt(2)\n\t"
    "ds_read_b128 v[208:211], v237 offset:256\n\t"
    "v_mfma_f32_16x16x32_bf16 v[104:107], v[212:215], v[8:11], v[104:107]\n\t"
    "v_mfma_f32_16x16x32_bf16 v[108:111], v[212:215], v[32:35], v[108:111]\n\t"
    "buffer_load_dwordx4 v244, s[12:15], s37 offen offset:2048 nt lds\n\t"
    "v_mfma_f32_16x16x32_bf16 v[112:115], v[212:215], v[56:59], v[112:115]\n\t"
    "v_mfma_f32_16x16x32_bf16 v[116:119], v[212:215], v[80:83], v[116:119]\n\t"
    "v_mfma_f32_16x16x32_bf16 v[120:123], v[212:215], a[240:243], v[120:123]\n\t"
    "s_waitcnt lgkmcnt(2)\n\t"
    "ds_read_b128 v[212:215], v236 offset:6144\n\t"
    "v_mfma_f32_16x16x32_bf16 v[104:107], v[216:219], v[12:15], v[104:107]\n\t"
    "s_add_u32 m0, s58, 15360\n\t"
    "v_mfma_f32_16x16x32_bf16 v[108:111], v[216:219], v[36:39], v[108:111]\n\t"
    "s_add_u32 s41, s37, 3072\n\t"
    "buffer_load_dwordx4 v242, s[12:15], s41 offen nt lds\n\t"
    "v_mfma_f32_16x16x32_bf16 v[112:115], v[216:219], v[60:63], v[112:115]\n\t"
    "v_mfma_f32_16x16x32_bf16 v[116:119], v[216:219], v[84:87], v[116:119]\n\t"
    "v_mfma_f32_16x16x32_bf16 v[120:123], v[216:219], a[244:247], v[120:123]\n\t"
    "s_waitcnt lgkmcnt(2)\n\t"
    "ds_read_b128 v[216:219], v237 offset:6144\n\t"
    "v_mfma_f32_16x16x32_bf16 v[104:107], v[204:207], v[16:19], v[104:107]\n\t"
    "v_mfma_f32_16x16x32_bf16 v[108:111], v[204:207], v[40:43], v[108:111]\n\t"
    "buffer_load_dwordx4 v243, s[12:15], s41 offen offset:1024 nt lds\n\t"
    "v_mfma_f32_16x16x32_bf16 v[112:115], v[204:207], v[64:67], v[112:115]\n\t"
    "v_mfma_f32_16x16x32_bf16 v[116:119], v[204:207], v[88:91], v[116:119]\n\t"
    "v_mfma_f32_16x16x32_bf16 v[120:123], v[204:207], a[248:251], v[120:123]\n\t"
    "s_waitcnt lgkmcnt(2)\n\t"
    "ds_read_b128 v[204:207], v236 offset:6272\n\t"
    "v_mfma_f32_16x16x32_bf16 v[104:107], v[208:211], v[20:23], v[104:107]\n\t"
    "v_mfma_f32_16x16x32_bf16 v[108:111], v[208:211], v[44:47], v[108:111]\n\t"
    "buffer_load_dwordx4 v244, s[12:15], s41 offen offset:2048 nt lds\n\t"
    "v_mfma_f32_16x16x32_bf16 v[112:115], v[208:211], v[68:71], v[112:115]\n\t"
    "s_add_u32 s37, s37, 12288\n\t"
    "v_mfma_f32_16x16x32_bf16 v[116:119], v[208:211], v[92:95], v[116:119]\n\t"
    "v_mfma_f32_16x16x32_bf16 v[120:123], v[208:211], a[252:255], v[120:123]\n\t"
    "s_waitcnt lgkmcnt(2)\n\t"
    "ds_read_b128 v[208:211], v237 offset:6272\n\t"
    "v_mfma_f32_16x16x32_bf16 v[124:127], v[212:215], v[0:3], v[144:147]\n\t"
    "v_mfma_f32_16x16x32_bf16 v[128:131], v[212:215], v[24:27], v[148:151]\n\t"
    "s_add_u32 s30, s28, 24576\n\t"
    "v_exp_f32 v230, v104\n\t"
    "v_mfma_f32_16x16x32_bf16 v[132:135], v[212:215], v[48:51], v[152:155]\n\t"
    "s_cmp_eq_u32 s30, 73728\n\t"
    "v_exp_f32 v231, v105\n\t"
    "v_mfma_f32_16x16x32_bf16 v[136:139], v[212:215], v[72:75], v[156:159]\n\t"
    "s_cselect_b32 s30, 0, s30\n\t"
    "v_exp_f32 v232, v106\n\t"
    "v_mfma_f32_16x16x32_bf16 v[140:143], v[212:215], v[96:99], v[160:163]\n\t"
    "s_waitcnt lgkmcnt(2)\n\t"
    "ds_read_b128 v[212:215], v236 offset:6400\n\t"
    "v_add_f32 v225, v230, v231\n\t"
    "v_mfma_f32_16x16x32_bf16 v[124:127], v[216:219], v[4:7], v[124:127]\n\t"
    "s_sub_u32 s31, s30, s28\n\t"
    "v_exp_f32 v233, v107\n\t"
    "v_mfma_f32_16x16x32_bf16 v[128:131], v[216:219], v[28:31], v[128:131]\n\t"
    "v_exp_f32 v234, v108\n\t"
    "v_mfma_f32_16x16x32_bf16 v[132:135], v[216:219], v[52:55], v[132:135]\n\t"
    "v_cvt_pk_bf16_f32 v164, v230, v231\n\t"
    "v_add_f32 v225, v232, v225\n\t"
    "v_mfma_f32_16x16x32_bf16 v[136:139], v[216:219], v[76:79], v[136:139]\n\t"
    "v_exp_f32 v235, v109\n\t"
    "v_mfma_f32_16x16x32_bf16 v[140:143], v[216:219], v[100:103], v[140:143]\n\t"
    "s_waitcnt lgkmcnt(2)\n\t"
    "ds_read_b128 v[216:219], v237 offset:6400\n\t"
    "v_add_f32 v225, v233, v225\n\t"
    "v_mfma_f32_16x16x32_bf16 v[124:127], v[204:207], v[8:11], v[124:127]\n\t"
    "v_add_u32 v236, s31, v236\n\t"
    "v_add_u32 v237, s31, v237\n\t"
    "v_mfma_f32_16x16x32_bf16 v[128:131], v[204:207], v[32:35], v[128:131]\n\t"
    "s_add_u32 s75, s28, s34\n\t"
    "v_exp_f32 v230, v110\n\t"
    "v_mfma_f32_16x16x32_bf16 v[132:135], v[204:207], v[56:59], v[132:135]\n\t"
    "v_cvt_pk_bf16_f32 v165, v232, v233\n\t"
    "v_add_f32 v226, v234, v235\n\t"
    "v_mfma_f32_16x16x32_bf16 v[136:139], v[204:207], v[80:83], v[136:139]\n\t"
    "v_exp_f32 v231, v111\n\t"
    "v_mfma_f32_16x16x32_bf16 v[140:143], v[204:207], a[240:243], v[140:143]\n\t"
    "s_waitcnt vmcnt(24) lgkmcnt(0)\n\t"
    "s_barrier\n\t"
    "ds_read_b64_tr_b16 v[204:205], v238 offset:0\n\t"
    "v_cvt_pk_bf16_f32 v168, v234, v235\n\t"
    "v_mfma_f32_16x16x32_bf16 v[124:127], v[208:211], v[12:15], v[124:127]\n\t"
    "ds_read_b64_tr_b16 v[206:207], v238 offset:6144\n\t"
    "v_add_f32 v226, v230, v226\n\t"
    "v_mfma_f32_16x16x32_bf16 v[128:131], v[208:211], v[36:39], v[128:131]\n\t"
    "s_add_u32 m0, s75, 0\n\t"
    "v_exp_f32 v232, v112\n\t"
    "v_mfma_f32_16x16x32_bf16 v[132:135], v[208:211], v[60:63], v[132:135]\n\t"
    "buffer_load_dwordx4 v242, s[8:11], s36 offen nt lds\n\t"
    "v_add_f32 v226, v231, v226\n\t"
    "v_mfma_f32_16x16x32_bf16 v[136:139], v[208:211], v[84:87], v[136:139]\n\t"
    "v_exp_f32 v233, v113\n\t"
    "v_mfma_f32_16x16x32_bf16 v[140:143], v[208:211], a[244:247], v[140:143]\n\t"
    "ds_read_b64_tr_b16 v[208:209], v239 offset:0\n\t"
    "v_cvt_pk_bf16_f32 v169, v230, v231\n\t"
    "v_mfma_f32_16x16x32_bf16 v[124:127], v[212:215], v[16:19], v[124:127]\n\t"
    "ds_read_b64_tr_b16 v[210:211], v239 offset:6144\n\t"
    "v_add_f32 v227, v232, v233\n\t"
    "v_mfma_f32_16x16x32_bf16 v[128:131], v[212:215], v[40:43], v[128:131]\n\t"
    "v_exp_f32 v234, v114\n\t"
    "v_mfma_f32_16x16x32_bf16 v[132:135], v[212:215], v[64:67], v[132:135]\n\t"
    "v_exp_f32 v235, v115\n\t"
    "v_mfma_f32_16x16x32_bf16 v[136:139], v[212:215], v[88:91], v[136:139]\n\t"
    "v_exp_f32 v230, v116\n\t"
    "v_mfma_f32_16x16x32_bf16 v[140:143], v[212:215], a[248:251], v[140:143]\n\t"
    "ds_read_b64_tr_b16 v[212:213], v240 offset:0\n\t"
    "v_cvt_pk_bf16_f32 v172, v232, v233\n\t"
    "v_mfma_f32_16x16x32_bf16 v[124:127], v[216:219], v[20:23], v[124:127]\n\t"
    "ds_read_b64_tr_b16 v[214:215], v240 offset:6144\n\t"
    "v_add_f32 v227, v234, v227\n\t"
    "v_mfma_f32_16x16x32_bf16 v[128:131], v[216:219], v[44:47], v[128:131]\n\t"
    "v_exp_f32 v231, v117\n\t"
    "v_mfma_f32_16x16x32_bf16 v[132:135], v[216:219], v[68:71], v[132:135]\n\t"
    "buffer_load_dwordx4 v243, s[8:11], s36 offen offset:1024 nt lds\n\t"
    "v_add_f32 v227, v235, v227\n\t"
    "v_mfma_f32_16x16x32_bf16 v[136:139], v[216:219], v[92:95], v[136:139]\n\t"
    "v_exp_f32 v232, v118\n\t"
    "v_mfma_f32_16x16x32_bf16 v[140:143], v[216:219], a[252:255], v[140:143]\n\t"
    "s_waitcnt lgkmcnt(4)\n\t"
    "ds_read_b64_tr_b16 v[216:217], v241 offset:0\n\t"
    "v_cvt_pk_bf16_f32 v173, v234, v235\n\t"
    "v_mfma_f32_16x16x32_bf16 a[0:3], v[204:207], v[184:187], a[0:3]\n\t"
    "ds_read_b64_tr_b16 v[218:219], v241 offset:6144\n\t"
    "v_add_f32 v228, v230, v231\n\t"
    "v_mfma_f32_16x16x32_bf16 a[4:7], v[204:207], v[188:191], a[4:7]\n\t"
    "v_exp_f32 v233, v119\n\t"
    "v_mfma_f32_16x16x32_bf16 a[8:11], v[204:207], v[192:195], a[8:11]\n\t"
    "v_exp_f32 v234, v120\n\t"
    "v_mfma_f32_16x16x32_bf16 a[12:15], v[204:207], v[196:199], a[12:15]\n\t"
    "v_cvt_pk_bf16_f32 v176, v230, v231\n\t"
    "v_add_f32 v228, v232, v228\n\t"
    "v_mfma_f32_16x16x32_bf16 a[16:19], v[204:207], v[200:203], a[16:19]\n\t"
    "s_waitcnt lgkmcnt(4)\n\t"
    "ds_read_b64_tr_b16 v[204:205], v238 offset:128\n\t"
    "v_add_f32 v228, v233, v228\n\t"
    "v_mfma_f32_16x16x32_bf16 a[20:23], v[208:211], v[184:187], a[20:23]\n\t"
    "ds_read_b64_tr_b16 v[206:207], v238 offset:6272\n\t"
    "v_cvt_pk_bf16_f32 v177, v232, v233\n\t"
    "v_mfma_f32_16x16x32_bf16 a[24:27], v[208:211], v[188:191], a[24:27]\n\t"
    "v_exp_f32 v235, v121\n\t"
    "v_mfma_f32_16x16x32_bf16 a[28:31], v[208:211], v[192:195], a[28:31]\n\t"
    "buffer_load_dwordx4 v244, s[8:11], s36 offen offset:2048 nt lds\n\t"
    "v_add_f32 v229, v234, v235\n\t"
    "v_mfma_f32_16x16x32_bf16 a[32:35], v[208:211], v[196:199], a[32:35]\n\t"
    "v_exp_f32 v230, v122\n\t"
    "v_mfma_f32_16x16x32_bf16 a[36:39], v[208:211], v[200:203], a[36:39]\n\t"
    "s_waitcnt lgkmcnt(4)\n\t"
    "ds_read_b64_tr_b16 v[208:209], v239 offset:128\n\t"
    "v_cvt_pk_bf16_f32 v180, v234, v235\n\t"
    "v_mfma_f32_16x16x32_bf16 a[40:43], v[212:215], v[184:187], a[40:43]\n\t"
    "ds_read_b64_tr_b16 v[210:211], v239 offset:6272\n\t"
    "v_add_f32 v229, v230, v229\n\t"
    "v_mfma_f32_16x16x32_bf16 a[44:47], v[212:215], v[188:191], a[44:47]\n\t"
    "v_exp_f32 v231, v123\n\t"
    "v_mfma_f32_16x16x32_bf16 a[48:51], v[212:215], v[192:195], a[48:51]\n\t"
    "v_exp_f32 v232, v124\n\t"
    "v_mfma_f32_16x16x32_bf16 a[52:55], v[212:215], v[196:199], a[52:55]\n\t"
    "v_exp_f32 v233, v125\n\t"
    "v_mfma_f32_16x16x32_bf16 a[56:59], v[212:215], v[200:203], a[56:59]\n\t"
    "s_waitcnt lgkmcnt(4)\n\t"
    "ds_read_b64_tr_b16 v[212:213], v240 offset:128\n\t"
    "v_add_f32 v229, v231, v229\n\t"
    "v_mfma_f32_16x16x32_bf16 a[60:63], v[216:219], v[184:187], a[60:63]\n\t"
    "ds_read_b64_tr_b16 v[214:215], v240 offset:6272\n\t"
    "v_cvt_pk_bf16_f32 v181, v230, v231\n\t"
    "v_mfma_f32_16x16x32_bf16 a[64:67], v[216:219], v[188:191], a[64:67]\n\t"
    "s_add_u32 m0, s75, 3072\n\t"
    "v_exp_f32 v234, v126\n\t"
    "v_mfma_f32_16x16x32_bf16 a[68:71], v[216:219], v[192:195], a[68:71]\n\t"
    "s_add_u32 s40, s36, 3072\n\t"
    "buffer_load_dwordx4 v242, s[8:11], s40 offen nt lds\n\t"
    "v_add_f32 v225, v232, v225\n\t"
    "v_mfma_f32_16x16x32_bf16 a[72:75], v[216:219], v[196:199], a[72:75]\n\t"
    "v_exp_f32 v235, v127\n\t"
    "v_mfma_f32_16x16x32_bf16 a[76:79], v[216:219], v[200:203], a[76:79]\n\t"
    "s_waitcnt lgkmcnt(4)\n\t"
    "ds_read_b64_tr_b16 v[216:217], v241 offset:128\n\t"
    "v_add_f32 v225, v233, v225\n\t"
    "v_mfma_f32_16x16x32_bf16 a[80:83], v[204:207], v[184:187], a[80:83]\n\t"
    "ds_read_b64_tr_b16 v[218:219], v241 offset:6272\n\t"
    "v_cvt_pk_bf16_f32 v166, v232, v233\n\t"
    "v_mfma_f32_16x16x32_bf16 a[84:87], v[204:207], v[188:191], a[84:87]\n\t"
    "v_exp_f32 v230, v128\n\t"
    "v_mfma_f32_16x16x32_bf16 a[88:91], v[204:207], v[192:195], a[88:91]\n\t"
    "v_exp_f32 v231, v129\n\t"
    "v_mfma_f32_16x16x32_bf16 a[92:95], v[204:207], v[196:199], a[92:95]\n\t"
    "v_exp_f32 v232, v130\n\t"
    "v_mfma_f32_16x16x32_bf16 a[96:99], v[204:207], v[200:203], a[96:99]\n\t"
    "s_waitcnt lgkmcnt(4)\n\t"
    "ds_read_b64_tr_b16 v[204:205], v238 offset:256\n\t"
    "v_add_f32 v225, v234, v225\n\t"
    "v_mfma_f32_16x16x32_bf16 a[100:103], v[208:211], v[184:187], a[100:103]\n\t"
    "ds_read_b64_tr_b16 v[206:207], v238 offset:6400\n\t"
    "v_add_f32 v225, v235, v225\n\t"
    "v_mfma_f32_16x16x32_bf16 a[104:107], v[208:211], v[188:191], a[104:107]\n\t"
    "v_cvt_pk_bf16_f32 v167, v234, v235\n\t"
    "v_add_f32 v226, v230, v226\n\t"
    "v_mfma_f32_16x16x32_bf16 a[108:111], v[208:211], v[192:195], a[108:111]\n\t"
    "buffer_load_dwordx4 v243, s[8:11], s40 offen offset:1024 nt lds\n\t"
    "v_add_f32 v226, v231, v226\n\t"
    "v_mfma_f32_16x16x32_bf16 a[112:115], v[208:211], v[196:199], a[112:115]\n\t"
    "v_exp_f32 v233, v131\n\t"
    "v_mfma_f32_16x16x32_bf16 a[116:119], v[208:211], v[200:203], a[116:119]\n\t"
    "s_waitcnt lgkmcnt(4)\n\t"
    "ds_read_b64_tr_b16 v[208:209], v239 offset:256\n\t"
    "v_cvt_pk_bf16_f32 v170, v230, v231\n\t"
    "v_mfma_f32_16x16x32_bf16 a[120:123], v[212:215], v[184:187], a[120:123]\n\t"
    "ds_read_b64_tr_b16 v[210:211], v239 offset:6400\n\t"
    "v_add_f32 v226, v232, v226\n\t"
    "v_mfma_f32_16x16x32_bf16 a[124:127], v[212:215], v[188:191], a[124:127]\n\t"
    "v_exp_f32 v234, v132\n\t"
    "v_mfma_f32_16x16x32_bf16 a[128:131], v[212:215], v[192:195], a[128:131]\n\t"
    "v_exp_f32 v235, v133\n\t"
    "v_mfma_f32_16x16x32_bf16 a[132:135], v[212:215], v[196:199], a[132:135]\n\t"
    "v_exp_f32 v230, v134\n\t"
    "v_mfma_f32_16x16x32_bf16 a[136:139], v[212:215], v[200:203], a[136:139]\n\t"
    "s_waitcnt lgkmcnt(4)\n\t"
    "ds_read_b64_tr_b16 v[212:213], v240 offset:256\n\t"
    "v_add_f32 v226, v233, v226\n\t"
    "v_mfma_f32_16x16x32_bf16 a[140:143], v[216:219], v[184:187], a[140:143]\n\t"
    "ds_read_b64_tr_b16 v[214:215], v240 offset:6400\n\t"
    "v_cvt_pk_bf16_f32 v171, v232, v233\n\t"
    "v_mfma_f32_16x16x32_bf16 a[144:147], v[216:219], v[188:191], a[144:147]\n\t"
    "v_exp_f32 v231, v135\n\t"
    "v_mfma_f32_16x16x32_bf16 a[148:151], v[216:219], v[192:195], a[148:151]\n\t"
    "buffer_load_dwordx4 v244, s[8:11], s40 offen offset:2048 nt lds\n\t"
    "v_add_f32 v227, v234, v227\n\t"
    "v_mfma_f32_16x16x32_bf16 a[152:155], v[216:219], v[196:199], a[152:155]\n\t"
    "s_add_u32 s36, s36, 12288\n\t"
    "v_exp_f32 v232, v136\n\t"
    "v_mfma_f32_16x16x32_bf16 a[156:159], v[216:219], v[200:203], a[156:159]\n\t"
    "s_waitcnt lgkmcnt(4)\n\t"
    "ds_read_b64_tr_b16 v[216:217], v241 offset:256\n\t"
    "v_add_f32 v227, v235, v227\n\t"
    "v_mfma_f32_16x16x32_bf16 a[160:163], v[204:207], v[184:187], a[160:163]\n\t"
    "ds_read_b64_tr_b16 v[218:219], v241 offset:6400\n\t"
    "v_cvt_pk_bf16_f32 v174, v234, v235\n\t"
    "v_mfma_f32_16x16x32_bf16 a[164:167], v[204:207], v[188:191], a[164:167]\n\t"
    "v_exp_f32 v233, v137\n\t"
    "v_mfma_f32_16x16x32_bf16 a[168:171], v[204:207], v[192:195], a[168:171]\n\t"
    "v_exp_f32 v234, v138\n\t"
    "v_mfma_f32_16x16x32_bf16 a[172:175], v[204:207], v[196:199], a[172:175]\n\t"
    "v_add_f32 v227, v230, v227\n\t"
    "v_add_f32 v227, v231, v227\n\t"
    "v_mfma_f32_16x16x32_bf16 a[176:179], v[204:207], v[200:203], a[176:179]\n\t"
    "s_waitcnt lgkmcnt(4)\n\t"
    "v_cvt_pk_bf16_f32 v175, v230, v231\n\t"
    "v_add_f32 v228, v232, v228\n\t"
    "v_mfma_f32_16x16x32_bf16 a[180:183], v[208:211], v[184:187], a[180:183]\n\t"
    "v_exp_f32 v235, v139\n\t"
    "v_mfma_f32_16x16x32_bf16 a[184:187], v[208:211], v[188:191], a[184:187]\n\t"
    "v_exp_f32 v230, v140\n\t"
    "v_mfma_f32_16x16x32_bf16 a[188:191], v[208:211], v[192:195], a[188:191]\n\t"
    "v_add_f32 v228, v233, v228\n\t"
    "v_cvt_pk_bf16_f32 v178, v232, v233\n\t"
    "v_mfma_f32_16x16x32_bf16 a[192:195], v[208:211], v[196:199], a[192:195]\n\t"
    "s_waitcnt vmcnt(24) lgkmcnt(0)\n\t"
    "s_barrier\n\t"
    "ds_read_b128 v[204:207], v236 offset:0\n\t"
    "v_add_f32 v228, v234, v228\n\t"
    "v_mfma_f32_16x16x32_bf16 a[196:199], v[208:211], v[200:203], a[196:199]\n\t"
    "v_exp_f32 v231, v141\n\t"
    "v_mfma_f32_16x16x32_bf16 a[200:203], v[212:215], v[184:187], a[200:203]\n\t"
    "s_mov_b32 s29, s28\n\t"
    "ds_read_b128 v[208:211], v237 offset:0\n\t"
    "v_add_f32 v228, v235, v228\n\t"
    "v_mfma_f32_16x16x32_bf16 a[204:207], v[212:215], v[188:191], a[204:207]\n\t"
    "s_mov_b32 s28, s30\n\t"
    "v_exp_f32 v232, v142\n\t"
    "v_mfma_f32_16x16x32_bf16 a[208:211], v[212:215], v[192:195], a[208:211]\n\t"
    "s_add_u32 s58, s29, s34\n\t"
    "v_cvt_pk_bf16_f32 v179, v234, v235\n\t"
    "v_add_f32 v229, v230, v229\n\t"
    "v_add_f32 v229, v231, v229\n\t"
    "v_mfma_f32_16x16x32_bf16 a[212:215], v[212:215], v[196:199], a[212:215]\n\t"
    "s_add_u32 s59, s29, s35\n\t"
    "v_exp_f32 v233, v143\n\t"
    "v_mfma_f32_16x16x32_bf16 a[216:219], v[212:215], v[200:203], a[216:219]\n\t"
    "v_cvt_pk_bf16_f32 v182, v230, v231\n\t"
    "v_add_f32 v229, v232, v229\n\t"
    "v_add_f32 v229, v233, v229\n\t"
    "v_mfma_f32_16x16x32_bf16 a[220:223], v[216:219], v[184:187], a[220:223]\n\t"
    "ds_read_b128 v[212:215], v236 offset:128\n\t"
    "v_cvt_pk_bf16_f32 v183, v232, v233\n\t"
    "v_mfma_f32_16x16x32_bf16 a[224:227], v[216:219], v[188:191], a[224:227]\n\t"
    "v_max3_f32 v253, v225, v226, v227\n\t"
    "v_max3_f32 v253, v253, v228, v229\n\t"
    "v_mfma_f32_16x16x32_bf16 a[228:231], v[216:219], v[192:195], a[228:231]\n\t"
    "v_cmp_nge_f32 vcc, s50, v253\n\t"
    "v_mfma_f32_16x16x32_bf16 a[232:235], v[216:219], v[196:199], a[232:235]\n\t"
    "v_mfma_f32_16x16x32_bf16 a[236:239], v[216:219], v[200:203], a[236:239]\n\t"
    "s_waitcnt lgkmcnt(2)\n\t"
    "ds_read_b128 v[216:219], v237 offset:128\n\t"
    "s_cbranch_vccnz .Lk1_stub_s11\n\t"
    ".Lk1_cont_s11:\n\t"
    "v_mfma_f32_16x16x32_bf16 v[104:107], v[204:207], v[0:3], v[144:147]\n\t"
    "s_add_u32 m0, s58, 12288\n\t"
    "v_add_u32 v238, s31, v238\n\t"
    "v_add_u32 v239, s31, v239\n\t"
    "v_mfma_f32_16x16x32_bf16 v[108:111], v[204:207], v[24:27], v[148:151]\n\t"
    "buffer_load_dwordx4 v242, s[12:15], s37 offen nt lds\n\t"
    "v_mfma_f32_16x16x32_bf16 v[112:115], v[204:207], v[48:51], v[152:155]\n\t"
    "v_add_u32 v240, s31, v240\n\t"
    "v_add_u32 v241, s31, v241\n\t"
    "v_mfma_f32_16x16x32_bf16 v[116:119], v[204:207], v[72:75], v[156:159]\n\t"
    "v_add_f32 v220, v225, v220\n\t"
    "v_add_f32 v221, v226, v221\n\t"
    "v_mfma_f32_16x16x32_bf16 v[120:123], v[204:207], v[96:99], v[160:163]\n\t"
    "s_waitcnt lgkmcnt(2)\n\t"
    "ds_read_b128 v[204:207], v236 offset:256\n\t"
    "v_add_f32 v222, v227, v222\n\t"
    "v_mfma_f32_16x16x32_bf16 v[104:107], v[208:211], v[4:7], v[104:107]\n\t"
    "v_add_f32 v223, v228, v223\n\t"
    "v_add_f32 v224, v229, v224\n\t"
    "v_mfma_f32_16x16x32_bf16 v[108:111], v[208:211], v[28:31], v[108:111]\n\t"
    "buffer_load_dwordx4 v243, s[12:15], s37 offen offset:1024 nt lds\n\t"
    "v_mfma_f32_16x16x32_bf16 v[112:115], v[208:211], v[52:55], v[112:115]\n\t"
    "v_mfma_f32_16x16x32_bf16 v[116:119], v[208:211], v[76:79], v[116:119]\n\t"
    "v_mfma_f32_16x16x32_bf16 v[120:123], v[208:211], v[100:103], v[120:123]\n\t"
    "s_waitcnt lgkmcnt(2)\n\t"
    "ds_read_b128 v[208:211], v237 offset:256\n\t"
    "v_mfma_f32_16x16x32_bf16 v[104:107], v[212:215], v[8:11], v[104:107]\n\t"
    "v_mfma_f32_16x16x32_bf16 v[108:111], v[212:215], v[32:35], v[108:111]\n\t"
    "buffer_load_dwordx4 v244, s[12:15], s37 offen offset:2048 nt lds\n\t"
    "v_mfma_f32_16x16x32_bf16 v[112:115], v[212:215], v[56:59], v[112:115]\n\t"
    "v_mfma_f32_16x16x32_bf16 v[116:119], v[212:215], v[80:83], v[116:119]\n\t"
    "v_mfma_f32_16x16x32_bf16 v[120:123], v[212:215], a[240:243], v[120:123]\n\t"
    "s_waitcnt lgkmcnt(2)\n\t"
    "ds_read_b128 v[212:215], v236 offset:6144\n\t"
    "v_mfma_f32_16x16x32_bf16 v[104:107], v[216:219], v[12:15], v[104:107]\n\t"
    "s_add_u32 m0, s58, 15360\n\t"
    "v_mfma_f32_16x16x32_bf16 v[108:111], v[216:219], v[36:39], v[108:111]\n\t"
    "s_add_u32 s41, s37, 3072\n\t"
    "buffer_load_dwordx4 v242, s[12:15], s41 offen nt lds\n\t"
    "v_mfma_f32_16x16x32_bf16 v[112:115], v[216:219], v[60:63], v[112:115]\n\t"
    "v_mfma_f32_16x16x32_bf16 v[116:119], v[216:219], v[84:87], v[116:119]\n\t"
    "v_mfma_f32_16x16x32_bf16 v[120:123], v[216:219], a[244:247], v[120:123]\n\t"
    "s_waitcnt lgkmcnt(2)\n\t"
    "ds_read_b128 v[216:219], v237 offset:6144\n\t"
    "v_mfma_f32_16x16x32_bf16 v[104:107], v[204:207], v[16:19], v[104:107]\n\t"
    "v_mfma_f32_16x16x32_bf16 v[108:111], v[204:207], v[40:43], v[108:111]\n\t"
    "buffer_load_dwordx4 v243, s[12:15], s41 offen offset:1024 nt lds\n\t"
    "v_mfma_f32_16x16x32_bf16 v[112:115], v[204:207], v[64:67], v[112:115]\n\t"
    "v_mfma_f32_16x16x32_bf16 v[116:119], v[204:207], v[88:91], v[116:119]\n\t"
    "v_mfma_f32_16x16x32_bf16 v[120:123], v[204:207], a[248:251], v[120:123]\n\t"
    "s_waitcnt lgkmcnt(2)\n\t"
    "ds_read_b128 v[204:207], v236 offset:6272\n\t"
    "v_mfma_f32_16x16x32_bf16 v[104:107], v[208:211], v[20:23], v[104:107]\n\t"
    "v_mfma_f32_16x16x32_bf16 v[108:111], v[208:211], v[44:47], v[108:111]\n\t"
    "buffer_load_dwordx4 v244, s[12:15], s41 offen offset:2048 nt lds\n\t"
    "v_mfma_f32_16x16x32_bf16 v[112:115], v[208:211], v[68:71], v[112:115]\n\t"
    "s_add_u32 s37, s37, 12288\n\t"
    "v_mfma_f32_16x16x32_bf16 v[116:119], v[208:211], v[92:95], v[116:119]\n\t"
    "v_mfma_f32_16x16x32_bf16 v[120:123], v[208:211], a[252:255], v[120:123]\n\t"
    "s_waitcnt lgkmcnt(2)\n\t"
    "ds_read_b128 v[208:211], v237 offset:6272\n\t"
    "v_mfma_f32_16x16x32_bf16 v[124:127], v[212:215], v[0:3], v[144:147]\n\t"
    "v_mfma_f32_16x16x32_bf16 v[128:131], v[212:215], v[24:27], v[148:151]\n\t"
    "s_add_u32 s30, s28, 24576\n\t"
    "v_exp_f32 v230, v104\n\t"
    "v_mfma_f32_16x16x32_bf16 v[132:135], v[212:215], v[48:51], v[152:155]\n\t"
    "s_cmp_eq_u32 s30, 73728\n\t"
    "v_exp_f32 v231, v105\n\t"
    "v_mfma_f32_16x16x32_bf16 v[136:139], v[212:215], v[72:75], v[156:159]\n\t"
    "s_cselect_b32 s30, 0, s30\n\t"
    "v_exp_f32 v232, v106\n\t"
    "v_mfma_f32_16x16x32_bf16 v[140:143], v[212:215], v[96:99], v[160:163]\n\t"
    "s_waitcnt lgkmcnt(2)\n\t"
    "ds_read_b128 v[212:215], v236 offset:6400\n\t"
    "v_add_f32 v225, v230, v231\n\t"
    "v_mfma_f32_16x16x32_bf16 v[124:127], v[216:219], v[4:7], v[124:127]\n\t"
    "s_sub_u32 s31, s30, s28\n\t"
    "v_exp_f32 v233, v107\n\t"
    "v_mfma_f32_16x16x32_bf16 v[128:131], v[216:219], v[28:31], v[128:131]\n\t"
    "v_exp_f32 v234, v108\n\t"
    "v_mfma_f32_16x16x32_bf16 v[132:135], v[216:219], v[52:55], v[132:135]\n\t"
    "v_cvt_pk_bf16_f32 v184, v230, v231\n\t"
    "v_add_f32 v225, v232, v225\n\t"
    "v_mfma_f32_16x16x32_bf16 v[136:139], v[216:219], v[76:79], v[136:139]\n\t"
    "v_exp_f32 v235, v109\n\t"
    "v_mfma_f32_16x16x32_bf16 v[140:143], v[216:219], v[100:103], v[140:143]\n\t"
    "s_waitcnt lgkmcnt(2)\n\t"
    "ds_read_b128 v[216:219], v237 offset:6400\n\t"
    "v_add_f32 v225, v233, v225\n\t"
    "v_mfma_f32_16x16x32_bf16 v[124:127], v[204:207], v[8:11], v[124:127]\n\t"
    "v_add_u32 v236, s31, v236\n\t"
    "v_add_u32 v237, s31, v237\n\t"
    "v_mfma_f32_16x16x32_bf16 v[128:131], v[204:207], v[32:35], v[128:131]\n\t"
    "v_exp_f32 v230, v110\n\t"
    "v_mfma_f32_16x16x32_bf16 v[132:135], v[204:207], v[56:59], v[132:135]\n\t"
    "v_cvt_pk_bf16_f32 v185, v232, v233\n\t"
    "v_add_f32 v226, v234, v235\n\t"
    "v_mfma_f32_16x16x32_bf16 v[136:139], v[204:207], v[80:83], v[136:139]\n\t"
    "v_exp_f32 v231, v111\n\t"
    "v_mfma_f32_16x16x32_bf16 v[140:143], v[204:207], a[240:243], v[140:143]\n\t"
    "s_waitcnt vmcnt(24) lgkmcnt(0)\n\t"
    "s_barrier\n\t"
    "ds_read_b64_tr_b16 v[204:205], v238 offset:0\n\t"
    "v_cvt_pk_bf16_f32 v188, v234, v235\n\t"
    "v_mfma_f32_16x16x32_bf16 v[124:127], v[208:211], v[12:15], v[124:127]\n\t"
    "ds_read_b64_tr_b16 v[206:207], v238 offset:6144\n\t"
    "v_add_f32 v226, v230, v226\n\t"
    "v_mfma_f32_16x16x32_bf16 v[128:131], v[208:211], v[36:39], v[128:131]\n\t"
    "v_exp_f32 v232, v112\n\t"
    "v_mfma_f32_16x16x32_bf16 v[132:135], v[208:211], v[60:63], v[132:135]\n\t"
    "v_exp_f32 v233, v113\n\t"
    "v_mfma_f32_16x16x32_bf16 v[136:139], v[208:211], v[84:87], v[136:139]\n\t"
    "v_exp_f32 v234, v114\n\t"
    "v_mfma_f32_16x16x32_bf16 v[140:143], v[208:211], a[244:247], v[140:143]\n\t"
    "ds_read_b64_tr_b16 v[208:209], v239 offset:0\n\t"
    "v_add_f32 v226, v231, v226\n\t"
    "v_mfma_f32_16x16x32_bf16 v[124:127], v[212:215], v[16:19], v[124:127]\n\t"
    "ds_read_b64_tr_b16 v[210:211], v239 offset:6144\n\t"
    "v_cvt_pk_bf16_f32 v189, v230, v231\n\t"
    "v_mfma_f32_16x16x32_bf16 v[128:131], v[212:215], v[40:43], v[128:131]\n\t"
    "v_exp_f32 v235, v115\n\t"
    "v_mfma_f32_16x16x32_bf16 v[132:135], v[212:215], v[64:67], v[132:135]\n\t"
    "v_exp_f32 v230, v116\n\t"
    "v_mfma_f32_16x16x32_bf16 v[136:139], v[212:215], v[88:91], v[136:139]\n\t"
    "v_add_f32 v227, v232, v233\n\t"
    "v_cvt_pk_bf16_f32 v192, v232, v233\n\t"
    "v_mfma_f32_16x16x32_bf16 v[140:143], v[212:215], a[248:251], v[140:143]\n\t"
    "ds_read_b64_tr_b16 v[212:213], v240 offset:0\n\t"
    "v_add_f32 v227, v234, v227\n\t"
    "v_mfma_f32_16x16x32_bf16 v[124:127], v[216:219], v[20:23], v[124:127]\n\t"
    "ds_read_b64_tr_b16 v[214:215], v240 offset:6144\n\t"
    "v_add_f32 v227, v235, v227\n\t"
    "v_mfma_f32_16x16x32_bf16 v[128:131], v[216:219], v[44:47], v[128:131]\n\t"
    "v_exp_f32 v231, v117\n\t"
    "v_mfma_f32_16x16x32_bf16 v[132:135], v[216:219], v[68:71], v[132:135]\n\t"
    "v_exp_f32 v232, v118\n\t"
    "v_mfma_f32_16x16x32_bf16 v[136:139], v[216:219], v[92:95], v[136:139]\n\t"
    "v_cvt_pk_bf16_f32 v193, v234, v235\n\t"
    "v_add_f32 v228, v230, v231\n\t"
    "v_mfma_f32_16x16x32_bf16 v[140:143], v[216:219], a[252:255], v[140:143]\n\t"
    "s_waitcnt lgkmcnt(4)\n\t"
    "ds_read_b64_tr_b16 v[216:217], v241 offset:0\n\t"
    "v_cvt_pk_bf16_f32 v196, v230, v231\n\t"
    "v_mfma_f32_16x16x32_bf16 a[0:3], v[204:207], v[164:167], a[0:3]\n\t"
    "ds_read_b64_tr_b16 v[218:219], v241 offset:6144\n\t"
    "v_add_f32 v228, v232, v228\n\t"
    "v_mfma_f32_16x16x32_bf16 a[4:7], v[204:207], v[168:171], a[4:7]\n\t"
    "v_exp_f32 v233, v119\n\t"
    "v_mfma_f32_16x16x32_bf16 a[8:11], v[204:207], v[172:175], a[8:11]\n\t"
    "v_exp_f32 v234, v120\n\t"
    "v_mfma_f32_16x16x32_bf16 a[12:15], v[204:207], v[176:179], a[12:15]\n\t"
    "v_exp_f32 v235, v121\n\t"
    "v_mfma_f32_16x16x32_bf16 a[16:19], v[204:207], v[180:183], a[16:19]\n\t"
    "s_waitcnt lgkmcnt(4)\n\t"
    "ds_read_b64_tr_b16 v[204:205], v238 offset:128\n\t"
    "v_add_f32 v228, v233, v228\n\t"
    "v_mfma_f32_16x16x32_bf16 a[20:23], v[208:211], v[164:167], a[20:23]\n\t"
    "ds_read_b64_tr_b16 v[206:207], v238 offset:6272\n\t"
    "v_cvt_pk_bf16_f32 v197, v232, v233\n\t"
    "v_mfma_f32_16x16x32_bf16 a[24:27], v[208:211], v[168:171], a[24:27]\n\t"
    "v_exp_f32 v230, v122\n\t"
    "v_mfma_f32_16x16x32_bf16 a[28:31], v[208:211], v[172:175], a[28:31]\n\t"
    "v_exp_f32 v231, v123\n\t"
    "v_mfma_f32_16x16x32_bf16 a[32:35], v[208:211], v[176:179], a[32:35]\n\t"
    "v_exp_f32 v232, v124\n\t"
    "v_mfma_f32_16x16x32_bf16 a[36:39], v[208:211], v[180:183], a[36:39]\n\t"
    "s_waitcnt lgkmcnt(4)\n\t"
    "ds_read_b64_tr_b16 v[208:209], v239 offset:128\n\t"
    "v_add_f32 v229, v234, v235\n\t"
    "v_mfma_f32_16x16x32_bf16 a[40:43], v[212:215], v[164:167], a[40:43]\n\t"
    "ds_read_b64_tr_b16 v[210:211], v239 offset:6272\n\t"
    "v_cvt_pk_bf16_f32 v200, v234, v235\n\t"
    "v_mfma_f32_16x16x32_bf16 a[44:47], v[212:215], v[168:171], a[44:47]\n\t"
    "v_exp_f32 v233, v125\n\t"
    "v_mfma_f32_16x16x32_bf16 a[48:51], v[212:215], v[172:175], a[48:51]\n\t"
    "v_exp_f32 v234, v126\n\t"
    "v_mfma_f32_16x16x32_bf16 a[52:55], v[212:215], v[176:179], a[52:55]\n\t"
    "v_add_f32 v229, v230, v229\n\t"
    "v_add_f32 v229, v231, v229\n\t"
    "v_mfma_f32_16x16x32_bf16 a[56:59], v[212:215], v[180:183], a[56:59]\n\t"
    "s_waitcnt lgkmcnt(4)\n\t"
    "ds_read_b64_tr_b16 v[212:213], v240 offset:128\n\t"
    "v_cvt_pk_bf16_f32 v201, v230, v231\n\t"
    "v_mfma_f32_16x16x32_bf16 a[60:63], v[216:219], v[164:167], a[60:63]\n\t"
    "ds_read_b64_tr_b16 v[214:215], v240 offset:6272\n\t"
    "v_add_f32 v225, v232, v225\n\t"
    "v_mfma_f32_16x16x32_bf16 a[64:67], v[216:219], v[168:171], a[64:67]\n\t"
    "v_exp_f32 v235, v127\n\t"
    "v_mfma_f32_16x16x32_bf16 a[68:71], v[216:219], v[172:175], a[68:71]\n\t"
    "v_exp_f32 v230, v128\n\t"
    "v_mfma_f32_16x16x32_bf16 a[72:75], v[216:219], v[176:179], a[72:75]\n\t"
    "v_add_f32 v225, v233, v225\n\t"
    "v_cvt_pk_bf16_f32 v186, v232, v233\n\t"
    "v_mfma_f32_16x16x32_bf16 a[76:79], v[216:219], v[180:183], a[76:79]\n\t"
    "s_waitcnt lgkmcnt(4)\n\t"
    "ds_read_b64_tr_b16 v[216:217], v241 offset:128\n\t"
    "v_add_f32 v225, v234, v225\n\t"
    "v_mfma_f32_16x16x32_bf16 a[80:83], v[204:207], v[164:167], a[80:83]\n\t"
    "ds_read_b64_tr_b16 v[218:219], v241 offset:6272\n\t"
    "v_add_f32 v225, v235, v225\n\t"
    "v_mfma_f32_16x16x32_bf16 a[84:87], v[204:207], v[168:171], a[84:87]\n\t"
    "v_exp_f32 v231, v129\n\t"
    "v_mfma_f32_16x16x32_bf16 a[88:91], v[204:207], v[172:175], a[88:91]\n\t"
    "v_exp_f32 v232, v130\n\t"
    "v_mfma_f32_16x16x32_bf16 a[92:95], v[204:207], v[176:179], a[92:95]\n\t"
    "v_cvt_pk_bf16_f32 v187, v234, v235\n\t"
    "v_add_f32 v226, v230, v226\n\t"
    "v_mfma_f32_16x16x32_bf16 a[96:99], v[204:207], v[180:183], a[96:99]\n\t"
    "s_waitcnt lgkmcnt(4)\n\t"
    "ds_read_b64_tr_b16 v[204:205], v238 offset:256\n\t"
    "v_add_f32 v226, v231, v226\n\t"
    "v_mfma_f32_16x16x32_bf16 a[100:103], v[208:211], v[164:167], a[100:103]\n\t"
    "ds_read_b64_tr_b16 v[206:207], v238 offset:6400\n\t"
    "v_cvt_pk_bf16_f32 v190, v230, v231\n\t"
    "v_mfma_f32_16x16x32_bf16 a[104:107], v[208:211], v[168:171], a[104:107]\n\t"
    "v_exp_f32 v233, v131\n\t"
    "v_mfma_f32_16x16x32_bf16 a[108:111], v[208:211], v[172:175], a[108:111]\n\t"
    "v_exp_f32 v234, v132\n\t"
    "v_mfma_f32_16x16x32_bf16 a[112:115], v[208:211], v[176:179], a[112:115]\n\t"
    "v_exp_f32 v235, v133\n\t"
    "v_mfma_f32_16x16x32_bf16 a[116:119], v[208:211], v[180:183], a[116:119]\n\t"
    "s_waitcnt lgkmcnt(4)\n\t"
    "ds_read_b64_tr_b16 v[208:209], v239 offset:256\n\t"
    "v_add_f32 v226, v232, v226\n\t"
    "v_mfma_f32_16x16x32_bf16 a[120:123], v[212:215], v[164:167], a[120:123]\n\t"
    "ds_read_b64_tr_b16 v[210:211], v239 offset:6400\n\t"
    "v_add_f32 v226, v233, v226\n\t"
    "v_mfma_f32_16x16x32_bf16 a[124:127], v[212:215], v[168:171], a[124:127]\n\t"
    "v_exp_f32 v230, v134\n\t"
    "v_mfma_f32_16x16x32_bf16 a[128:131], v[212:215], v[172:175], a[128:131]\n\t"
    "v_cvt_pk_bf16_f32 v191, v232, v233\n\t"
    "v_add_f32 v227, v234, v227\n\t"
    "v_mfma_f32_16x16x32_bf16 a[132:135], v[212:215], v[176:179], a[132:135]\n\t"
    "v_exp_f32 v231, v135\n\t"
    "v_mfma_f32_16x16x32_bf16 a[136:139], v[212:215], v[180:183], a[136:139]\n\t"
    "s_waitcnt lgkmcnt(4)\n\t"
    "ds_read_b64_tr_b16 v[212:213], v240 offset:256\n\t"
    "v_add_f32 v227, v235, v227\n\t"
    "v_mfma_f32_16x16x32_bf16 a[140:143], v[216:219], v[164:167], a[140:143]\n\t"
    "ds_read_b64_tr_b16 v[214:215], v240 offset:6400\n\t"
    "v_cvt_pk_bf16_f32 v194, v234, v235\n\t"
    "v_mfma_f32_16x16x32_bf16 a[144:147], v[216:219], v[168:171], a[144:147]\n\t"
    "v_exp_f32 v232, v136\n\t"
    "v_mfma_f32_16x16x32_bf16 a[148:151], v[216:219], v[172:175], a[148:151]\n\t"
    "v_exp_f32 v233, v137\n\t"
    "v_mfma_f32_16x16x32_bf16 a[152:155], v[216:219], v[176:179], a[152:155]\n\t"
    "v_exp_f32 v234, v138\n\t"
    "v_mfma_f32_16x16x32_bf16 a[156:159], v[216:219], v[180:183], a[156:159]\n\t"
    "s_waitcnt lgkmcnt(4)\n\t"
    "ds_read_b64_tr_b16 v[216:217], v241 offset:256\n\t"
    "v_add_f32 v227, v230, v227\n\t"
    "v_mfma_f32_16x16x32_bf16 a[160:163], v[204:207], v[164:167], a[160:163]\n\t"
    "ds_read_b64_tr_b16 v[218:219], v241 offset:6400\n\t"
    "v_add_f32 v227, v231, v227\n\t"
    "v_mfma_f32_16x16x32_bf16 a[164:167], v[204:207], v[168:171], a[164:167]\n\t"
    "v_cvt_pk_bf16_f32 v195, v230, v231\n\t"
    "v_add_f32 v228, v232, v228\n\t"
    "v_mfma_f32_16x16x32_bf16 a[168:171], v[204:207], v[172:175], a[168:171]\n\t"
    "v_exp_f32 v235, v139\n\t"
    "v_mfma_f32_16x16x32_bf16 a[172:175], v[204:207], v[176:179], a[172:175]\n\t"
    "v_exp_f32 v230, v140\n\t"
    "v_mfma_f32_16x16x32_bf16 a[176:179], v[204:207], v[180:183], a[176:179]\n\t"
    "s_waitcnt lgkmcnt(4)\n\t"
    "v_add_f32 v228, v233, v228\n\t"
    "v_cvt_pk_bf16_f32 v198, v232, v233\n\t"
    "v_mfma_f32_16x16x32_bf16 a[180:183], v[208:211], v[164:167], a[180:183]\n\t"
    "v_exp_f32 v231, v141\n\t"
    "v_mfma_f32_16x16x32_bf16 a[184:187], v[208:211], v[168:171], a[184:187]\n\t"
    "v_exp_f32 v232, v142\n\t"
    "v_mfma_f32_16x16x32_bf16 a[188:191], v[208:211], v[172:175], a[188:191]\n\t"
    "v_add_f32 v228, v234, v228\n\t"
    "v_add_f32 v228, v235, v228\n\t"
    "v_mfma_f32_16x16x32_bf16 a[192:195], v[208:211], v[176:179], a[192:195]\n\t"
    "s_waitcnt vmcnt(18) lgkmcnt(0)\n\t"
    "s_barrier\n\t"
    "ds_read_b128 v[204:207], v236 offset:0\n\t"
    "v_cvt_pk_bf16_f32 v199, v234, v235\n\t"
    "v_mfma_f32_16x16x32_bf16 a[196:199], v[208:211], v[180:183], a[196:199]\n\t"
    "v_exp_f32 v233, v143\n\t"
    "v_mfma_f32_16x16x32_bf16 a[200:203], v[212:215], v[164:167], a[200:203]\n\t"
    "s_mov_b32 s29, s28\n\t"
    "ds_read_b128 v[208:211], v237 offset:0\n\t"
    "v_add_f32 v229, v230, v229\n\t"
    "v_mfma_f32_16x16x32_bf16 a[204:207], v[212:215], v[168:171], a[204:207]\n\t"
    "s_mov_b32 s28, s30\n\t"
    "v_add_f32 v229, v231, v229\n\t"
    "v_cvt_pk_bf16_f32 v202, v230, v231\n\t"
    "v_mfma_f32_16x16x32_bf16 a[208:211], v[212:215], v[172:175], a[208:211]\n\t"
    "s_add_u32 s58, s29, s34\n\t"
    "v_add_f32 v229, v232, v229\n\t"
    "v_add_f32 v229, v233, v229\n\t"
    "v_mfma_f32_16x16x32_bf16 a[212:215], v[212:215], v[176:179], a[212:215]\n\t"
    "s_add_u32 s59, s29, s35\n\t"
    "v_cvt_pk_bf16_f32 v203, v232, v233\n\t"
    "v_max3_f32 v253, v225, v226, v227\n\t"
    "v_mfma_f32_16x16x32_bf16 a[216:219], v[212:215], v[180:183], a[216:219]\n\t"
    "v_max3_f32 v253, v253, v228, v229\n\t"
    "v_cmp_nge_f32 vcc, s50, v253\n\t"
    "v_mfma_f32_16x16x32_bf16 a[220:223], v[216:219], v[164:167], a[220:223]\n\t"
    "ds_read_b128 v[212:215], v236 offset:128\n\t"
    "v_mfma_f32_16x16x32_bf16 a[224:227], v[216:219], v[168:171], a[224:227]\n\t"
    "v_mfma_f32_16x16x32_bf16 a[228:231], v[216:219], v[172:175], a[228:231]\n\t"
    "v_mfma_f32_16x16x32_bf16 a[232:235], v[216:219], v[176:179], a[232:235]\n\t"
    "v_mfma_f32_16x16x32_bf16 a[236:239], v[216:219], v[180:183], a[236:239]\n\t"
    "s_waitcnt lgkmcnt(2)\n\t"
    "ds_read_b128 v[216:219], v237 offset:128\n\t"
    "s_cbranch_vccnz .Lk1_stub_s12\n\t"
    ".Lk1_cont_s12:\n\t"
    "v_mfma_f32_16x16x32_bf16 v[104:107], v[204:207], v[0:3], v[144:147]\n\t"
    "s_add_u32 m0, s58, 12288\n\t"
    "v_add_u32 v238, s31, v238\n\t"
    "v_add_u32 v239, s31, v239\n\t"
    "v_mfma_f32_16x16x32_bf16 v[108:111], v[204:207], v[24:27], v[148:151]\n\t"
    "buffer_load_dwordx4 v242, s[12:15], s37 offen nt lds\n\t"
    "v_mfma_f32_16x16x32_bf16 v[112:115], v[204:207], v[48:51], v[152:155]\n\t"
    "v_add_u32 v240, s31, v240\n\t"
    "v_add_u32 v241, s31, v241\n\t"
    "v_mfma_f32_16x16x32_bf16 v[116:119], v[204:207], v[72:75], v[156:159]\n\t"
    "v_add_f32 v220, v225, v220\n\t"
    "v_add_f32 v221, v226, v221\n\t"
    "v_mfma_f32_16x16x32_bf16 v[120:123], v[204:207], v[96:99], v[160:163]\n\t"
    "s_waitcnt lgkmcnt(2)\n\t"
    "ds_read_b128 v[204:207], v236 offset:256\n\t"
    "v_add_f32 v222, v227, v222\n\t"
    "v_mfma_f32_16x16x32_bf16 v[104:107], v[208:211], v[4:7], v[104:107]\n\t"
    "v_add_f32 v223, v228, v223\n\t"
    "v_add_f32 v224, v229, v224\n\t"
    "v_mfma_f32_16x16x32_bf16 v[108:111], v[208:211], v[28:31], v[108:111]\n\t"
    "buffer_load_dwordx4 v243, s[12:15], s37 offen offset:1024 nt lds\n\t"
    "v_mfma_f32_16x16x32_bf16 v[112:115], v[208:211], v[52:55], v[112:115]\n\t"
    "v_mfma_f32_16x16x32_bf16 v[116:119], v[208:211], v[76:79], v[116:119]\n\t"
    "v_mfma_f32_16x16x32_bf16 v[120:123], v[208:211], v[100:103], v[120:123]\n\t"
    "s_waitcnt lgkmcnt(2)\n\t"
    "ds_read_b128 v[208:211], v237 offset:256\n\t"
    "v_mfma_f32_16x16x32_bf16 v[104:107], v[212:215], v[8:11], v[104:107]\n\t"
    "v_mfma_f32_16x16x32_bf16 v[108:111], v[212:215], v[32:35], v[108:111]\n\t"
    "buffer_load_dwordx4 v244, s[12:15], s37 offen offset:2048 nt lds\n\t"
    "v_mfma_f32_16x16x32_bf16 v[112:115], v[212:215], v[56:59], v[112:115]\n\t"
    "v_mfma_f32_16x16x32_bf16 v[116:119], v[212:215], v[80:83], v[116:119]\n\t"
    "v_mfma_f32_16x16x32_bf16 v[120:123], v[212:215], a[240:243], v[120:123]\n\t"
    "s_waitcnt lgkmcnt(2)\n\t"
    "ds_read_b128 v[212:215], v236 offset:6144\n\t"
    "v_mfma_f32_16x16x32_bf16 v[104:107], v[216:219], v[12:15], v[104:107]\n\t"
    "s_add_u32 m0, s58, 15360\n\t"
    "v_mfma_f32_16x16x32_bf16 v[108:111], v[216:219], v[36:39], v[108:111]\n\t"
    "s_add_u32 s41, s37, 3072\n\t"
    "buffer_load_dwordx4 v242, s[12:15], s41 offen nt lds\n\t"
    "v_mfma_f32_16x16x32_bf16 v[112:115], v[216:219], v[60:63], v[112:115]\n\t"
    "v_mfma_f32_16x16x32_bf16 v[116:119], v[216:219], v[84:87], v[116:119]\n\t"
    "v_mfma_f32_16x16x32_bf16 v[120:123], v[216:219], a[244:247], v[120:123]\n\t"
    "s_waitcnt lgkmcnt(2)\n\t"
    "ds_read_b128 v[216:219], v237 offset:6144\n\t"
    "v_mfma_f32_16x16x32_bf16 v[104:107], v[204:207], v[16:19], v[104:107]\n\t"
    "v_mfma_f32_16x16x32_bf16 v[108:111], v[204:207], v[40:43], v[108:111]\n\t"
    "buffer_load_dwordx4 v243, s[12:15], s41 offen offset:1024 nt lds\n\t"
    "v_mfma_f32_16x16x32_bf16 v[112:115], v[204:207], v[64:67], v[112:115]\n\t"
    "v_mfma_f32_16x16x32_bf16 v[116:119], v[204:207], v[88:91], v[116:119]\n\t"
    "v_mfma_f32_16x16x32_bf16 v[120:123], v[204:207], a[248:251], v[120:123]\n\t"
    "s_waitcnt lgkmcnt(2)\n\t"
    "ds_read_b128 v[204:207], v236 offset:6272\n\t"
    "v_mfma_f32_16x16x32_bf16 v[104:107], v[208:211], v[20:23], v[104:107]\n\t"
    "v_mfma_f32_16x16x32_bf16 v[108:111], v[208:211], v[44:47], v[108:111]\n\t"
    "buffer_load_dwordx4 v244, s[12:15], s41 offen offset:2048 nt lds\n\t"
    "v_mfma_f32_16x16x32_bf16 v[112:115], v[208:211], v[68:71], v[112:115]\n\t"
    "s_add_u32 s37, s37, 12288\n\t"
    "v_mfma_f32_16x16x32_bf16 v[116:119], v[208:211], v[92:95], v[116:119]\n\t"
    "v_mfma_f32_16x16x32_bf16 v[120:123], v[208:211], a[252:255], v[120:123]\n\t"
    "s_waitcnt lgkmcnt(2)\n\t"
    "ds_read_b128 v[208:211], v237 offset:6272\n\t"
    "v_mfma_f32_16x16x32_bf16 v[124:127], v[212:215], v[0:3], v[144:147]\n\t"
    "v_mfma_f32_16x16x32_bf16 v[128:131], v[212:215], v[24:27], v[148:151]\n\t"
    "s_add_u32 s30, s28, 24576\n\t"
    "v_exp_f32 v230, v104\n\t"
    "v_mfma_f32_16x16x32_bf16 v[132:135], v[212:215], v[48:51], v[152:155]\n\t"
    "s_cmp_eq_u32 s30, 73728\n\t"
    "v_exp_f32 v231, v105\n\t"
    "v_mfma_f32_16x16x32_bf16 v[136:139], v[212:215], v[72:75], v[156:159]\n\t"
    "s_cselect_b32 s30, 0, s30\n\t"
    "v_exp_f32 v232, v106\n\t"
    "v_mfma_f32_16x16x32_bf16 v[140:143], v[212:215], v[96:99], v[160:163]\n\t"
    "s_waitcnt lgkmcnt(2)\n\t"
    "ds_read_b128 v[212:215], v236 offset:6400\n\t"
    "v_add_f32 v225, v230, v231\n\t"
    "v_mfma_f32_16x16x32_bf16 v[124:127], v[216:219], v[4:7], v[124:127]\n\t"
    "s_sub_u32 s31, s30, s28\n\t"
    "v_exp_f32 v233, v107\n\t"
    "v_mfma_f32_16x16x32_bf16 v[128:131], v[216:219], v[28:31], v[128:131]\n\t"
    "v_exp_f32 v234, v108\n\t"
    "v_mfma_f32_16x16x32_bf16 v[132:135], v[216:219], v[52:55], v[132:135]\n\t"
    "v_cvt_pk_bf16_f32 v164, v230, v231\n\t"
    "v_add_f32 v225, v232, v225\n\t"
    "v_mfma_f32_16x16x32_bf16 v[136:139], v[216:219], v[76:79], v[136:139]\n\t"
    "v_exp_f32 v235, v109\n\t"
    "v_mfma_f32_16x16x32_bf16 v[140:143], v[216:219], v[100:103], v[140:143]\n\t"
    "s_waitcnt lgkmcnt(2)\n\t"
    "ds_read_b128 v[216:219], v237 offset:6400\n\t"
    "v_add_f32 v225, v233, v225\n\t"
    "v_mfma_f32_16x16x32_bf16 v[124:127], v[204:207], v[8:11], v[124:127]\n\t"
    "v_add_u32 v236, s31, v236\n\t"
    "v_add_u32 v237, s31, v237\n\t"
    "v_mfma_f32_16x16x32_bf16 v[128:131], v[204:207], v[32:35], v[128:131]\n\t"
    "v_exp_f32 v230, v110\n\t"
    "v_mfma_f32_16x16x32_bf16 v[132:135], v[204:207], v[56:59], v[132:135]\n\t"
    "v_cvt_pk_bf16_f32 v165, v232, v233\n\t"
    "v_add_f32 v226, v234, v235\n\t"
    "v_mfma_f32_16x16x32_bf16 v[136:139], v[204:207], v[80:83], v[136:139]\n\t"
    "v_exp_f32 v231, v111\n\t"
    "v_mfma_f32_16x16x32_bf16 v[140:143], v[204:207], a[240:243], v[140:143]\n\t"
    "s_waitcnt vmcnt(18) lgkmcnt(0)\n\t"
    "s_barrier\n\t"
    "ds_read_b64_tr_b16 v[204:205], v238 offset:0\n\t"
    "v_cvt_pk_bf16_f32 v168, v234, v235\n\t"
    "v_mfma_f32_16x16x32_bf16 v[124:127], v[208:211], v[12:15], v[124:127]\n\t"
    "ds_read_b64_tr_b16 v[206:207], v238 offset:6144\n\t"
    "v_add_f32 v226, v230, v226\n\t"
    "v_mfma_f32_16x16x32_bf16 v[128:131], v[208:211], v[36:39], v[128:131]\n\t"
    "v_exp_f32 v232, v112\n\t"
    "v_mfma_f32_16x16x32_bf16 v[132:135], v[208:211], v[60:63], v[132:135]\n\t"
    "v_exp_f32 v233, v113\n\t"
    "v_mfma_f32_16x16x32_bf16 v[136:139], v[208:211], v[84:87], v[136:139]\n\t"
    "v_exp_f32 v234, v114\n\t"
    "v_mfma_f32_16x16x32_bf16 v[140:143], v[208:211], a[244:247], v[140:143]\n\t"
    "ds_read_b64_tr_b16 v[208:209], v239 offset:0\n\t"
    "v_add_f32 v226, v231, v226\n\t"
    "v_mfma_f32_16x16x32_bf16 v[124:127], v[212:215], v[16:19], v[124:127]\n\t"
    "ds_read_b64_tr_b16 v[210:211], v239 offset:6144\n\t"
    "v_cvt_pk_bf16_f32 v169, v230, v231\n\t"
    "v_mfma_f32_16x16x32_bf16 v[128:131], v[212:215], v[40:43], v[128:131]\n\t"
    "v_exp_f32 v235, v115\n\t"
    "v_mfma_f32_16x16x32_bf16 v[132:135], v[212:215], v[64:67], v[132:135]\n\t"
    "v_exp_f32 v230, v116\n\t"
    "v_mfma_f32_16x16x32_bf16 v[136:139], v[212:215], v[88:91], v[136:139]\n\t"
    "v_add_f32 v227, v232, v233\n\t"
    "v_cvt_pk_bf16_f32 v172, v232, v233\n\t"
    "v_mfma_f32_16x16x32_bf16 v[140:143], v[212:215], a[248:251], v[140:143]\n\t"
    "ds_read_b64_tr_b16 v[212:213], v240 offset:0\n\t"
    "v_add_f32 v227, v234, v227\n\t"
    "v_mfma_f32_16x16x32_bf16 v[124:127], v[216:219], v[20:23], v[124:127]\n\t"
    "ds_read_b64_tr_b16 v[214:215], v240 offset:6144\n\t"
    "v_add_f32 v227, v235, v227\n\t"
    "v_mfma_f32_16x16x32_bf16 v[128:131], v[216:219], v[44:47], v[128:131]\n\t"
    "v_exp_f32 v231, v117\n\t"
    "v_mfma_f32_16x16x32_bf16 v[132:135], v[216:219], v[68:71], v[132:135]\n\t"
    "v_exp_f32 v232, v118\n\t"
    "v_mfma_f32_16x16x32_bf16 v[136:139], v[216:219], v[92:95], v[136:139]\n\t"
    "v_cvt_pk_bf16_f32 v173, v234, v235\n\t"
    "v_add_f32 v228, v230, v231\n\t"
    "v_mfma_f32_16x16x32_bf16 v[140:143], v[216:219], a[252:255], v[140:143]\n\t"
    "s_waitcnt lgkmcnt(4)\n\t"
    "ds_read_b64_tr_b16 v[216:217], v241 offset:0\n\t"
    "v_cvt_pk_bf16_f32 v176, v230, v231\n\t"
    "v_mfma_f32_16x16x32_bf16 a[0:3], v[204:207], v[184:187], a[0:3]\n\t"
    "ds_read_b64_tr_b16 v[218:219], v241 offset:6144\n\t"
    "v_add_f32 v228, v232, v228\n\t"
    "v_mfma_f32_16x16x32_bf16 a[4:7], v[204:207], v[188:191], a[4:7]\n\t"
    "v_exp_f32 v233, v119\n\t"
    "v_mfma_f32_16x16x32_bf16 a[8:11], v[204:207], v[192:195], a[8:11]\n\t"
    "v_exp_f32 v234, v120\n\t"
    "v_mfma_f32_16x16x32_bf16 a[12:15], v[204:207], v[196:199], a[12:15]\n\t"
    "v_exp_f32 v235, v121\n\t"
    "v_mfma_f32_16x16x32_bf16 a[16:19], v[204:207], v[200:203], a[16:19]\n\t"
    "s_waitcnt lgkmcnt(4)\n\t"
    "ds_read_b64_tr_b16 v[204:205], v238 offset:128\n\t"
    "v_add_f32 v228, v233, v228\n\t"
    "v_mfma_f32_16x16x32_bf16 a[20:23], v[208:211], v[184:187], a[20:23]\n\t"
    "ds_read_b64_tr_b16 v[206:207], v238 offset:6272\n\t"
    "v_cvt_pk_bf16_f32 v177, v232, v233\n\t"
    "v_mfma_f32_16x16x32_bf16 a[24:27], v[208:211], v[188:191], a[24:27]\n\t"
    "v_exp_f32 v230, v122\n\t"
    "v_mfma_f32_16x16x32_bf16 a[28:31], v[208:211], v[192:195], a[28:31]\n\t"
    "v_exp_f32 v231, v123\n\t"
    "v_mfma_f32_16x16x32_bf16 a[32:35], v[208:211], v[196:199], a[32:35]\n\t"
    "v_exp_f32 v232, v124\n\t"
    "v_mfma_f32_16x16x32_bf16 a[36:39], v[208:211], v[200:203], a[36:39]\n\t"
    "s_waitcnt lgkmcnt(4)\n\t"
    "ds_read_b64_tr_b16 v[208:209], v239 offset:128\n\t"
    "v_add_f32 v229, v234, v235\n\t"
    "v_mfma_f32_16x16x32_bf16 a[40:43], v[212:215], v[184:187], a[40:43]\n\t"
    "ds_read_b64_tr_b16 v[210:211], v239 offset:6272\n\t"
    "v_cvt_pk_bf16_f32 v180, v234, v235\n\t"
    "v_mfma_f32_16x16x32_bf16 a[44:47], v[212:215], v[188:191], a[44:47]\n\t"
    "v_exp_f32 v233, v125\n\t"
    "v_mfma_f32_16x16x32_bf16 a[48:51], v[212:215], v[192:195], a[48:51]\n\t"
    "v_exp_f32 v234, v126\n\t"
    "v_mfma_f32_16x16x32_bf16 a[52:55], v[212:215], v[196:199], a[52:55]\n\t"
    "v_add_f32 v229, v230, v229\n\t"
    "v_add_f32 v229, v231, v229\n\t"
    "v_mfma_f32_16x16x32_bf16 a[56:59], v[212:215], v[200:203], a[56:59]\n\t"
    "s_waitcnt lgkmcnt(4)\n\t"
    "ds_read_b64_tr_b16 v[212:213], v240 offset:128\n\t"
    "v_cvt_pk_bf16_f32 v181, v230, v231\n\t"
    "v_mfma_f32_16x16x32_bf16 a[60:63], v[216:219], v[184:187], a[60:63]\n\t"
    "ds_read_b64_tr_b16 v[214:215], v240 offset:6272\n\t"
    "v_add_f32 v225, v232, v225\n\t"
    "v_mfma_f32_16x16x32_bf16 a[64:67], v[216:219], v[188:191], a[64:67]\n\t"
    "v_exp_f32 v235, v127\n\t"
    "v_mfma_f32_16x16x32_bf16 a[68:71], v[216:219], v[192:195], a[68:71]\n\t"
    "v_exp_f32 v230, v128\n\t"
    "v_mfma_f32_16x16x32_bf16 a[72:75], v[216:219], v[196:199], a[72:75]\n\t"
    "v_add_f32 v225, v233, v225\n\t"
    "v_cvt_pk_bf16_f32 v166, v232, v233\n\t"
    "v_mfma_f32_16x16x32_bf16 a[76:79], v[216:219], v[200:203], a[76:79]\n\t"
    "s_waitcnt lgkmcnt(4)\n\t"
    "ds_read_b64_tr_b16 v[216:217], v241 offset:128\n\t"
    "v_add_f32 v225, v234, v225\n\t"
    "v_mfma_f32_16x16x32_bf16 a[80:83], v[204:207], v[184:187], a[80:83]\n\t"
    "ds_read_b64_tr_b16 v[218:219], v241 offset:6272\n\t"
    "v_add_f32 v225, v235, v225\n\t"
    "v_mfma_f32_16x16x32_bf16 a[84:87], v[204:207], v[188:191], a[84:87]\n\t"
    "v_exp_f32 v231, v129\n\t"
    "v_mfma_f32_16x16x32_bf16 a[88:91], v[204:207], v[192:195], a[88:91]\n\t"
    "v_exp_f32 v232, v130\n\t"
    "v_mfma_f32_16x16x32_bf16 a[92:95], v[204:207], v[196:199], a[92:95]\n\t"
    "v_cvt_pk_bf16_f32 v167, v234, v235\n\t"
    "v_add_f32 v226, v230, v226\n\t"
    "v_mfma_f32_16x16x32_bf16 a[96:99], v[204:207], v[200:203], a[96:99]\n\t"
    "s_waitcnt lgkmcnt(4)\n\t"
    "ds_read_b64_tr_b16 v[204:205], v238 offset:256\n\t"
    "v_add_f32 v226, v231, v226\n\t"
    "v_mfma_f32_16x16x32_bf16 a[100:103], v[208:211], v[184:187], a[100:103]\n\t"
    "ds_read_b64_tr_b16 v[206:207], v238 offset:6400\n\t"
    "v_cvt_pk_bf16_f32 v170, v230, v231\n\t"
    "v_mfma_f32_16x16x32_bf16 a[104:107], v[208:211], v[188:191], a[104:107]\n\t"
    "v_exp_f32 v233, v131\n\t"
    "v_mfma_f32_16x16x32_bf16 a[108:111], v[208:211], v[192:195], a[108:111]\n\t"
    "v_exp_f32 v234, v132\n\t"
    "v_mfma_f32_16x16x32_bf16 a[112:115], v[208:211], v[196:199], a[112:115]\n\t"
    "v_exp_f32 v235, v133\n\t"
    "v_mfma_f32_16x16x32_bf16 a[116:119], v[208:211], v[200:203], a[116:119]\n\t"
    "s_waitcnt lgkmcnt(4)\n\t"
    "ds_read_b64_tr_b16 v[208:209], v239 offset:256\n\t"
    "v_add_f32 v226, v232, v226\n\t"
    "v_mfma_f32_16x16x32_bf16 a[120:123], v[212:215], v[184:187], a[120:123]\n\t"
    "ds_read_b64_tr_b16 v[210:211], v239 offset:6400\n\t"
    "v_add_f32 v226, v233, v226\n\t"
    "v_mfma_f32_16x16x32_bf16 a[124:127], v[212:215], v[188:191], a[124:127]\n\t"
    "v_exp_f32 v230, v134\n\t"
    "v_mfma_f32_16x16x32_bf16 a[128:131], v[212:215], v[192:195], a[128:131]\n\t"
    "v_cvt_pk_bf16_f32 v171, v232, v233\n\t"
    "v_add_f32 v227, v234, v227\n\t"
    "v_mfma_f32_16x16x32_bf16 a[132:135], v[212:215], v[196:199], a[132:135]\n\t"
    "v_exp_f32 v231, v135\n\t"
    "v_mfma_f32_16x16x32_bf16 a[136:139], v[212:215], v[200:203], a[136:139]\n\t"
    "s_waitcnt lgkmcnt(4)\n\t"
    "ds_read_b64_tr_b16 v[212:213], v240 offset:256\n\t"
    "v_add_f32 v227, v235, v227\n\t"
    "v_mfma_f32_16x16x32_bf16 a[140:143], v[216:219], v[184:187], a[140:143]\n\t"
    "ds_read_b64_tr_b16 v[214:215], v240 offset:6400\n\t"
    "v_cvt_pk_bf16_f32 v174, v234, v235\n\t"
    "v_mfma_f32_16x16x32_bf16 a[144:147], v[216:219], v[188:191], a[144:147]\n\t"
    "v_exp_f32 v232, v136\n\t"
    "v_mfma_f32_16x16x32_bf16 a[148:151], v[216:219], v[192:195], a[148:151]\n\t"
    "v_exp_f32 v233, v137\n\t"
    "v_mfma_f32_16x16x32_bf16 a[152:155], v[216:219], v[196:199], a[152:155]\n\t"
    "v_exp_f32 v234, v138\n\t"
    "v_mfma_f32_16x16x32_bf16 a[156:159], v[216:219], v[200:203], a[156:159]\n\t"
    "s_waitcnt lgkmcnt(4)\n\t"
    "ds_read_b64_tr_b16 v[216:217], v241 offset:256\n\t"
    "v_add_f32 v227, v230, v227\n\t"
    "v_mfma_f32_16x16x32_bf16 a[160:163], v[204:207], v[184:187], a[160:163]\n\t"
    "ds_read_b64_tr_b16 v[218:219], v241 offset:6400\n\t"
    "v_add_f32 v227, v231, v227\n\t"
    "v_mfma_f32_16x16x32_bf16 a[164:167], v[204:207], v[188:191], a[164:167]\n\t"
    "v_cvt_pk_bf16_f32 v175, v230, v231\n\t"
    "v_add_f32 v228, v232, v228\n\t"
    "v_mfma_f32_16x16x32_bf16 a[168:171], v[204:207], v[192:195], a[168:171]\n\t"
    "v_exp_f32 v235, v139\n\t"
    "v_mfma_f32_16x16x32_bf16 a[172:175], v[204:207], v[196:199], a[172:175]\n\t"
    "v_exp_f32 v230, v140\n\t"
    "v_mfma_f32_16x16x32_bf16 a[176:179], v[204:207], v[200:203], a[176:179]\n\t"
    "s_waitcnt lgkmcnt(4)\n\t"
    "v_add_f32 v228, v233, v228\n\t"
    "v_cvt_pk_bf16_f32 v178, v232, v233\n\t"
    "v_mfma_f32_16x16x32_bf16 a[180:183], v[208:211], v[184:187], a[180:183]\n\t"
    "v_exp_f32 v231, v141\n\t"
    "v_mfma_f32_16x16x32_bf16 a[184:187], v[208:211], v[188:191], a[184:187]\n\t"
    "v_exp_f32 v232, v142\n\t"
    "v_mfma_f32_16x16x32_bf16 a[188:191], v[208:211], v[192:195], a[188:191]\n\t"
    "v_add_f32 v228, v234, v228\n\t"
    "v_add_f32 v228, v235, v228\n\t"
    "v_mfma_f32_16x16x32_bf16 a[192:195], v[208:211], v[196:199], a[192:195]\n\t"
    "s_waitcnt vmcnt(12) lgkmcnt(0)\n\t"
    "s_barrier\n\t"
    "ds_read_b128 v[204:207], v236 offset:0\n\t"
    "v_cvt_pk_bf16_f32 v179, v234, v235\n\t"
    "v_mfma_f32_16x16x32_bf16 a[196:199], v[208:211], v[200:203], a[196:199]\n\t"
    "v_exp_f32 v233, v143\n\t"
    "v_mfma_f32_16x16x32_bf16 a[200:203], v[212:215], v[184:187], a[200:203]\n\t"
    "s_mov_b32 s29, s28\n\t"
    "ds_read_b128 v[208:211], v237 offset:0\n\t"
    "v_add_f32 v229, v230, v229\n\t"
    "v_mfma_f32_16x16x32_bf16 a[204:207], v[212:215], v[188:191], a[204:207]\n\t"
    "s_mov_b32 s28, s30\n\t"
    "v_add_f32 v229, v231, v229\n\t"
    "v_cvt_pk_bf16_f32 v182, v230, v231\n\t"
    "v_mfma_f32_16x16x32_bf16 a[208:211], v[212:215], v[192:195], a[208:211]\n\t"
    "s_add_u32 s58, s29, s34\n\t"
    "v_add_f32 v229, v232, v229\n\t"
    "v_add_f32 v229, v233, v229\n\t"
    "v_mfma_f32_16x16x32_bf16 a[212:215], v[212:215], v[196:199], a[212:215]\n\t"
    "s_add_u32 s59, s29, s35\n\t"
    "v_cvt_pk_bf16_f32 v183, v232, v233\n\t"
    "v_max3_f32 v253, v225, v226, v227\n\t"
    "v_mfma_f32_16x16x32_bf16 a[216:219], v[212:215], v[200:203], a[216:219]\n\t"
    "v_max3_f32 v253, v253, v228, v229\n\t"
    "v_cmp_nge_f32 vcc, s50, v253\n\t"
    "v_mfma_f32_16x16x32_bf16 a[220:223], v[216:219], v[184:187], a[220:223]\n\t"
    "ds_read_b128 v[212:215], v236 offset:128\n\t"
    "v_mfma_f32_16x16x32_bf16 a[224:227], v[216:219], v[188:191], a[224:227]\n\t"
    "v_mfma_f32_16x16x32_bf16 a[228:231], v[216:219], v[192:195], a[228:231]\n\t"
    "v_mfma_f32_16x16x32_bf16 a[232:235], v[216:219], v[196:199], a[232:235]\n\t"
    "v_mfma_f32_16x16x32_bf16 a[236:239], v[216:219], v[200:203], a[236:239]\n\t"
    "s_waitcnt lgkmcnt(2)\n\t"
    "ds_read_b128 v[216:219], v237 offset:128\n\t"
    "s_cbranch_vccnz .Lk1_stub_s13\n\t"
    ".Lk1_cont_s13:\n\t"
    "s_cmp_eq_u32 s45, 0\n\t"
    "s_cbranch_scc0 .Lk1_last\n\t"
    "s_lshl_b32 s42, s70, 5\n\t"
    "s_add_u32 s42, s42, s79\n\t"
    "s_mul_i32 s42, s42, 196608\n\t"
    "s_add_u32 s8, s62, s42\n\t"
    "s_addc_u32 s9, s63, 0\n\t"
    "s_add_u32 s12, s64, s42\n\t"
    "s_addc_u32 s13, s65, 0\n\t"
    "s_mul_i32 s36, s51, 6144\n\t"
    "s_mov_b32 s37, s36\n\t"
    "v_mfma_f32_16x16x32_bf16 v[104:107], v[204:207], v[0:3], v[144:147]\n\t"
    "s_add_u32 m0, s58, 0\n\t"
    "v_add_u32 v238, s31, v238\n\t"
    "v_add_u32 v239, s31, v239\n\t"
    "v_mfma_f32_16x16x32_bf16 v[108:111], v[204:207], v[24:27], v[148:151]\n\t"
    "buffer_load_dwordx4 v245, s[16:19], s38 offen lds\n\t"
    "v_mfma_f32_16x16x32_bf16 v[112:115], v[204:207], v[48:51], v[152:155]\n\t"
    "v_add_u32 v240, s31, v240\n\t"
    "v_add_u32 v241, s31, v241\n\t"
    "v_mfma_f32_16x16x32_bf16 v[116:119], v[204:207], v[72:75], v[156:159]\n\t"
    "buffer_load_dwordx4 v246, s[16:19], s38 offen offset:1024 lds\n\t"
    "v_mfma_f32_16x16x32_bf16 v[120:123], v[204:207], v[96:99], v[160:163]\n\t"
    "s_waitcnt lgkmcnt(2)\n\t"
    "ds_read_b128 v[204:207], v236 offset:256\n\t"
    "v_add_f32 v220, v225, v220\n\t"
    "v_mfma_f32_16x16x32_bf16 v[104:107], v[208:211], v[4:7], v[104:107]\n\t"
    "v_add_f32 v221, v226, v221\n\t"
    "v_add_f32 v222, v227, v222\n\t"
    "v_mfma_f32_16x16x32_bf16 v[108:111], v[208:211], v[28:31], v[108:111]\n\t"
    "buffer_load_dwordx4 v247, s[16:19], s38 offen offset:2048 lds\n\t"
    "v_mfma_f32_16x16x32_bf16 v[112:115], v[208:211], v[52:55], v[112:115]\n\t"
    "s_add_u32 m0, s58, 3072\n\t"
    "v_add_f32 v223, v228, v223\n\t"
    "v_add_f32 v224, v229, v224\n\t"
    "v_mfma_f32_16x16x32_bf16 v[116:119], v[208:211], v[76:79], v[116:119]\n\t"
    "s_add_u32 s40, s38, 98304\n\t"
    "buffer_load_dwordx4 v245, s[16:19], s40 offen lds\n\t"
    "v_mfma_f32_16x16x32_bf16 v[120:123], v[208:211], v[100:103], v[120:123]\n\t"
    "s_waitcnt lgkmcnt(2)\n\t"
    "ds_read_b128 v[208:211], v237 offset:256\n\t"
    "v_mfma_f32_16x16x32_bf16 v[104:107], v[212:215], v[8:11], v[104:107]\n\t"
    "v_mfma_f32_16x16x32_bf16 v[108:111], v[212:215], v[32:35], v[108:111]\n\t"
    "buffer_load_dwordx4 v246, s[16:19], s40 offen offset:1024 lds\n\t"
    "v_mfma_f32_16x16x32_bf16 v[112:115], v[212:215], v[56:59], v[112:115]\n\t"
    "v_mfma_f32_16x16x32_bf16 v[116:119], v[212:215], v[80:83], v[116:119]\n\t"
    "buffer_load_dwordx4 v247, s[16:19], s40 offen offset:2048 lds\n\t"
    "v_mfma_f32_16x16x32_bf16 v[120:123], v[212:215], a[240:243], v[120:123]\n\t"
    "s_waitcnt lgkmcnt(2)\n\t"
    "ds_read_b128 v[212:215], v236 offset:6144\n\t"
    "v_mfma_f32_16x16x32_bf16 v[104:107], v[216:219], v[12:15], v[104:107]\n\t"
    "s_add_u32 m0, s59, 0\n\t"
    "v_mfma_f32_16x16x32_bf16 v[108:111], v[216:219], v[36:39], v[108:111]\n\t"
    "buffer_load_dwordx4 v248, s[20:23], s39 offen lds\n\t"
    "v_mfma_f32_16x16x32_bf16 v[112:115], v[216:219], v[60:63], v[112:115]\n\t"
    "s_add_u32 m0, s59, 4096\n\t"
    "v_mfma_f32_16x16x32_bf16 v[116:119], v[216:219], v[84:87], v[116:119]\n\t"
    "s_add_u32 s41, s39, 81920\n\t"
    "buffer_load_dwordx4 v248, s[20:23], s41 offen lds\n\t"
    "v_mfma_f32_16x16x32_bf16 v[120:123], v[216:219], a[244:247], v[120:123]\n\t"
    "s_waitcnt lgkmcnt(2)\n\t"
    "ds_read_b128 v[216:219], v237 offset:6144\n\t"
    "v_mfma_f32_16x16x32_bf16 v[104:107], v[204:207], v[16:19], v[104:107]\n\t"
    "s_add_u32 m0, s59, 8192\n\t"
    "v_mfma_f32_16x16x32_bf16 v[108:111], v[204:207], v[40:43], v[108:111]\n\t"
    "s_add_u32 s40, s39, 163840\n\t"
    "buffer_load_dwordx4 v248, s[20:23], s40 offen lds\n\t"
    "v_mfma_f32_16x16x32_bf16 v[112:115], v[204:207], v[64:67], v[112:115]\n\t"
    "s_add_u32 s38, s38, 393216\n\t"
    "v_mfma_f32_16x16x32_bf16 v[116:119], v[204:207], v[88:91], v[116:119]\n\t"
    "s_add_u32 s39, s39, 64\n\t"
    "v_mfma_f32_16x16x32_bf16 v[120:123], v[204:207], a[248:251], v[120:123]\n\t"
    "s_waitcnt lgkmcnt(2)\n\t"
    "ds_read_b128 v[204:207], v236 offset:6272\n\t"
    "v_mfma_f32_16x16x32_bf16 v[104:107], v[208:211], v[20:23], v[104:107]\n\t"
    "v_mfma_f32_16x16x32_bf16 v[108:111], v[208:211], v[44:47], v[108:111]\n\t"
    "v_mfma_f32_16x16x32_bf16 v[112:115], v[208:211], v[68:71], v[112:115]\n\t"
    "v_mfma_f32_16x16x32_bf16 v[116:119], v[208:211], v[92:95], v[116:119]\n\t"
    "v_mfma_f32_16x16x32_bf16 v[120:123], v[208:211], a[252:255], v[120:123]\n\t"
    "s_waitcnt lgkmcnt(2)\n\t"
    "ds_read_b128 v[208:211], v237 offset:6272\n\t"
    "v_mfma_f32_16x16x32_bf16 v[124:127], v[212:215], v[0:3], v[144:147]\n\t"
    "v_mfma_f32_16x16x32_bf16 v[128:131], v[212:215], v[24:27], v[148:151]\n\t"
    "s_add_u32 s30, s28, 24576\n\t"
    "v_exp_f32 v230, v104\n\t"
    "v_mfma_f32_16x16x32_bf16 v[132:135], v[212:215], v[48:51], v[152:155]\n\t"
    "s_cmp_eq_u32 s30, 73728\n\t"
    "v_exp_f32 v231, v105\n\t"
    "v_mfma_f32_16x16x32_bf16 v[136:139], v[212:215], v[72:75], v[156:159]\n\t"
    "s_cselect_b32 s30, 0, s30\n\t"
    "v_exp_f32 v232, v106\n\t"
    "v_mfma_f32_16x16x32_bf16 v[140:143], v[212:215], v[96:99], v[160:163]\n\t"
    "s_waitcnt lgkmcnt(2)\n\t"
    "ds_read_b128 v[212:215], v236 offset:6400\n\t"
    "v_add_f32 v225, v230, v231\n\t"
    "v_mfma_f32_16x16x32_bf16 v[124:127], v[216:219], v[4:7], v[124:127]\n\t"
    "s_sub_u32 s31, s30, s28\n\t"
    "v_exp_f32 v233, v107\n\t"
    "v_mfma_f32_16x16x32_bf16 v[128:131], v[216:219], v[28:31], v[128:131]\n\t"
    "v_exp_f32 v234, v108\n\t"
    "v_mfma_f32_16x16x32_bf16 v[132:135], v[216:219], v[52:55], v[132:135]\n\t"
    "v_cvt_pk_bf16_f32 v184, v230, v231\n\t"
    "v_add_f32 v225, v232, v225\n\t"
    "v_mfma_f32_16x16x32_bf16 v[136:139], v[216:219], v[76:79], v[136:139]\n\t"
    "v_exp_f32 v235, v109\n\t"
    "v_mfma_f32_16x16x32_bf16 v[140:143], v[216:219], v[100:103], v[140:143]\n\t"
    "s_waitcnt lgkmcnt(2)\n\t"
    "ds_read_b128 v[216:219], v237 offset:6400\n\t"
    "v_add_f32 v225, v233, v225\n\t"
    "v_mfma_f32_16x16x32_bf16 v[124:127], v[204:207], v[8:11], v[124:127]\n\t"
    "v_add_u32 v236, s31, v236\n\t"
    "v_add_u32 v237, s31, v237\n\t"
    "v_mfma_f32_16x16x32_bf16 v[128:131], v[204:207], v[32:35], v[128:131]\n\t"
    "v_exp_f32 v230, v110\n\t"
    "v_mfma_f32_16x16x32_bf16 v[132:135], v[204:207], v[56:59], v[132:135]\n\t"
    "v_cvt_pk_bf16_f32 v185, v232, v233\n\t"
    "v_add_f32 v226, v234, v235\n\t"
    "v_mfma_f32_16x16x32_bf16 v[136:139], v[204:207], v[80:83], v[136:139]\n\t"
    "v_exp_f32 v231, v111\n\t"
    "v_mfma_f32_16x16x32_bf16 v[140:143], v[204:207], a[240:243], v[140:143]\n\t"
    "s_waitcnt vmcnt(15) lgkmcnt(0)\n\t"
    "s_barrier\n\t"
    "ds_read_b64_tr_b16 v[204:205], v238 offset:0\n\t"
    "v_cvt_pk_bf16_f32 v188, v234, v235\n\t"
    "v_mfma_f32_16x16x32_bf16 v[124:127], v[208:211], v[12:15], v[124:127]\n\t"
    "ds_read_b64_tr_b16 v[206:207], v238 offset:6144\n\t"
    "v_add_f32 v226, v230, v226\n\t"
    "v_mfma_f32_16x16x32_bf16 v[128:131], v[208:211], v[36:39], v[128:131]\n\t"
    "v_exp_f32 v232, v112\n\t"
    "v_mfma_f32_16x16x32_bf16 v[132:135], v[208:211], v[60:63], v[132:135]\n\t"
    "v_exp_f32 v233, v113\n\t"
    "v_mfma_f32_16x16x32_bf16 v[136:139], v[208:211], v[84:87], v[136:139]\n\t"
    "v_exp_f32 v234, v114\n\t"
    "v_mfma_f32_16x16x32_bf16 v[140:143], v[208:211], a[244:247], v[140:143]\n\t"
    "ds_read_b64_tr_b16 v[208:209], v239 offset:0\n\t"
    "v_add_f32 v226, v231, v226\n\t"
    "v_mfma_f32_16x16x32_bf16 v[124:127], v[212:215], v[16:19], v[124:127]\n\t"
    "ds_read_b64_tr_b16 v[210:211], v239 offset:6144\n\t"
    "v_cvt_pk_bf16_f32 v189, v230, v231\n\t"
    "v_mfma_f32_16x16x32_bf16 v[128:131], v[212:215], v[40:43], v[128:131]\n\t"
    "v_exp_f32 v235, v115\n\t"
    "v_mfma_f32_16x16x32_bf16 v[132:135], v[212:215], v[64:67], v[132:135]\n\t"
    "v_exp_f32 v230, v116\n\t"
    "v_mfma_f32_16x16x32_bf16 v[136:139], v[212:215], v[88:91], v[136:139]\n\t"
    "v_add_f32 v227, v232, v233\n\t"
    "v_cvt_pk_bf16_f32 v192, v232, v233\n\t"
    "v_mfma_f32_16x16x32_bf16 v[140:143], v[212:215], a[248:251], v[140:143]\n\t"
    "ds_read_b64_tr_b16 v[212:213], v240 offset:0\n\t"
    "v_add_f32 v227, v234, v227\n\t"
    "v_mfma_f32_16x16x32_bf16 v[124:127], v[216:219], v[20:23], v[124:127]\n\t"
    "ds_read_b64_tr_b16 v[214:215], v240 offset:6144\n\t"
    "v_add_f32 v227, v235, v227\n\t"
    "v_mfma_f32_16x16x32_bf16 v[128:131], v[216:219], v[44:47], v[128:131]\n\t"
    "v_exp_f32 v231, v117\n\t"
    "v_mfma_f32_16x16x32_bf16 v[132:135], v[216:219], v[68:71], v[132:135]\n\t"
    "v_exp_f32 v232, v118\n\t"
    "v_mfma_f32_16x16x32_bf16 v[136:139], v[216:219], v[92:95], v[136:139]\n\t"
    "v_cvt_pk_bf16_f32 v193, v234, v235\n\t"
    "v_add_f32 v228, v230, v231\n\t"
    "v_mfma_f32_16x16x32_bf16 v[140:143], v[216:219], a[252:255], v[140:143]\n\t"
    "s_waitcnt lgkmcnt(4)\n\t"
    "ds_read_b64_tr_b16 v[216:217], v241 offset:0\n\t"
    "v_cvt_pk_bf16_f32 v196, v230, v231\n\t"
    "v_mfma_f32_16x16x32_bf16 a[0:3], v[204:207], v[164:167], a[0:3]\n\t"
    "ds_read_b64_tr_b16 v[218:219], v241 offset:6144\n\t"
    "v_add_f32 v228, v232, v228\n\t"
    "v_mfma_f32_16x16x32_bf16 a[4:7], v[204:207], v[168:171], a[4:7]\n\t"
    "v_exp_f32 v233, v119\n\t"
    "v_mfma_f32_16x16x32_bf16 a[8:11], v[204:207], v[172:175], a[8:11]\n\t"
    "v_exp_f32 v234, v120\n\t"
    "v_mfma_f32_16x16x32_bf16 a[12:15], v[204:207], v[176:179], a[12:15]\n\t"
    "v_exp_f32 v235, v121\n\t"
    "v_mfma_f32_16x16x32_bf16 a[16:19], v[204:207], v[180:183], a[16:19]\n\t"
    "s_waitcnt lgkmcnt(4)\n\t"
    "ds_read_b64_tr_b16 v[204:205], v238 offset:128\n\t"
    "v_add_f32 v228, v233, v228\n\t"
    "v_mfma_f32_16x16x32_bf16 a[20:23], v[208:211], v[164:167], a[20:23]\n\t"
    "ds_read_b64_tr_b16 v[206:207], v238 offset:6272\n\t"
    "v_cvt_pk_bf16_f32 v197, v232, v233\n\t"
    "v_mfma_f32_16x16x32_bf16 a[24:27], v[208:211], v[168:171], a[24:27]\n\t"
    "v_exp_f32 v230, v122\n\t"
    "v_mfma_f32_16x16x32_bf16 a[28:31], v[208:211], v[172:175], a[28:31]\n\t"
    "v_exp_f32 v231, v123\n\t"
    "v_mfma_f32_16x16x32_bf16 a[32:35], v[208:211], v[176:179], a[32:35]\n\t"
    "v_exp_f32 v232, v124\n\t"
    "v_mfma_f32_16x16x32_bf16 a[36:39], v[208:211], v[180:183], a[36:39]\n\t"
    "s_waitcnt lgkmcnt(4)\n\t"
    "ds_read_b64_tr_b16 v[208:209], v239 offset:128\n\t"
    "v_add_f32 v229, v234, v235\n\t"
    "v_mfma_f32_16x16x32_bf16 a[40:43], v[212:215], v[164:167], a[40:43]\n\t"
    "ds_read_b64_tr_b16 v[210:211], v239 offset:6272\n\t"
    "v_cvt_pk_bf16_f32 v200, v234, v235\n\t"
    "v_mfma_f32_16x16x32_bf16 a[44:47], v[212:215], v[168:171], a[44:47]\n\t"
    "v_exp_f32 v233, v125\n\t"
    "v_mfma_f32_16x16x32_bf16 a[48:51], v[212:215], v[172:175], a[48:51]\n\t"
    "v_exp_f32 v234, v126\n\t"
    "v_mfma_f32_16x16x32_bf16 a[52:55], v[212:215], v[176:179], a[52:55]\n\t"
    "v_add_f32 v229, v230, v229\n\t"
    "v_add_f32 v229, v231, v229\n\t"
    "v_mfma_f32_16x16x32_bf16 a[56:59], v[212:215], v[180:183], a[56:59]\n\t"
    "s_waitcnt lgkmcnt(4)\n\t"
    "ds_read_b64_tr_b16 v[212:213], v240 offset:128\n\t"
    "v_cvt_pk_bf16_f32 v201, v230, v231\n\t"
    "v_mfma_f32_16x16x32_bf16 a[60:63], v[216:219], v[164:167], a[60:63]\n\t"
    "ds_read_b64_tr_b16 v[214:215], v240 offset:6272\n\t"
    "v_add_f32 v225, v232, v225\n\t"
    "v_mfma_f32_16x16x32_bf16 a[64:67], v[216:219], v[168:171], a[64:67]\n\t"
    "v_exp_f32 v235, v127\n\t"
    "v_mfma_f32_16x16x32_bf16 a[68:71], v[216:219], v[172:175], a[68:71]\n\t"
    "v_exp_f32 v230, v128\n\t"
    "v_mfma_f32_16x16x32_bf16 a[72:75], v[216:219], v[176:179], a[72:75]\n\t"
    "v_add_f32 v225, v233, v225\n\t"
    "v_cvt_pk_bf16_f32 v186, v232, v233\n\t"
    "v_mfma_f32_16x16x32_bf16 a[76:79], v[216:219], v[180:183], a[76:79]\n\t"
    "s_waitcnt lgkmcnt(4)\n\t"
    "ds_read_b64_tr_b16 v[216:217], v241 offset:128\n\t"
    "v_add_f32 v225, v234, v225\n\t"
    "v_mfma_f32_16x16x32_bf16 a[80:83], v[204:207], v[164:167], a[80:83]\n\t"
    "ds_read_b64_tr_b16 v[218:219], v241 offset:6272\n\t"
    "v_add_f32 v225, v235, v225\n\t"
    "v_mfma_f32_16x16x32_bf16 a[84:87], v[204:207], v[168:171], a[84:87]\n\t"
    "v_exp_f32 v231, v129\n\t"
    "v_mfma_f32_16x16x32_bf16 a[88:91], v[204:207], v[172:175], a[88:91]\n\t"
    "v_exp_f32 v232, v130\n\t"
    "v_mfma_f32_16x16x32_bf16 a[92:95], v[204:207], v[176:179], a[92:95]\n\t"
    "v_cvt_pk_bf16_f32 v187, v234, v235\n\t"
    "v_add_f32 v226, v230, v226\n\t"
    "v_mfma_f32_16x16x32_bf16 a[96:99], v[204:207], v[180:183], a[96:99]\n\t"
    "s_waitcnt lgkmcnt(4)\n\t"
    "ds_read_b64_tr_b16 v[204:205], v238 offset:256\n\t"
    "v_add_f32 v226, v231, v226\n\t"
    "v_mfma_f32_16x16x32_bf16 a[100:103], v[208:211], v[164:167], a[100:103]\n\t"
    "ds_read_b64_tr_b16 v[206:207], v238 offset:6400\n\t"
    "v_cvt_pk_bf16_f32 v190, v230, v231\n\t"
    "v_mfma_f32_16x16x32_bf16 a[104:107], v[208:211], v[168:171], a[104:107]\n\t"
    "v_exp_f32 v233, v131\n\t"
    "v_mfma_f32_16x16x32_bf16 a[108:111], v[208:211], v[172:175], a[108:111]\n\t"
    "v_exp_f32 v234, v132\n\t"
    "v_mfma_f32_16x16x32_bf16 a[112:115], v[208:211], v[176:179], a[112:115]\n\t"
    "v_exp_f32 v235, v133\n\t"
    "v_mfma_f32_16x16x32_bf16 a[116:119], v[208:211], v[180:183], a[116:119]\n\t"
    "s_waitcnt lgkmcnt(4)\n\t"
    "ds_read_b64_tr_b16 v[208:209], v239 offset:256\n\t"
    "v_add_f32 v226, v232, v226\n\t"
    "v_mfma_f32_16x16x32_bf16 a[120:123], v[212:215], v[164:167], a[120:123]\n\t"
    "ds_read_b64_tr_b16 v[210:211], v239 offset:6400\n\t"
    "v_add_f32 v226, v233, v226\n\t"
    "v_mfma_f32_16x16x32_bf16 a[124:127], v[212:215], v[168:171], a[124:127]\n\t"
    "v_exp_f32 v230, v134\n\t"
    "v_mfma_f32_16x16x32_bf16 a[128:131], v[212:215], v[172:175], a[128:131]\n\t"
    "v_cvt_pk_bf16_f32 v191, v232, v233\n\t"
    "v_add_f32 v227, v234, v227\n\t"
    "v_mfma_f32_16x16x32_bf16 a[132:135], v[212:215], v[176:179], a[132:135]\n\t"
    "v_exp_f32 v231, v135\n\t"
    "v_mfma_f32_16x16x32_bf16 a[136:139], v[212:215], v[180:183], a[136:139]\n\t"
    "s_waitcnt lgkmcnt(4)\n\t"
    "ds_read_b64_tr_b16 v[212:213], v240 offset:256\n\t"
    "v_add_f32 v227, v235, v227\n\t"
    "v_mfma_f32_16x16x32_bf16 a[140:143], v[216:219], v[164:167], a[140:143]\n\t"
    "ds_read_b64_tr_b16 v[214:215], v240 offset:6400\n\t"
    "v_cvt_pk_bf16_f32 v194, v234, v235\n\t"
    "v_mfma_f32_16x16x32_bf16 a[144:147], v[216:219], v[168:171], a[144:147]\n\t"
    "v_exp_f32 v232, v136\n\t"
    "v_mfma_f32_16x16x32_bf16 a[148:151], v[216:219], v[172:175], a[148:151]\n\t"
    "v_exp_f32 v233, v137\n\t"
    "v_mfma_f32_16x16x32_bf16 a[152:155], v[216:219], v[176:179], a[152:155]\n\t"
    "v_exp_f32 v234, v138\n\t"
    "v_mfma_f32_16x16x32_bf16 a[156:159], v[216:219], v[180:183], a[156:159]\n\t"
    "s_waitcnt lgkmcnt(4)\n\t"
    "ds_read_b64_tr_b16 v[216:217], v241 offset:256\n\t"
    "v_add_f32 v227, v230, v227\n\t"
    "v_mfma_f32_16x16x32_bf16 a[160:163], v[204:207], v[164:167], a[160:163]\n\t"
    "ds_read_b64_tr_b16 v[218:219], v241 offset:6400\n\t"
    "v_add_f32 v227, v231, v227\n\t"
    "v_mfma_f32_16x16x32_bf16 a[164:167], v[204:207], v[168:171], a[164:167]\n\t"
    "v_add_u32 v238, s31, v238\n\t"
    "v_cvt_pk_bf16_f32 v195, v230, v231\n\t"
    "v_add_f32 v228, v232, v228\n\t"
    "v_mfma_f32_16x16x32_bf16 a[168:171], v[204:207], v[172:175], a[168:171]\n\t"
    "v_add_u32 v239, s31, v239\n\t"
    "v_exp_f32 v235, v139\n\t"
    "v_mfma_f32_16x16x32_bf16 a[172:175], v[204:207], v[176:179], a[172:175]\n\t"
    "v_add_u32 v240, s31, v240\n\t"
    "v_exp_f32 v230, v140\n\t"
    "v_mfma_f32_16x16x32_bf16 a[176:179], v[204:207], v[180:183], a[176:179]\n\t"
    "s_waitcnt lgkmcnt(4)\n\t"
    "v_add_u32 v241, s31, v241\n\t"
    "v_add_f32 v228, v233, v228\n\t"
    "v_cvt_pk_bf16_f32 v198, v232, v233\n\t"
    "v_mfma_f32_16x16x32_bf16 a[180:183], v[208:211], v[164:167], a[180:183]\n\t"
    "v_exp_f32 v231, v141\n\t"
    "v_mfma_f32_16x16x32_bf16 a[184:187], v[208:211], v[168:171], a[184:187]\n\t"
    "v_exp_f32 v232, v142\n\t"
    "v_mfma_f32_16x16x32_bf16 a[188:191], v[208:211], v[172:175], a[188:191]\n\t"
    "v_add_f32 v228, v234, v228\n\t"
    "v_add_f32 v228, v235, v228\n\t"
    "v_mfma_f32_16x16x32_bf16 a[192:195], v[208:211], v[176:179], a[192:195]\n\t"
    "s_waitcnt vmcnt(9) lgkmcnt(0)\n\t"
    "s_barrier\n\t"
    "ds_read_b64_tr_b16 v[204:205], v238 offset:0\n\t"
    "v_cvt_pk_bf16_f32 v199, v234, v235\n\t"
    "v_add_f32 v229, v230, v229\n\t"
    "v_mfma_f32_16x16x32_bf16 a[196:199], v[208:211], v[180:183], a[196:199]\n\t"
    "ds_read_b64_tr_b16 v[206:207], v238 offset:6144\n\t"
    "v_add_f32 v229, v231, v229\n\t"
    "v_cvt_pk_bf16_f32 v202, v230, v231\n\t"
    "v_mfma_f32_16x16x32_bf16 a[200:203], v[212:215], v[164:167], a[200:203]\n\t"
    "s_mov_b32 s29, s28\n\t"
    "ds_read_b64_tr_b16 v[208:209], v239 offset:0\n\t"
    "v_add_f32 v229, v232, v229\n\t"
    "v_mfma_f32_16x16x32_bf16 a[204:207], v[212:215], v[168:171], a[204:207]\n\t"
    "s_mov_b32 s28, s30\n\t"
    "ds_read_b64_tr_b16 v[210:211], v239 offset:6144\n\t"
    "v_mfma_f32_16x16x32_bf16 a[208:211], v[212:215], v[172:175], a[208:211]\n\t"
    "s_add_u32 s58, s29, s34\n\t"
    "v_exp_f32 v233, v143\n\t"
    "v_mfma_f32_16x16x32_bf16 a[212:215], v[212:215], v[176:179], a[212:215]\n\t"
    "s_add_u32 s59, s29, s35\n\t"
    "v_add_f32 v229, v233, v229\n\t"
    "v_cvt_pk_bf16_f32 v203, v232, v233\n\t"
    "v_mfma_f32_16x16x32_bf16 a[216:219], v[212:215], v[180:183], a[216:219]\n\t"
    "v_max3_f32 v253, v225, v226, v227\n\t"
    "v_max3_f32 v253, v253, v228, v229\n\t"
    "v_mfma_f32_16x16x32_bf16 a[220:223], v[216:219], v[164:167], a[220:223]\n\t"
    "ds_read_b64_tr_b16 v[212:213], v240 offset:0\n\t"
    "v_cmp_nge_f32 vcc, s50, v253\n\t"
    "v_mfma_f32_16x16x32_bf16 a[224:227], v[216:219], v[168:171], a[224:227]\n\t"
    "ds_read_b64_tr_b16 v[214:215], v240 offset:6144\n\t"
    "v_mfma_f32_16x16x32_bf16 a[228:231], v[216:219], v[172:175], a[228:231]\n\t"
    "v_mfma_f32_16x16x32_bf16 a[232:235], v[216:219], v[176:179], a[232:235]\n\t"
    "v_mfma_f32_16x16x32_bf16 a[236:239], v[216:219], v[180:183], a[236:239]\n\t"
    "s_waitcnt lgkmcnt(4)\n\t"
    "ds_read_b64_tr_b16 v[216:217], v241 offset:0\n\t"
    "s_cbranch_vccnz .Lk1_stub_s14q\n\t"
    ".Lk1_cont_s14q:\n\t"
    "v_mfma_f32_16x16x32_bf16 a[0:3], v[204:207], v[184:187], a[0:3]\n\t"
    "s_add_u32 m0, s58, 0\n\t"
    "ds_read_b64_tr_b16 v[218:219], v241 offset:6144\n\t"
    "v_mfma_f32_16x16x32_bf16 a[4:7], v[204:207], v[188:191], a[4:7]\n\t"
    "buffer_load_dwordx4 v245, s[16:19], s38 offen lds\n\t"
    "v_mfma_f32_16x16x32_bf16 a[8:11], v[204:207], v[192:195], a[8:11]\n\t"
    "v_add_f32 v220, v225, v220\n\t"
    "v_add_f32 v221, v226, v221\n\t"
    "v_mfma_f32_16x16x32_bf16 a[12:15], v[204:207], v[196:199], a[12:15]\n\t"
    "v_add_f32 v222, v227, v222\n\t"
    "v_add_f32 v223, v228, v223\n\t"
    "v_mfma_f32_16x16x32_bf16 a[16:19], v[204:207], v[200:203], a[16:19]\n\t"
    "s_waitcnt lgkmcnt(4)\n\t"
    "ds_read_b64_tr_b16 v[204:205], v238 offset:128\n\t"
    "v_add_f32 v224, v229, v224\n\t"
    "v_mfma_f32_16x16x32_bf16 a[20:23], v[208:211], v[184:187], a[20:23]\n\t"
    "ds_read_b64_tr_b16 v[206:207], v238 offset:6272\n\t"
    "v_mfma_f32_16x16x32_bf16 a[24:27], v[208:211], v[188:191], a[24:27]\n\t"
    "buffer_load_dwordx4 v246, s[16:19], s38 offen offset:1024 lds\n\t"
    "v_mfma_f32_16x16x32_bf16 a[28:31], v[208:211], v[192:195], a[28:31]\n\t"
    "v_mfma_f32_16x16x32_bf16 a[32:35], v[208:211], v[196:199], a[32:35]\n\t"
    "buffer_load_dwordx4 v247, s[16:19], s38 offen offset:2048 lds\n\t"
    "v_mfma_f32_16x16x32_bf16 a[36:39], v[208:211], v[200:203], a[36:39]\n\t"
    "s_waitcnt lgkmcnt(4)\n\t"
    "ds_read_b64_tr_b16 v[208:209], v239 offset:128\n\t"
    "v_mov_b32 v230, v220\n\t"
    "v_mfma_f32_16x16x32_bf16 a[40:43], v[212:215], v[184:187], a[40:43]\n\t"
    "ds_read_b64_tr_b16 v[210:211], v239 offset:6272\n\t"
    "v_mov_b32 v231, v221\n\t"
    "v_mfma_f32_16x16x32_bf16 a[44:47], v[212:215], v[188:191], a[44:47]\n\t"
    "s_add_u32 m0, s58, 3072\n\t"
    "v_mov_b32 v232, v222\n\t"
    "v_mov_b32 v233, v223\n\t"
    "v_mfma_f32_16x16x32_bf16 a[48:51], v[212:215], v[192:195], a[48:51]\n\t"
    "s_add_u32 s40, s38, 98304\n\t"
    "buffer_load_dwordx4 v245, s[16:19], s40 offen lds\n\t"
    "v_mfma_f32_16x16x32_bf16 a[52:55], v[212:215], v[196:199], a[52:55]\n\t"
    "v_mov_b32 v234, v224\n\t"
    "v_mfma_f32_16x16x32_bf16 a[56:59], v[212:215], v[200:203], a[56:59]\n\t"
    "s_waitcnt lgkmcnt(4)\n\t"
    "ds_read_b64_tr_b16 v[212:213], v240 offset:128\n\t"
    "v_mfma_f32_16x16x32_bf16 a[60:63], v[216:219], v[184:187], a[60:63]\n\t"
    "ds_read_b64_tr_b16 v[214:215], v240 offset:6272\n\t"
    "v_permlane32_swap_b32 v220, v230\n\t"
    "v_mfma_f32_16x16x32_bf16 a[64:67], v[216:219], v[188:191], a[64:67]\n\t"
    "buffer_load_dwordx4 v246, s[16:19], s40 offen offset:1024 lds\n\t"
    "v_mfma_f32_16x16x32_bf16 a[68:71], v[216:219], v[192:195], a[68:71]\n\t"
    "v_permlane32_swap_b32 v221, v231\n\t"
    "v_permlane32_swap_b32 v222, v232\n\t"
    "v_mfma_f32_16x16x32_bf16 a[72:75], v[216:219], v[196:199], a[72:75]\n\t"
    "v_permlane32_swap_b32 v223, v233\n\t"
    "v_permlane32_swap_b32 v224, v234\n\t"
    "v_mfma_f32_16x16x32_bf16 a[76:79], v[216:219], v[200:203], a[76:79]\n\t"
    "s_waitcnt lgkmcnt(4)\n\t"
    "ds_read_b64_tr_b16 v[216:217], v241 offset:128\n\t"
    "v_add_f32 v220, v220, v230\n\t"
    "v_mfma_f32_16x16x32_bf16 a[80:83], v[204:207], v[184:187], a[80:83]\n\t"
    "ds_read_b64_tr_b16 v[218:219], v241 offset:6272\n\t"
    "v_add_f32 v221, v221, v231\n\t"
    "v_mfma_f32_16x16x32_bf16 a[84:87], v[204:207], v[188:191], a[84:87]\n\t"
    "buffer_load_dwordx4 v247, s[16:19], s40 offen offset:2048 lds\n\t"
    "v_mfma_f32_16x16x32_bf16 a[88:91], v[204:207], v[192:195], a[88:91]\n\t"
    "s_add_u32 m0, s59, 0\n\t"
    "v_add_f32 v222, v222, v232\n\t"
    "v_add_f32 v223, v223, v233\n\t"
    "v_mfma_f32_16x16x32_bf16 a[92:95], v[204:207], v[196:199], a[92:95]\n\t"
    "buffer_load_dwordx4 v248, s[20:23], s39 offen lds\n\t"
    "v_mfma_f32_16x16x32_bf16 a[96:99], v[204:207], v[200:203], a[96:99]\n\t"
    "s_waitcnt lgkmcnt(4)\n\t"
    "ds_read_b64_tr_b16 v[204:205], v238 offset:256\n\t"
    "v_add_f32 v224, v224, v234\n\t"
    "v_mfma_f32_16x16x32_bf16 a[100:103], v[208:211], v[184:187], a[100:103]\n\t"
    "ds_read_b64_tr_b16 v[206:207], v238 offset:6400\n\t"
    "v_mfma_f32_16x16x32_bf16 a[104:107], v[208:211], v[188:191], a[104:107]\n\t"
    "s_add_u32 m0, s59, 4096\n\t"
    "v_mov_b32 v230, v220\n\t"
    "v_mov_b32 v231, v221\n\t"
    "v_mfma_f32_16x16x32_bf16 a[108:111], v[208:211], v[192:195], a[108:111]\n\t"
    "s_add_u32 s41, s39, 81920\n\t"
    "buffer_load_dwordx4 v248, s[20:23], s41 offen lds\n\t"
    "v_mfma_f32_16x16x32_bf16 a[112:115], v[208:211], v[196:199], a[112:115]\n\t"
    "v_mov_b32 v232, v222\n\t"
    "v_mov_b32 v233, v223\n\t"
    "v_mfma_f32_16x16x32_bf16 a[116:119], v[208:211], v[200:203], a[116:119]\n\t"
    "s_waitcnt lgkmcnt(4)\n\t"
    "ds_read_b64_tr_b16 v[208:209], v239 offset:256\n\t"
    "v_mov_b32 v234, v224\n\t"
    "v_mfma_f32_16x16x32_bf16 a[120:123], v[212:215], v[184:187], a[120:123]\n\t"
    "s_add_u32 m0, s59, 8192\n\t"
    "ds_read_b64_tr_b16 v[210:211], v239 offset:6400\n\t"
    "v_mfma_f32_16x16x32_bf16 a[124:127], v[212:215], v[188:191], a[124:127]\n\t"
    "s_add_u32 s40, s39, 163840\n\t"
    "buffer_load_dwordx4 v248, s[20:23], s40 offen lds\n\t"
    "v_mfma_f32_16x16x32_bf16 a[128:131], v[212:215], v[192:195], a[128:131]\n\t"
    "s_add_u32 s38, s38, 393216\n\t"
    "v_permlane16_swap_b32 v220, v230\n\t"
    "v_permlane16_swap_b32 v221, v231\n\t"
    "v_mfma_f32_16x16x32_bf16 a[132:135], v[212:215], v[196:199], a[132:135]\n\t"
    "s_add_u32 s39, s39, 64\n\t"
    "v_permlane16_swap_b32 v222, v232\n\t"
    "v_permlane16_swap_b32 v223, v233\n\t"
    "v_mfma_f32_16x16x32_bf16 a[136:139], v[212:215], v[200:203], a[136:139]\n\t"
    "s_waitcnt lgkmcnt(4)\n\t"
    "ds_read_b64_tr_b16 v[212:213], v240 offset:256\n\t"
    "v_permlane16_swap_b32 v224, v234\n\t"
    "v_mfma_f32_16x16x32_bf16 a[140:143], v[216:219], v[184:187], a[140:143]\n\t"
    "ds_read_b64_tr_b16 v[214:215], v240 offset:6400\n\t"
    "v_add_f32 v220, v220, v230\n\t"
    "v_mfma_f32_16x16x32_bf16 a[144:147], v[216:219], v[188:191], a[144:147]\n\t"
    "v_add_f32 v221, v221, v231\n\t"
    "v_add_f32 v222, v222, v232\n\t"
    "v_mfma_f32_16x16x32_bf16 a[148:151], v[216:219], v[192:195], a[148:151]\n\t"
    "v_add_f32 v223, v223, v233\n\t"
    "v_add_f32 v224, v224, v234\n\t"
    "v_mfma_f32_16x16x32_bf16 a[152:155], v[216:219], v[196:199], a[152:155]\n\t"
    "v_mfma_f32_16x16x32_bf16 a[156:159], v[216:219], v[200:203], a[156:159]\n\t"
    "s_waitcnt lgkmcnt(4)\n\t"
    "ds_read_b64_tr_b16 v[216:217], v241 offset:256\n\t"
    "v_mfma_f32_16x16x32_bf16 a[160:163], v[204:207], v[184:187], a[160:163]\n\t"
    "s_mov_b32 s46, s28\n\t"
    "ds_read_b64_tr_b16 v[218:219], v241 offset:6400\n\t"
    "v_mfma_f32_16x16x32_bf16 a[164:167], v[204:207], v[188:191], a[164:167]\n\t"
    "s_add_u32 s30, s28, 24576\n\t"
    "v_rcp_f32 v144, v220\n\t"
    "v_mfma_f32_16x16x32_bf16 a[168:171], v[204:207], v[192:195], a[168:171]\n\t"
    "s_cmp_eq_u32 s30, 73728\n\t"
    "v_rcp_f32 v146, v221\n\t"
    "v_mfma_f32_16x16x32_bf16 a[172:175], v[204:207], v[196:199], a[172:175]\n\t"
    "s_cselect_b32 s30, 0, s30\n\t"
    "v_rcp_f32 v148, v222\n\t"
    "v_mfma_f32_16x16x32_bf16 a[176:179], v[204:207], v[200:203], a[176:179]\n\t"
    "s_waitcnt lgkmcnt(4)\n\t"
    "v_rcp_f32 v150, v223\n\t"
    "v_mfma_f32_16x16x32_bf16 a[180:183], v[208:211], v[184:187], a[180:183]\n\t"
    "s_sub_u32 s31, s30, s28\n\t"
    "v_rcp_f32 v152, v224\n\t"
    "v_mfma_f32_16x16x32_bf16 a[184:187], v[208:211], v[188:191], a[184:187]\n\t"
    "s_waitcnt vmcnt(9) lgkmcnt(0)\n\t"
    "s_barrier\n\t"
    "v_mfma_f32_16x16x32_bf16 a[188:191], v[208:211], v[192:195], a[188:191]\n\t"
    "s_mov_b32 s29, s28\n\t"
    "v_mfma_f32_16x16x32_bf16 a[192:195], v[208:211], v[196:199], a[192:195]\n\t"
    "s_mov_b32 s28, s30\n\t"
    "v_mfma_f32_16x16x32_bf16 a[196:199], v[208:211], v[200:203], a[196:199]\n\t"
    "s_add_u32 s58, s29, s34\n\t"
    "v_mfma_f32_16x16x32_bf16 a[200:203], v[212:215], v[184:187], a[200:203]\n\t"
    "s_add_u32 s59, s29, s35\n\t"
    "v_mfma_f32_16x16x32_bf16 a[204:207], v[212:215], v[188:191], a[204:207]\n\t"
    "v_mfma_f32_16x16x32_bf16 a[208:211], v[212:215], v[192:195], a[208:211]\n\t"
    "v_mfma_f32_16x16x32_bf16 a[212:215], v[212:215], v[196:199], a[212:215]\n\t"
    "v_mfma_f32_16x16x32_bf16 a[216:219], v[212:215], v[200:203], a[216:219]\n\t"
    "v_mfma_f32_16x16x32_bf16 a[220:223], v[216:219], v[184:187], a[220:223]\n\t"
    "v_mfma_f32_16x16x32_bf16 a[224:227], v[216:219], v[188:191], a[224:227]\n\t"
    "v_mfma_f32_16x16x32_bf16 a[228:231], v[216:219], v[192:195], a[228:231]\n\t"
    "v_mfma_f32_16x16x32_bf16 a[232:235], v[216:219], v[196:199], a[232:235]\n\t"
    "v_mfma_f32_16x16x32_bf16 a[236:239], v[216:219], v[200:203], a[236:239]\n\t"
    "s_add_u32 m0, s58, 0\n\t"
    "s_nop 0\n\t"
    "buffer_load_dwordx4 v245, s[16:19], s38 offen lds\n\t"
    "buffer_load_dwordx4 v246, s[16:19], s38 offen offset:1024 lds\n\t"
    "buffer_load_dwordx4 v247, s[16:19], s38 offen offset:2048 lds\n\t"
    "s_add_u32 m0, s58, 3072\n\t"
    "s_add_u32 s40, s38, 98304\n\t"
    "buffer_load_dwordx4 v245, s[16:19], s40 offen lds\n\t"
    "buffer_load_dwordx4 v246, s[16:19], s40 offen offset:1024 lds\n\t"
    "buffer_load_dwordx4 v247, s[16:19], s40 offen offset:2048 lds\n\t"
    "s_add_u32 m0, s59, 0\n\t"
    "s_nop 0\n\t"
    "buffer_load_dwordx4 v248, s[20:23], s39 offen lds\n\t"
    "s_add_u32 m0, s59, 4096\n\t"
    "s_add_u32 s41, s39, 81920\n\t"
    "buffer_load_dwordx4 v248, s[20:23], s41 offen lds\n\t"
    "s_add_u32 m0, s59, 8192\n\t"
    "s_add_u32 s40, s39, 163840\n\t"
    "buffer_load_dwordx4 v248, s[20:23], s40 offen lds\n\t"
    "s_add_u32 s38, s38, 393216\n\t"
    "s_add_u32 s39, s39, 64\n\t"
    "s_mov_b32 s42, 0\n\t"
    "s_mov_b32 s43, 4096\n\t"
    "v_accvgpr_read_b32 v0, a0\n\t"
    "v_accvgpr_read_b32 v1, a1\n\t"
    "v_accvgpr_read_b32 v2, a2\n\t"
    "v_accvgpr_read_b32 v3, a3\n\t"
    "v_accvgpr_read_b32 v4, a20\n\t"
    "v_accvgpr_read_b32 v5, a21\n\t"
    "v_accvgpr_read_b32 v6, a22\n\t"
    "v_accvgpr_read_b32 v7, a23\n\t"
    "v_pk_mul_f32 v[0:1], v[0:1], v[144:145] op_sel_hi:[1,0]\n\t"
    "v_pk_mul_f32 v[2:3], v[2:3], v[144:145] op_sel_hi:[1,0]\n\t"
    "v_pk_mul_f32 v[4:5], v[4:5], v[144:145] op_sel_hi:[1,0]\n\t"
    "v_pk_mul_f32 v[6:7], v[6:7], v[144:145] op_sel_hi:[1,0]\n\t"
    "v_cvt_pk_bf16_f32 v0, v0, v1\n\t"
    "v_cvt_pk_bf16_f32 v1, v2, v3\n\t"
    "v_cvt_pk_bf16_f32 v2, v4, v5\n\t"
    "v_cvt_pk_bf16_f32 v3, v6, v7\n\t"
    "buffer_store_dwordx4 v[0:3], v252, s[24:27], s42 offen sc1\n\t"
    "v_accvgpr_read_b32 v8, a4\n\t"
    "v_accvgpr_read_b32 v9, a5\n\t"
    "v_accvgpr_read_b32 v10, a6\n\t"
    "v_accvgpr_read_b32 v11, a7\n\t"
    "v_accvgpr_read_b32 v12, a24\n\t"
    "v_accvgpr_read_b32 v13, a25\n\t"
    "v_accvgpr_read_b32 v14, a26\n\t"
    "v_accvgpr_read_b32 v15, a27\n\t"
    "v_pk_mul_f32 v[8:9], v[8:9], v[146:147] op_sel_hi:[1,0]\n\t"
    "v_pk_mul_f32 v[10:11], v[10:11], v[146:147] op_sel_hi:[1,0]\n\t"
    "v_pk_mul_f32 v[12:13], v[12:13], v[146:147] op_sel_hi:[1,0]\n\t"
    "v_pk_mul_f32 v[14:15], v[14:15], v[146:147] op_sel_hi:[1,0]\n\t"
    "v_cvt_pk_bf16_f32 v8, v8, v9\n\t"
    "v_cvt_pk_bf16_f32 v9, v10, v11\n\t"
    "v_cvt_pk_bf16_f32 v10, v12, v13\n\t"
    "v_cvt_pk_bf16_f32 v11, v14, v15\n\t"
    "buffer_store_dwordx4 v[8:11], v252, s[24:27], s42 offen offset:1024 sc1\n\t"
    "v_accvgpr_read_b32 v16, a8\n\t"
    "v_accvgpr_read_b32 v17, a9\n\t"
    "v_accvgpr_read_b32 v18, a10\n\t"
    "v_accvgpr_read_b32 v19, a11\n\t"
    "v_accvgpr_read_b32 v20, a28\n\t"
    "v_accvgpr_read_b32 v21, a29\n\t"
    "v_accvgpr_read_b32 v22, a30\n\t"
    "v_accvgpr_read_b32 v23, a31\n\t"
    "v_pk_mul_f32 v[16:17], v[16:17], v[148:149] op_sel_hi:[1,0]\n\t"
    "v_pk_mul_f32 v[18:19], v[18:19], v[148:149] op_sel_hi:[1,0]\n\t"
    "v_pk_mul_f32 v[20:21], v[20:21], v[148:149] op_sel_hi:[1,0]\n\t"
    "v_pk_mul_f32 v[22:23], v[22:23], v[148:149] op_sel_hi:[1,0]\n\t"
    "v_cvt_pk_bf16_f32 v16, v16, v17\n\t"
    "v_cvt_pk_bf16_f32 v17, v18, v19\n\t"
    "v_cvt_pk_bf16_f32 v18, v20, v21\n\t"
    "v_cvt_pk_bf16_f32 v19, v22, v23\n\t"
    "buffer_store_dwordx4 v[16:19], v252, s[24:27], s42 offen offset:2048 sc1\n\t"
    "v_accvgpr_read_b32 v24, a12\n\t"
    "v_accvgpr_read_b32 v25, a13\n\t"
    "v_accvgpr_read_b32 v26, a14\n\t"
    "v_accvgpr_read_b32 v27, a15\n\t"
    "v_accvgpr_read_b32 v28, a32\n\t"
    "v_accvgpr_read_b32 v29, a33\n\t"
    "v_accvgpr_read_b32 v30, a34\n\t"
    "v_accvgpr_read_b32 v31, a35\n\t"
    "v_pk_mul_f32 v[24:25], v[24:25], v[150:151] op_sel_hi:[1,0]\n\t"
    "v_pk_mul_f32 v[26:27], v[26:27], v[150:151] op_sel_hi:[1,0]\n\t"
    "v_pk_mul_f32 v[28:29], v[28:29], v[150:151] op_sel_hi:[1,0]\n\t"
    "v_pk_mul_f32 v[30:31], v[30:31], v[150:151] op_sel_hi:[1,0]\n\t"
    "v_cvt_pk_bf16_f32 v24, v24, v25\n\t"
    "v_cvt_pk_bf16_f32 v25, v26, v27\n\t"
    "v_cvt_pk_bf16_f32 v26, v28, v29\n\t"
    "v_cvt_pk_bf16_f32 v27, v30, v31\n\t"
    "buffer_store_dwordx4 v[24:27], v252, s[24:27], s42 offen offset:3072 sc1\n\t"
    "v_accvgpr_read_b32 v32, a16\n\t"
    "v_accvgpr_read_b32 v33, a17\n\t"
    "v_accvgpr_read_b32 v34, a18\n\t"
    "v_accvgpr_read_b32 v35, a19\n\t"
    "v_accvgpr_read_b32 v36, a36\n\t"
    "v_accvgpr_read_b32 v37, a37\n\t"
    "v_accvgpr_read_b32 v38, a38\n\t"
    "v_accvgpr_read_b32 v39, a39\n\t"
    "v_pk_mul_f32 v[32:33], v[32:33], v[152:153] op_sel_hi:[1,0]\n\t"
    "v_pk_mul_f32 v[34:35], v[34:35], v[152:153] op_sel_hi:[1,0]\n\t"
    "v_pk_mul_f32 v[36:37], v[36:37], v[152:153] op_sel_hi:[1,0]\n\t"
    "v_pk_mul_f32 v[38:39], v[38:39], v[152:153] op_sel_hi:[1,0]\n\t"
    "v_cvt_pk_bf16_f32 v32, v32, v33\n\t"
    "v_cvt_pk_bf16_f32 v33, v34, v35\n\t"
    "v_cvt_pk_bf16_f32 v34, v36, v37\n\t"
    "v_cvt_pk_bf16_f32 v35, v38, v39\n\t"
    "buffer_store_dwordx4 v[32:35], v252, s[24:27], s43 offen sc1\n\t"
    "s_mov_b32 s42, 10240\n\t"
    "s_mov_b32 s43, 14336\n\t"
    "v_accvgpr_read_b32 v40, a40\n\t"
    "v_accvgpr_read_b32 v41, a41\n\t"
    "v_accvgpr_read_b32 v42, a42\n\t"
    "v_accvgpr_read_b32 v43, a43\n\t"
    "v_accvgpr_read_b32 v44, a60\n\t"
    "v_accvgpr_read_b32 v45, a61\n\t"
    "v_accvgpr_read_b32 v46, a62\n\t"
    "v_accvgpr_read_b32 v47, a63\n\t"
    "v_pk_mul_f32 v[40:41], v[40:41], v[144:145] op_sel_hi:[1,0]\n\t"
    "v_pk_mul_f32 v[42:43], v[42:43], v[144:145] op_sel_hi:[1,0]\n\t"
    "v_pk_mul_f32 v[44:45], v[44:45], v[144:145] op_sel_hi:[1,0]\n\t"
    "v_pk_mul_f32 v[46:47], v[46:47], v[144:145] op_sel_hi:[1,0]\n\t"
    "v_cvt_pk_bf16_f32 v40, v40, v41\n\t"
    "v_cvt_pk_bf16_f32 v41, v42, v43\n\t"
    "v_cvt_pk_bf16_f32 v42, v44, v45\n\t"
    "v_cvt_pk_bf16_f32 v43, v46, v47\n\t"
    "buffer_store_dwordx4 v[40:43], v252, s[24:27], s42 offen sc1\n\t"
    "v_accvgpr_read_b32 v48, a44\n\t"
    "v_accvgpr_read_b32 v49, a45\n\t"
    "v_accvgpr_read_b32 v50, a46\n\t"
    "v_accvgpr_read_b32 v51, a47\n\t"
    "v_accvgpr_read_b32 v52, a64\n\t"
    "v_accvgpr_read_b32 v53, a65\n\t"
    "v_accvgpr_read_b32 v54, a66\n\t"
    "v_accvgpr_read_b32 v55, a67\n\t"
    "v_pk_mul_f32 v[48:49], v[48:49], v[146:147] op_sel_hi:[1,0]\n\t"
    "v_pk_mul_f32 v[50:51], v[50:51], v[146:147] op_sel_hi:[1,0]\n\t"
    "v_pk_mul_f32 v[52:53], v[52:53], v[146:147] op_sel_hi:[1,0]\n\t"
    "v_pk_mul_f32 v[54:55], v[54:55], v[146:147] op_sel_hi:[1,0]\n\t"
    "v_cvt_pk_bf16_f32 v48, v48, v49\n\t"
    "v_cvt_pk_bf16_f32 v49, v50, v51\n\t"
    "v_cvt_pk_bf16_f32 v50, v52, v53\n\t"
    "v_cvt_pk_bf16_f32 v51, v54, v55\n\t"
    "buffer_store_dwordx4 v[48:51], v252, s[24:27], s42 offen offset:1024 sc1\n\t"
    "v_accvgpr_read_b32 v56, a48\n\t"
    "v_accvgpr_read_b32 v57, a49\n\t"
    "v_accvgpr_read_b32 v58, a50\n\t"
    "v_accvgpr_read_b32 v59, a51\n\t"
    "v_accvgpr_read_b32 v60, a68\n\t"
    "v_accvgpr_read_b32 v61, a69\n\t"
    "v_accvgpr_read_b32 v62, a70\n\t"
    "v_accvgpr_read_b32 v63, a71\n\t"
    "v_pk_mul_f32 v[56:57], v[56:57], v[148:149] op_sel_hi:[1,0]\n\t"
    "v_pk_mul_f32 v[58:59], v[58:59], v[148:149] op_sel_hi:[1,0]\n\t"
    "v_pk_mul_f32 v[60:61], v[60:61], v[148:149] op_sel_hi:[1,0]\n\t"
    "v_pk_mul_f32 v[62:63], v[62:63], v[148:149] op_sel_hi:[1,0]\n\t"
    "v_cvt_pk_bf16_f32 v56, v56, v57\n\t"
    "v_cvt_pk_bf16_f32 v57, v58, v59\n\t"
    "v_cvt_pk_bf16_f32 v58, v60, v61\n\t"
    "v_cvt_pk_bf16_f32 v59, v62, v63\n\t"
    "buffer_store_dwordx4 v[56:59], v252, s[24:27], s42 offen offset:2048 sc1\n\t"
    "v_accvgpr_read_b32 v64, a52\n\t"
    "v_accvgpr_read_b32 v65, a53\n\t"
    "v_accvgpr_read_b32 v66, a54\n\t"
    "v_accvgpr_read_b32 v67, a55\n\t"
    "v_accvgpr_read_b32 v68, a72\n\t"
    "v_accvgpr_read_b32 v69, a73\n\t"
    "v_accvgpr_read_b32 v70, a74\n\t"
    "v_accvgpr_read_b32 v71, a75\n\t"
    "v_pk_mul_f32 v[64:65], v[64:65], v[150:151] op_sel_hi:[1,0]\n\t"
    "v_pk_mul_f32 v[66:67], v[66:67], v[150:151] op_sel_hi:[1,0]\n\t"
    "v_pk_mul_f32 v[68:69], v[68:69], v[150:151] op_sel_hi:[1,0]\n\t"
    "v_pk_mul_f32 v[70:71], v[70:71], v[150:151] op_sel_hi:[1,0]\n\t"
    "v_cvt_pk_bf16_f32 v64, v64, v65\n\t"
    "v_cvt_pk_bf16_f32 v65, v66, v67\n\t"
    "v_cvt_pk_bf16_f32 v66, v68, v69\n\t"
    "v_cvt_pk_bf16_f32 v67, v70, v71\n\t"
    "buffer_store_dwordx4 v[64:67], v252, s[24:27], s42 offen offset:3072 sc1\n\t"
    "v_accvgpr_read_b32 v72, a56\n\t"
    "v_accvgpr_read_b32 v73, a57\n\t"
    "v_accvgpr_read_b32 v74, a58\n\t"
    "v_accvgpr_read_b32 v75, a59\n\t"
    "v_accvgpr_read_b32 v76, a76\n\t"
    "v_accvgpr_read_b32 v77, a77\n\t"
    "v_accvgpr_read_b32 v78, a78\n\t"
    "v_accvgpr_read_b32 v79, a79\n\t"
    "v_pk_mul_f32 v[72:73], v[72:73], v[152:153] op_sel_hi:[1,0]\n\t"
    "v_pk_mul_f32 v[74:75], v[74:75], v[152:153] op_sel_hi:[1,0]\n\t"
    "v_pk_mul_f32 v[76:77], v[76:77], v[152:153] op_sel_hi:[1,0]\n\t"
    "v_pk_mul_f32 v[78:79], v[78:79], v[152:153] op_sel_hi:[1,0]\n\t"
    "v_cvt_pk_bf16_f32 v72, v72, v73\n\t"
    "v_cvt_pk_bf16_f32 v73, v74, v75\n\t"
    "v_cvt_pk_bf16_f32 v74, v76, v77\n\t"
    "v_cvt_pk_bf16_f32 v75, v78, v79\n\t"
    "buffer_store_dwordx4 v[72:75], v252, s[24:27], s43 offen sc1\n\t"
    "s_mov_b32 s42, 20480\n\t"
    "s_mov_b32 s43, 24576\n\t"
    "v_accvgpr_read_b32 v80, a80\n\t"
    "v_accvgpr_read_b32 v81, a81\n\t"
    "v_accvgpr_read_b32 v82, a82\n\t"
    "v_accvgpr_read_b32 v83, a83\n\t"
    "v_accvgpr_read_b32 v84, a100\n\t"
    "v_accvgpr_read_b32 v85, a101\n\t"
    "v_accvgpr_read_b32 v86, a102\n\t"
    "v_accvgpr_read_b32 v87, a103\n\t"
    "v_pk_mul_f32 v[80:81], v[80:81], v[144:145] op_sel_hi:[1,0]\n\t"
    "v_pk_mul_f32 v[82:83], v[82:83], v[144:145] op_sel_hi:[1,0]\n\t"
    "v_pk_mul_f32 v[84:85], v[84:85], v[144:145] op_sel_hi:[1,0]\n\t"
    "v_pk_mul_f32 v[86:87], v[86:87], v[144:145] op_sel_hi:[1,0]\n\t"
    "v_cvt_pk_bf16_f32 v80, v80, v81\n\t"
    "v_cvt_pk_bf16_f32 v81, v82, v83\n\t"
    "v_cvt_pk_bf16_f32 v82, v84, v85\n\t"
    "v_cvt_pk_bf16_f32 v83, v86, v87\n\t"
    "buffer_store_dwordx4 v[80:83], v252, s[24:27], s42 offen sc1\n\t"
    "v_accvgpr_read_b32 v88, a84\n\t"
    "v_accvgpr_read_b32 v89, a85\n\t"
    "v_accvgpr_read_b32 v90, a86\n\t"
    "v_accvgpr_read_b32 v91, a87\n\t"
    "v_accvgpr_read_b32 v92, a104\n\t"
    "v_accvgpr_read_b32 v93, a105\n\t"
    "v_accvgpr_read_b32 v94, a106\n\t"
    "v_accvgpr_read_b32 v95, a107\n\t"
    "v_pk_mul_f32 v[88:89], v[88:89], v[146:147] op_sel_hi:[1,0]\n\t"
    "v_pk_mul_f32 v[90:91], v[90:91], v[146:147] op_sel_hi:[1,0]\n\t"
    "v_pk_mul_f32 v[92:93], v[92:93], v[146:147] op_sel_hi:[1,0]\n\t"
    "v_pk_mul_f32 v[94:95], v[94:95], v[146:147] op_sel_hi:[1,0]\n\t"
    "v_cvt_pk_bf16_f32 v88, v88, v89\n\t"
    "v_cvt_pk_bf16_f32 v89, v90, v91\n\t"
    "v_cvt_pk_bf16_f32 v90, v92, v93\n\t"
    "v_cvt_pk_bf16_f32 v91, v94, v95\n\t"
    "buffer_store_dwordx4 v[88:91], v252, s[24:27], s42 offen offset:1024 sc1\n\t"
    "v_accvgpr_read_b32 v0, a88\n\t"
    "v_accvgpr_read_b32 v1, a89\n\t"
    "v_accvgpr_read_b32 v2, a90\n\t"
    "v_accvgpr_read_b32 v3, a91\n\t"
    "v_accvgpr_read_b32 v4, a108\n\t"
    "v_accvgpr_read_b32 v5, a109\n\t"
    "v_accvgpr_read_b32 v6, a110\n\t"
    "v_accvgpr_read_b32 v7, a111\n\t"
    "v_pk_mul_f32 v[0:1], v[0:1], v[148:149] op_sel_hi:[1,0]\n\t"
    "v_pk_mul_f32 v[2:3], v[2:3], v[148:149] op_sel_hi:[1,0]\n\t"
    "v_pk_mul_f32 v[4:5], v[4:5], v[148:149] op_sel_hi:[1,0]\n\t"
    "v_pk_mul_f32 v[6:7], v[6:7], v[148:149] op_sel_hi:[1,0]\n\t"
    "v_cvt_pk_bf16_f32 v0, v0, v1\n\t"
    "v_cvt_pk_bf16_f32 v1, v2, v3\n\t"
    "v_cvt_pk_bf16_f32 v2, v4, v5\n\t"
    "v_cvt_pk_bf16_f32 v3, v6, v7\n\t"
    "buffer_store_dwordx4 v[0:3], v252, s[24:27], s42 offen offset:2048 sc1\n\t"
    "v_accvgpr_read_b32 v8, a92\n\t"
    "v_accvgpr_read_b32 v9, a93\n\t"
    "v_accvgpr_read_b32 v10, a94\n\t"
    "v_accvgpr_read_b32 v11, a95\n\t"
    "v_accvgpr_read_b32 v12, a112\n\t"
    "v_accvgpr_read_b32 v13, a113\n\t"
    "v_accvgpr_read_b32 v14, a114\n\t"
    "v_accvgpr_read_b32 v15, a115\n\t"
    "v_pk_mul_f32 v[8:9], v[8:9], v[150:151] op_sel_hi:[1,0]\n\t"
    "v_pk_mul_f32 v[10:11], v[10:11], v[150:151] op_sel_hi:[1,0]\n\t"
    "v_pk_mul_f32 v[12:13], v[12:13], v[150:151] op_sel_hi:[1,0]\n\t"
    "v_pk_mul_f32 v[14:15], v[14:15], v[150:151] op_sel_hi:[1,0]\n\t"
    "v_cvt_pk_bf16_f32 v8, v8, v9\n\t"
    "v_cvt_pk_bf16_f32 v9, v10, v11\n\t"
    "v_cvt_pk_bf16_f32 v10, v12, v13\n\t"
    "v_cvt_pk_bf16_f32 v11, v14, v15\n\t"
    "buffer_store_dwordx4 v[8:11], v252, s[24:27], s42 offen offset:3072 sc1\n\t"
    "v_accvgpr_read_b32 v16, a96\n\t"
    "v_accvgpr_read_b32 v17, a97\n\t"
    "v_accvgpr_read_b32 v18, a98\n\t"
    "v_accvgpr_read_b32 v19, a99\n\t"
    "v_accvgpr_read_b32 v20, a116\n\t"
    "v_accvgpr_read_b32 v21, a117\n\t"
    "v_accvgpr_read_b32 v22, a118\n\t"
    "v_accvgpr_read_b32 v23, a119\n\t"
    "v_pk_mul_f32 v[16:17], v[16:17], v[152:153] op_sel_hi:[1,0]\n\t"
    "v_pk_mul_f32 v[18:19], v[18:19], v[152:153] op_sel_hi:[1,0]\n\t"
    "v_pk_mul_f32 v[20:21], v[20:21], v[152:153] op_sel_hi:[1,0]\n\t"
    "v_pk_mul_f32 v[22:23], v[22:23], v[152:153] op_sel_hi:[1,0]\n\t"
    "v_cvt_pk_bf16_f32 v16, v16, v17\n\t"
    "v_cvt_pk_bf16_f32 v17, v18, v19\n\t"
    "v_cvt_pk_bf16_f32 v18, v20, v21\n\t"
    "v_cvt_pk_bf16_f32 v19, v22, v23\n\t"
    "buffer_store_dwordx4 v[16:19], v252, s[24:27], s43 offen sc1\n\t"
    "s_mov_b32 s42, 30720\n\t"
    "s_mov_b32 s43, 34816\n\t"
    "v_accvgpr_read_b32 v24, a120\n\t"
    "v_accvgpr_read_b32 v25, a121\n\t"
    "v_accvgpr_read_b32 v26, a122\n\t"
    "v_accvgpr_read_b32 v27, a123\n\t"
    "v_accvgpr_read_b32 v28, a140\n\t"
    "v_accvgpr_read_b32 v29, a141\n\t"
    "v_accvgpr_read_b32 v30, a142\n\t"
    "v_accvgpr_read_b32 v31, a143\n\t"
    "v_pk_mul_f32 v[24:25], v[24:25], v[144:145] op_sel_hi:[1,0]\n\t"
    "v_pk_mul_f32 v[26:27], v[26:27], v[144:145] op_sel_hi:[1,0]\n\t"
    "v_pk_mul_f32 v[28:29], v[28:29], v[144:145] op_sel_hi:[1,0]\n\t"
    "v_pk_mul_f32 v[30:31], v[30:31], v[144:145] op_sel_hi:[1,0]\n\t"
    "v_cvt_pk_bf16_f32 v24, v24, v25\n\t"
    "v_cvt_pk_bf16_f32 v25, v26, v27\n\t"
    "v_cvt_pk_bf16_f32 v26, v28, v29\n\t"
    "v_cvt_pk_bf16_f32 v27, v30, v31\n\t"
    "buffer_store_dwordx4 v[24:27], v252, s[24:27], s42 offen sc1\n\t"
    "v_accvgpr_read_b32 v32, a124\n\t"
    "v_accvgpr_read_b32 v33, a125\n\t"
    "v_accvgpr_read_b32 v34, a126\n\t"
    "v_accvgpr_read_b32 v35, a127\n\t"
    "v_accvgpr_read_b32 v36, a144\n\t"
    "v_accvgpr_read_b32 v37, a145\n\t"
    "v_accvgpr_read_b32 v38, a146\n\t"
    "v_accvgpr_read_b32 v39, a147\n\t"
    "v_pk_mul_f32 v[32:33], v[32:33], v[146:147] op_sel_hi:[1,0]\n\t"
    "v_pk_mul_f32 v[34:35], v[34:35], v[146:147] op_sel_hi:[1,0]\n\t"
    "v_pk_mul_f32 v[36:37], v[36:37], v[146:147] op_sel_hi:[1,0]\n\t"
    "v_pk_mul_f32 v[38:39], v[38:39], v[146:147] op_sel_hi:[1,0]\n\t"
    "v_cvt_pk_bf16_f32 v32, v32, v33\n\t"
    "v_cvt_pk_bf16_f32 v33, v34, v35\n\t"
    "v_cvt_pk_bf16_f32 v34, v36, v37\n\t"
    "v_cvt_pk_bf16_f32 v35, v38, v39\n\t"
    "buffer_store_dwordx4 v[32:35], v252, s[24:27], s42 offen offset:1024 sc1\n\t"
    "v_accvgpr_read_b32 v40, a128\n\t"
    "v_accvgpr_read_b32 v41, a129\n\t"
    "v_accvgpr_read_b32 v42, a130\n\t"
    "v_accvgpr_read_b32 v43, a131\n\t"
    "v_accvgpr_read_b32 v44, a148\n\t"
    "v_accvgpr_read_b32 v45, a149\n\t"
    "v_accvgpr_read_b32 v46, a150\n\t"
    "v_accvgpr_read_b32 v47, a151\n\t"
    "v_pk_mul_f32 v[40:41], v[40:41], v[148:149] op_sel_hi:[1,0]\n\t"
    "v_pk_mul_f32 v[42:43], v[42:43], v[148:149] op_sel_hi:[1,0]\n\t"
    "v_pk_mul_f32 v[44:45], v[44:45], v[148:149] op_sel_hi:[1,0]\n\t"
    "v_pk_mul_f32 v[46:47], v[46:47], v[148:149] op_sel_hi:[1,0]\n\t"
    "v_cvt_pk_bf16_f32 v40, v40, v41\n\t"
    "v_cvt_pk_bf16_f32 v41, v42, v43\n\t"
    "v_cvt_pk_bf16_f32 v42, v44, v45\n\t"
    "v_cvt_pk_bf16_f32 v43, v46, v47\n\t"
    "buffer_store_dwordx4 v[40:43], v252, s[24:27], s42 offen offset:2048 sc1\n\t"
    "v_accvgpr_read_b32 v48, a132\n\t"
    "v_accvgpr_read_b32 v49, a133\n\t"
    "v_accvgpr_read_b32 v50, a134\n\t"
    "v_accvgpr_read_b32 v51, a135\n\t"
    "v_accvgpr_read_b32 v52, a152\n\t"
    "v_accvgpr_read_b32 v53, a153\n\t"
    "v_accvgpr_read_b32 v54, a154\n\t"
    "v_accvgpr_read_b32 v55, a155\n\t"
    "v_pk_mul_f32 v[48:49], v[48:49], v[150:151] op_sel_hi:[1,0]\n\t"
    "v_pk_mul_f32 v[50:51], v[50:51], v[150:151] op_sel_hi:[1,0]\n\t"
    "v_pk_mul_f32 v[52:53], v[52:53], v[150:151] op_sel_hi:[1,0]\n\t"
    "v_pk_mul_f32 v[54:55], v[54:55], v[150:151] op_sel_hi:[1,0]\n\t"
    "v_cvt_pk_bf16_f32 v48, v48, v49\n\t"
    "v_cvt_pk_bf16_f32 v49, v50, v51\n\t"
    "v_cvt_pk_bf16_f32 v50, v52, v53\n\t"
    "v_cvt_pk_bf16_f32 v51, v54, v55\n\t"
    "buffer_store_dwordx4 v[48:51], v252, s[24:27], s42 offen offset:3072 sc1\n\t"
    "v_accvgpr_read_b32 v56, a136\n\t"
    "v_accvgpr_read_b32 v57, a137\n\t"
    "v_accvgpr_read_b32 v58, a138\n\t"
    "v_accvgpr_read_b32 v59, a139\n\t"
    "v_accvgpr_read_b32 v60, a156\n\t"
    "v_accvgpr_read_b32 v61, a157\n\t"
    "v_accvgpr_read_b32 v62, a158\n\t"
    "v_accvgpr_read_b32 v63, a159\n\t"
    "v_pk_mul_f32 v[56:57], v[56:57], v[152:153] op_sel_hi:[1,0]\n\t"
    "v_pk_mul_f32 v[58:59], v[58:59], v[152:153] op_sel_hi:[1,0]\n\t"
    "v_pk_mul_f32 v[60:61], v[60:61], v[152:153] op_sel_hi:[1,0]\n\t"
    "v_pk_mul_f32 v[62:63], v[62:63], v[152:153] op_sel_hi:[1,0]\n\t"
    "v_cvt_pk_bf16_f32 v56, v56, v57\n\t"
    "v_cvt_pk_bf16_f32 v57, v58, v59\n\t"
    "v_cvt_pk_bf16_f32 v58, v60, v61\n\t"
    "v_cvt_pk_bf16_f32 v59, v62, v63\n\t"
    "buffer_store_dwordx4 v[56:59], v252, s[24:27], s43 offen sc1\n\t"
    "s_mov_b32 s42, 40960\n\t"
    "s_mov_b32 s43, 45056\n\t"
    "v_accvgpr_read_b32 v64, a160\n\t"
    "v_accvgpr_read_b32 v65, a161\n\t"
    "v_accvgpr_read_b32 v66, a162\n\t"
    "v_accvgpr_read_b32 v67, a163\n\t"
    "v_accvgpr_read_b32 v68, a180\n\t"
    "v_accvgpr_read_b32 v69, a181\n\t"
    "v_accvgpr_read_b32 v70, a182\n\t"
    "v_accvgpr_read_b32 v71, a183\n\t"
    "v_pk_mul_f32 v[64:65], v[64:65], v[144:145] op_sel_hi:[1,0]\n\t"
    "v_pk_mul_f32 v[66:67], v[66:67], v[144:145] op_sel_hi:[1,0]\n\t"
    "v_pk_mul_f32 v[68:69], v[68:69], v[144:145] op_sel_hi:[1,0]\n\t"
    "v_pk_mul_f32 v[70:71], v[70:71], v[144:145] op_sel_hi:[1,0]\n\t"
    "v_cvt_pk_bf16_f32 v64, v64, v65\n\t"
    "v_cvt_pk_bf16_f32 v65, v66, v67\n\t"
    "v_cvt_pk_bf16_f32 v66, v68, v69\n\t"
    "v_cvt_pk_bf16_f32 v67, v70, v71\n\t"
    "buffer_store_dwordx4 v[64:67], v252, s[24:27], s42 offen sc1\n\t"
    "v_accvgpr_read_b32 v72, a164\n\t"
    "v_accvgpr_read_b32 v73, a165\n\t"
    "v_accvgpr_read_b32 v74, a166\n\t"
    "v_accvgpr_read_b32 v75, a167\n\t"
    "v_accvgpr_read_b32 v76, a184\n\t"
    "v_accvgpr_read_b32 v77, a185\n\t"
    "v_accvgpr_read_b32 v78, a186\n\t"
    "v_accvgpr_read_b32 v79, a187\n\t"
    "v_pk_mul_f32 v[72:73], v[72:73], v[146:147] op_sel_hi:[1,0]\n\t"
    "v_pk_mul_f32 v[74:75], v[74:75], v[146:147] op_sel_hi:[1,0]\n\t"
    "v_pk_mul_f32 v[76:77], v[76:77], v[146:147] op_sel_hi:[1,0]\n\t"
    "v_pk_mul_f32 v[78:79], v[78:79], v[146:147] op_sel_hi:[1,0]\n\t"
    "v_cvt_pk_bf16_f32 v72, v72, v73\n\t"
    "v_cvt_pk_bf16_f32 v73, v74, v75\n\t"
    "v_cvt_pk_bf16_f32 v74, v76, v77\n\t"
    "v_cvt_pk_bf16_f32 v75, v78, v79\n\t"
    "buffer_store_dwordx4 v[72:75], v252, s[24:27], s42 offen offset:1024 sc1\n\t"
    "v_accvgpr_read_b32 v80, a168\n\t"
    "v_accvgpr_read_b32 v81, a169\n\t"
    "v_accvgpr_read_b32 v82, a170\n\t"
    "v_accvgpr_read_b32 v83, a171\n\t"
    "v_accvgpr_read_b32 v84, a188\n\t"
    "v_accvgpr_read_b32 v85, a189\n\t"
    "v_accvgpr_read_b32 v86, a190\n\t"
    "v_accvgpr_read_b32 v87, a191\n\t"
    "v_pk_mul_f32 v[80:81], v[80:81], v[148:149] op_sel_hi:[1,0]\n\t"
    "v_pk_mul_f32 v[82:83], v[82:83], v[148:149] op_sel_hi:[1,0]\n\t"
    "v_pk_mul_f32 v[84:85], v[84:85], v[148:149] op_sel_hi:[1,0]\n\t"
    "v_pk_mul_f32 v[86:87], v[86:87], v[148:149] op_sel_hi:[1,0]\n\t"
    "v_cvt_pk_bf16_f32 v80, v80, v81\n\t"
    "v_cvt_pk_bf16_f32 v81, v82, v83\n\t"
    "v_cvt_pk_bf16_f32 v82, v84, v85\n\t"
    "v_cvt_pk_bf16_f32 v83, v86, v87\n\t"
    "buffer_store_dwordx4 v[80:83], v252, s[24:27], s42 offen offset:2048 sc1\n\t"
    "v_accvgpr_read_b32 v88, a172\n\t"
    "v_accvgpr_read_b32 v89, a173\n\t"
    "v_accvgpr_read_b32 v90, a174\n\t"
    "v_accvgpr_read_b32 v91, a175\n\t"
    "v_accvgpr_read_b32 v92, a192\n\t"
    "v_accvgpr_read_b32 v93, a193\n\t"
    "v_accvgpr_read_b32 v94, a194\n\t"
    "v_accvgpr_read_b32 v95, a195\n\t"
    "v_pk_mul_f32 v[88:89], v[88:89], v[150:151] op_sel_hi:[1,0]\n\t"
    "v_pk_mul_f32 v[90:91], v[90:91], v[150:151] op_sel_hi:[1,0]\n\t"
    "v_pk_mul_f32 v[92:93], v[92:93], v[150:151] op_sel_hi:[1,0]\n\t"
    "v_pk_mul_f32 v[94:95], v[94:95], v[150:151] op_sel_hi:[1,0]\n\t"
    "v_cvt_pk_bf16_f32 v88, v88, v89\n\t"
    "v_cvt_pk_bf16_f32 v89, v90, v91\n\t"
    "v_cvt_pk_bf16_f32 v90, v92, v93\n\t"
    "v_cvt_pk_bf16_f32 v91, v94, v95\n\t"
    "buffer_store_dwordx4 v[88:91], v252, s[24:27], s42 offen offset:3072 sc1\n\t"
    "v_accvgpr_read_b32 v0, a176\n\t"
    "v_accvgpr_read_b32 v1, a177\n\t"
    "v_accvgpr_read_b32 v2, a178\n\t"
    "v_accvgpr_read_b32 v3, a179\n\t"
    "v_accvgpr_read_b32 v4, a196\n\t"
    "v_accvgpr_read_b32 v5, a197\n\t"
    "v_accvgpr_read_b32 v6, a198\n\t"
    "v_accvgpr_read_b32 v7, a199\n\t"
    "v_pk_mul_f32 v[0:1], v[0:1], v[152:153] op_sel_hi:[1,0]\n\t"
    "v_pk_mul_f32 v[2:3], v[2:3], v[152:153] op_sel_hi:[1,0]\n\t"
    "v_pk_mul_f32 v[4:5], v[4:5], v[152:153] op_sel_hi:[1,0]\n\t"
    "v_pk_mul_f32 v[6:7], v[6:7], v[152:153] op_sel_hi:[1,0]\n\t"
    "v_cvt_pk_bf16_f32 v0, v0, v1\n\t"
    "v_cvt_pk_bf16_f32 v1, v2, v3\n\t"
    "v_cvt_pk_bf16_f32 v2, v4, v5\n\t"
    "v_cvt_pk_bf16_f32 v3, v6, v7\n\t"
    "buffer_store_dwordx4 v[0:3], v252, s[24:27], s43 offen sc1\n\t"
    "s_mov_b32 s42, 51200\n\t"
    "s_mov_b32 s43, 55296\n\t"
    "v_accvgpr_read_b32 v8, a200\n\t"
    "v_accvgpr_read_b32 v9, a201\n\t"
    "v_accvgpr_read_b32 v10, a202\n\t"
    "v_accvgpr_read_b32 v11, a203\n\t"
    "v_accvgpr_read_b32 v12, a220\n\t"
    "v_accvgpr_read_b32 v13, a221\n\t"
    "v_accvgpr_read_b32 v14, a222\n\t"
    "v_accvgpr_read_b32 v15, a223\n\t"
    "v_pk_mul_f32 v[8:9], v[8:9], v[144:145] op_sel_hi:[1,0]\n\t"
    "v_pk_mul_f32 v[10:11], v[10:11], v[144:145] op_sel_hi:[1,0]\n\t"
    "v_pk_mul_f32 v[12:13], v[12:13], v[144:145] op_sel_hi:[1,0]\n\t"
    "v_pk_mul_f32 v[14:15], v[14:15], v[144:145] op_sel_hi:[1,0]\n\t"
    "v_cvt_pk_bf16_f32 v8, v8, v9\n\t"
    "v_cvt_pk_bf16_f32 v9, v10, v11\n\t"
    "v_cvt_pk_bf16_f32 v10, v12, v13\n\t"
    "v_cvt_pk_bf16_f32 v11, v14, v15\n\t"
    "buffer_store_dwordx4 v[8:11], v252, s[24:27], s42 offen sc1\n\t"
    "v_accvgpr_read_b32 v16, a204\n\t"
    "v_accvgpr_read_b32 v17, a205\n\t"
    "v_accvgpr_read_b32 v18, a206\n\t"
    "v_accvgpr_read_b32 v19, a207\n\t"
    "v_accvgpr_read_b32 v20, a224\n\t"
    "v_accvgpr_read_b32 v21, a225\n\t"
    "v_accvgpr_read_b32 v22, a226\n\t"
    "v_accvgpr_read_b32 v23, a227\n\t"
    "v_pk_mul_f32 v[16:17], v[16:17], v[146:147] op_sel_hi:[1,0]\n\t"
    "v_pk_mul_f32 v[18:19], v[18:19], v[146:147] op_sel_hi:[1,0]\n\t"
    "v_pk_mul_f32 v[20:21], v[20:21], v[146:147] op_sel_hi:[1,0]\n\t"
    "v_pk_mul_f32 v[22:23], v[22:23], v[146:147] op_sel_hi:[1,0]\n\t"
    "v_cvt_pk_bf16_f32 v16, v16, v17\n\t"
    "v_cvt_pk_bf16_f32 v17, v18, v19\n\t"
    "v_cvt_pk_bf16_f32 v18, v20, v21\n\t"
    "v_cvt_pk_bf16_f32 v19, v22, v23\n\t"
    "buffer_store_dwordx4 v[16:19], v252, s[24:27], s42 offen offset:1024 sc1\n\t"
    "v_accvgpr_read_b32 v24, a208\n\t"
    "v_accvgpr_read_b32 v25, a209\n\t"
    "v_accvgpr_read_b32 v26, a210\n\t"
    "v_accvgpr_read_b32 v27, a211\n\t"
    "v_accvgpr_read_b32 v28, a228\n\t"
    "v_accvgpr_read_b32 v29, a229\n\t"
    "v_accvgpr_read_b32 v30, a230\n\t"
    "v_accvgpr_read_b32 v31, a231\n\t"
    "v_pk_mul_f32 v[24:25], v[24:25], v[148:149] op_sel_hi:[1,0]\n\t"
    "v_pk_mul_f32 v[26:27], v[26:27], v[148:149] op_sel_hi:[1,0]\n\t"
    "v_pk_mul_f32 v[28:29], v[28:29], v[148:149] op_sel_hi:[1,0]\n\t"
    "v_pk_mul_f32 v[30:31], v[30:31], v[148:149] op_sel_hi:[1,0]\n\t"
    "v_cvt_pk_bf16_f32 v24, v24, v25\n\t"
    "v_cvt_pk_bf16_f32 v25, v26, v27\n\t"
    "v_cvt_pk_bf16_f32 v26, v28, v29\n\t"
    "v_cvt_pk_bf16_f32 v27, v30, v31\n\t"
    "buffer_store_dwordx4 v[24:27], v252, s[24:27], s42 offen offset:2048 sc1\n\t"
    "v_accvgpr_read_b32 v32, a212\n\t"
    "v_accvgpr_read_b32 v33, a213\n\t"
    "v_accvgpr_read_b32 v34, a214\n\t"
    "v_accvgpr_read_b32 v35, a215\n\t"
    "v_accvgpr_read_b32 v36, a232\n\t"
    "v_accvgpr_read_b32 v37, a233\n\t"
    "v_accvgpr_read_b32 v38, a234\n\t"
    "v_accvgpr_read_b32 v39, a235\n\t"
    "v_pk_mul_f32 v[32:33], v[32:33], v[150:151] op_sel_hi:[1,0]\n\t"
    "v_pk_mul_f32 v[34:35], v[34:35], v[150:151] op_sel_hi:[1,0]\n\t"
    "v_pk_mul_f32 v[36:37], v[36:37], v[150:151] op_sel_hi:[1,0]\n\t"
    "v_pk_mul_f32 v[38:39], v[38:39], v[150:151] op_sel_hi:[1,0]\n\t"
    "v_cvt_pk_bf16_f32 v32, v32, v33\n\t"
    "v_cvt_pk_bf16_f32 v33, v34, v35\n\t"
    "v_cvt_pk_bf16_f32 v34, v36, v37\n\t"
    "v_cvt_pk_bf16_f32 v35, v38, v39\n\t"
    "buffer_store_dwordx4 v[32:35], v252, s[24:27], s42 offen offset:3072 sc1\n\t"
    "v_accvgpr_read_b32 v40, a216\n\t"
    "v_accvgpr_read_b32 v41, a217\n\t"
    "v_accvgpr_read_b32 v42, a218\n\t"
    "v_accvgpr_read_b32 v43, a219\n\t"
    "v_accvgpr_read_b32 v44, a236\n\t"
    "v_accvgpr_read_b32 v45, a237\n\t"
    "v_accvgpr_read_b32 v46, a238\n\t"
    "v_accvgpr_read_b32 v47, a239\n\t"
    "v_pk_mul_f32 v[40:41], v[40:41], v[152:153] op_sel_hi:[1,0]\n\t"
    "v_pk_mul_f32 v[42:43], v[42:43], v[152:153] op_sel_hi:[1,0]\n\t"
    "v_pk_mul_f32 v[44:45], v[44:45], v[152:153] op_sel_hi:[1,0]\n\t"
    "v_pk_mul_f32 v[46:47], v[46:47], v[152:153] op_sel_hi:[1,0]\n\t"
    "v_cvt_pk_bf16_f32 v40, v40, v41\n\t"
    "v_cvt_pk_bf16_f32 v41, v42, v43\n\t"
    "v_cvt_pk_bf16_f32 v42, v44, v45\n\t"
    "v_cvt_pk_bf16_f32 v43, v46, v47\n\t"
    "buffer_store_dwordx4 v[40:43], v252, s[24:27], s43 offen sc1\n\t"
    "s_lshl_b32 s42, s6, 4\n\t"
    "s_add_u32 s42, s42, s70\n\t"
    "s_lshl_b32 s42, s42, 5\n\t"
    "s_add_u32 s42, s42, s79\n\t"
    "s_mul_i32 s42, s42, 61440\n\t"
    "s_add_u32 s24, s68, s42\n\t"
    "s_addc_u32 s25, s69, 0\n\t"
    "v_mbcnt_lo_u32_b32 v1, -1, 0\n\t"
    "s_lshr_b32 s42, s78, 6\n\t"
    "s_lshl_b32 s42, s42, 2\n\t"
    "s_add_u32 s42, s42, 8\n\t"
    "v_add_u32 v0, s42, v254\n\t"
    "s_lshl_b32 s42, s78, 7\n\t"
    "s_xor_b32 s42, s42, 128\n\t"
    "s_add_u32 s42, s42, 24\n\t"
    "v_cmp_le_u32 vcc, 4, v1\n\t"
    "v_mov_b32 v1, s42\n\t"
    "s_nop 0\n\t"
    "v_cndmask_b32 v0, v0, v1, vcc\n\t"
    "global_load_dword v1, v0, s[60:61] sc1\n\t"
    "s_mov_b32 s45, 1\n\t"
    "s_branch .Lk1_q\n\t"
    ".Lk1_last:\n\t"
    "global_load_dword v247, v254, s[60:61] sc1\n\t"
    "v_mfma_f32_16x16x32_bf16 v[104:107], v[204:207], v[0:3], v[144:147]\n\t"
    "v_add_u32 v238, s31, v238\n\t"
    "v_add_u32 v239, s31, v239\n\t"
    "v_mfma_f32_16x16x32_bf16 v[108:111], v[204:207], v[24:27], v[148:151]\n\t"
    "v_add_u32 v240, s31, v240\n\t"
    "v_add_u32 v241, s31, v241\n\t"
    "v_mfma_f32_16x16x32_bf16 v[112:115], v[204:207], v[48:51], v[152:155]\n\t"
    "v_add_f32 v220, v225, v220\n\t"
    "v_add_f32 v221, v226, v221\n\t"
    "v_mfma_f32_16x16x32_bf16 v[116:119], v[204:207], v[72:75], v[156:159]\n\t"
    "v_add_f32 v222, v227, v222\n\t"
    "v_add_f32 v223, v228, v223\n\t"
    "v_mfma_f32_16x16x32_bf16 v[120:123], v[204:207], v[96:99], v[160:163]\n\t"
    "s_waitcnt lgkmcnt(2)\n\t"
    "ds_read_b128 v[204:207], v236 offset:256\n\t"
    "v_add_f32 v224, v229, v224\n\t"
    "v_mfma_f32_16x16x32_bf16 v[104:107], v[208:211], v[4:7], v[104:107]\n\t"
    "v_mfma_f32_16x16x32_bf16 v[108:111], v[208:211], v[28:31], v[108:111]\n\t"
    "v_mfma_f32_16x16x32_bf16 v[112:115], v[208:211], v[52:55], v[112:115]\n\t"
    "v_mfma_f32_16x16x32_bf16 v[116:119], v[208:211], v[76:79], v[116:119]\n\t"
    "v_mfma_f32_16x16x32_bf16 v[120:123], v[208:211], v[100:103], v[120:123]\n\t"
    "s_waitcnt lgkmcnt(2)\n\t"
    "ds_read_b128 v[208:211], v237 offset:256\n\t"
    "v_mfma_f32_16x16x32_bf16 v[104:107], v[212:215], v[8:11], v[104:107]\n\t"
    "v_mfma_f32_16x16x32_bf16 v[108:111], v[212:215], v[32:35], v[108:111]\n\t"
    "v_mfma_f32_16x16x32_bf16 v[112:115], v[212:215], v[56:59], v[112:115]\n\t"
    "v_mfma_f32_16x16x32_bf16 v[116:119], v[212:215], v[80:83], v[116:119]\n\t"
    "v_mfma_f32_16x16x32_bf16 v[120:123], v[212:215], a[240:243], v[120:123]\n\t"
    "s_waitcnt lgkmcnt(2)\n\t"
    "ds_read_b128 v[212:215], v236 offset:6144\n\t"
    "v_mfma_f32_16x16x32_bf16 v[104:107], v[216:219], v[12:15], v[104:107]\n\t"
    "v_mfma_f32_16x16x32_bf16 v[108:111], v[216:219], v[36:39], v[108:111]\n\t"
    "v_mfma_f32_16x16x32_bf16 v[112:115], v[216:219], v[60:63], v[112:115]\n\t"
    "v_mfma_f32_16x16x32_bf16 v[116:119], v[216:219], v[84:87], v[116:119]\n\t"
    "v_mfma_f32_16x16x32_bf16 v[120:123], v[216:219], a[244:247], v[120:123]\n\t"
    "s_waitcnt lgkmcnt(2)\n\t"
    "ds_read_b128 v[216:219], v237 offset:6144\n\t"
    "v_mfma_f32_16x16x32_bf16 v[104:107], v[204:207], v[16:19], v[104:107]\n\t"
    "v_mfma_f32_16x16x32_bf16 v[108:111], v[204:207], v[40:43], v[108:111]\n\t"
    "v_mfma_f32_16x16x32_bf16 v[112:115], v[204:207], v[64:67], v[112:115]\n\t"
    "v_mfma_f32_16x16x32_bf16 v[116:119], v[204:207], v[88:91], v[116:119]\n\t"
    "v_mfma_f32_16x16x32_bf16 v[120:123], v[204:207], a[248:251], v[120:123]\n\t"
    "s_waitcnt lgkmcnt(2)\n\t"
    "ds_read_b128 v[204:207], v236 offset:6272\n\t"
    "v_mfma_f32_16x16x32_bf16 v[104:107], v[208:211], v[20:23], v[104:107]\n\t"
    "v_mfma_f32_16x16x32_bf16 v[108:111], v[208:211], v[44:47], v[108:111]\n\t"
    "v_mfma_f32_16x16x32_bf16 v[112:115], v[208:211], v[68:71], v[112:115]\n\t"
    "v_mfma_f32_16x16x32_bf16 v[116:119], v[208:211], v[92:95], v[116:119]\n\t"
    "v_mfma_f32_16x16x32_bf16 v[120:123], v[208:211], a[252:255], v[120:123]\n\t"
    "s_waitcnt lgkmcnt(2)\n\t"
    "ds_read_b128 v[208:211], v237 offset:6272\n\t"
    "v_mfma_f32_16x16x32_bf16 v[124:127], v[212:215], v[0:3], v[144:147]\n\t"
    "v_mfma_f32_16x16x32_bf16 v[128:131], v[212:215], v[24:27], v[148:151]\n\t"
    "s_add_u32 s30, s28, 24576\n\t"
    "v_exp_f32 v230, v104\n\t"
    "v_mfma_f32_16x16x32_bf16 v[132:135], v[212:215], v[48:51], v[152:155]\n\t"
    "s_cmp_eq_u32 s30, 73728\n\t"
    "v_exp_f32 v231, v105\n\t"
    "v_mfma_f32_16x16x32_bf16 v[136:139], v[212:215], v[72:75], v[156:159]\n\t"
    "s_cselect_b32 s30, 0, s30\n\t"
    "v_exp_f32 v232, v106\n\t"
    "v_mfma_f32_16x16x32_bf16 v[140:143], v[212:215], v[96:99], v[160:163]\n\t"
    "s_waitcnt lgkmcnt(2)\n\t"
    "ds_read_b128 v[212:215], v236 offset:6400\n\t"
    "v_add_f32 v225, v230, v231\n\t"
    "v_mfma_f32_16x16x32_bf16 v[124:127], v[216:219], v[4:7], v[124:127]\n\t"
    "s_sub_u32 s31, s30, s28\n\t"
    "v_exp_f32 v233, v107\n\t"
    "v_mfma_f32_16x16x32_bf16 v[128:131], v[216:219], v[28:31], v[128:131]\n\t"
    "v_exp_f32 v234, v108\n\t"
    "v_mfma_f32_16x16x32_bf16 v[132:135], v[216:219], v[52:55], v[132:135]\n\t"
    "v_cvt_pk_bf16_f32 v184, v230, v231\n\t"
    "v_add_f32 v225, v232, v225\n\t"
    "v_mfma_f32_16x16x32_bf16 v[136:139], v[216:219], v[76:79], v[136:139]\n\t"
    "v_exp_f32 v235, v109\n\t"
    "v_mfma_f32_16x16x32_bf16 v[140:143], v[216:219], v[100:103], v[140:143]\n\t"
    "s_waitcnt lgkmcnt(2)\n\t"
    "ds_read_b128 v[216:219], v237 offset:6400\n\t"
    "v_add_f32 v225, v233, v225\n\t"
    "v_mfma_f32_16x16x32_bf16 v[124:127], v[204:207], v[8:11], v[124:127]\n\t"
    "v_add_u32 v236, s31, v236\n\t"
    "v_add_u32 v237, s31, v237\n\t"
    "v_mfma_f32_16x16x32_bf16 v[128:131], v[204:207], v[32:35], v[128:131]\n\t"
    "v_exp_f32 v230, v110\n\t"
    "v_mfma_f32_16x16x32_bf16 v[132:135], v[204:207], v[56:59], v[132:135]\n\t"
    "v_cvt_pk_bf16_f32 v185, v232, v233\n\t"
    "v_add_f32 v226, v234, v235\n\t"
    "v_mfma_f32_16x16x32_bf16 v[136:139], v[204:207], v[80:83], v[136:139]\n\t"
    "v_exp_f32 v231, v111\n\t"
    "v_mfma_f32_16x16x32_bf16 v[140:143], v[204:207], a[240:243], v[140:143]\n\t"
    "s_waitcnt vmcnt(7) lgkmcnt(0)\n\t"
    "s_barrier\n\t"
    "ds_read_b64_tr_b16 v[204:205], v238 offset:0\n\t"
    "v_cvt_pk_bf16_f32 v188, v234, v235\n\t"
    "v_mfma_f32_16x16x32_bf16 v[124:127], v[208:211], v[12:15], v[124:127]\n\t"
    "ds_read_b64_tr_b16 v[206:207], v238 offset:6144\n\t"
    "v_add_f32 v226, v230, v226\n\t"
    "v_mfma_f32_16x16x32_bf16 v[128:131], v[208:211], v[36:39], v[128:131]\n\t"
    "v_exp_f32 v232, v112\n\t"
    "v_mfma_f32_16x16x32_bf16 v[132:135], v[208:211], v[60:63], v[132:135]\n\t"
    "v_exp_f32 v233, v113\n\t"
    "v_mfma_f32_16x16x32_bf16 v[136:139], v[208:211], v[84:87], v[136:139]\n\t"
    "v_exp_f32 v234, v114\n\t"
    "v_mfma_f32_16x16x32_bf16 v[140:143], v[208:211], a[244:247], v[140:143]\n\t"
    "ds_read_b64_tr_b16 v[208:209], v239 offset:0\n\t"
    "v_add_f32 v226, v231, v226\n\t"
    "v_mfma_f32_16x16x32_bf16 v[124:127], v[212:215], v[16:19], v[124:127]\n\t"
    "ds_read_b64_tr_b16 v[210:211], v239 offset:6144\n\t"
    "v_cvt_pk_bf16_f32 v189, v230, v231\n\t"
    "v_mfma_f32_16x16x32_bf16 v[128:131], v[212:215], v[40:43], v[128:131]\n\t"
    "v_exp_f32 v235, v115\n\t"
    "v_mfma_f32_16x16x32_bf16 v[132:135], v[212:215], v[64:67], v[132:135]\n\t"
    "v_exp_f32 v230, v116\n\t"
    "v_mfma_f32_16x16x32_bf16 v[136:139], v[212:215], v[88:91], v[136:139]\n\t"
    "v_add_f32 v227, v232, v233\n\t"
    "v_cvt_pk_bf16_f32 v192, v232, v233\n\t"
    "v_mfma_f32_16x16x32_bf16 v[140:143], v[212:215], a[248:251], v[140:143]\n\t"
    "ds_read_b64_tr_b16 v[212:213], v240 offset:0\n\t"
    "v_add_f32 v227, v234, v227\n\t"
    "v_mfma_f32_16x16x32_bf16 v[124:127], v[216:219], v[20:23], v[124:127]\n\t"
    "ds_read_b64_tr_b16 v[214:215], v240 offset:6144\n\t"
    "v_add_f32 v227, v235, v227\n\t"
    "v_mfma_f32_16x16x32_bf16 v[128:131], v[216:219], v[44:47], v[128:131]\n\t"
    "v_exp_f32 v231, v117\n\t"
    "v_mfma_f32_16x16x32_bf16 v[132:135], v[216:219], v[68:71], v[132:135]\n\t"
    "v_exp_f32 v232, v118\n\t"
    "v_mfma_f32_16x16x32_bf16 v[136:139], v[216:219], v[92:95], v[136:139]\n\t"
    "v_cvt_pk_bf16_f32 v193, v234, v235\n\t"
    "v_add_f32 v228, v230, v231\n\t"
    "v_mfma_f32_16x16x32_bf16 v[140:143], v[216:219], a[252:255], v[140:143]\n\t"
    "s_waitcnt lgkmcnt(4)\n\t"
    "ds_read_b64_tr_b16 v[216:217], v241 offset:0\n\t"
    "v_cvt_pk_bf16_f32 v196, v230, v231\n\t"
    "v_mfma_f32_16x16x32_bf16 a[0:3], v[204:207], v[164:167], a[0:3]\n\t"
    "ds_read_b64_tr_b16 v[218:219], v241 offset:6144\n\t"
    "v_add_f32 v228, v232, v228\n\t"
    "v_mfma_f32_16x16x32_bf16 a[4:7], v[204:207], v[168:171], a[4:7]\n\t"
    "v_exp_f32 v233, v119\n\t"
    "v_mfma_f32_16x16x32_bf16 a[8:11], v[204:207], v[172:175], a[8:11]\n\t"
    "v_exp_f32 v234, v120\n\t"
    "v_mfma_f32_16x16x32_bf16 a[12:15], v[204:207], v[176:179], a[12:15]\n\t"
    "v_exp_f32 v235, v121\n\t"
    "v_mfma_f32_16x16x32_bf16 a[16:19], v[204:207], v[180:183], a[16:19]\n\t"
    "s_waitcnt lgkmcnt(4)\n\t"
    "ds_read_b64_tr_b16 v[204:205], v238 offset:128\n\t"
    "v_add_f32 v228, v233, v228\n\t"
    "v_mfma_f32_16x16x32_bf16 a[20:23], v[208:211], v[164:167], a[20:23]\n\t"
    "ds_read_b64_tr_b16 v[206:207], v238 offset:6272\n\t"
    "v_cvt_pk_bf16_f32 v197, v232, v233\n\t"
    "v_mfma_f32_16x16x32_bf16 a[24:27], v[208:211], v[168:171], a[24:27]\n\t"
    "v_exp_f32 v230, v122\n\t"
    "v_mfma_f32_16x16x32_bf16 a[28:31], v[208:211], v[172:175], a[28:31]\n\t"
    "v_exp_f32 v231, v123\n\t"
    "v_mfma_f32_16x16x32_bf16 a[32:35], v[208:211], v[176:179], a[32:35]\n\t"
    "v_exp_f32 v232, v124\n\t"
    "v_mfma_f32_16x16x32_bf16 a[36:39], v[208:211], v[180:183], a[36:39]\n\t"
    "s_waitcnt lgkmcnt(4)\n\t"
    "ds_read_b64_tr_b16 v[208:209], v239 offset:128\n\t"
    "v_add_f32 v229, v234, v235\n\t"
    "v_mfma_f32_16x16x32_bf16 a[40:43], v[212:215], v[164:167], a[40:43]\n\t"
    "ds_read_b64_tr_b16 v[210:211], v239 offset:6272\n\t"
    "v_cvt_pk_bf16_f32 v200, v234, v235\n\t"
    "v_mfma_f32_16x16x32_bf16 a[44:47], v[212:215], v[168:171], a[44:47]\n\t"
    "v_exp_f32 v233, v125\n\t"
    "v_mfma_f32_16x16x32_bf16 a[48:51], v[212:215], v[172:175], a[48:51]\n\t"
    "v_exp_f32 v234, v126\n\t"
    "v_mfma_f32_16x16x32_bf16 a[52:55], v[212:215], v[176:179], a[52:55]\n\t"
    "v_add_f32 v229, v230, v229\n\t"
    "v_add_f32 v229, v231, v229\n\t"
    "v_mfma_f32_16x16x32_bf16 a[56:59], v[212:215], v[180:183], a[56:59]\n\t"
    "s_waitcnt lgkmcnt(4)\n\t"
    "ds_read_b64_tr_b16 v[212:213], v240 offset:128\n\t"
    "v_cvt_pk_bf16_f32 v201, v230, v231\n\t"
    "v_mfma_f32_16x16x32_bf16 a[60:63], v[216:219], v[164:167], a[60:63]\n\t"
    "ds_read_b64_tr_b16 v[214:215], v240 offset:6272\n\t"
    "v_add_f32 v225, v232, v225\n\t"
    "v_mfma_f32_16x16x32_bf16 a[64:67], v[216:219], v[168:171], a[64:67]\n\t"
    "v_exp_f32 v235, v127\n\t"
    "v_mfma_f32_16x16x32_bf16 a[68:71], v[216:219], v[172:175], a[68:71]\n\t"
    "v_exp_f32 v230, v128\n\t"
    "v_mfma_f32_16x16x32_bf16 a[72:75], v[216:219], v[176:179], a[72:75]\n\t"
    "v_add_f32 v225, v233, v225\n\t"
    "v_cvt_pk_bf16_f32 v186, v232, v233\n\t"
    "v_mfma_f32_16x16x32_bf16 a[76:79], v[216:219], v[180:183], a[76:79]\n\t"
    "s_waitcnt lgkmcnt(4)\n\t"
    "ds_read_b64_tr_b16 v[216:217], v241 offset:128\n\t"
    "v_add_f32 v225, v234, v225\n\t"
    "v_mfma_f32_16x16x32_bf16 a[80:83], v[204:207], v[164:167], a[80:83]\n\t"
    "ds_read_b64_tr_b16 v[218:219], v241 offset:6272\n\t"
    "v_add_f32 v225, v235, v225\n\t"
    "v_mfma_f32_16x16x32_bf16 a[84:87], v[204:207], v[168:171], a[84:87]\n\t"
    "v_exp_f32 v231, v129\n\t"
    "v_mfma_f32_16x16x32_bf16 a[88:91], v[204:207], v[172:175], a[88:91]\n\t"
    "v_exp_f32 v232, v130\n\t"
    "v_mfma_f32_16x16x32_bf16 a[92:95], v[204:207], v[176:179], a[92:95]\n\t"
    "v_cvt_pk_bf16_f32 v187, v234, v235\n\t"
    "v_add_f32 v226, v230, v226\n\t"
    "v_mfma_f32_16x16x32_bf16 a[96:99], v[204:207], v[180:183], a[96:99]\n\t"
    "s_waitcnt lgkmcnt(4)\n\t"
    "ds_read_b64_tr_b16 v[204:205], v238 offset:256\n\t"
    "v_add_f32 v226, v231, v226\n\t"
    "v_mfma_f32_16x16x32_bf16 a[100:103], v[208:211], v[164:167], a[100:103]\n\t"
    "ds_read_b64_tr_b16 v[206:207], v238 offset:6400\n\t"
    "v_cvt_pk_bf16_f32 v190, v230, v231\n\t"
    "v_mfma_f32_16x16x32_bf16 a[104:107], v[208:211], v[168:171], a[104:107]\n\t"
    "v_exp_f32 v233, v131\n\t"
    "v_mfma_f32_16x16x32_bf16 a[108:111], v[208:211], v[172:175], a[108:111]\n\t"
    "v_exp_f32 v234, v132\n\t"
    "v_mfma_f32_16x16x32_bf16 a[112:115], v[208:211], v[176:179], a[112:115]\n\t"
    "v_exp_f32 v235, v133\n\t"
    "v_mfma_f32_16x16x32_bf16 a[116:119], v[208:211], v[180:183], a[116:119]\n\t"
    "s_waitcnt lgkmcnt(4)\n\t"
    "ds_read_b64_tr_b16 v[208:209], v239 offset:256\n\t"
    "v_add_f32 v226, v232, v226\n\t"
    "v_mfma_f32_16x16x32_bf16 a[120:123], v[212:215], v[164:167], a[120:123]\n\t"
    "ds_read_b64_tr_b16 v[210:211], v239 offset:6400\n\t"
    "v_add_f32 v226, v233, v226\n\t"
    "v_mfma_f32_16x16x32_bf16 a[124:127], v[212:215], v[168:171], a[124:127]\n\t"
    "v_exp_f32 v230, v134\n\t"
    "v_mfma_f32_16x16x32_bf16 a[128:131], v[212:215], v[172:175], a[128:131]\n\t"
    "v_cvt_pk_bf16_f32 v191, v232, v233\n\t"
    "v_add_f32 v227, v234, v227\n\t"
    "v_mfma_f32_16x16x32_bf16 a[132:135], v[212:215], v[176:179], a[132:135]\n\t"
    "v_exp_f32 v231, v135\n\t"
    "v_mfma_f32_16x16x32_bf16 a[136:139], v[212:215], v[180:183], a[136:139]\n\t"
    "s_waitcnt lgkmcnt(4)\n\t"
    "ds_read_b64_tr_b16 v[212:213], v240 offset:256\n\t"
    "v_add_f32 v227, v235, v227\n\t"
    "v_mfma_f32_16x16x32_bf16 a[140:143], v[216:219], v[164:167], a[140:143]\n\t"
    "ds_read_b64_tr_b16 v[214:215], v240 offset:6400\n\t"
    "v_cvt_pk_bf16_f32 v194, v234, v235\n\t"
    "v_mfma_f32_16x16x32_bf16 a[144:147], v[216:219], v[168:171], a[144:147]\n\t"
    "v_exp_f32 v232, v136\n\t"
    "v_mfma_f32_16x16x32_bf16 a[148:151], v[216:219], v[172:175], a[148:151]\n\t"
    "v_exp_f32 v233, v137\n\t"
    "v_mfma_f32_16x16x32_bf16 a[152:155], v[216:219], v[176:179], a[152:155]\n\t"
    "v_exp_f32 v234, v138\n\t"
    "v_mfma_f32_16x16x32_bf16 a[156:159], v[216:219], v[180:183], a[156:159]\n\t"
    "s_waitcnt lgkmcnt(4)\n\t"
    "ds_read_b64_tr_b16 v[216:217], v241 offset:256\n\t"
    "v_add_f32 v227, v230, v227\n\t"
    "v_mfma_f32_16x16x32_bf16 a[160:163], v[204:207], v[164:167], a[160:163]\n\t"
    "ds_read_b64_tr_b16 v[218:219], v241 offset:6400\n\t"
    "v_add_f32 v227, v231, v227\n\t"
    "v_mfma_f32_16x16x32_bf16 a[164:167], v[204:207], v[168:171], a[164:167]\n\t"
    "v_add_u32 v238, s31, v238\n\t"
    "v_cvt_pk_bf16_f32 v195, v230, v231\n\t"
    "v_add_f32 v228, v232, v228\n\t"
    "v_mfma_f32_16x16x32_bf16 a[168:171], v[204:207], v[172:175], a[168:171]\n\t"
    "v_add_u32 v239, s31, v239\n\t"
    "v_exp_f32 v235, v139\n\t"
    "v_mfma_f32_16x16x32_bf16 a[172:175], v[204:207], v[176:179], a[172:175]\n\t"
    "v_add_u32 v240, s31, v240\n\t"
    "v_exp_f32 v230, v140\n\t"
    "v_mfma_f32_16x16x32_bf16 a[176:179], v[204:207], v[180:183], a[176:179]\n\t"
    "s_waitcnt lgkmcnt(4)\n\t"
    "v_add_u32 v241, s31, v241\n\t"
    "v_add_f32 v228, v233, v228\n\t"
    "v_cvt_pk_bf16_f32 v198, v232, v233\n\t"
    "v_mfma_f32_16x16x32_bf16 a[180:183], v[208:211], v[164:167], a[180:183]\n\t"
    "v_exp_f32 v231, v141\n\t"
    "v_mfma_f32_16x16x32_bf16 a[184:187], v[208:211], v[168:171], a[184:187]\n\t"
    "v_exp_f32 v232, v142\n\t"
    "v_mfma_f32_16x16x32_bf16 a[188:191], v[208:211], v[172:175], a[188:191]\n\t"
    "v_add_f32 v228, v234, v228\n\t"
    "v_add_f32 v228, v235, v228\n\t"
    "v_mfma_f32_16x16x32_bf16 a[192:195], v[208:211], v[176:179], a[192:195]\n\t"
    "s_waitcnt vmcnt(1) lgkmcnt(0)\n\t"
    "s_barrier\n\t"
    "ds_read_b64_tr_b16 v[204:205], v238 offset:0\n\t"
    "v_cvt_pk_bf16_f32 v199, v234, v235\n\t"
    "v_add_f32 v229, v230, v229\n\t"
    "v_mfma_f32_16x16x32_bf16 a[196:199], v[208:211], v[180:183], a[196:199]\n\t"
    "ds_read_b64_tr_b16 v[206:207], v238 offset:6144\n\t"
    "v_add_f32 v229, v231, v229\n\t"
    "v_cvt_pk_bf16_f32 v202, v230, v231\n\t"
    "v_mfma_f32_16x16x32_bf16 a[200:203], v[212:215], v[164:167], a[200:203]\n\t"
    "s_mov_b32 s29, s28\n\t"
    "ds_read_b64_tr_b16 v[208:209], v239 offset:0\n\t"
    "v_add_f32 v229, v232, v229\n\t"
    "v_mfma_f32_16x16x32_bf16 a[204:207], v[212:215], v[168:171], a[204:207]\n\t"
    "s_mov_b32 s28, s30\n\t"
    "ds_read_b64_tr_b16 v[210:211], v239 offset:6144\n\t"
    "v_mfma_f32_16x16x32_bf16 a[208:211], v[212:215], v[172:175], a[208:211]\n\t"
    "s_add_u32 s58, s29, s34\n\t"
    "v_exp_f32 v233, v143\n\t"
    "v_mfma_f32_16x16x32_bf16 a[212:215], v[212:215], v[176:179], a[212:215]\n\t"
    "s_add_u32 s59, s29, s35\n\t"
    "v_add_f32 v229, v233, v229\n\t"
    "v_cvt_pk_bf16_f32 v203, v232, v233\n\t"
    "v_mfma_f32_16x16x32_bf16 a[216:219], v[212:215], v[180:183], a[216:219]\n\t"
    "v_max3_f32 v253, v225, v226, v227\n\t"
    "v_max3_f32 v253, v253, v228, v229\n\t"
    "v_mfma_f32_16x16x32_bf16 a[220:223], v[216:219], v[164:167], a[220:223]\n\t"
    "ds_read_b64_tr_b16 v[212:213], v240 offset:0\n\t"
    "v_cmp_nge_f32 vcc, s50, v253\n\t"
    "v_mfma_f32_16x16x32_bf16 a[224:227], v[216:219], v[168:171], a[224:227]\n\t"
    "ds_read_b64_tr_b16 v[214:215], v240 offset:6144\n\t"
    "v_mfma_f32_16x16x32_bf16 a[228:231], v[216:219], v[172:175], a[228:231]\n\t"
    "v_mfma_f32_16x16x32_bf16 a[232:235], v[216:219], v[176:179], a[232:235]\n\t"
    "v_mfma_f32_16x16x32_bf16 a[236:239], v[216:219], v[180:183], a[236:239]\n\t"
    "s_waitcnt lgkmcnt(4)\n\t"
    "ds_read_b64_tr_b16 v[216:217], v241 offset:0\n\t"
    "s_cbranch_vccnz .Lk1_stub_s14e\n\t"
    ".Lk1_cont_s14e:\n\t"
    "v_mfma_f32_16x16x32_bf16 a[0:3], v[204:207], v[184:187], a[0:3]\n\t"
    "ds_read_b64_tr_b16 v[218:219], v241 offset:6144\n\t"
    "v_mfma_f32_16x16x32_bf16 a[4:7], v[204:207], v[188:191], a[4:7]\n\t"
    "v_add_f32 v220, v225, v220\n\t"
    "v_add_f32 v221, v226, v221\n\t"
    "v_mfma_f32_16x16x32_bf16 a[8:11], v[204:207], v[192:195], a[8:11]\n\t"
    "v_add_f32 v222, v227, v222\n\t"
    "v_add_f32 v223, v228, v223\n\t"
    "v_mfma_f32_16x16x32_bf16 a[12:15], v[204:207], v[196:199], a[12:15]\n\t"
    "v_add_f32 v224, v229, v224\n\t"
    "v_mfma_f32_16x16x32_bf16 a[16:19], v[204:207], v[200:203], a[16:19]\n\t"
    "s_waitcnt lgkmcnt(4)\n\t"
    "ds_read_b64_tr_b16 v[204:205], v238 offset:128\n\t"
    "v_mfma_f32_16x16x32_bf16 a[20:23], v[208:211], v[184:187], a[20:23]\n\t"
    "ds_read_b64_tr_b16 v[206:207], v238 offset:6272\n\t"
    "v_mfma_f32_16x16x32_bf16 a[24:27], v[208:211], v[188:191], a[24:27]\n\t"
    "v_mfma_f32_16x16x32_bf16 a[28:31], v[208:211], v[192:195], a[28:31]\n\t"
    "v_mfma_f32_16x16x32_bf16 a[32:35], v[208:211], v[196:199], a[32:35]\n\t"
    "v_mov_b32 v230, v220\n\t"
    "v_mov_b32 v231, v221\n\t"
    "v_mfma_f32_16x16x32_bf16 a[36:39], v[208:211], v[200:203], a[36:39]\n\t"
    "s_waitcnt lgkmcnt(4)\n\t"
    "ds_read_b64_tr_b16 v[208:209], v239 offset:128\n\t"
    "v_mov_b32 v232, v222\n\t"
    "v_mfma_f32_16x16x32_bf16 a[40:43], v[212:215], v[184:187], a[40:43]\n\t"
    "ds_read_b64_tr_b16 v[210:211], v239 offset:6272\n\t"
    "v_mov_b32 v233, v223\n\t"
    "v_mfma_f32_16x16x32_bf16 a[44:47], v[212:215], v[188:191], a[44:47]\n\t"
    "v_mov_b32 v234, v224\n\t"
    "v_mfma_f32_16x16x32_bf16 a[48:51], v[212:215], v[192:195], a[48:51]\n\t"
    "v_mfma_f32_16x16x32_bf16 a[52:55], v[212:215], v[196:199], a[52:55]\n\t"
    "v_permlane32_swap_b32 v220, v230\n\t"
    "v_permlane32_swap_b32 v221, v231\n\t"
    "v_mfma_f32_16x16x32_bf16 a[56:59], v[212:215], v[200:203], a[56:59]\n\t"
    "s_waitcnt lgkmcnt(4)\n\t"
    "ds_read_b64_tr_b16 v[212:213], v240 offset:128\n\t"
    "v_permlane32_swap_b32 v222, v232\n\t"
    "v_mfma_f32_16x16x32_bf16 a[60:63], v[216:219], v[184:187], a[60:63]\n\t"
    "ds_read_b64_tr_b16 v[214:215], v240 offset:6272\n\t"
    "v_permlane32_swap_b32 v223, v233\n\t"
    "v_mfma_f32_16x16x32_bf16 a[64:67], v[216:219], v[188:191], a[64:67]\n\t"
    "v_permlane32_swap_b32 v224, v234\n\t"
    "v_mfma_f32_16x16x32_bf16 a[68:71], v[216:219], v[192:195], a[68:71]\n\t"
    "v_add_f32 v220, v220, v230\n\t"
    "v_add_f32 v221, v221, v231\n\t"
    "v_mfma_f32_16x16x32_bf16 a[72:75], v[216:219], v[196:199], a[72:75]\n\t"
    "v_add_f32 v222, v222, v232\n\t"
    "v_add_f32 v223, v223, v233\n\t"
    "v_mfma_f32_16x16x32_bf16 a[76:79], v[216:219], v[200:203], a[76:79]\n\t"
    "s_waitcnt lgkmcnt(4)\n\t"
    "ds_read_b64_tr_b16 v[216:217], v241 offset:128\n\t"
    "v_add_f32 v224, v224, v234\n\t"
    "v_mfma_f32_16x16x32_bf16 a[80:83], v[204:207], v[184:187], a[80:83]\n\t"
    "ds_read_b64_tr_b16 v[218:219], v241 offset:6272\n\t"
    "v_mfma_f32_16x16x32_bf16 a[84:87], v[204:207], v[188:191], a[84:87]\n\t"
    "v_mov_b32 v230, v220\n\t"
    "v_mov_b32 v231, v221\n\t"
    "v_mfma_f32_16x16x32_bf16 a[88:91], v[204:207], v[192:195], a[88:91]\n\t"
    "v_mov_b32 v232, v222\n\t"
    "v_mov_b32 v233, v223\n\t"
    "v_mfma_f32_16x16x32_bf16 a[92:95], v[204:207], v[196:199], a[92:95]\n\t"
    "v_mov_b32 v234, v224\n\t"
    "v_mfma_f32_16x16x32_bf16 a[96:99], v[204:207], v[200:203], a[96:99]\n\t"
    "s_waitcnt lgkmcnt(4)\n\t"
    "ds_read_b64_tr_b16 v[204:205], v238 offset:256\n\t"
    "v_mfma_f32_16x16x32_bf16 a[100:103], v[208:211], v[184:187], a[100:103]\n\t"
    "ds_read_b64_tr_b16 v[206:207], v238 offset:6400\n\t"
    "v_permlane16_swap_b32 v220, v230\n\t"
    "v_mfma_f32_16x16x32_bf16 a[104:107], v[208:211], v[188:191], a[104:107]\n\t"
    "v_permlane16_swap_b32 v221, v231\n\t"
    "v_permlane16_swap_b32 v222, v232\n\t"
    "v_mfma_f32_16x16x32_bf16 a[108:111], v[208:211], v[192:195], a[108:111]\n\t"
    "v_permlane16_swap_b32 v223, v233\n\t"
    "v_permlane16_swap_b32 v224, v234\n\t"
    "v_mfma_f32_16x16x32_bf16 a[112:115], v[208:211], v[196:199], a[112:115]\n\t"
    "v_add_f32 v220, v220, v230\n\t"
    "v_add_f32 v221, v221, v231\n\t"
    "v_mfma_f32_16x16x32_bf16 a[116:119], v[208:211], v[200:203], a[116:119]\n\t"
    "s_waitcnt lgkmcnt(4)\n\t"
    "ds_read_b64_tr_b16 v[208:209], v239 offset:256\n\t"
    "v_add_f32 v222, v222, v232\n\t"
    "v_mfma_f32_16x16x32_bf16 a[120:123], v[212:215], v[184:187], a[120:123]\n\t"
    "ds_read_b64_tr_b16 v[210:211], v239 offset:6400\n\t"
    "v_add_f32 v223, v223, v233\n\t"
    "v_mfma_f32_16x16x32_bf16 a[124:127], v[212:215], v[188:191], a[124:127]\n\t"
    "v_add_f32 v224, v224, v234\n\t"
    "v_mfma_f32_16x16x32_bf16 a[128:131], v[212:215], v[192:195], a[128:131]\n\t"
    "v_mfma_f32_16x16x32_bf16 a[132:135], v[212:215], v[196:199], a[132:135]\n\t"
    "v_rcp_f32 v144, v220\n\t"
    "v_mfma_f32_16x16x32_bf16 a[136:139], v[212:215], v[200:203], a[136:139]\n\t"
    "s_waitcnt lgkmcnt(4)\n\t"
    "ds_read_b64_tr_b16 v[212:213], v240 offset:256\n\t"
    "v_mfma_f32_16x16x32_bf16 a[140:143], v[216:219], v[184:187], a[140:143]\n\t"
    "ds_read_b64_tr_b16 v[214:215], v240 offset:6400\n\t"
    "v_mfma_f32_16x16x32_bf16 a[144:147], v[216:219], v[188:191], a[144:147]\n\t"
    "v_rcp_f32 v146, v221\n\t"
    "v_mfma_f32_16x16x32_bf16 a[148:151], v[216:219], v[192:195], a[148:151]\n\t"
    "v_rcp_f32 v148, v222\n\t"
    "v_mfma_f32_16x16x32_bf16 a[152:155], v[216:219], v[196:199], a[152:155]\n\t"
    "v_rcp_f32 v150, v223\n\t"
    "v_mfma_f32_16x16x32_bf16 a[156:159], v[216:219], v[200:203], a[156:159]\n\t"
    "s_waitcnt lgkmcnt(4)\n\t"
    "ds_read_b64_tr_b16 v[216:217], v241 offset:256\n\t"
    "v_mfma_f32_16x16x32_bf16 a[160:163], v[204:207], v[184:187], a[160:163]\n\t"
    "ds_read_b64_tr_b16 v[218:219], v241 offset:6400\n\t"
    "v_mfma_f32_16x16x32_bf16 a[164:167], v[204:207], v[188:191], a[164:167]\n\t"
    "v_rcp_f32 v152, v224\n\t"
    "v_mfma_f32_16x16x32_bf16 a[168:171], v[204:207], v[192:195], a[168:171]\n\t"
    "v_mfma_f32_16x16x32_bf16 a[172:175], v[204:207], v[196:199], a[172:175]\n\t"
    "v_mfma_f32_16x16x32_bf16 a[176:179], v[204:207], v[200:203], a[176:179]\n\t"
    "s_waitcnt lgkmcnt(4)\n\t"
    "v_mfma_f32_16x16x32_bf16 a[180:183], v[208:211], v[184:187], a[180:183]\n\t"
    "v_mfma_f32_16x16x32_bf16 a[184:187], v[208:211], v[188:191], a[184:187]\n\t"
    "s_waitcnt vmcnt(0) lgkmcnt(0)\n\t"
    "s_barrier\n\t"
    "v_mfma_f32_16x16x32_bf16 a[188:191], v[208:211], v[192:195], a[188:191]\n\t"
    "v_mfma_f32_16x16x32_bf16 a[192:195], v[208:211], v[196:199], a[192:195]\n\t"
    "v_mfma_f32_16x16x32_bf16 a[196:199], v[208:211], v[200:203], a[196:199]\n\t"
    "v_mfma_f32_16x16x32_bf16 a[200:203], v[212:215], v[184:187], a[200:203]\n\t"
    "v_mfma_f32_16x16x32_bf16 a[204:207], v[212:215], v[188:191], a[204:207]\n\t"
    "v_mfma_f32_16x16x32_bf16 a[208:211], v[212:215], v[192:195], a[208:211]\n\t"
    "v_mfma_f32_16x16x32_bf16 a[212:215], v[212:215], v[196:199], a[212:215]\n\t"
    "v_mfma_f32_16x16x32_bf16 a[216:219], v[212:215], v[200:203], a[216:219]\n\t"
    "v_mfma_f32_16x16x32_bf16 a[220:223], v[216:219], v[184:187], a[220:223]\n\t"
    "v_mfma_f32_16x16x32_bf16 a[224:227], v[216:219], v[188:191], a[224:227]\n\t"
    "v_mfma_f32_16x16x32_bf16 a[228:231], v[216:219], v[192:195], a[228:231]\n\t"
    "v_mfma_f32_16x16x32_bf16 a[232:235], v[216:219], v[196:199], a[232:235]\n\t"
    "v_mfma_f32_16x16x32_bf16 a[236:239], v[216:219], v[200:203], a[236:239]\n\t"
    "s_mov_b32 s41, 32768\n\t"
    ".Lo_pollz:\n\t"
    "v_cmp_ne_u32 vcc, v255, v247\n\t"
    "s_and_b32 s40, vcc_lo, 0xf\n\t"
    "s_cbranch_scc0 .Lo_okz ; scc-ok\n\t"
    "s_sleep 1\n\t"
    "global_load_dword v247, v254, s[60:61] sc1\n\t"
    "s_sub_u32 s41, s41, 1\n\t"
    "s_waitcnt vmcnt(0)\n\t"
    "s_cbranch_scc0 .Lo_pollz ; scc-ok\n\t"
    ".Lo_okz:\n\t"
    "s_cmp_eq_u32 s74, 0\n\t"
    "s_cbranch_scc0 .Lk1_tyb\n\t"
    "v_mbcnt_lo_u32_b32 v115, -1, 0\n\t"
    "v_mbcnt_hi_u32_b32 v115, -1, v115\n\t"
    "v_lshlrev_b32 v214, 4, v115\n\t"
    "v_add_u32 v116, 0, v115\n\t"
    "v_mul_u32_u24 v117, 2731, v116\n\t"
    "v_lshrrev_b32 v117, 16, v117\n\t"
    "v_mul_u32_u24 v118, 24, v117\n\t"
    "v_sub_u32 v118, v116, v118\n\t"
    "v_bfe_u32 v119, v117, 1, 2\n\t"
    "v_lshlrev_b32 v119, 1, v119\n\t"
    "v_xor_b32 v118, v118, v119\n\t"
    "v_mul_u32_u24 v119, 1280, v117\n\t"
    "v_lshl_add_u32 v118, v118, 4, v119\n\t"
    "v_subrev_u32 v207, 0, v118\n\t"
    "v_add_u32 v116, 64, v115\n\t"
    "v_mul_u32_u24 v117, 2731, v116\n\t"
    "v_lshrrev_b32 v117, 16, v117\n\t"
    "v_mul_u32_u24 v118, 24, v117\n\t"
    "v_sub_u32 v118, v116, v118\n\t"
    "v_bfe_u32 v119, v117, 1, 2\n\t"
    "v_lshlrev_b32 v119, 1, v119\n\t"
    "v_xor_b32 v118, v118, v119\n\t"
    "v_mul_u32_u24 v119, 1280, v117\n\t"
    "v_lshl_add_u32 v118, v118, 4, v119\n\t"
    "v_subrev_u32 v208, 1024, v118\n\t"
    "v_add_u32 v116, 128, v115\n\t"
    "v_mul_u32_u24 v117, 2731, v116\n\t"
    "v_lshrrev_b32 v117, 16, v117\n\t"
    "v_mul_u32_u24 v118, 24, v117\n\t"
    "v_sub_u32 v118, v116, v118\n\t"
    "v_bfe_u32 v119, v117, 1, 2\n\t"
    "v_lshlrev_b32 v119, 1, v119\n\t"
    "v_xor_b32 v118, v118, v119\n\t"
    "v_mul_u32_u24 v119, 1280, v117\n\t"
    "v_lshl_add_u32 v118, v118, 4, v119\n\t"
    "v_subrev_u32 v209, 2048, v118\n\t"
    "v_add_u32 v116, 192, v115\n\t"
    "v_mul_u32_u24 v117, 2731, v116\n\t"
    "v_lshrrev_b32 v117, 16, v117\n\t"
    "v_mul_u32_u24 v118, 24, v117\n\t"
    "v_sub_u32 v118, v116, v118\n\t"
    "v_bfe_u32 v119, v117, 1, 2\n\t"
    "v_lshlrev_b32 v119, 1, v119\n\t"
    "v_xor_b32 v118, v118, v119\n\t"
    "v_mul_u32_u24 v119, 1280, v117\n\t"
    "v_lshl_add_u32 v118, v118, 4, v119\n\t"
    "v_subrev_u32 v210, 3072, v118\n\t"
    "v_add_u32 v116, 256, v115\n\t"
    "v_mul_u32_u24 v117, 2731, v116\n\t"
    "v_lshrrev_b32 v117, 16, v117\n\t"
    "v_mul_u32_u24 v118, 24, v117\n\t"
    "v_sub_u32 v118, v116, v118\n\t"
    "v_bfe_u32 v119, v117, 1, 2\n\t"
    "v_lshlrev_b32 v119, 1, v119\n\t"
    "v_xor_b32 v118, v118, v119\n\t"
    "v_mul_u32_u24 v119, 1280, v117\n\t"
    "v_lshl_add_u32 v118, v118, 4, v119\n\t"
    "v_subrev_u32 v211, 0, v118\n\t"
    "v_add_u32 v116, 320, v115\n\t"
    "v_mul_u32_u24 v117, 2731, v116\n\t"
    "v_lshrrev_b32 v117, 16, v117\n\t"
    "v_mul_u32_u24 v118, 24, v117\n\t"
    "v_sub_u32 v118, v116, v118\n\t"
    "v_bfe_u32 v119, v117, 1, 2\n\t"
    "v_lshlrev_b32 v119, 1, v119\n\t"
    "v_xor_b32 v118, v118, v119\n\t"
    "v_mul_u32_u24 v119, 1280, v117\n\t"
    "v_lshl_add_u32 v118, v118, 4, v119\n\t"
    "v_subrev_u32 v212, 1024, v118\n\t"
    "s_lshr_b32 s38, s7, 1\n\t"
    "s_and_b32 s39, s7, 1\n\t"
    "s_and_b32 s40, s78, 1\n\t"
    "s_mul_i32 s41, s40, 3932160\n\t"
    "s_bfe_u32 s42, s78, 0x10006\n\t"
    "s_mul_i32 s42, s42, 384\n\t"
    "s_mov_b32 s20, s42\n\t"
    "s_add_u32 s41, s41, s42\n\t"
    "s_add_u32 s8, s94, s41\n\t"
    "s_addc_u32 s9, s95, 0\n\t"
    "s_sub_u32 s10, 3932160, s42\n\t"
    "s_lshl_b32 s41, s6, 4\n\t"
    "s_add_u32 s41, s41, s70\n\t"
    "s_lshl_b32 s41, s41, 1\n\t"
    "s_add_u32 s41, s41, s40\n\t"
    "s_mul_i32 s41, s41, 983040\n\t"
    "s_add_u32 s12, s68, s41\n\t"
    "s_addc_u32 s13, s69, 0\n\t"
    "s_mov_b32 s14, 983040\n\t"
    "s_mul_i32 s36, s7, 20480\n\t"
    "s_mul_i32 s37, s7, 5120\n\t"
    "s_mul_i32 s34, s7, 6144\n\t"
    "s_add_u32 s35, s37, 24576\n\t"
    "s_add_u32 s58, s34, 0\n\t"
    "s_add_u32 s59, s35, 0\n\t"
    "s_add_u32 m0, s59, 0\n\t"
    "s_nop 0\n\t"
    "buffer_load_dwordx4 v214, s[12:15], s37 offen sc1 lds\n\t"
    "buffer_load_dwordx4 v214, s[12:15], s37 offen offset:1024 sc1 lds\n\t"
    "buffer_load_dwordx4 v214, s[12:15], s37 offen offset:2048 sc1 lds\n\t"
    "buffer_load_dwordx4 v214, s[12:15], s37 offen offset:3072 sc1 lds\n\t"
    "s_add_u32 m0, s59, 4096\n\t"
    "s_add_u32 s40, s37, 4096\n\t"
    "s_nop 0\n\t"
    "buffer_load_dwordx4 v214, s[12:15], s40 offen sc1 lds\n\t"
    "s_add_u32 s37, s37, 20480\n\t"
    "s_add_u32 m0, s58, 0\n\t"
    "s_nop 0\n\t"
    "buffer_load_dwordx4 v207, s[8:11], s36 offen lds\n\t"
    "buffer_load_dwordx4 v208, s[8:11], s36 offen offset:1024 lds\n\t"
    "buffer_load_dwordx4 v209, s[8:11], s36 offen offset:2048 lds\n\t"
    "buffer_load_dwordx4 v210, s[8:11], s36 offen offset:3072 lds\n\t"
    "s_add_u32 m0, s58, 4096\n\t"
    "s_nop 0\n\t"
    "buffer_load_dwordx4 v211, s[8:11], s36 offen lds\n\t"
    "buffer_load_dwordx4 v212, s[8:11], s36 offen offset:1024 lds\n\t"
    "s_add_u32 s36, s36, 81920\n\t"
    "v_lshrrev_b32 v116, 4, v115\n\t"
    "v_bfe_u32 v117, v115, 2, 2\n\t"
    "v_and_b32 v118, 3, v115\n\t"
    "v_lshl_add_u32 v117, v116, 2, v117\n\t"
    "v_bfe_u32 v120, v117, 1, 2\n\t"
    "v_mul_u32_u24 v117, 384, v117\n\t"
    "v_lshl_add_u32 v117, v118, 3, v117\n\t"
    "s_mul_i32 s40, s39, 6\n\t"
    "s_add_u32 s41, s40, 0\n\t"
    "v_xor_b32 v119, s41, v120\n\t"
    "v_lshl_add_u32 v204, v119, 5, v117\n\t"
    "s_add_u32 s41, s40, 1\n\t"
    "v_xor_b32 v119, s41, v120\n\t"
    "v_lshl_add_u32 v205, v119, 5, v117\n\t"
    "s_add_u32 s41, s40, 2\n\t"
    "v_xor_b32 v119, s41, v120\n\t"
    "v_lshl_add_u32 v222, v119, 5, v117\n\t"
    "s_add_u32 s41, s40, 3\n\t"
    "v_xor_b32 v119, s41, v120\n\t"
    "v_lshl_add_u32 v223, v119, 5, v117\n\t"
    "s_mul_i32 s40, s38, 5120\n\t"
    "s_add_u32 s40, s40, 24576\n\t"
    "v_add_u32 v206, s40, v214\n\t"
    "s_nop 4\n\t"
    "s_mov_b32 s42, 0\n\t"
    "s_mov_b32 s43, 4096\n\t"
    "v_accvgpr_read_b32 v0, a0\n\t"
    "v_accvgpr_read_b32 v1, a1\n\t"
    "v_accvgpr_read_b32 v2, a2\n\t"
    "v_accvgpr_read_b32 v3, a3\n\t"
    "v_accvgpr_read_b32 v4, a20\n\t"
    "v_accvgpr_read_b32 v5, a21\n\t"
    "v_accvgpr_read_b32 v6, a22\n\t"
    "v_accvgpr_read_b32 v7, a23\n\t"
    "v_pk_mul_f32 v[0:1], v[0:1], v[144:145] op_sel_hi:[1,0]\n\t"
    "v_pk_mul_f32 v[2:3], v[2:3], v[144:145] op_sel_hi:[1,0]\n\t"
    "v_pk_mul_f32 v[4:5], v[4:5], v[144:145] op_sel_hi:[1,0]\n\t"
    "v_pk_mul_f32 v[6:7], v[6:7], v[144:145] op_sel_hi:[1,0]\n\t"
    "v_cvt_pk_bf16_f32 v0, v0, v1\n\t"
    "v_cvt_pk_bf16_f32 v1, v2, v3\n\t"
    "v_cvt_pk_bf16_f32 v2, v4, v5\n\t"
    "v_cvt_pk_bf16_f32 v3, v6, v7\n\t"
    "buffer_store_dwordx4 v[0:3], v252, s[24:27], s42 offen sc1\n\t"
    "s_add_u32 s58, s34, 45056\n\t"
    "s_add_u32 s59, s35, 45056\n\t"
    "s_add_u32 m0, s59, 0\n\t"
    "s_nop 0\n\t"
    "buffer_load_dwordx4 v214, s[12:15], s37 offen sc1 lds\n\t"
    "buffer_load_dwordx4 v214, s[12:15], s37 offen offset:1024 sc1 lds\n\t"
    "v_accvgpr_read_b32 v8, a4\n\t"
    "v_accvgpr_read_b32 v9, a5\n\t"
    "v_accvgpr_read_b32 v10, a6\n\t"
    "v_accvgpr_read_b32 v11, a7\n\t"
    "v_accvgpr_read_b32 v12, a24\n\t"
    "v_accvgpr_read_b32 v13, a25\n\t"
    "v_accvgpr_read_b32 v14, a26\n\t"
    "v_accvgpr_read_b32 v15, a27\n\t"
    "v_pk_mul_f32 v[8:9], v[8:9], v[146:147] op_sel_hi:[1,0]\n\t"
    "v_pk_mul_f32 v[10:11], v[10:11], v[146:147] op_sel_hi:[1,0]\n\t"
    "v_pk_mul_f32 v[12:13], v[12:13], v[146:147] op_sel_hi:[1,0]\n\t"
    "v_pk_mul_f32 v[14:15], v[14:15], v[146:147] op_sel_hi:[1,0]\n\t"
    "v_cvt_pk_bf16_f32 v8, v8, v9\n\t"
    "v_cvt_pk_bf16_f32 v9, v10, v11\n\t"
    "v_cvt_pk_bf16_f32 v10, v12, v13\n\t"
    "v_cvt_pk_bf16_f32 v11, v14, v15\n\t"
    "buffer_store_dwordx4 v[8:11], v252, s[24:27], s42 offen offset:1024 sc1\n\t"
    "buffer_load_dwordx4 v214, s[12:15], s37 offen offset:2048 sc1 lds\n\t"
    "buffer_load_dwordx4 v214, s[12:15], s37 offen offset:3072 sc1 lds\n\t"
    "v_accvgpr_read_b32 v16, a8\n\t"
    "v_accvgpr_read_b32 v17, a9\n\t"
    "v_accvgpr_read_b32 v18, a10\n\t"
    "v_accvgpr_read_b32 v19, a11\n\t"
    "v_accvgpr_read_b32 v20, a28\n\t"
    "v_accvgpr_read_b32 v21, a29\n\t"
    "v_accvgpr_read_b32 v22, a30\n\t"
    "v_accvgpr_read_b32 v23, a31\n\t"
    "v_pk_mul_f32 v[16:17], v[16:17], v[148:149] op_sel_hi:[1,0]\n\t"
    "v_pk_mul_f32 v[18:19], v[18:19], v[148:149] op_sel_hi:[1,0]\n\t"
    "v_pk_mul_f32 v[20:21], v[20:21], v[148:149] op_sel_hi:[1,0]\n\t"
    "v_pk_mul_f32 v[22:23], v[22:23], v[148:149] op_sel_hi:[1,0]\n\t"
    "v_cvt_pk_bf16_f32 v16, v16, v17\n\t"
    "v_cvt_pk_bf16_f32 v17, v18, v19\n\t"
    "v_cvt_pk_bf16_f32 v18, v20, v21\n\t"
    "v_cvt_pk_bf16_f32 v19, v22, v23\n\t"
    "buffer_store_dwordx4 v[16:19], v252, s[24:27], s42 offen offset:2048 sc1\n\t"
    "s_add_u32 m0, s59, 4096\n\t"
    "s_add_u32 s40, s37, 4096\n\t"
    "s_nop 0\n\t"
    "buffer_load_dwordx4 v214, s[12:15], s40 offen sc1 lds\n\t"
    "s_add_u32 s37, s37, 20480\n\t"
    "s_add_u32 m0, s58, 0\n\t"
    "s_nop 0\n\t"
    "buffer_load_dwordx4 v207, s[8:11], s36 offen lds\n\t"
    "v_accvgpr_read_b32 v24, a12\n\t"
    "v_accvgpr_read_b32 v25, a13\n\t"
    "v_accvgpr_read_b32 v26, a14\n\t"
    "v_accvgpr_read_b32 v27, a15\n\t"
    "v_accvgpr_read_b32 v28, a32\n\t"
    "v_accvgpr_read_b32 v29, a33\n\t"
    "v_accvgpr_read_b32 v30, a34\n\t"
    "v_accvgpr_read_b32 v31, a35\n\t"
    "v_pk_mul_f32 v[24:25], v[24:25], v[150:151] op_sel_hi:[1,0]\n\t"
    "v_pk_mul_f32 v[26:27], v[26:27], v[150:151] op_sel_hi:[1,0]\n\t"
    "v_pk_mul_f32 v[28:29], v[28:29], v[150:151] op_sel_hi:[1,0]\n\t"
    "v_pk_mul_f32 v[30:31], v[30:31], v[150:151] op_sel_hi:[1,0]\n\t"
    "v_cvt_pk_bf16_f32 v24, v24, v25\n\t"
    "v_cvt_pk_bf16_f32 v25, v26, v27\n\t"
    "v_cvt_pk_bf16_f32 v26, v28, v29\n\t"
    "v_cvt_pk_bf16_f32 v27, v30, v31\n\t"
    "buffer_store_dwordx4 v[24:27], v252, s[24:27], s42 offen offset:3072 sc1\n\t"
    "buffer_load_dwordx4 v208, s[8:11], s36 offen offset:1024 lds\n\t"
    "buffer_load_dwordx4 v209, s[8:11], s36 offen offset:2048 lds\n\t"
    "v_accvgpr_read_b32 v32, a16\n\t"
    "v_accvgpr_read_b32 v33, a17\n\t"
    "v_accvgpr_read_b32 v34, a18\n\t"
    "v_accvgpr_read_b32 v35, a19\n\t"
    "v_accvgpr_read_b32 v36, a36\n\t"
    "v_accvgpr_read_b32 v37, a37\n\t"
    "v_accvgpr_read_b32 v38, a38\n\t"
    "v_accvgpr_read_b32 v39, a39\n\t"
    "v_pk_mul_f32 v[32:33], v[32:33], v[152:153] op_sel_hi:[1,0]\n\t"
    "v_pk_mul_f32 v[34:35], v[34:35], v[152:153] op_sel_hi:[1,0]\n\t"
    "v_pk_mul_f32 v[36:37], v[36:37], v[152:153] op_sel_hi:[1,0]\n\t"
    "v_pk_mul_f32 v[38:39], v[38:39], v[152:153] op_sel_hi:[1,0]\n\t"
    "v_cvt_pk_bf16_f32 v32, v32, v33\n\t"
    "v_cvt_pk_bf16_f32 v33, v34, v35\n\t"
    "v_cvt_pk_bf16_f32 v34, v36, v37\n\t"
    "v_cvt_pk_bf16_f32 v35, v38, v39\n\t"
    "buffer_store_dwordx4 v[32:35], v252, s[24:27], s43 offen sc1\n\t"
    "buffer_load_dwordx4 v210, s[8:11], s36 offen offset:3072 lds\n\t"
    "s_add_u32 m0, s58, 4096\n\t"
    "s_nop 0\n\t"
    "buffer_load_dwordx4 v211, s[8:11], s36 offen lds\n\t"
    "s_mov_b32 s42, 10240\n\t"
    "s_mov_b32 s43, 14336\n\t"
    "v_accvgpr_read_b32 v40, a40\n\t"
    "v_accvgpr_read_b32 v41, a41\n\t"
    "v_accvgpr_read_b32 v42, a42\n\t"
    "v_accvgpr_read_b32 v43, a43\n\t"
    "v_accvgpr_read_b32 v44, a60\n\t"
    "v_accvgpr_read_b32 v45, a61\n\t"
    "v_accvgpr_read_b32 v46, a62\n\t"
    "v_accvgpr_read_b32 v47, a63\n\t"
    "v_pk_mul_f32 v[40:41], v[40:41], v[144:145] op_sel_hi:[1,0]\n\t"
    "v_pk_mul_f32 v[42:43], v[42:43], v[144:145] op_sel_hi:[1,0]\n\t"
    "v_pk_mul_f32 v[44:45], v[44:45], v[144:145] op_sel_hi:[1,0]\n\t"
    "v_pk_mul_f32 v[46:47], v[46:47], v[144:145] op_sel_hi:[1,0]\n\t"
    "v_cvt_pk_bf16_f32 v40, v40, v41\n\t"
    "v_cvt_pk_bf16_f32 v41, v42, v43\n\t"
    "v_cvt_pk_bf16_f32 v42, v44, v45\n\t"
    "v_cvt_pk_bf16_f32 v43, v46, v47\n\t"
    "buffer_store_dwordx4 v[40:43], v252, s[24:27], s42 offen sc1\n\t"
    "buffer_load_dwordx4 v212, s[8:11], s36 offen offset:1024 lds\n\t"
    "s_add_u32 s36, s36, 81920\n\t"
    "s_add_u32 s58, s34, 90112\n\t"
    "s_add_u32 s59, s35, 90112\n\t"
    "s_add_u32 m0, s59, 0\n\t"
    "s_nop 0\n\t"
    "buffer_load_dwordx4 v214, s[12:15], s37 offen sc1 lds\n\t"
    "v_accvgpr_read_b32 v48, a44\n\t"
    "v_accvgpr_read_b32 v49, a45\n\t"
    "v_accvgpr_read_b32 v50, a46\n\t"
    "v_accvgpr_read_b32 v51, a47\n\t"
    "v_accvgpr_read_b32 v52, a64\n\t"
    "v_accvgpr_read_b32 v53, a65\n\t"
    "v_accvgpr_read_b32 v54, a66\n\t"
    "v_accvgpr_read_b32 v55, a67\n\t"
    "v_pk_mul_f32 v[48:49], v[48:49], v[146:147] op_sel_hi:[1,0]\n\t"
    "v_pk_mul_f32 v[50:51], v[50:51], v[146:147] op_sel_hi:[1,0]\n\t"
    "v_pk_mul_f32 v[52:53], v[52:53], v[146:147] op_sel_hi:[1,0]\n\t"
    "v_pk_mul_f32 v[54:55], v[54:55], v[146:147] op_sel_hi:[1,0]\n\t"
    "v_cvt_pk_bf16_f32 v48, v48, v49\n\t"
    "v_cvt_pk_bf16_f32 v49, v50, v51\n\t"
    "v_cvt_pk_bf16_f32 v50, v52, v53\n\t"
    "v_cvt_pk_bf16_f32 v51, v54, v55\n\t"
    "buffer_store_dwordx4 v[48:51], v252, s[24:27], s42 offen offset:1024 sc1\n\t"
    "buffer_load_dwordx4 v214, s[12:15], s37 offen offset:1024 sc1 lds\n\t"
    "buffer_load_dwordx4 v214, s[12:15], s37 offen offset:2048 sc1 lds\n\t"
    "v_accvgpr_read_b32 v56, a48\n\t"
    "v_accvgpr_read_b32 v57, a49\n\t"
    "v_accvgpr_read_b32 v58, a50\n\t"
    "v_accvgpr_read_b32 v59, a51\n\t"
    "v_accvgpr_read_b32 v60, a68\n\t"
    "v_accvgpr_read_b32 v61, a69\n\t"
    "v_accvgpr_read_b32 v62, a70\n\t"
    "v_accvgpr_read_b32 v63, a71\n\t"
    "v_pk_mul_f32 v[56:57], v[56:57], v[148:149] op_sel_hi:[1,0]\n\t"
    "v_pk_mul_f32 v[58:59], v[58:59], v[148:149] op_sel_hi:[1,0]\n\t"
    "v_pk_mul_f32 v[60:61], v[60:61], v[148:149] op_sel_hi:[1,0]\n\t"
    "v_pk_mul_f32 v[62:63], v[62:63], v[148:149] op_sel_hi:[1,0]\n\t"
    "v_cvt_pk_bf16_f32 v56, v56, v57\n\t"
    "v_cvt_pk_bf16_f32 v57, v58, v59\n\t"
    "v_cvt_pk_bf16_f32 v58, v60, v61\n\t"
    "v_cvt_pk_bf16_f32 v59, v62, v63\n\t"
    "buffer_store_dwordx4 v[56:59], v252, s[24:27], s42 offen offset:2048 sc1\n\t"
    "buffer_load_dwordx4 v214, s[12:15], s37 offen offset:3072 sc1 lds\n\t"
    "s_add_u32 m0, s59, 4096\n\t"
    "s_add_u32 s40, s37, 4096\n\t"
    "s_nop 0\n\t"
    "buffer_load_dwordx4 v214, s[12:15], s40 offen sc1 lds\n\t"
    "s_add_u32 s37, s37, 20480\n\t"
    "v_accvgpr_read_b32 v64, a52\n\t"
    "v_accvgpr_read_b32 v65, a53\n\t"
    "v_accvgpr_read_b32 v66, a54\n\t"
    "v_accvgpr_read_b32 v67, a55\n\t"
    "v_accvgpr_read_b32 v68, a72\n\t"
    "v_accvgpr_read_b32 v69, a73\n\t"
    "v_accvgpr_read_b32 v70, a74\n\t"
    "v_accvgpr_read_b32 v71, a75\n\t"
    "v_pk_mul_f32 v[64:65], v[64:65], v[150:151] op_sel_hi:[1,0]\n\t"
    "v_pk_mul_f32 v[66:67], v[66:67], v[150:151] op_sel_hi:[1,0]\n\t"
    "v_pk_mul_f32 v[68:69], v[68:69], v[150:151] op_sel_hi:[1,0]\n\t"
    "v_pk_mul_f32 v[70:71], v[70:71], v[150:151] op_sel_hi:[1,0]\n\t"
    "v_cvt_pk_bf16_f32 v64, v64, v65\n\t"
    "v_cvt_pk_bf16_f32 v65, v66, v67\n\t"
    "v_cvt_pk_bf16_f32 v66, v68, v69\n\t"
    "v_cvt_pk_bf16_f32 v67, v70, v71\n\t"
    "buffer_store_dwordx4 v[64:67], v252, s[24:27], s42 offen offset:3072 sc1\n\t"
    "v_accvgpr_read_b32 v72, a56\n\t"
    "v_accvgpr_read_b32 v73, a57\n\t"
    "v_accvgpr_read_b32 v74, a58\n\t"
    "v_accvgpr_read_b32 v75, a59\n\t"
    "v_accvgpr_read_b32 v76, a76\n\t"
    "v_accvgpr_read_b32 v77, a77\n\t"
    "v_accvgpr_read_b32 v78, a78\n\t"
    "v_accvgpr_read_b32 v79, a79\n\t"
    "v_pk_mul_f32 v[72:73], v[72:73], v[152:153] op_sel_hi:[1,0]\n\t"
    "v_pk_mul_f32 v[74:75], v[74:75], v[152:153] op_sel_hi:[1,0]\n\t"
    "v_pk_mul_f32 v[76:77], v[76:77], v[152:153] op_sel_hi:[1,0]\n\t"
    "v_pk_mul_f32 v[78:79], v[78:79], v[152:153] op_sel_hi:[1,0]\n\t"
    "v_cvt_pk_bf16_f32 v72, v72, v73\n\t"
    "v_cvt_pk_bf16_f32 v73, v74, v75\n\t"
    "v_cvt_pk_bf16_f32 v74, v76, v77\n\t"
    "v_cvt_pk_bf16_f32 v75, v78, v79\n\t"
    "buffer_store_dwordx4 v[72:75], v252, s[24:27], s43 offen sc1\n\t"
    "s_mov_b32 s42, 20480\n\t"
    "s_mov_b32 s43, 24576\n\t"
    "v_accvgpr_read_b32 v80, a80\n\t"
    "v_accvgpr_read_b32 v81, a81\n\t"
    "v_accvgpr_read_b32 v82, a82\n\t"
    "v_accvgpr_read_b32 v83, a83\n\t"
    "v_accvgpr_read_b32 v84, a100\n\t"
    "v_accvgpr_read_b32 v85, a101\n\t"
    "v_accvgpr_read_b32 v86, a102\n\t"
    "v_accvgpr_read_b32 v87, a103\n\t"
    "v_pk_mul_f32 v[80:81], v[80:81], v[144:145] op_sel_hi:[1,0]\n\t"
    "v_pk_mul_f32 v[82:83], v[82:83], v[144:145] op_sel_hi:[1,0]\n\t"
    "v_pk_mul_f32 v[84:85], v[84:85], v[144:145] op_sel_hi:[1,0]\n\t"
    "v_pk_mul_f32 v[86:87], v[86:87], v[144:145] op_sel_hi:[1,0]\n\t"
    "v_cvt_pk_bf16_f32 v80, v80, v81\n\t"
    "v_cvt_pk_bf16_f32 v81, v82, v83\n\t"
    "v_cvt_pk_bf16_f32 v82, v84, v85\n\t"
    "v_cvt_pk_bf16_f32 v83, v86, v87\n\t"
    "buffer_store_dwordx4 v[80:83], v252, s[24:27], s42 offen sc1\n\t"
    "v_accvgpr_read_b32 v88, a84\n\t"
    "v_accvgpr_read_b32 v89, a85\n\t"
    "v_accvgpr_read_b32 v90, a86\n\t"
    "v_accvgpr_read_b32 v91, a87\n\t"
    "v_accvgpr_read_b32 v92, a104\n\t"
    "v_accvgpr_read_b32 v93, a105\n\t"
    "v_accvgpr_read_b32 v94, a106\n\t"
    "v_accvgpr_read_b32 v95, a107\n\t"
    "v_pk_mul_f32 v[88:89], v[88:89], v[146:147] op_sel_hi:[1,0]\n\t"
    "v_pk_mul_f32 v[90:91], v[90:91], v[146:147] op_sel_hi:[1,0]\n\t"
    "v_pk_mul_f32 v[92:93], v[92:93], v[146:147] op_sel_hi:[1,0]\n\t"
    "v_pk_mul_f32 v[94:95], v[94:95], v[146:147] op_sel_hi:[1,0]\n\t"
    "v_cvt_pk_bf16_f32 v88, v88, v89\n\t"
    "v_cvt_pk_bf16_f32 v89, v90, v91\n\t"
    "v_cvt_pk_bf16_f32 v90, v92, v93\n\t"
    "v_cvt_pk_bf16_f32 v91, v94, v95\n\t"
    "buffer_store_dwordx4 v[88:91], v252, s[24:27], s42 offen offset:1024 sc1\n\t"
    "v_accvgpr_read_b32 v0, a88\n\t"
    "v_accvgpr_read_b32 v1, a89\n\t"
    "v_accvgpr_read_b32 v2, a90\n\t"
    "v_accvgpr_read_b32 v3, a91\n\t"
    "v_accvgpr_read_b32 v4, a108\n\t"
    "v_accvgpr_read_b32 v5, a109\n\t"
    "v_accvgpr_read_b32 v6, a110\n\t"
    "v_accvgpr_read_b32 v7, a111\n\t"
    "v_pk_mul_f32 v[0:1], v[0:1], v[148:149] op_sel_hi:[1,0]\n\t"
    "v_pk_mul_f32 v[2:3], v[2:3], v[148:149] op_sel_hi:[1,0]\n\t"
    "v_pk_mul_f32 v[4:5], v[4:5], v[148:149] op_sel_hi:[1,0]\n\t"
    "v_pk_mul_f32 v[6:7], v[6:7], v[148:149] op_sel_hi:[1,0]\n\t"
    "v_cvt_pk_bf16_f32 v0, v0, v1\n\t"
    "v_cvt_pk_bf16_f32 v1, v2, v3\n\t"
    "v_cvt_pk_bf16_f32 v2, v4, v5\n\t"
    "v_cvt_pk_bf16_f32 v3, v6, v7\n\t"
    "buffer_store_dwordx4 v[0:3], v252, s[24:27], s42 offen offset:2048 sc1\n\t"
    "v_accvgpr_read_b32 v8, a92\n\t"
    "v_accvgpr_read_b32 v9, a93\n\t"
    "v_accvgpr_read_b32 v10, a94\n\t"
    "v_accvgpr_read_b32 v11, a95\n\t"
    "v_accvgpr_read_b32 v12, a112\n\t"
    "v_accvgpr_read_b32 v13, a113\n\t"
    "v_accvgpr_read_b32 v14, a114\n\t"
    "v_accvgpr_read_b32 v15, a115\n\t"
    "v_pk_mul_f32 v[8:9], v[8:9], v[150:151] op_sel_hi:[1,0]\n\t"
    "v_pk_mul_f32 v[10:11], v[10:11], v[150:151] op_sel_hi:[1,0]\n\t"
    "v_pk_mul_f32 v[12:13], v[12:13], v[150:151] op_sel_hi:[1,0]\n\t"
    "v_pk_mul_f32 v[14:15], v[14:15], v[150:151] op_sel_hi:[1,0]\n\t"
    "v_cvt_pk_bf16_f32 v8, v8, v9\n\t"
    "v_cvt_pk_bf16_f32 v9, v10, v11\n\t"
    "v_cvt_pk_bf16_f32 v10, v12, v13\n\t"
    "v_cvt_pk_bf16_f32 v11, v14, v15\n\t"
    "buffer_store_dwordx4 v[8:11], v252, s[24:27], s42 offen offset:3072 sc1\n\t"
    "v_accvgpr_read_b32 v16, a96\n\t"
    "v_accvgpr_read_b32 v17, a97\n\t"
    "v_accvgpr_read_b32 v18, a98\n\t"
    "v_accvgpr_read_b32 v19, a99\n\t"
    "v_accvgpr_read_b32 v20, a116\n\t"
    "v_accvgpr_read_b32 v21, a117\n\t"
    "v_accvgpr_read_b32 v22, a118\n\t"
    "v_accvgpr_read_b32 v23, a119\n\t"
    "v_pk_mul_f32 v[16:17], v[16:17], v[152:153] op_sel_hi:[1,0]\n\t"
    "v_pk_mul_f32 v[18:19], v[18:19], v[152:153] op_sel_hi:[1,0]\n\t"
    "v_pk_mul_f32 v[20:21], v[20:21], v[152:153] op_sel_hi:[1,0]\n\t"
    "v_pk_mul_f32 v[22:23], v[22:23], v[152:153] op_sel_hi:[1,0]\n\t"
    "v_cvt_pk_bf16_f32 v16, v16, v17\n\t"
    "v_cvt_pk_bf16_f32 v17, v18, v19\n\t"
    "v_cvt_pk_bf16_f32 v18, v20, v21\n\t"
    "v_cvt_pk_bf16_f32 v19, v22, v23\n\t"
    "buffer_store_dwordx4 v[16:19], v252, s[24:27], s43 offen sc1\n\t"
    "s_mov_b32 s42, 30720\n\t"
    "s_mov_b32 s43, 34816\n\t"
    "v_accvgpr_read_b32 v24, a120\n\t"
    "v_accvgpr_read_b32 v25, a121\n\t"
    "v_accvgpr_read_b32 v26, a122\n\t"
    "v_accvgpr_read_b32 v27, a123\n\t"
    "v_accvgpr_read_b32 v28, a140\n\t"
    "v_accvgpr_read_b32 v29, a141\n\t"
    "v_accvgpr_read_b32 v30, a142\n\t"
    "v_accvgpr_read_b32 v31, a143\n\t"
    "v_pk_mul_f32 v[24:25], v[24:25], v[144:145] op_sel_hi:[1,0]\n\t"
    "v_pk_mul_f32 v[26:27], v[26:27], v[144:145] op_sel_hi:[1,0]\n\t"
    "v_pk_mul_f32 v[28:29], v[28:29], v[144:145] op_sel_hi:[1,0]\n\t"
    "v_pk_mul_f32 v[30:31], v[30:31], v[144:145] op_sel_hi:[1,0]\n\t"
    "v_cvt_pk_bf16_f32 v24, v24, v25\n\t"
    "v_cvt_pk_bf16_f32 v25, v26, v27\n\t"
    "v_cvt_pk_bf16_f32 v26, v28, v29\n\t"
    "v_cvt_pk_bf16_f32 v27, v30, v31\n\t"
    "buffer_store_dwordx4 v[24:27], v252, s[24:27], s42 offen sc1\n\t"
    "v_accvgpr_read_b32 v32, a124\n\t"
    "v_accvgpr_read_b32 v33, a125\n\t"
    "v_accvgpr_read_b32 v34, a126\n\t"
    "v_accvgpr_read_b32 v35, a127\n\t"
    "v_accvgpr_read_b32 v36, a144\n\t"
    "v_accvgpr_read_b32 v37, a145\n\t"
    "v_accvgpr_read_b32 v38, a146\n\t"
    "v_accvgpr_read_b32 v39, a147\n\t"
    "v_pk_mul_f32 v[32:33], v[32:33], v[146:147] op_sel_hi:[1,0]\n\t"
    "v_pk_mul_f32 v[34:35], v[34:35], v[146:147] op_sel_hi:[1,0]\n\t"
    "v_pk_mul_f32 v[36:37], v[36:37], v[146:147] op_sel_hi:[1,0]\n\t"
    "v_pk_mul_f32 v[38:39], v[38:39], v[146:147] op_sel_hi:[1,0]\n\t"
    "v_cvt_pk_bf16_f32 v32, v32, v33\n\t"
    "v_cvt_pk_bf16_f32 v33, v34, v35\n\t"
    "v_cvt_pk_bf16_f32 v34, v36, v37\n\t"
    "v_cvt_pk_bf16_f32 v35, v38, v39\n\t"
    "buffer_store_dwordx4 v[32:35], v252, s[24:27], s42 offen offset:1024 sc1\n\t"
    "v_accvgpr_read_b32 v40, a128\n\t"
    "v_accvgpr_read_b32 v41, a129\n\t"
    "v_accvgpr_read_b32 v42, a130\n\t"
    "v_accvgpr_read_b32 v43, a131\n\t"
    "v_accvgpr_read_b32 v44, a148\n\t"
    "v_accvgpr_read_b32 v45, a149\n\t"
    "v_accvgpr_read_b32 v46, a150\n\t"
    "v_accvgpr_read_b32 v47, a151\n\t"
    "v_pk_mul_f32 v[40:41], v[40:41], v[148:149] op_sel_hi:[1,0]\n\t"
    "v_pk_mul_f32 v[42:43], v[42:43], v[148:149] op_sel_hi:[1,0]\n\t"
    "v_pk_mul_f32 v[44:45], v[44:45], v[148:149] op_sel_hi:[1,0]\n\t"
    "v_pk_mul_f32 v[46:47], v[46:47], v[148:149] op_sel_hi:[1,0]\n\t"
    "v_cvt_pk_bf16_f32 v40, v40, v41\n\t"
    "v_cvt_pk_bf16_f32 v41, v42, v43\n\t"
    "v_cvt_pk_bf16_f32 v42, v44, v45\n\t"
    "v_cvt_pk_bf16_f32 v43, v46, v47\n\t"
    "buffer_store_dwordx4 v[40:43], v252, s[24:27], s42 offen offset:2048 sc1\n\t"
    "v_accvgpr_read_b32 v48, a132\n\t"
    "v_accvgpr_read_b32 v49, a133\n\t"
    "v_accvgpr_read_b32 v50, a134\n\t"
    "v_accvgpr_read_b32 v51, a135\n\t"
    "v_accvgpr_read_b32 v52, a152\n\t"
    "v_accvgpr_read_b32 v53, a153\n\t"
    "v_accvgpr_read_b32 v54, a154\n\t"
    "v_accvgpr_read_b32 v55, a155\n\t"
    "v_pk_mul_f32 v[48:49], v[48:49], v[150:151] op_sel_hi:[1,0]\n\t"
    "v_pk_mul_f32 v[50:51], v[50:51], v[150:151] op_sel_hi:[1,0]\n\t"
    "v_pk_mul_f32 v[52:53], v[52:53], v[150:151] op_sel_hi:[1,0]\n\t"
    "v_pk_mul_f32 v[54:55], v[54:55], v[150:151] op_sel_hi:[1,0]\n\t"
    "v_cvt_pk_bf16_f32 v48, v48, v49\n\t"
    "v_cvt_pk_bf16_f32 v49, v50, v51\n\t"
    "v_cvt_pk_bf16_f32 v50, v52, v53\n\t"
    "v_cvt_pk_bf16_f32 v51, v54, v55\n\t"
    "buffer_store_dwordx4 v[48:51], v252, s[24:27], s42 offen offset:3072 sc1\n\t"
    "v_accvgpr_read_b32 v56, a136\n\t"
    "v_accvgpr_read_b32 v57, a137\n\t"
    "v_accvgpr_read_b32 v58, a138\n\t"
    "v_accvgpr_read_b32 v59, a139\n\t"
    "v_accvgpr_read_b32 v60, a156\n\t"
    "v_accvgpr_read_b32 v61, a157\n\t"
    "v_accvgpr_read_b32 v62, a158\n\t"
    "v_accvgpr_read_b32 v63, a159\n\t"
    "v_pk_mul_f32 v[56:57], v[56:57], v[152:153] op_sel_hi:[1,0]\n\t"
    "v_pk_mul_f32 v[58:59], v[58:59], v[152:153] op_sel_hi:[1,0]\n\t"
    "v_pk_mul_f32 v[60:61], v[60:61], v[152:153] op_sel_hi:[1,0]\n\t"
    "v_pk_mul_f32 v[62:63], v[62:63], v[152:153] op_sel_hi:[1,0]\n\t"
    "v_cvt_pk_bf16_f32 v56, v56, v57\n\t"
    "v_cvt_pk_bf16_f32 v57, v58, v59\n\t"
    "v_cvt_pk_bf16_f32 v58, v60, v61\n\t"
    "v_cvt_pk_bf16_f32 v59, v62, v63\n\t"
    "buffer_store_dwordx4 v[56:59], v252, s[24:27], s43 offen sc1\n\t"
    "s_mov_b32 s42, 40960\n\t"
    "s_mov_b32 s43, 45056\n\t"
    "v_accvgpr_read_b32 v64, a160\n\t"
    "v_accvgpr_read_b32 v65, a161\n\t"
    "v_accvgpr_read_b32 v66, a162\n\t"
    "v_accvgpr_read_b32 v67, a163\n\t"
    "v_accvgpr_read_b32 v68, a180\n\t"
    "v_accvgpr_read_b32 v69, a181\n\t"
    "v_accvgpr_read_b32 v70, a182\n\t"
    "v_accvgpr_read_b32 v71, a183\n\t"
    "v_pk_mul_f32 v[64:65], v[64:65], v[144:145] op_sel_hi:[1,0]\n\t"
    "v_pk_mul_f32 v[66:67], v[66:67], v[144:145] op_sel_hi:[1,0]\n\t"
    "v_pk_mul_f32 v[68:69], v[68:69], v[144:145] op_sel_hi:[1,0]\n\t"
    "v_pk_mul_f32 v[70:71], v[70:71], v[144:145] op_sel_hi:[1,0]\n\t"
    "v_cvt_pk_bf16_f32 v64, v64, v65\n\t"
    "v_cvt_pk_bf16_f32 v65, v66, v67\n\t"
    "v_cvt_pk_bf16_f32 v66, v68, v69\n\t"
    "v_cvt_pk_bf16_f32 v67, v70, v71\n\t"
    "buffer_store_dwordx4 v[64:67], v252, s[24:27], s42 offen sc1\n\t"
    "v_accvgpr_read_b32 v72, a164\n\t"
    "v_accvgpr_read_b32 v73, a165\n\t"
    "v_accvgpr_read_b32 v74, a166\n\t"
    "v_accvgpr_read_b32 v75, a167\n\t"
    "v_accvgpr_read_b32 v76, a184\n\t"
    "v_accvgpr_read_b32 v77, a185\n\t"
    "v_accvgpr_read_b32 v78, a186\n\t"
    "v_accvgpr_read_b32 v79, a187\n\t"
    "v_pk_mul_f32 v[72:73], v[72:73], v[146:147] op_sel_hi:[1,0]\n\t"
    "v_pk_mul_f32 v[74:75], v[74:75], v[146:147] op_sel_hi:[1,0]\n\t"
    "v_pk_mul_f32 v[76:77], v[76:77], v[146:147] op_sel_hi:[1,0]\n\t"
    "v_pk_mul_f32 v[78:79], v[78:79], v[146:147] op_sel_hi:[1,0]\n\t"
    "v_cvt_pk_bf16_f32 v72, v72, v73\n\t"
    "v_cvt_pk_bf16_f32 v73, v74, v75\n\t"
    "v_cvt_pk_bf16_f32 v74, v76, v77\n\t"
    "v_cvt_pk_bf16_f32 v75, v78, v79\n\t"
    "buffer_store_dwordx4 v[72:75], v252, s[24:27], s42 offen offset:1024 sc1\n\t"
    "v_accvgpr_read_b32 v80, a168\n\t"
    "v_accvgpr_read_b32 v81, a169\n\t"
    "v_accvgpr_read_b32 v82, a170\n\t"
    "v_accvgpr_read_b32 v83, a171\n\t"
    "v_accvgpr_read_b32 v84, a188\n\t"
    "v_accvgpr_read_b32 v85, a189\n\t"
    "v_accvgpr_read_b32 v86, a190\n\t"
    "v_accvgpr_read_b32 v87, a191\n\t"
    "v_pk_mul_f32 v[80:81], v[80:81], v[148:149] op_sel_hi:[1,0]\n\t"
    "v_pk_mul_f32 v[82:83], v[82:83], v[148:149] op_sel_hi:[1,0]\n\t"
    "v_pk_mul_f32 v[84:85], v[84:85], v[148:149] op_sel_hi:[1,0]\n\t"
    "v_pk_mul_f32 v[86:87], v[86:87], v[148:149] op_sel_hi:[1,0]\n\t"
    "v_cvt_pk_bf16_f32 v80, v80, v81\n\t"
    "v_cvt_pk_bf16_f32 v81, v82, v83\n\t"
    "v_cvt_pk_bf16_f32 v82, v84, v85\n\t"
    "v_cvt_pk_bf16_f32 v83, v86, v87\n\t"
    "buffer_store_dwordx4 v[80:83], v252, s[24:27], s42 offen offset:2048 sc1\n\t"
    "v_accvgpr_read_b32 v88, a172\n\t"
    "v_accvgpr_read_b32 v89, a173\n\t"
    "v_accvgpr_read_b32 v90, a174\n\t"
    "v_accvgpr_read_b32 v91, a175\n\t"
    "v_accvgpr_read_b32 v92, a192\n\t"
    "v_accvgpr_read_b32 v93, a193\n\t"
    "v_accvgpr_read_b32 v94, a194\n\t"
    "v_accvgpr_read_b32 v95, a195\n\t"
    "v_pk_mul_f32 v[88:89], v[88:89], v[150:151] op_sel_hi:[1,0]\n\t"
    "v_pk_mul_f32 v[90:91], v[90:91], v[150:151] op_sel_hi:[1,0]\n\t"
    "v_pk_mul_f32 v[92:93], v[92:93], v[150:151] op_sel_hi:[1,0]\n\t"
    "v_pk_mul_f32 v[94:95], v[94:95], v[150:151] op_sel_hi:[1,0]\n\t"
    "v_cvt_pk_bf16_f32 v88, v88, v89\n\t"
    "v_cvt_pk_bf16_f32 v89, v90, v91\n\t"
    "v_cvt_pk_bf16_f32 v90, v92, v93\n\t"
    "v_cvt_pk_bf16_f32 v91, v94, v95\n\t"
    "buffer_store_dwordx4 v[88:91], v252, s[24:27], s42 offen offset:3072 sc1\n\t"
    "v_accvgpr_read_b32 v0, a176\n\t"
    "v_accvgpr_read_b32 v1, a177\n\t"
    "v_accvgpr_read_b32 v2, a178\n\t"
    "v_accvgpr_read_b32 v3, a179\n\t"
    "v_accvgpr_read_b32 v4, a196\n\t"
    "v_accvgpr_read_b32 v5, a197\n\t"
    "v_accvgpr_read_b32 v6, a198\n\t"
    "v_accvgpr_read_b32 v7, a199\n\t"
    "v_pk_mul_f32 v[0:1], v[0:1], v[152:153] op_sel_hi:[1,0]\n\t"
    "v_pk_mul_f32 v[2:3], v[2:3], v[152:153] op_sel_hi:[1,0]\n\t"
    "v_pk_mul_f32 v[4:5], v[4:5], v[152:153] op_sel_hi:[1,0]\n\t"
    "v_pk_mul_f32 v[6:7], v[6:7], v[152:153] op_sel_hi:[1,0]\n\t"
    "v_cvt_pk_bf16_f32 v0, v0, v1\n\t"
    "v_cvt_pk_bf16_f32 v1, v2, v3\n\t"
    "v_cvt_pk_bf16_f32 v2, v4, v5\n\t"
    "v_cvt_pk_bf16_f32 v3, v6, v7\n\t"
    "buffer_store_dwordx4 v[0:3], v252, s[24:27], s43 offen sc1\n\t"
    "s_mov_b32 s42, 51200\n\t"
    "s_mov_b32 s43, 55296\n\t"
    "v_accvgpr_read_b32 v8, a200\n\t"
    "v_accvgpr_read_b32 v9, a201\n\t"
    "v_accvgpr_read_b32 v10, a202\n\t"
    "v_accvgpr_read_b32 v11, a203\n\t"
    "v_accvgpr_read_b32 v12, a220\n\t"
    "v_accvgpr_read_b32 v13, a221\n\t"
    "v_accvgpr_read_b32 v14, a222\n\t"
    "v_accvgpr_read_b32 v15, a223\n\t"
    "v_pk_mul_f32 v[8:9], v[8:9], v[144:145] op_sel_hi:[1,0]\n\t"
    "v_pk_mul_f32 v[10:11], v[10:11], v[144:145] op_sel_hi:[1,0]\n\t"
    "v_pk_mul_f32 v[12:13], v[12:13], v[144:145] op_sel_hi:[1,0]\n\t"
    "v_pk_mul_f32 v[14:15], v[14:15], v[144:145] op_sel_hi:[1,0]\n\t"
    "v_cvt_pk_bf16_f32 v8, v8, v9\n\t"
    "v_cvt_pk_bf16_f32 v9, v10, v11\n\t"
    "v_cvt_pk_bf16_f32 v10, v12, v13\n\t"
    "v_cvt_pk_bf16_f32 v11, v14, v15\n\t"
    "buffer_store_dwordx4 v[8:11], v252, s[24:27], s42 offen sc1\n\t"
    "v_accvgpr_read_b32 v16, a204\n\t"
    "v_accvgpr_read_b32 v17, a205\n\t"
    "v_accvgpr_read_b32 v18, a206\n\t"
    "v_accvgpr_read_b32 v19, a207\n\t"
    "v_accvgpr_read_b32 v20, a224\n\t"
    "v_accvgpr_read_b32 v21, a225\n\t"
    "v_accvgpr_read_b32 v22, a226\n\t"
    "v_accvgpr_read_b32 v23, a227\n\t"
    "v_pk_mul_f32 v[16:17], v[16:17], v[146:147] op_sel_hi:[1,0]\n\t"
    "v_pk_mul_f32 v[18:19], v[18:19], v[146:147] op_sel_hi:[1,0]\n\t"
    "v_pk_mul_f32 v[20:21], v[20:21], v[146:147] op_sel_hi:[1,0]\n\t"
    "v_pk_mul_f32 v[22:23], v[22:23], v[146:147] op_sel_hi:[1,0]\n\t"
    "v_cvt_pk_bf16_f32 v16, v16, v17\n\t"
    "v_cvt_pk_bf16_f32 v17, v18, v19\n\t"
    "v_cvt_pk_bf16_f32 v18, v20, v21\n\t"
    "v_cvt_pk_bf16_f32 v19, v22, v23\n\t"
    "buffer_store_dwordx4 v[16:19], v252, s[24:27], s42 offen offset:1024 sc1\n\t"
    "v_accvgpr_read_b32 v24, a208\n\t"
    "v_accvgpr_read_b32 v25, a209\n\t"
    "v_accvgpr_read_b32 v26, a210\n\t"
    "v_accvgpr_read_b32 v27, a211\n\t"
    "v_accvgpr_read_b32 v28, a228\n\t"
    "v_accvgpr_read_b32 v29, a229\n\t"
    "v_accvgpr_read_b32 v30, a230\n\t"
    "v_accvgpr_read_b32 v31, a231\n\t"
    "v_pk_mul_f32 v[24:25], v[24:25], v[148:149] op_sel_hi:[1,0]\n\t"
    "v_pk_mul_f32 v[26:27], v[26:27], v[148:149] op_sel_hi:[1,0]\n\t"
    "v_pk_mul_f32 v[28:29], v[28:29], v[148:149] op_sel_hi:[1,0]\n\t"
    "v_pk_mul_f32 v[30:31], v[30:31], v[148:149] op_sel_hi:[1,0]\n\t"
    "v_cvt_pk_bf16_f32 v24, v24, v25\n\t"
    "v_cvt_pk_bf16_f32 v25, v26, v27\n\t"
    "v_cvt_pk_bf16_f32 v26, v28, v29\n\t"
    "v_cvt_pk_bf16_f32 v27, v30, v31\n\t"
    "buffer_store_dwordx4 v[24:27], v252, s[24:27], s42 offen offset:2048 sc1\n\t"
    "v_accvgpr_read_b32 v32, a212\n\t"
    "v_accvgpr_read_b32 v33, a213\n\t"
    "v_accvgpr_read_b32 v34, a214\n\t"
    "v_accvgpr_read_b32 v35, a215\n\t"
    "v_accvgpr_read_b32 v36, a232\n\t"
    "v_accvgpr_read_b32 v37, a233\n\t"
    "v_accvgpr_read_b32 v38, a234\n\t"
    "v_accvgpr_read_b32 v39, a235\n\t"
    "v_pk_mul_f32 v[32:33], v[32:33], v[150:151] op_sel_hi:[1,0]\n\t"
    "v_pk_mul_f32 v[34:35], v[34:35], v[150:151] op_sel_hi:[1,0]\n\t"
    "v_pk_mul_f32 v[36:37], v[36:37], v[150:151] op_sel_hi:[1,0]\n\t"
    "v_pk_mul_f32 v[38:39], v[38:39], v[150:151] op_sel_hi:[1,0]\n\t"
    "v_cvt_pk_bf16_f32 v32, v32, v33\n\t"
    "v_cvt_pk_bf16_f32 v33, v34, v35\n\t"
    "v_cvt_pk_bf16_f32 v34, v36, v37\n\t"
    "v_cvt_pk_bf16_f32 v35, v38, v39\n\t"
    "buffer_store_dwordx4 v[32:35], v252, s[24:27], s42 offen offset:3072 sc1\n\t"
    "v_accvgpr_read_b32 v40, a216\n\t"
    "v_accvgpr_read_b32 v41, a217\n\t"
    "v_accvgpr_read_b32 v42, a218\n\t"
    "v_accvgpr_read_b32 v43, a219\n\t"
    "v_accvgpr_read_b32 v44, a236\n\t"
    "v_accvgpr_read_b32 v45, a237\n\t"
    "v_accvgpr_read_b32 v46, a238\n\t"
    "v_accvgpr_read_b32 v47, a239\n\t"
    "v_pk_mul_f32 v[40:41], v[40:41], v[152:153] op_sel_hi:[1,0]\n\t"
    "v_pk_mul_f32 v[42:43], v[42:43], v[152:153] op_sel_hi:[1,0]\n\t"
    "v_pk_mul_f32 v[44:45], v[44:45], v[152:153] op_sel_hi:[1,0]\n\t"
    "v_pk_mul_f32 v[46:47], v[46:47], v[152:153] op_sel_hi:[1,0]\n\t"
    "v_cvt_pk_bf16_f32 v40, v40, v41\n\t"
    "v_cvt_pk_bf16_f32 v41, v42, v43\n\t"
    "v_cvt_pk_bf16_f32 v42, v44, v45\n\t"
    "v_cvt_pk_bf16_f32 v43, v46, v47\n\t"
    "buffer_store_dwordx4 v[40:43], v252, s[24:27], s43 offen sc1\n\t"
    "s_mov_b32 s28, 0\n\t"
    "v_accvgpr_write_b32 a0, 0\n\t"
    "v_accvgpr_write_b32 a1, 0\n\t"
    "v_accvgpr_write_b32 a2, 0\n\t"
    "v_accvgpr_write_b32 a3, 0\n\t"
    "v_accvgpr_write_b32 a4, 0\n\t"
    "v_accvgpr_write_b32 a5, 0\n\t"
    "v_accvgpr_write_b32 a6, 0\n\t"
    "v_accvgpr_write_b32 a7, 0\n\t"
    "v_accvgpr_write_b32 a8, 0\n\t"
    "v_accvgpr_write_b32 a9, 0\n\t"
    "v_accvgpr_write_b32 a10, 0\n\t"
    "v_accvgpr_write_b32 a11, 0\n\t"
    "v_accvgpr_write_b32 a12, 0\n\t"
    "v_accvgpr_write_b32 a13, 0\n\t"
    "v_accvgpr_write_b32 a14, 0\n\t"
    "v_accvgpr_write_b32 a15, 0\n\t"
    "v_accvgpr_write_b32 a16, 0\n\t"
    "v_accvgpr_write_b32 a17, 0\n\t"
    "v_accvgpr_write_b32 a18, 0\n\t"
    "v_accvgpr_write_b32 a19, 0\n\t"
    "v_accvgpr_write_b32 a20, 0\n\t"
    "v_accvgpr_write_b32 a21, 0\n\t"
    "v_accvgpr_write_b32 a22, 0\n\t"
    "v_accvgpr_write_b32 a23, 0\n\t"
    "v_accvgpr_write_b32 a24, 0\n\t"
    "v_accvgpr_write_b32 a25, 0\n\t"
    "v_accvgpr_write_b32 a26, 0\n\t"
    "v_accvgpr_write_b32 a27, 0\n\t"
    "v_accvgpr_write_b32 a28, 0\n\t"
    "v_accvgpr_write_b32 a29, 0\n\t"
    "v_accvgpr_write_b32 a30, 0\n\t"
    "v_accvgpr_write_b32 a31, 0\n\t"
    "v_accvgpr_write_b32 a32, 0\n\t"
    "v_accvgpr_write_b32 a33, 0\n\t"
    "v_accvgpr_write_b32 a34, 0\n\t"
    "v_accvgpr_write_b32 a35, 0\n\t"
    "v_accvgpr_write_b32 a36, 0\n\t"
    "v_accvgpr_write_b32 a37, 0\n\t"
    "v_accvgpr_write_b32 a38, 0\n\t"
    "v_accvgpr_write_b32 a39, 0\n\t"
    "v_accvgpr_write_b32 a40, 0\n\t"
    "v_accvgpr_write_b32 a41, 0\n\t"
    "v_accvgpr_write_b32 a42, 0\n\t"
    "v_accvgpr_write_b32 a43, 0\n\t"
    "v_accvgpr_write_b32 a44, 0\n\t"
    "v_accvgpr_write_b32 a45, 0\n\t"
    "v_accvgpr_write_b32 a46, 0\n\t"
    "v_accvgpr_write_b32 a47, 0\n\t"
    "v_accvgpr_write_b32 a48, 0\n\t"
    "v_accvgpr_write_b32 a49, 0\n\t"
    "v_accvgpr_write_b32 a50, 0\n\t"
    "v_accvgpr_write_b32 a51, 0\n\t"
    "v_accvgpr_write_b32 a52, 0\n\t"
    "v_accvgpr_write_b32 a53, 0\n\t"
    "v_accvgpr_write_b32 a54, 0\n\t"
    "v_accvgpr_write_b32 a55, 0\n\t"
    "v_accvgpr_write_b32 a56, 0\n\t"
    "v_accvgpr_write_b32 a57, 0\n\t"
    "v_accvgpr_write_b32 a58, 0\n\t"
    "v_accvgpr_write_b32 a59, 0\n\t"
    "v_accvgpr_write_b32 a60, 0\n\t"
    "v_accvgpr_write_b32 a61, 0\n\t"
    "v_accvgpr_write_b32 a62, 0\n\t"
    "v_accvgpr_write_b32 a63, 0\n\t"
    "v_accvgpr_write_b32 a64, 0\n\t"
    "v_accvgpr_write_b32 a65, 0\n\t"
    "v_accvgpr_write_b32 a66, 0\n\t"
    "v_accvgpr_write_b32 a67, 0\n\t"
    "v_accvgpr_write_b32 a68, 0\n\t"
    "v_accvgpr_write_b32 a69, 0\n\t"
    "v_accvgpr_write_b32 a70, 0\n\t"
    "v_accvgpr_write_b32 a71, 0\n\t"
    "v_accvgpr_write_b32 a72, 0\n\t"
    "v_accvgpr_write_b32 a73, 0\n\t"
    "v_accvgpr_write_b32 a74, 0\n\t"
    "v_accvgpr_write_b32 a75, 0\n\t"
    "v_accvgpr_write_b32 a76, 0\n\t"
    "v_accvgpr_write_b32 a77, 0\n\t"
    "v_accvgpr_write_b32 a78, 0\n\t"
    "v_accvgpr_write_b32 a79, 0\n\t"
    "v_accvgpr_write_b32 a80, 0\n\t"
    "v_accvgpr_write_b32 a81, 0\n\t"
    "v_accvgpr_write_b32 a82, 0\n\t"
    "v_accvgpr_write_b32 a83, 0\n\t"
    "v_accvgpr_write_b32 a84, 0\n\t"
    "v_accvgpr_write_b32 a85, 0\n\t"
    "v_accvgpr_write_b32 a86, 0\n\t"
    "v_accvgpr_write_b32 a87, 0\n\t"
    "v_accvgpr_write_b32 a88, 0\n\t"
    "v_accvgpr_write_b32 a89, 0\n\t"
    "v_accvgpr_write_b32 a90, 0\n\t"
    "v_accvgpr_write_b32 a91, 0\n\t"
    "v_accvgpr_write_b32 a92, 0\n\t"
    "v_accvgpr_write_b32 a93, 0\n\t"
    "v_accvgpr_write_b32 a94, 0\n\t"
    "v_accvgpr_write_b32 a95, 0\n\t"
    "v_accvgpr_write_b32 a96, 0\n\t"
    "v_accvgpr_write_b32 a97, 0\n\t"
    "v_accvgpr_write_b32 a98, 0\n\t"
    "v_accvgpr_write_b32 a99, 0\n\t"
    "v_accvgpr_write_b32 a100, 0\n\t"
    "v_accvgpr_write_b32 a101, 0\n\t"
    "v_accvgpr_write_b32 a102, 0\n\t"
    "v_accvgpr_write_b32 a103, 0\n\t"
    "v_accvgpr_write_b32 a104, 0\n\t"
    "v_accvgpr_write_b32 a105, 0\n\t"
    "v_accvgpr_write_b32 a106, 0\n\t"
    "v_accvgpr_write_b32 a107, 0\n\t"
    "v_accvgpr_write_b32 a108, 0\n\t"
    "v_accvgpr_write_b32 a109, 0\n\t"
    "v_accvgpr_write_b32 a110, 0\n\t"
    "v_accvgpr_write_b32 a111, 0\n\t"
    "v_accvgpr_write_b32 a112, 0\n\t"
    "v_accvgpr_write_b32 a113, 0\n\t"
    "v_accvgpr_write_b32 a114, 0\n\t"
    "v_accvgpr_write_b32 a115, 0\n\t"
    "v_accvgpr_write_b32 a116, 0\n\t"
    "v_accvgpr_write_b32 a117, 0\n\t"
    "v_accvgpr_write_b32 a118, 0\n\t"
    "v_accvgpr_write_b32 a119, 0\n\t"
    "s_waitcnt vmcnt(46)\n\t"
    "s_barrier\n\t"
    "ds_read_b64_tr_b16 v[0:1], v204 offset:0\n\t"
    "ds_read_b64_tr_b16 v[2:3], v204 offset:6144\n\t"
    "ds_read_b128 v[56:59], v206 offset:0\n\t"
    "ds_read_b128 v[60:63], v206 offset:1024\n\t"
    "ds_read_b128 v[64:67], v206 offset:2048\n\t"
    "ds_read_b128 v[68:71], v206 offset:3072\n\t"
    "ds_read_b128 v[72:75], v206 offset:4096\n\t"
    "ds_read_b64_tr_b16 v[4:5], v205 offset:0\n\t"
    "ds_read_b64_tr_b16 v[6:7], v205 offset:6144\n\t"
    "ds_read_b64_tr_b16 v[8:9], v222 offset:0\n\t"
    "ds_read_b64_tr_b16 v[10:11], v222 offset:6144\n\t"
    "ds_read_b64_tr_b16 v[12:13], v223 offset:0\n\t"
    "ds_read_b64_tr_b16 v[14:15], v223 offset:6144\n\t"
    "ds_read_b64_tr_b16 v[16:17], v204 offset:128\n\t"
    "ds_read_b64_tr_b16 v[18:19], v204 offset:6272\n\t"
    "ds_read_b64_tr_b16 v[20:21], v205 offset:128\n\t"
    "ds_read_b64_tr_b16 v[22:23], v205 offset:6272\n\t"
    "s_waitcnt lgkmcnt(0)\n\t"
    "s_mov_b32 s44, 2\n\t"
    "s_getpc_b64 s[52:53]\n\t"
    ".Lo_pca0:\n\t"
    "s_add_u32 s52, s52, .Lo_reta0-.Lo_pca0\n\t"
    "s_addc_u32 s53, s53, 0\n\t"
    "s_branch .Lo_loopa\n\t"
    ".Lo_reta0:\n\t"
    "s_lshr_b32 s40, s78, 6\n\t"
    "s_add_u32 s40, s40, 4\n\t"
    "s_lshl_b64 exec, 1, s40\n\t"
    "global_store_dword v254, v255, s[60:61] sc1\n\t"
    "s_mov_b64 exec, -1\n\t"
    "s_mov_b32 s44, 17\n\t"
    "s_getpc_b64 s[52:53]\n\t"
    ".Lo_pca1:\n\t"
    "s_add_u32 s52, s52, .Lo_reta1-.Lo_pca1\n\t"
    "s_addc_u32 s53, s53, 0\n\t"
    "s_branch .Lo_loopa\n\t"
    ".Lo_reta1:\n\t"
    "global_load_dword v218, v254, s[60:61] sc1\n\t"
    "s_mov_b32 s44, 2\n\t"
    "s_getpc_b64 s[52:53]\n\t"
    ".Lo_pca2:\n\t"
    "s_add_u32 s52, s52, .Lo_reta2-.Lo_pca2\n\t"
    "s_addc_u32 s53, s53, 0\n\t"
    "s_branch .Lo_loopa\n\t"
    ".Lo_reta2:\n\t"
    "s_mov_b32 s41, 32768\n\t"
    ".Lo_polla1:\n\t"
    "v_cmp_ne_u32 vcc, v255, v218\n\t"
    "s_and_b32 s40, vcc_lo, 0x30\n\t"
    "s_cbranch_scc0 .Lo_oka1 ; scc-ok\n\t"
    "s_sleep 1\n\t"
    "global_load_dword v218, v254, s[60:61] sc1\n\t"
    "s_sub_u32 s41, s41, 1\n\t"
    "s_waitcnt vmcnt(0)\n\t"
    "s_cbranch_scc0 .Lo_polla1 ; scc-ok\n\t"
    ".Lo_oka1:\n\t"
    "s_mov_b32 s44, 10\n\t"
    "s_getpc_b64 s[52:53]\n\t"
    ".Lo_pca3:\n\t"
    "s_add_u32 s52, s52, .Lo_reta3-.Lo_pca3\n\t"
    "s_addc_u32 s53, s53, 0\n\t"
    "s_branch .Lo_loopa\n\t"
    ".Lo_reta3:\n\t"
    "global_load_dword v218, v254, s[60:61] sc1\n\t"
    "s_mov_b32 s44, 2\n\t"
    "s_getpc_b64 s[52:53]\n\t"
    ".Lo_pca4:\n\t"
    "s_add_u32 s52, s52, .Lo_reta4-.Lo_pca4\n\t"
    "s_addc_u32 s53, s53, 0\n\t"
    "s_branch .Lo_loopa\n\t"
    ".Lo_reta4:\n\t"
    "s_mov_b32 s41, 32768\n\t"
    ".Lo_polla2:\n\t"
    "v_cmp_ne_u32 vcc, v255, v218\n\t"
    "s_and_b32 s40, vcc_lo, 0xc0\n\t"
    "s_cbranch_scc0 .Lo_oka2 ; scc-ok\n\t"
    "s_sleep 1\n\t"
    "global_load_dword v218, v254, s[60:61] sc1\n\t"
    "s_sub_u32 s41, s41, 1\n\t"
    "s_waitcnt vmcnt(0)\n\t"
    "s_cbranch_scc0 .Lo_polla2 ; scc-ok\n\t"
    ".Lo_oka2:\n\t"
    "s_mov_b32 s44, 15\n\t"
    "s_getpc_b64 s[52:53]\n\t"
    ".Lo_pca5:\n\t"
    "s_add_u32 s52, s52, .Lo_reta5-.Lo_pca5\n\t"
    "s_addc_u32 s53, s53, 0\n\t"
    "s_branch .Lo_loopa\n\t"
    ".Lo_reta5:\n\t"
    "s_branch .Lo_donea\n\t"
    ".Lo_loopa:\n\t"
    "v_mfma_f32_16x16x32_bf16 a[0:3], v[0:3], v[56:59], a[0:3]\n\t"
    "s_add_u32 s30, s28, 45056\n\t"
    "ds_read_b64_tr_b16 v[28:29], v204 offset:12288\n\t"
    "v_mfma_f32_16x16x32_bf16 a[4:7], v[0:3], v[60:63], a[4:7]\n\t"
    "s_add_u32 m0, s58, 0\n\t"
    "ds_read_b64_tr_b16 v[30:31], v204 offset:18432\n\t"
    "v_mfma_f32_16x16x32_bf16 a[8:11], v[0:3], v[64:67], a[8:11]\n\t"
    "buffer_load_dwordx4 v207, s[8:11], s36 offen lds\n\t"
    "v_mfma_f32_16x16x32_bf16 a[12:15], v[0:3], v[68:71], a[12:15]\n\t"
    "s_cmp_eq_u32 s30, 135168\n\t"
    "s_cselect_b32 s30, 0, s30\n\t"
    "ds_read_b128 v[76:79], v206 offset:10240\n\t"
    "v_mfma_f32_16x16x32_bf16 a[16:19], v[0:3], v[72:75], a[16:19]\n\t"
    "s_sub_u32 s31, s30, s28\n\t"
    "ds_read_b128 v[80:83], v206 offset:11264\n\t"
    "v_mfma_f32_16x16x32_bf16 a[20:23], v[4:7], v[56:59], a[20:23]\n\t"
    "ds_read_b128 v[84:87], v206 offset:12288\n\t"
    "v_mfma_f32_16x16x32_bf16 a[24:27], v[4:7], v[60:63], a[24:27]\n\t"
    "buffer_load_dwordx4 v208, s[8:11], s36 offen offset:1024 lds\n\t"
    "v_mfma_f32_16x16x32_bf16 a[28:31], v[4:7], v[64:67], a[28:31]\n\t"
    "ds_read_b128 v[88:91], v206 offset:13312\n\t"
    "v_mfma_f32_16x16x32_bf16 a[32:35], v[4:7], v[68:71], a[32:35]\n\t"
    "ds_read_b128 v[92:95], v206 offset:14336\n\t"
    "v_mfma_f32_16x16x32_bf16 a[36:39], v[4:7], v[72:75], a[36:39]\n\t"
    "ds_read_b64_tr_b16 v[32:33], v205 offset:12288\n\t"
    "v_mfma_f32_16x16x32_bf16 a[40:43], v[8:11], v[56:59], a[40:43]\n\t"
    "ds_read_b64_tr_b16 v[34:35], v205 offset:18432\n\t"
    "v_mfma_f32_16x16x32_bf16 a[44:47], v[8:11], v[60:63], a[44:47]\n\t"
    "buffer_load_dwordx4 v209, s[8:11], s36 offen offset:2048 lds\n\t"
    "v_mfma_f32_16x16x32_bf16 a[48:51], v[8:11], v[64:67], a[48:51]\n\t"
    "ds_read_b64_tr_b16 v[36:37], v222 offset:12288\n\t"
    "v_mfma_f32_16x16x32_bf16 a[52:55], v[8:11], v[68:71], a[52:55]\n\t"
    "ds_read_b64_tr_b16 v[38:39], v222 offset:18432\n\t"
    "v_mfma_f32_16x16x32_bf16 a[56:59], v[8:11], v[72:75], a[56:59]\n\t"
    "ds_read_b64_tr_b16 v[40:41], v223 offset:12288\n\t"
    "v_mfma_f32_16x16x32_bf16 a[60:63], v[12:15], v[56:59], a[60:63]\n\t"
    "buffer_load_dwordx4 v210, s[8:11], s36 offen offset:3072 lds\n\t"
    "v_mfma_f32_16x16x32_bf16 a[64:67], v[12:15], v[60:63], a[64:67]\n\t"
    "ds_read_b64_tr_b16 v[42:43], v223 offset:18432\n\t"
    "v_mfma_f32_16x16x32_bf16 a[68:71], v[12:15], v[64:67], a[68:71]\n\t"
    "ds_read_b64_tr_b16 v[44:45], v204 offset:12416\n\t"
    "v_mfma_f32_16x16x32_bf16 a[72:75], v[12:15], v[68:71], a[72:75]\n\t"
    "ds_read_b64_tr_b16 v[46:47], v204 offset:18560\n\t"
    "v_mfma_f32_16x16x32_bf16 a[76:79], v[12:15], v[72:75], a[76:79]\n\t"
    "s_add_u32 m0, s58, 4096\n\t"
    "ds_read_b64_tr_b16 v[48:49], v205 offset:12416\n\t"
    "v_mfma_f32_16x16x32_bf16 a[80:83], v[16:19], v[56:59], a[80:83]\n\t"
    "buffer_load_dwordx4 v211, s[8:11], s36 offen lds\n\t"
    "v_mfma_f32_16x16x32_bf16 a[84:87], v[16:19], v[60:63], a[84:87]\n\t"
    "ds_read_b64_tr_b16 v[50:51], v205 offset:18560\n\t"
    "v_mfma_f32_16x16x32_bf16 a[88:91], v[16:19], v[64:67], a[88:91]\n\t"
    "v_mfma_f32_16x16x32_bf16 a[92:95], v[16:19], v[68:71], a[92:95]\n\t"
    "v_mfma_f32_16x16x32_bf16 a[96:99], v[16:19], v[72:75], a[96:99]\n\t"
    "buffer_load_dwordx4 v212, s[8:11], s36 offen offset:1024 lds\n\t"
    "v_mfma_f32_16x16x32_bf16 a[100:103], v[20:23], v[56:59], a[100:103]\n\t"
    "s_add_u32 s36, s36, 81920\n\t"
    "v_mfma_f32_16x16x32_bf16 a[104:107], v[20:23], v[60:63], a[104:107]\n\t"
    "v_mfma_f32_16x16x32_bf16 a[108:111], v[20:23], v[64:67], a[108:111]\n\t"
    "v_mfma_f32_16x16x32_bf16 a[112:115], v[20:23], v[68:71], a[112:115]\n\t"
    "v_add_u32 v204, s31, v204\n\t"
    "v_add_u32 v205, s31, v205\n\t"
    "v_add_u32 v222, s31, v222\n\t"
    "v_add_u32 v223, s31, v223\n\t"
    "v_add_u32 v206, s31, v206\n\t"
    "v_mfma_f32_16x16x32_bf16 a[116:119], v[20:23], v[72:75], a[116:119]\n\t"
    "s_waitcnt vmcnt(11) lgkmcnt(0)\n\t"
    "s_barrier\n\t"
    "v_mfma_f32_16x16x32_bf16 a[0:3], v[28:31], v[76:79], a[0:3]\n\t"
    "s_add_u32 s58, s28, s34\n\t"
    "s_add_u32 s59, s28, s35\n\t"
    "s_mov_b32 s28, s30\n\t"
    "ds_read_b64_tr_b16 v[0:1], v204 offset:0\n\t"
    "v_mfma_f32_16x16x32_bf16 a[4:7], v[28:31], v[80:83], a[4:7]\n\t"
    "s_add_u32 m0, s59, 0\n\t"
    "ds_read_b64_tr_b16 v[2:3], v204 offset:6144\n\t"
    "v_mfma_f32_16x16x32_bf16 a[8:11], v[28:31], v[84:87], a[8:11]\n\t"
    "buffer_load_dwordx4 v214, s[12:15], s37 offen sc1 lds\n\t"
    "v_mfma_f32_16x16x32_bf16 a[12:15], v[28:31], v[88:91], a[12:15]\n\t"
    "ds_read_b128 v[56:59], v206 offset:0\n\t"
    "v_mfma_f32_16x16x32_bf16 a[16:19], v[28:31], v[92:95], a[16:19]\n\t"
    "ds_read_b128 v[60:63], v206 offset:1024\n\t"
    "v_mfma_f32_16x16x32_bf16 a[20:23], v[32:35], v[76:79], a[20:23]\n\t"
    "ds_read_b128 v[64:67], v206 offset:2048\n\t"
    "v_mfma_f32_16x16x32_bf16 a[24:27], v[32:35], v[80:83], a[24:27]\n\t"
    "ds_read_b128 v[68:71], v206 offset:3072\n\t"
    "v_mfma_f32_16x16x32_bf16 a[28:31], v[32:35], v[84:87], a[28:31]\n\t"
    "buffer_load_dwordx4 v214, s[12:15], s37 offen offset:1024 sc1 lds\n\t"
    "v_mfma_f32_16x16x32_bf16 a[32:35], v[32:35], v[88:91], a[32:35]\n\t"
    "ds_read_b128 v[72:75], v206 offset:4096\n\t"
    "v_mfma_f32_16x16x32_bf16 a[36:39], v[32:35], v[92:95], a[36:39]\n\t"
    "ds_read_b64_tr_b16 v[4:5], v205 offset:0\n\t"
    "v_mfma_f32_16x16x32_bf16 a[40:43], v[36:39], v[76:79], a[40:43]\n\t"
    "ds_read_b64_tr_b16 v[6:7], v205 offset:6144\n\t"
    "v_mfma_f32_16x16x32_bf16 a[44:47], v[36:39], v[80:83], a[44:47]\n\t"
    "ds_read_b64_tr_b16 v[8:9], v222 offset:0\n\t"
    "v_mfma_f32_16x16x32_bf16 a[48:51], v[36:39], v[84:87], a[48:51]\n\t"
    "buffer_load_dwordx4 v214, s[12:15], s37 offen offset:2048 sc1 lds\n\t"
    "v_mfma_f32_16x16x32_bf16 a[52:55], v[36:39], v[88:91], a[52:55]\n\t"
    "ds_read_b64_tr_b16 v[10:11], v222 offset:6144\n\t"
    "v_mfma_f32_16x16x32_bf16 a[56:59], v[36:39], v[92:95], a[56:59]\n\t"
    "ds_read_b64_tr_b16 v[12:13], v223 offset:0\n\t"
    "v_mfma_f32_16x16x32_bf16 a[60:63], v[40:43], v[76:79], a[60:63]\n\t"
    "ds_read_b64_tr_b16 v[14:15], v223 offset:6144\n\t"
    "v_mfma_f32_16x16x32_bf16 a[64:67], v[40:43], v[80:83], a[64:67]\n\t"
    "ds_read_b64_tr_b16 v[16:17], v204 offset:128\n\t"
    "v_mfma_f32_16x16x32_bf16 a[68:71], v[40:43], v[84:87], a[68:71]\n\t"
    "ds_read_b64_tr_b16 v[18:19], v204 offset:6272\n\t"
    "v_mfma_f32_16x16x32_bf16 a[72:75], v[40:43], v[88:91], a[72:75]\n\t"
    "buffer_load_dwordx4 v214, s[12:15], s37 offen offset:3072 sc1 lds\n\t"
    "v_mfma_f32_16x16x32_bf16 a[76:79], v[40:43], v[92:95], a[76:79]\n\t"
    "ds_read_b64_tr_b16 v[20:21], v205 offset:128\n\t"
    "v_mfma_f32_16x16x32_bf16 a[80:83], v[44:47], v[76:79], a[80:83]\n\t"
    "ds_read_b64_tr_b16 v[22:23], v205 offset:6272\n\t"
    "v_mfma_f32_16x16x32_bf16 a[84:87], v[44:47], v[80:83], a[84:87]\n\t"
    "v_mfma_f32_16x16x32_bf16 a[88:91], v[44:47], v[84:87], a[88:91]\n\t"
    "s_add_u32 m0, s59, 4096\n\t"
    "s_add_u32 s40, s37, 4096\n\t"
    "v_mfma_f32_16x16x32_bf16 a[92:95], v[44:47], v[88:91], a[92:95]\n\t"
    "buffer_load_dwordx4 v214, s[12:15], s40 offen sc1 lds\n\t"
    "v_mfma_f32_16x16x32_bf16 a[96:99], v[44:47], v[92:95], a[96:99]\n\t"
    "s_add_u32 s37, s37, 20480\n\t"
    "v_mfma_f32_16x16x32_bf16 a[100:103], v[48:51], v[76:79], a[100:103]\n\t"
    "v_mfma_f32_16x16x32_bf16 a[104:107], v[48:51], v[80:83], a[104:107]\n\t"
    "v_mfma_f32_16x16x32_bf16 a[108:111], v[48:51], v[84:87], a[108:111]\n\t"
    "v_mfma_f32_16x16x32_bf16 a[112:115], v[48:51], v[88:91], a[112:115]\n\t"
    "v_mfma_f32_16x16x32_bf16 a[116:119], v[48:51], v[92:95], a[116:119]\n\t"
    "s_waitcnt lgkmcnt(0)\n\t"
    "s_sub_u32 s44, s44, 1\n\t"
    "s_cmp_lg_u32 s44, 0\n\t"
    "s_cbranch_scc1 .Lo_loopa\n\t"
    "s_setpc_b64 s[52:53]\n\t"
    ".Lo_donea:\n\t"
    "s_lshr_b32 s40, s78, 1\n\t"
    "s_lshl_b32 s40, s40, 2\n\t"
    "s_add_u32 s40, s40, s7\n\t"
    "s_mul_i32 s40, s40, 35840\n\t"
    "s_add_u32 s40, s40, 62914560\n\t"
    "s_add_u32 s16, s68, s40\n\t"
    "s_addc_u32 s17, s69, 0\n\t"
    "s_mov_b32 s18, 35840\n\t"
    "s_and_b32 s41, s78, 1\n\t"
    "s_lshl_b32 s41, s41, 1\n\t"
    "s_add_u32 s44, s41, s38\n\t"
    "s_cmp_eq_u32 s44, 1\n\t"
    "s_cbranch_scc1 .Lo_snda1\n\t"
    "s_cmp_eq_u32 s44, 2\n\t"
    "s_cbranch_scc1 .Lo_snda2\n\t"
    "s_cmp_eq_u32 s44, 3\n\t"
    "s_cbranch_scc1 .Lo_snda3\n\t"
    "s_mov_b32 s40, 3072\n\t"
    "buffer_store_dwordx4 a[12:15], v214, s[16:19], s40 offen sc1\n\t"
    "s_mov_b32 s40, 4096\n\t"
    "buffer_store_dwordx4 a[16:19], v214, s[16:19], s40 offen sc1\n\t"
    "s_mov_b32 s40, 8192\n\t"
    "buffer_store_dwordx4 a[32:35], v214, s[16:19], s40 offen sc1\n\t"
    "s_mov_b32 s40, 9216\n\t"
    "buffer_store_dwordx4 a[36:39], v214, s[16:19], s40 offen sc1\n\t"
    "s_mov_b32 s40, 13312\n\t"
    "buffer_store_dwordx4 a[52:55], v214, s[16:19], s40 offen sc1\n\t"
    "s_mov_b32 s40, 14336\n\t"
    "buffer_store_dwordx4 a[56:59], v214, s[16:19], s40 offen sc1\n\t"
    "s_mov_b32 s40, 18432\n\t"
    "buffer_store_dwordx4 a[72:75], v214, s[16:19], s40 offen sc1\n\t"
    "s_mov_b32 s40, 19456\n\t"
    "buffer_store_dwordx4 a[76:79], v214, s[16:19], s40 offen sc1\n\t"
    "s_mov_b32 s40, 23552\n\t"
    "buffer_store_dwordx4 a[92:95], v214, s[16:19], s40 offen sc1\n\t"
    "s_mov_b32 s40, 24576\n\t"
    "buffer_store_dwordx4 a[96:99], v214, s[16:19], s40 offen sc1\n\t"
    "s_mov_b32 s40, 28672\n\t"
    "buffer_store_dwordx4 a[112:115], v214, s[16:19], s40 offen sc1\n\t"
    "s_mov_b32 s40, 29696\n\t"
    "buffer_store_dwordx4 a[116:119], v214, s[16:19], s40 offen sc1\n\t"
    "s_branch .Lo_senta\n\t"
    ".Lo_snda1:\n\t"
    "s_mov_b32 s40, 2048\n\t"
    "buffer_store_dwordx4 a[8:11], v214, s[16:19], s40 offen sc1\n\t"
    "s_mov_b32 s40, 3072\n\t"
    "buffer_store_dwordx4 a[12:15], v214, s[16:19], s40 offen sc1\n\t"
    "s_mov_b32 s40, 4096\n\t"
    "buffer_store_dwordx4 a[16:19], v214, s[16:19], s40 offen sc1\n\t"
    "s_mov_b32 s40, 7168\n\t"
    "buffer_store_dwordx4 a[28:31], v214, s[16:19], s40 offen sc1\n\t"
    "s_mov_b32 s40, 8192\n\t"
    "buffer_store_dwordx4 a[32:35], v214, s[16:19], s40 offen sc1\n\t"
    "s_mov_b32 s40, 9216\n\t"
    "buffer_store_dwordx4 a[36:39], v214, s[16:19], s40 offen sc1\n\t"
    "s_mov_b32 s40, 12288\n\t"
    "buffer_store_dwordx4 a[48:51], v214, s[16:19], s40 offen sc1\n\t"
    "s_mov_b32 s40, 13312\n\t"
    "buffer_store_dwordx4 a[52:55], v214, s[16:19], s40 offen sc1\n\t"
    "s_mov_b32 s40, 14336\n\t"
    "buffer_store_dwordx4 a[56:59], v214, s[16:19], s40 offen sc1\n\t"
    "s_mov_b32 s40, 17408\n\t"
    "buffer_store_dwordx4 a[68:71], v214, s[16:19], s40 offen sc1\n\t"
    "s_mov_b32 s40, 18432\n\t"
    "buffer_store_dwordx4 a[72:75], v214, s[16:19], s40 offen sc1\n\t"
    "s_mov_b32 s40, 19456\n\t"
    "buffer_store_dwordx4 a[76:79], v214, s[16:19], s40 offen sc1\n\t"
    "s_mov_b32 s40, 22528\n\t"
    "buffer_store_dwordx4 a[88:91], v214, s[16:19], s40 offen sc1\n\t"
    "s_mov_b32 s40, 23552\n\t"
    "buffer_store_dwordx4 a[92:95], v214, s[16:19], s40 offen sc1\n\t"
    "s_mov_b32 s40, 24576\n\t"
    "buffer_store_dwordx4 a[96:99], v214, s[16:19], s40 offen sc1\n\t"
    "s_mov_b32 s40, 27648\n\t"
    "buffer_store_dwordx4 a[108:111], v214, s[16:19], s40 offen sc1\n\t"
    "s_mov_b32 s40, 28672\n\t"
    "buffer_store_dwordx4 a[112:115], v214, s[16:19], s40 offen sc1\n\t"
    "s_mov_b32 s40, 29696\n\t"
    "buffer_store_dwordx4 a[116:119], v214, s[16:19], s40 offen sc1\n\t"
    "s_branch .Lo_senta\n\t"
    ".Lo_snda2:\n\t"
    "s_mov_b32 s40, 0\n\t"
    "buffer_store_dwordx4 a[0:3], v214, s[16:19], s40 offen sc1\n\t"
    "s_mov_b32 s40, 1024\n\t"
    "buffer_store_dwordx4 a[4:7], v214, s[16:19], s40 offen sc1\n\t"
    "s_mov_b32 s40, 2048\n\t"
    "buffer_store_dwordx4 a[8:11], v214, s[16:19], s40 offen sc1\n\t"
    "s_mov_b32 s40, 5120\n\t"
    "buffer_store_dwordx4 a[20:23], v214, s[16:19], s40 offen sc1\n\t"
    "s_mov_b32 s40, 6144\n\t"
    "buffer_store_dwordx4 a[24:27], v214, s[16:19], s40 offen sc1\n\t"
    "s_mov_b32 s40, 7168\n\t"
    "buffer_store_dwordx4 a[28:31], v214, s[16:19], s40 offen sc1\n\t"
    "s_mov_b32 s40, 10240\n\t"
    "buffer_store_dwordx4 a[40:43], v214, s[16:19], s40 offen sc1\n\t"
    "s_mov_b32 s40, 11264\n\t"
    "buffer_store_dwordx4 a[44:47], v214, s[16:19], s40 offen sc1\n\t"
    "s_mov_b32 s40, 12288\n\t"
    "buffer_store_dwordx4 a[48:51], v214, s[16:19], s40 offen sc1\n\t"
    "s_mov_b32 s40, 15360\n\t"
    "buffer_store_dwordx4 a[60:63], v214, s[16:19], s40 offen sc1\n\t"
    "s_mov_b32 s40, 16384\n\t"
    "buffer_store_dwordx4 a[64:67], v214, s[16:19], s40 offen sc1\n\t"
    "s_mov_b32 s40, 17408\n\t"
    "buffer_store_dwordx4 a[68:71], v214, s[16:19], s40 offen sc1\n\t"
    "s_mov_b32 s40, 20480\n\t"
    "buffer_store_dwordx4 a[80:83], v214, s[16:19], s40 offen sc1\n\t"
    "s_mov_b32 s40, 21504\n\t"
    "buffer_store_dwordx4 a[84:87], v214, s[16:19], s40 offen sc1\n\t"
    "s_mov_b32 s40, 22528\n\t"
    "buffer_store_dwordx4 a[88:91], v214, s[16:19], s40 offen sc1\n\t"
    "s_mov_b32 s40, 25600\n\t"
    "buffer_store_dwordx4 a[100:103], v214, s[16:19], s40 offen sc1\n\t"
    "s_mov_b32 s40, 26624\n\t"
    "buffer_store_dwordx4 a[104:107], v214, s[16:19], s40 offen sc1\n\t"
    "s_mov_b32 s40, 27648\n\t"
    "buffer_store_dwordx4 a[108:111], v214, s[16:19], s40 offen sc1\n\t"
    "s_branch .Lo_senta\n\t"
    ".Lo_snda3:\n\t"
    "s_mov_b32 s40, 0\n\t"
    "buffer_store_dwordx4 a[0:3], v214, s[16:19], s40 offen sc1\n\t"
    "s_mov_b32 s40, 1024\n\t"
    "buffer_store_dwordx4 a[4:7], v214, s[16:19], s40 offen sc1\n\t"
    "s_mov_b32 s40, 5120\n\t"
    "buffer_store_dwordx4 a[20:23], v214, s[16:19], s40 offen sc1\n\t"
    "s_mov_b32 s40, 6144\n\t"
    "buffer_store_dwordx4 a[24:27], v214, s[16:19], s40 offen sc1\n\t"
    "s_mov_b32 s40, 10240\n\t"
    "buffer_store_dwordx4 a[40:43], v214, s[16:19], s40 offen sc1\n\t"
    "s_mov_b32 s40, 11264\n\t"
    "buffer_store_dwordx4 a[44:47], v214, s[16:19], s40 offen sc1\n\t"
    "s_mov_b32 s40, 15360\n\t"
    "buffer_store_dwordx4 a[60:63], v214, s[16:19], s40 offen sc1\n\t"
    "s_mov_b32 s40, 16384\n\t"
    "buffer_store_dwordx4 a[64:67], v214, s[16:19], s40 offen sc1\n\t"
    "s_mov_b32 s40, 20480\n\t"
    "buffer_store_dwordx4 a[80:83], v214, s[16:19], s40 offen sc1\n\t"
    "s_mov_b32 s40, 21504\n\t"
    "buffer_store_dwordx4 a[84:87], v214, s[16:19], s40 offen sc1\n\t"
    "s_mov_b32 s40, 25600\n\t"
    "buffer_store_dwordx4 a[100:103], v214, s[16:19], s40 offen sc1\n\t"
    "s_mov_b32 s40, 26624\n\t"
    "buffer_store_dwordx4 a[104:107], v214, s[16:19], s40 offen sc1\n\t"
    ".Lo_senta:\n\t"
    "s_mul_i32 s40, s70, 409600\n\t"
    "s_mul_i32 s41, s6, 1280\n\t"
    "s_add_u32 s40, s40, s41\n\t"
    "s_add_u32 s40, s40, s20\n\t"
    "s_add_u32 s20, s4, s40\n\t"
    "s_addc_u32 s21, s5, 0\n\t"
    "s_mov_b32 s22, 407424\n\t"
    "s_bitcmp0_b32 s78, 0\n\t"
    "s_cselect_b32 s40, 48, 32\n\t"
    "s_cselect_b32 s42, 0, 48\n\t"
    "s_mul_i32 s41, s40, s38\n\t"
    "v_lshrrev_b32 v115, 4, v214\n\t"
    "v_and_b32 v116, 15, v115\n\t"
    "v_add_u32 v116, s41, v116\n\t"
    "v_mul_u32_u24 v116, 400, v116\n\t"
    "v_lshrrev_b32 v117, 4, v115\n\t"
    "v_lshl_add_u32 v116, v117, 3, v116\n\t"
    "s_mul_i32 s41, s39, 192\n\t"
    "v_add_u32 v96, s41, v116\n\t"
    "s_lshl_b32 s41, s7, 6\n\t"
    "v_add_u32 v115, s41, v115\n\t"
    "v_add_u32 v116, 0, v115\n\t"
    "v_mul_u32_u24 v117, 2731, v116\n\t"
    "v_lshrrev_b32 v117, 16, v117\n\t"
    "v_mul_u32_u24 v118, 24, v117\n\t"
    "v_sub_u32 v118, v116, v118\n\t"
    "v_lshlrev_b32 v118, 4, v118\n\t"
    "v_mul_u32_u24 v119, 400, v117\n\t"
    "v_add_u32 v97, v119, v118\n\t"
    "v_cmp_le_u32 vcc, s40, v117\n\t"
    "v_add_u32 v117, s42, v117\n\t"
    "v_add_u32 v119, 32, v117\n\t"
    "v_cndmask_b32 v117, v117, v119, vcc\n\t"
    "v_mul_u32_u24 v117, 2560, v117\n\t"
    "v_add_u32 v106, v117, v118\n\t"
    "v_add_u32 v116, 256, v115\n\t"
    "v_mul_u32_u24 v117, 2731, v116\n\t"
    "v_lshrrev_b32 v117, 16, v117\n\t"
    "v_mul_u32_u24 v118, 24, v117\n\t"
    "v_sub_u32 v118, v116, v118\n\t"
    "v_lshlrev_b32 v118, 4, v118\n\t"
    "v_mul_u32_u24 v119, 400, v117\n\t"
    "v_add_u32 v98, v119, v118\n\t"
    "v_cmp_le_u32 vcc, s40, v117\n\t"
    "v_add_u32 v117, s42, v117\n\t"
    "v_add_u32 v119, 32, v117\n\t"
    "v_cndmask_b32 v117, v117, v119, vcc\n\t"
    "v_mul_u32_u24 v117, 2560, v117\n\t"
    "v_add_u32 v107, v117, v118\n\t"
    "v_add_u32 v116, 512, v115\n\t"
    "v_mul_u32_u24 v117, 2731, v116\n\t"
    "v_lshrrev_b32 v117, 16, v117\n\t"
    "v_mul_u32_u24 v118, 24, v117\n\t"
    "v_sub_u32 v118, v116, v118\n\t"
    "v_lshlrev_b32 v118, 4, v118\n\t"
    "v_mul_u32_u24 v119, 400, v117\n\t"
    "v_add_u32 v99, v119, v118\n\t"
    "v_cmp_le_u32 vcc, s40, v117\n\t"
    "v_add_u32 v117, s42, v117\n\t"
    "v_add_u32 v119, 32, v117\n\t"
    "v_cndmask_b32 v117, v117, v119, vcc\n\t"
    "v_mul_u32_u24 v117, 2560, v117\n\t"
    "v_add_u32 v108, v117, v118\n\t"
    "v_add_u32 v116, 768, v115\n\t"
    "v_mul_u32_u24 v117, 2731, v116\n\t"
    "v_lshrrev_b32 v117, 16, v117\n\t"
    "v_mul_u32_u24 v118, 24, v117\n\t"
    "v_sub_u32 v118, v116, v118\n\t"
    "v_lshlrev_b32 v118, 4, v118\n\t"
    "v_mul_u32_u24 v119, 400, v117\n\t"
    "v_add_u32 v100, v119, v118\n\t"
    "v_cmp_le_u32 vcc, s40, v117\n\t"
    "v_add_u32 v117, s42, v117\n\t"
    "v_add_u32 v119, 32, v117\n\t"
    "v_cndmask_b32 v117, v117, v119, vcc\n\t"
    "v_mul_u32_u24 v117, 2560, v117\n\t"
    "v_add_u32 v109, v117, v118\n\t"
    "v_add_u32 v116, 1024, v115\n\t"
    "v_mul_u32_u24 v117, 2731, v116\n\t"
    "v_lshrrev_b32 v117, 16, v117\n\t"
    "v_mul_u32_u24 v118, 24, v117\n\t"
    "v_sub_u32 v118, v116, v118\n\t"
    "v_lshlrev_b32 v118, 4, v118\n\t"
    "v_mul_u32_u24 v119, 400, v117\n\t"
    "v_add_u32 v101, v119, v118\n\t"
    "v_cmp_le_u32 vcc, s40, v117\n\t"
    "v_add_u32 v117, s42, v117\n\t"
    "v_add_u32 v119, 32, v117\n\t"
    "v_cndmask_b32 v117, v117, v119, vcc\n\t"
    "v_mul_u32_u24 v117, 2560, v117\n\t"
    "v_add_u32 v110, v117, v118\n\t"
    "v_add_u32 v116, 1280, v115\n\t"
    "v_mul_u32_u24 v117, 2731, v116\n\t"
    "v_lshrrev_b32 v117, 16, v117\n\t"
    "v_mul_u32_u24 v118, 24, v117\n\t"
    "v_sub_u32 v118, v116, v118\n\t"
    "v_lshlrev_b32 v118, 4, v118\n\t"
    "v_mul_u32_u24 v119, 400, v117\n\t"
    "v_add_u32 v102, v119, v118\n\t"
    "v_cmp_le_u32 vcc, s40, v117\n\t"
    "v_add_u32 v117, s42, v117\n\t"
    "v_add_u32 v119, 32, v117\n\t"
    "v_cndmask_b32 v117, v117, v119, vcc\n\t"
    "v_mul_u32_u24 v117, 2560, v117\n\t"
    "v_add_u32 v111, v117, v118\n\t"
    "v_add_u32 v116, 1536, v115\n\t"
    "v_mul_u32_u24 v117, 2731, v116\n\t"
    "v_lshrrev_b32 v117, 16, v117\n\t"
    "v_mul_u32_u24 v118, 24, v117\n\t"
    "v_sub_u32 v118, v116, v118\n\t"
    "v_lshlrev_b32 v118, 4, v118\n\t"
    "v_mul_u32_u24 v119, 400, v117\n\t"
    "v_add_u32 v103, v119, v118\n\t"
    "v_cmp_le_u32 vcc, s40, v117\n\t"
    "v_add_u32 v117, s42, v117\n\t"
    "v_add_u32 v119, 32, v117\n\t"
    "v_cndmask_b32 v117, v117, v119, vcc\n\t"
    "v_mul_u32_u24 v117, 2560, v117\n\t"
    "v_add_u32 v112, v117, v118\n\t"
    "v_add_u32 v116, 1792, v115\n\t"
    "v_mul_u32_u24 v117, 2731, v116\n\t"
    "v_lshrrev_b32 v117, 16, v117\n\t"
    "v_mul_u32_u24 v118, 24, v117\n\t"
    "v_sub_u32 v118, v116, v118\n\t"
    "v_lshlrev_b32 v118, 4, v118\n\t"
    "v_mul_u32_u24 v119, 400, v117\n\t"
    "v_add_u32 v104, v119, v118\n\t"
    "v_cmp_le_u32 vcc, s40, v117\n\t"
    "v_add_u32 v117, s42, v117\n\t"
    "v_add_u32 v119, 32, v117\n\t"
    "v_cndmask_b32 v117, v117, v119, vcc\n\t"
    "v_mul_u32_u24 v117, 2560, v117\n\t"
    "v_add_u32 v113, v117, v118\n\t"
    "v_cmp_gt_u32 vcc, 1920, v116\n\t"
    "v_mov_b32 v119, 0x7fffffff\n\t"
    "s_nop 0\n\t"
    "v_cndmask_b32 v113, v119, v113, vcc\n\t"
    "s_waitcnt vmcnt(0) lgkmcnt(0)\n\t"
    "s_barrier\n\t"
    "s_mov_b64 exec, 0x400\n\t"
    "global_store_dword v254, v255, s[60:61] sc1\n\t"
    "s_mov_b64 exec, -1\n\t"
    "global_load_dword v218, v254, s[60:61] sc1\n\t"
    "s_waitcnt vmcnt(0)\n\t"
    "s_mov_b32 s41, 32768\n\t"
    ".Lo_pollax:\n\t"
    "v_cmp_ne_u32 vcc, v255, v218\n\t"
    "s_and_b32 s40, vcc_lo, 0x100\n\t"
    "s_cbranch_scc0 .Lo_okax ; scc-ok\n\t"
    "s_sleep 1\n\t"
    "global_load_dword v218, v254, s[60:61] sc1\n\t"
    "s_sub_u32 s41, s41, 1\n\t"
    "s_waitcnt vmcnt(0)\n\t"
    "s_cbranch_scc0 .Lo_pollax ; scc-ok\n\t"
    ".Lo_okax:\n\t"
    "s_cmp_eq_u32 s44, 1\n\t"
    "s_cbranch_scc1 .Lo_rcva1\n\t"
    "s_cmp_eq_u32 s44, 2\n\t"
    "s_cbranch_scc1 .Lo_rcva2\n\t"
    "s_cmp_eq_u32 s44, 3\n\t"
    "s_cbranch_scc1 .Lo_rcva3\n\t"
    "s_mov_b32 s40, 0\n\t"
    "buffer_load_dwordx4 v[124:127], v214, s[16:19], s40 offen sc1\n\t"
    "s_mov_b32 s40, 5120\n\t"
    "buffer_load_dwordx4 v[128:131], v214, s[16:19], s40 offen sc1\n\t"
    "s_mov_b32 s40, 10240\n\t"
    "buffer_load_dwordx4 v[132:135], v214, s[16:19], s40 offen sc1\n\t"
    "s_mov_b32 s40, 15360\n\t"
    "buffer_load_dwordx4 v[136:139], v214, s[16:19], s40 offen sc1\n\t"
    "s_mov_b32 s40, 20480\n\t"
    "buffer_load_dwordx4 v[140:143], v214, s[16:19], s40 offen sc1\n\t"
    "s_mov_b32 s40, 25600\n\t"
    "buffer_load_dwordx4 v[144:147], v214, s[16:19], s40 offen sc1\n\t"
    "s_mov_b32 s40, 1024\n\t"
    "buffer_load_dwordx4 v[148:151], v214, s[16:19], s40 offen sc1\n\t"
    "s_mov_b32 s40, 6144\n\t"
    "buffer_load_dwordx4 v[152:155], v214, s[16:19], s40 offen sc1\n\t"
    "s_mov_b32 s40, 11264\n\t"
    "buffer_load_dwordx4 v[156:159], v214, s[16:19], s40 offen sc1\n\t"
    "s_mov_b32 s40, 16384\n\t"
    "buffer_load_dwordx4 v[160:163], v214, s[16:19], s40 offen sc1\n\t"
    "s_mov_b32 s40, 21504\n\t"
    "buffer_load_dwordx4 v[164:167], v214, s[16:19], s40 offen sc1\n\t"
    "s_mov_b32 s40, 26624\n\t"
    "buffer_load_dwordx4 v[168:171], v214, s[16:19], s40 offen sc1\n\t"
    "s_mov_b32 s40, 2048\n\t"
    "buffer_load_dwordx4 v[172:175], v214, s[16:19], s40 offen sc1\n\t"
    "s_mov_b32 s40, 7168\n\t"
    "buffer_load_dwordx4 v[176:179], v214, s[16:19], s40 offen sc1\n\t"
    "s_mov_b32 s40, 12288\n\t"
    "buffer_load_dwordx4 v[180:183], v214, s[16:19], s40 offen sc1\n\t"
    "s_mov_b32 s40, 17408\n\t"
    "buffer_load_dwordx4 v[184:187], v214, s[16:19], s40 offen sc1\n\t"
    "s_mov_b32 s40, 22528\n\t"
    "buffer_load_dwordx4 v[188:191], v214, s[16:19], s40 offen sc1\n\t"
    "s_mov_b32 s40, 27648\n\t"
    "buffer_load_dwordx4 v[192:195], v214, s[16:19], s40 offen sc1\n\t"
    "v_accvgpr_read_b32 v0, a0\n\t"
    "v_accvgpr_read_b32 v1, a1\n\t"
    "v_accvgpr_read_b32 v2, a2\n\t"
    "v_accvgpr_read_b32 v3, a3\n\t"
    "s_waitcnt vmcnt(17)\n\t"
    "v_pk_add_f32 v[0:1], v[0:1], v[124:125]\n\t"
    "v_pk_add_f32 v[2:3], v[2:3], v[126:127]\n\t"
    "v_cvt_pk_bf16_f32 v0, v0, v1\n\t"
    "v_cvt_pk_bf16_f32 v1, v2, v3\n\t"
    "ds_write_b64 v96, v[0:1] offset:0\n\t"
    "v_accvgpr_read_b32 v4, a20\n\t"
    "v_accvgpr_read_b32 v5, a21\n\t"
    "v_accvgpr_read_b32 v6, a22\n\t"
    "v_accvgpr_read_b32 v7, a23\n\t"
    "s_waitcnt vmcnt(16)\n\t"
    "v_pk_add_f32 v[4:5], v[4:5], v[128:129]\n\t"
    "v_pk_add_f32 v[6:7], v[6:7], v[130:131]\n\t"
    "v_cvt_pk_bf16_f32 v4, v4, v5\n\t"
    "v_cvt_pk_bf16_f32 v5, v6, v7\n\t"
    "ds_write_b64 v96, v[4:5] offset:32\n\t"
    "v_accvgpr_read_b32 v8, a40\n\t"
    "v_accvgpr_read_b32 v9, a41\n\t"
    "v_accvgpr_read_b32 v10, a42\n\t"
    "v_accvgpr_read_b32 v11, a43\n\t"
    "s_waitcnt vmcnt(15)\n\t"
    "v_pk_add_f32 v[8:9], v[8:9], v[132:133]\n\t"
    "v_pk_add_f32 v[10:11], v[10:11], v[134:135]\n\t"
    "v_cvt_pk_bf16_f32 v8, v8, v9\n\t"
    "v_cvt_pk_bf16_f32 v9, v10, v11\n\t"
    "ds_write_b64 v96, v[8:9] offset:64\n\t"
    "v_accvgpr_read_b32 v12, a60\n\t"
    "v_accvgpr_read_b32 v13, a61\n\t"
    "v_accvgpr_read_b32 v14, a62\n\t"
    "v_accvgpr_read_b32 v15, a63\n\t"
    "s_waitcnt vmcnt(14)\n\t"
    "v_pk_add_f32 v[12:13], v[12:13], v[136:137]\n\t"
    "v_pk_add_f32 v[14:15], v[14:15], v[138:139]\n\t"
    "v_cvt_pk_bf16_f32 v12, v12, v13\n\t"
    "v_cvt_pk_bf16_f32 v13, v14, v15\n\t"
    "ds_write_b64 v96, v[12:13] offset:96\n\t"
    "v_accvgpr_read_b32 v16, a80\n\t"
    "v_accvgpr_read_b32 v17, a81\n\t"
    "v_accvgpr_read_b32 v18, a82\n\t"
    "v_accvgpr_read_b32 v19, a83\n\t"
    "s_waitcnt vmcnt(13)\n\t"
    "v_pk_add_f32 v[16:17], v[16:17], v[140:141]\n\t"
    "v_pk_add_f32 v[18:19], v[18:19], v[142:143]\n\t"
    "v_cvt_pk_bf16_f32 v16, v16, v17\n\t"
    "v_cvt_pk_bf16_f32 v17, v18, v19\n\t"
    "ds_write_b64 v96, v[16:17] offset:128\n\t"
    "v_accvgpr_read_b32 v20, a100\n\t"
    "v_accvgpr_read_b32 v21, a101\n\t"
    "v_accvgpr_read_b32 v22, a102\n\t"
    "v_accvgpr_read_b32 v23, a103\n\t"
    "s_waitcnt vmcnt(12)\n\t"
    "v_pk_add_f32 v[20:21], v[20:21], v[144:145]\n\t"
    "v_pk_add_f32 v[22:23], v[22:23], v[146:147]\n\t"
    "v_cvt_pk_bf16_f32 v20, v20, v21\n\t"
    "v_cvt_pk_bf16_f32 v21, v22, v23\n\t"
    "ds_write_b64 v96, v[20:21] offset:160\n\t"
    "v_accvgpr_read_b32 v24, a4\n\t"
    "v_accvgpr_read_b32 v25, a5\n\t"
    "v_accvgpr_read_b32 v26, a6\n\t"
    "v_accvgpr_read_b32 v27, a7\n\t"
    "s_waitcnt vmcnt(11)\n\t"
    "v_pk_add_f32 v[24:25], v[24:25], v[148:149]\n\t"
    "v_pk_add_f32 v[26:27], v[26:27], v[150:151]\n\t"
    "v_cvt_pk_bf16_f32 v24, v24, v25\n\t"
    "v_cvt_pk_bf16_f32 v25, v26, v27\n\t"
    "ds_write_b64 v96, v[24:25] offset:6400\n\t"
    "v_accvgpr_read_b32 v28, a24\n\t"
    "v_accvgpr_read_b32 v29, a25\n\t"
    "v_accvgpr_read_b32 v30, a26\n\t"
    "v_accvgpr_read_b32 v31, a27\n\t"
    "s_waitcnt vmcnt(10)\n\t"
    "v_pk_add_f32 v[28:29], v[28:29], v[152:153]\n\t"
    "v_pk_add_f32 v[30:31], v[30:31], v[154:155]\n\t"
    "v_cvt_pk_bf16_f32 v28, v28, v29\n\t"
    "v_cvt_pk_bf16_f32 v29, v30, v31\n\t"
    "ds_write_b64 v96, v[28:29] offset:6432\n\t"
    "v_accvgpr_read_b32 v0, a44\n\t"
    "v_accvgpr_read_b32 v1, a45\n\t"
    "v_accvgpr_read_b32 v2, a46\n\t"
    "v_accvgpr_read_b32 v3, a47\n\t"
    "s_waitcnt vmcnt(9)\n\t"
    "v_pk_add_f32 v[0:1], v[0:1], v[156:157]\n\t"
    "v_pk_add_f32 v[2:3], v[2:3], v[158:159]\n\t"
    "v_cvt_pk_bf16_f32 v0, v0, v1\n\t"
    "v_cvt_pk_bf16_f32 v1, v2, v3\n\t"
    "ds_write_b64 v96, v[0:1] offset:6464\n\t"
    "v_accvgpr_read_b32 v4, a64\n\t"
    "v_accvgpr_read_b32 v5, a65\n\t"
    "v_accvgpr_read_b32 v6, a66\n\t"
    "v_accvgpr_read_b32 v7, a67\n\t"
    "s_waitcnt vmcnt(8)\n\t"
    "v_pk_add_f32 v[4:5], v[4:5], v[160:161]\n\t"
    "v_pk_add_f32 v[6:7], v[6:7], v[162:163]\n\t"
    "v_cvt_pk_bf16_f32 v4, v4, v5\n\t"
    "v_cvt_pk_bf16_f32 v5, v6, v7\n\t"
    "ds_write_b64 v96, v[4:5] offset:6496\n\t"
    "v_accvgpr_read_b32 v8, a84\n\t"
    "v_accvgpr_read_b32 v9, a85\n\t"
    "v_accvgpr_read_b32 v10, a86\n\t"
    "v_accvgpr_read_b32 v11, a87\n\t"
    "s_waitcnt vmcnt(7)\n\t"
    "v_pk_add_f32 v[8:9], v[8:9], v[164:165]\n\t"
    "v_pk_add_f32 v[10:11], v[10:11], v[166:167]\n\t"
    "v_cvt_pk_bf16_f32 v8, v8, v9\n\t"
    "v_cvt_pk_bf16_f32 v9, v10, v11\n\t"
    "ds_write_b64 v96, v[8:9] offset:6528\n\t"
    "v_accvgpr_read_b32 v12, a104\n\t"
    "v_accvgpr_read_b32 v13, a105\n\t"
    "v_accvgpr_read_b32 v14, a106\n\t"
    "v_accvgpr_read_b32 v15, a107\n\t"
    "s_waitcnt vmcnt(6)\n\t"
    "v_pk_add_f32 v[12:13], v[12:13], v[168:169]\n\t"
    "v_pk_add_f32 v[14:15], v[14:15], v[170:171]\n\t"
    "v_cvt_pk_bf16_f32 v12, v12, v13\n\t"
    "v_cvt_pk_bf16_f32 v13, v14, v15\n\t"
    "ds_write_b64 v96, v[12:13] offset:6560\n\t"
    "v_accvgpr_read_b32 v16, a8\n\t"
    "v_accvgpr_read_b32 v17, a9\n\t"
    "v_accvgpr_read_b32 v18, a10\n\t"
    "v_accvgpr_read_b32 v19, a11\n\t"
    "s_waitcnt vmcnt(5)\n\t"
    "v_pk_add_f32 v[16:17], v[16:17], v[172:173]\n\t"
    "v_pk_add_f32 v[18:19], v[18:19], v[174:175]\n\t"
    "v_cvt_pk_bf16_f32 v16, v16, v17\n\t"
    "v_cvt_pk_bf16_f32 v17, v18, v19\n\t"
    "ds_write_b64 v96, v[16:17] offset:12800\n\t"
    "v_accvgpr_read_b32 v20, a28\n\t"
    "v_accvgpr_read_b32 v21, a29\n\t"
    "v_accvgpr_read_b32 v22, a30\n\t"
    "v_accvgpr_read_b32 v23, a31\n\t"
    "s_waitcnt vmcnt(4)\n\t"
    "v_pk_add_f32 v[20:21], v[20:21], v[176:177]\n\t"
    "v_pk_add_f32 v[22:23], v[22:23], v[178:179]\n\t"
    "v_cvt_pk_bf16_f32 v20, v20, v21\n\t"
    "v_cvt_pk_bf16_f32 v21, v22, v23\n\t"
    "ds_write_b64 v96, v[20:21] offset:12832\n\t"
    "v_accvgpr_read_b32 v24, a48\n\t"
    "v_accvgpr_read_b32 v25, a49\n\t"
    "v_accvgpr_read_b32 v26, a50\n\t"
    "v_accvgpr_read_b32 v27, a51\n\t"
    "s_waitcnt vmcnt(3)\n\t"
    "v_pk_add_f32 v[24:25], v[24:25], v[180:181]\n\t"
    "v_pk_add_f32 v[26:27], v[26:27], v[182:183]\n\t"
    "v_cvt_pk_bf16_f32 v24, v24, v25\n\t"
    "v_cvt_pk_bf16_f32 v25, v26, v27\n\t"
    "ds_write_b64 v96, v[24:25] offset:12864\n\t"
    "v_accvgpr_read_b32 v28, a68\n\t"
    "v_accvgpr_read_b32 v29, a69\n\t"
    "v_accvgpr_read_b32 v30, a70\n\t"
    "v_accvgpr_read_b32 v31, a71\n\t"
    "s_waitcnt vmcnt(2)\n\t"
    "v_pk_add_f32 v[28:29], v[28:29], v[184:185]\n\t"
    "v_pk_add_f32 v[30:31], v[30:31], v[186:187]\n\t"
    "v_cvt_pk_bf16_f32 v28, v28, v29\n\t"
    "v_cvt_pk_bf16_f32 v29, v30, v31\n\t"
    "ds_write_b64 v96, v[28:29] offset:12896\n\t"
    "v_accvgpr_read_b32 v0, a88\n\t"
    "v_accvgpr_read_b32 v1, a89\n\t"
    "v_accvgpr_read_b32 v2, a90\n\t"
    "v_accvgpr_read_b32 v3, a91\n\t"
    "s_waitcnt vmcnt(1)\n\t"
    "v_pk_add_f32 v[0:1], v[0:1], v[188:189]\n\t"
    "v_pk_add_f32 v[2:3], v[2:3], v[190:191]\n\t"
    "v_cvt_pk_bf16_f32 v0, v0, v1\n\t"
    "v_cvt_pk_bf16_f32 v1, v2, v3\n\t"
    "ds_write_b64 v96, v[0:1] offset:12928\n\t"
    "v_accvgpr_read_b32 v4, a108\n\t"
    "v_accvgpr_read_b32 v5, a109\n\t"
    "v_accvgpr_read_b32 v6, a110\n\t"
    "v_accvgpr_read_b32 v7, a111\n\t"
    "s_waitcnt vmcnt(0)\n\t"
    "v_pk_add_f32 v[4:5], v[4:5], v[192:193]\n\t"
    "v_pk_add_f32 v[6:7], v[6:7], v[194:195]\n\t"
    "v_cvt_pk_bf16_f32 v4, v4, v5\n\t"
    "v_cvt_pk_bf16_f32 v5, v6, v7\n\t"
    "ds_write_b64 v96, v[4:5] offset:12960\n\t"
    "s_branch .Lo_stageda\n\t"
    ".Lo_rcva1:\n\t"
    "s_mov_b32 s40, 0\n\t"
    "buffer_load_dwordx4 v[124:127], v214, s[16:19], s40 offen sc1\n\t"
    "s_mov_b32 s40, 5120\n\t"
    "buffer_load_dwordx4 v[128:131], v214, s[16:19], s40 offen sc1\n\t"
    "s_mov_b32 s40, 10240\n\t"
    "buffer_load_dwordx4 v[132:135], v214, s[16:19], s40 offen sc1\n\t"
    "s_mov_b32 s40, 15360\n\t"
    "buffer_load_dwordx4 v[136:139], v214, s[16:19], s40 offen sc1\n\t"
    "s_mov_b32 s40, 20480\n\t"
    "buffer_load_dwordx4 v[140:143], v214, s[16:19], s40 offen sc1\n\t"
    "s_mov_b32 s40, 25600\n\t"
    "buffer_load_dwordx4 v[144:147], v214, s[16:19], s40 offen sc1\n\t"
    "s_mov_b32 s40, 1024\n\t"
    "buffer_load_dwordx4 v[148:151], v214, s[16:19], s40 offen sc1\n\t"
    "s_mov_b32 s40, 6144\n\t"
    "buffer_load_dwordx4 v[152:155], v214, s[16:19], s40 offen sc1\n\t"
    "s_mov_b32 s40, 11264\n\t"
    "buffer_load_dwordx4 v[156:159], v214, s[16:19], s40 offen sc1\n\t"
    "s_mov_b32 s40, 16384\n\t"
    "buffer_load_dwordx4 v[160:163], v214, s[16:19], s40 offen sc1\n\t"
    "s_mov_b32 s40, 21504\n\t"
    "buffer_load_dwordx4 v[164:167], v214, s[16:19], s40 offen sc1\n\t"
    "s_mov_b32 s40, 26624\n\t"
    "buffer_load_dwordx4 v[168:171], v214, s[16:19], s40 offen sc1\n\t"
    "v_accvgpr_read_b32 v0, a0\n\t"
    "v_accvgpr_read_b32 v1, a1\n\t"
    "v_accvgpr_read_b32 v2, a2\n\t"
    "v_accvgpr_read_b32 v3, a3\n\t"
    "s_waitcnt vmcnt(11)\n\t"
    "v_pk_add_f32 v[0:1], v[0:1], v[124:125]\n\t"
    "v_pk_add_f32 v[2:3], v[2:3], v[126:127]\n\t"
    "v_cvt_pk_bf16_f32 v0, v0, v1\n\t"
    "v_cvt_pk_bf16_f32 v1, v2, v3\n\t"
    "ds_write_b64 v96, v[0:1] offset:0\n\t"
    "v_accvgpr_read_b32 v4, a20\n\t"
    "v_accvgpr_read_b32 v5, a21\n\t"
    "v_accvgpr_read_b32 v6, a22\n\t"
    "v_accvgpr_read_b32 v7, a23\n\t"
    "s_waitcnt vmcnt(10)\n\t"
    "v_pk_add_f32 v[4:5], v[4:5], v[128:129]\n\t"
    "v_pk_add_f32 v[6:7], v[6:7], v[130:131]\n\t"
    "v_cvt_pk_bf16_f32 v4, v4, v5\n\t"
    "v_cvt_pk_bf16_f32 v5, v6, v7\n\t"
    "ds_write_b64 v96, v[4:5] offset:32\n\t"
    "v_accvgpr_read_b32 v8, a40\n\t"
    "v_accvgpr_read_b32 v9, a41\n\t"
    "v_accvgpr_read_b32 v10, a42\n\t"
    "v_accvgpr_read_b32 v11, a43\n\t"
    "s_waitcnt vmcnt(9)\n\t"
    "v_pk_add_f32 v[8:9], v[8:9], v[132:133]\n\t"
    "v_pk_add_f32 v[10:11], v[10:11], v[134:135]\n\t"
    "v_cvt_pk_bf16_f32 v8, v8, v9\n\t"
    "v_cvt_pk_bf16_f32 v9, v10, v11\n\t"
    "ds_write_b64 v96, v[8:9] offset:64\n\t"
    "v_accvgpr_read_b32 v12, a60\n\t"
    "v_accvgpr_read_b32 v13, a61\n\t"
    "v_accvgpr_read_b32 v14, a62\n\t"
    "v_accvgpr_read_b32 v15, a63\n\t"
    "s_waitcnt vmcnt(8)\n\t"
    "v_pk_add_f32 v[12:13], v[12:13], v[136:137]\n\t"
    "v_pk_add_f32 v[14:15], v[14:15], v[138:139]\n\t"
    "v_cvt_pk_bf16_f32 v12, v12, v13\n\t"
    "v_cvt_pk_bf16_f32 v13, v14, v15\n\t"
    "ds_write_b64 v96, v[12:13] offset:96\n\t"
    "v_accvgpr_read_b32 v16, a80\n\t"
    "v_accvgpr_read_b32 v17, a81\n\t"
    "v_accvgpr_read_b32 v18, a82\n\t"
    "v_accvgpr_read_b32 v19, a83\n\t"
    "s_waitcnt vmcnt(7)\n\t"
    "v_pk_add_f32 v[16:17], v[16:17], v[140:141]\n\t"
    "v_pk_add_f32 v[18:19], v[18:19], v[142:143]\n\t"
    "v_cvt_pk_bf16_f32 v16, v16, v17\n\t"
    "v_cvt_pk_bf16_f32 v17, v18, v19\n\t"
    "ds_write_b64 v96, v[16:17] offset:128\n\t"
    "v_accvgpr_read_b32 v20, a100\n\t"
    "v_accvgpr_read_b32 v21, a101\n\t"
    "v_accvgpr_read_b32 v22, a102\n\t"
    "v_accvgpr_read_b32 v23, a103\n\t"
    "s_waitcnt vmcnt(6)\n\t"
    "v_pk_add_f32 v[20:21], v[20:21], v[144:145]\n\t"
    "v_pk_add_f32 v[22:23], v[22:23], v[146:147]\n\t"
    "v_cvt_pk_bf16_f32 v20, v20, v21\n\t"
    "v_cvt_pk_bf16_f32 v21, v22, v23\n\t"
    "ds_write_b64 v96, v[20:21] offset:160\n\t"
    "v_accvgpr_read_b32 v24, a4\n\t"
    "v_accvgpr_read_b32 v25, a5\n\t"
    "v_accvgpr_read_b32 v26, a6\n\t"
    "v_accvgpr_read_b32 v27, a7\n\t"
    "s_waitcnt vmcnt(5)\n\t"
    "v_pk_add_f32 v[24:25], v[24:25], v[148:149]\n\t"
    "v_pk_add_f32 v[26:27], v[26:27], v[150:151]\n\t"
    "v_cvt_pk_bf16_f32 v24, v24, v25\n\t"
    "v_cvt_pk_bf16_f32 v25, v26, v27\n\t"
    "ds_write_b64 v96, v[24:25] offset:6400\n\t"
    "v_accvgpr_read_b32 v28, a24\n\t"
    "v_accvgpr_read_b32 v29, a25\n\t"
    "v_accvgpr_read_b32 v30, a26\n\t"
    "v_accvgpr_read_b32 v31, a27\n\t"
    "s_waitcnt vmcnt(4)\n\t"
    "v_pk_add_f32 v[28:29], v[28:29], v[152:153]\n\t"
    "v_pk_add_f32 v[30:31], v[30:31], v[154:155]\n\t"
    "v_cvt_pk_bf16_f32 v28, v28, v29\n\t"
    "v_cvt_pk_bf16_f32 v29, v30, v31\n\t"
    "ds_write_b64 v96, v[28:29] offset:6432\n\t"
    "v_accvgpr_read_b32 v0, a44\n\t"
    "v_accvgpr_read_b32 v1, a45\n\t"
    "v_accvgpr_read_b32 v2, a46\n\t"
    "v_accvgpr_read_b32 v3, a47\n\t"
    "s_waitcnt vmcnt(3)\n\t"
    "v_pk_add_f32 v[0:1], v[0:1], v[156:157]\n\t"
    "v_pk_add_f32 v[2:3], v[2:3], v[158:159]\n\t"
    "v_cvt_pk_bf16_f32 v0, v0, v1\n\t"
    "v_cvt_pk_bf16_f32 v1, v2, v3\n\t"
    "ds_write_b64 v96, v[0:1] offset:6464\n\t"
    "v_accvgpr_read_b32 v4, a64\n\t"
    "v_accvgpr_read_b32 v5, a65\n\t"
    "v_accvgpr_read_b32 v6, a66\n\t"
    "v_accvgpr_read_b32 v7, a67\n\t"
    "s_waitcnt vmcnt(2)\n\t"
    "v_pk_add_f32 v[4:5], v[4:5], v[160:161]\n\t"
    "v_pk_add_f32 v[6:7], v[6:7], v[162:163]\n\t"
    "v_cvt_pk_bf16_f32 v4, v4, v5\n\t"
    "v_cvt_pk_bf16_f32 v5, v6, v7\n\t"
    "ds_write_b64 v96, v[4:5] offset:6496\n\t"
    "v_accvgpr_read_b32 v8, a84\n\t"
    "v_accvgpr_read_b32 v9, a85\n\t"
    "v_accvgpr_read_b32 v10, a86\n\t"
    "v_accvgpr_read_b32 v11, a87\n\t"
    "s_waitcnt vmcnt(1)\n\t"
    "v_pk_add_f32 v[8:9], v[8:9], v[164:165]\n\t"
    "v_pk_add_f32 v[10:11], v[10:11], v[166:167]\n\t"
    "v_cvt_pk_bf16_f32 v8, v8, v9\n\t"
    "v_cvt_pk_bf16_f32 v9, v10, v11\n\t"
    "ds_write_b64 v96, v[8:9] offset:6528\n\t"
    "v_accvgpr_read_b32 v12, a104\n\t"
    "v_accvgpr_read_b32 v13, a105\n\t"
    "v_accvgpr_read_b32 v14, a106\n\t"
    "v_accvgpr_read_b32 v15, a107\n\t"
    "s_waitcnt vmcnt(0)\n\t"
    "v_pk_add_f32 v[12:13], v[12:13], v[168:169]\n\t"
    "v_pk_add_f32 v[14:15], v[14:15], v[170:171]\n\t"
    "v_cvt_pk_bf16_f32 v12, v12, v13\n\t"
    "v_cvt_pk_bf16_f32 v13, v14, v15\n\t"
    "ds_write_b64 v96, v[12:13] offset:6560\n\t"
    "s_branch .Lo_stageda\n\t"
    ".Lo_rcva2:\n\t"
    "s_mov_b32 s40, 3072\n\t"
    "buffer_load_dwordx4 v[124:127], v214, s[16:19], s40 offen sc1\n\t"
    "s_mov_b32 s40, 8192\n\t"
    "buffer_load_dwordx4 v[128:131], v214, s[16:19], s40 offen sc1\n\t"
    "s_mov_b32 s40, 13312\n\t"
    "buffer_load_dwordx4 v[132:135], v214, s[16:19], s40 offen sc1\n\t"
    "s_mov_b32 s40, 18432\n\t"
    "buffer_load_dwordx4 v[136:139], v214, s[16:19], s40 offen sc1\n\t"
    "s_mov_b32 s40, 23552\n\t"
    "buffer_load_dwordx4 v[140:143], v214, s[16:19], s40 offen sc1\n\t"
    "s_mov_b32 s40, 28672\n\t"
    "buffer_load_dwordx4 v[144:147], v214, s[16:19], s40 offen sc1\n\t"
    "s_mov_b32 s40, 4096\n\t"
    "buffer_load_dwordx4 v[148:151], v214, s[16:19], s40 offen sc1\n\t"
    "s_mov_b32 s40, 9216\n\t"
    "buffer_load_dwordx4 v[152:155], v214, s[16:19], s40 offen sc1\n\t"
    "s_mov_b32 s40, 14336\n\t"
    "buffer_load_dwordx4 v[156:159], v214, s[16:19], s40 offen sc1\n\t"
    "s_mov_b32 s40, 19456\n\t"
    "buffer_load_dwordx4 v[160:163], v214, s[16:19], s40 offen sc1\n\t"
    "s_mov_b32 s40, 24576\n\t"
    "buffer_load_dwordx4 v[164:167], v214, s[16:19], s40 offen sc1\n\t"
    "s_mov_b32 s40, 29696\n\t"
    "buffer_load_dwordx4 v[168:171], v214, s[16:19], s40 offen sc1\n\t"
    "v_accvgpr_read_b32 v0, a12\n\t"
    "v_accvgpr_read_b32 v1, a13\n\t"
    "v_accvgpr_read_b32 v2, a14\n\t"
    "v_accvgpr_read_b32 v3, a15\n\t"
    "s_waitcnt vmcnt(11)\n\t"
    "v_pk_add_f32 v[0:1], v[0:1], v[124:125]\n\t"
    "v_pk_add_f32 v[2:3], v[2:3], v[126:127]\n\t"
    "v_cvt_pk_bf16_f32 v0, v0, v1\n\t"
    "v_cvt_pk_bf16_f32 v1, v2, v3\n\t"
    "ds_write_b64 v96, v[0:1] offset:0\n\t"
    "v_accvgpr_read_b32 v4, a32\n\t"
    "v_accvgpr_read_b32 v5, a33\n\t"
    "v_accvgpr_read_b32 v6, a34\n\t"
    "v_accvgpr_read_b32 v7, a35\n\t"
    "s_waitcnt vmcnt(10)\n\t"
    "v_pk_add_f32 v[4:5], v[4:5], v[128:129]\n\t"
    "v_pk_add_f32 v[6:7], v[6:7], v[130:131]\n\t"
    "v_cvt_pk_bf16_f32 v4, v4, v5\n\t"
    "v_cvt_pk_bf16_f32 v5, v6, v7\n\t"
    "ds_write_b64 v96, v[4:5] offset:32\n\t"
    "v_accvgpr_read_b32 v8, a52\n\t"
    "v_accvgpr_read_b32 v9, a53\n\t"
    "v_accvgpr_read_b32 v10, a54\n\t"
    "v_accvgpr_read_b32 v11, a55\n\t"
    "s_waitcnt vmcnt(9)\n\t"
    "v_pk_add_f32 v[8:9], v[8:9], v[132:133]\n\t"
    "v_pk_add_f32 v[10:11], v[10:11], v[134:135]\n\t"
    "v_cvt_pk_bf16_f32 v8, v8, v9\n\t"
    "v_cvt_pk_bf16_f32 v9, v10, v11\n\t"
    "ds_write_b64 v96, v[8:9] offset:64\n\t"
    "v_accvgpr_read_b32 v12, a72\n\t"
    "v_accvgpr_read_b32 v13, a73\n\t"
    "v_accvgpr_read_b32 v14, a74\n\t"
    "v_accvgpr_read_b32 v15, a75\n\t"
    "s_waitcnt vmcnt(8)\n\t"
    "v_pk_add_f32 v[12:13], v[12:13], v[136:137]\n\t"
    "v_pk_add_f32 v[14:15], v[14:15], v[138:139]\n\t"
    "v_cvt_pk_bf16_f32 v12, v12, v13\n\t"
    "v_cvt_pk_bf16_f32 v13, v14, v15\n\t"
    "ds_write_b64 v96, v[12:13] offset:96\n\t"
    "v_accvgpr_read_b32 v16, a92\n\t"
    "v_accvgpr_read_b32 v17, a93\n\t"
    "v_accvgpr_read_b32 v18, a94\n\t"
    "v_accvgpr_read_b32 v19, a95\n\t"
    "s_waitcnt vmcnt(7)\n\t"
    "v_pk_add_f32 v[16:17], v[16:17], v[140:141]\n\t"
    "v_pk_add_f32 v[18:19], v[18:19], v[142:143]\n\t"
    "v_cvt_pk_bf16_f32 v16, v16, v17\n\t"
    "v_cvt_pk_bf16_f32 v17, v18, v19\n\t"
    "ds_write_b64 v96, v[16:17] offset:128\n\t"
    "v_accvgpr_read_b32 v20, a112\n\t"
    "v_accvgpr_read_b32 v21, a113\n\t"
    "v_accvgpr_read_b32 v22, a114\n\t"
    "v_accvgpr_read_b32 v23, a115\n\t"
    "s_waitcnt vmcnt(6)\n\t"
    "v_pk_add_f32 v[20:21], v[20:21], v[144:145]\n\t"
    "v_pk_add_f32 v[22:23], v[22:23], v[146:147]\n\t"
    "v_cvt_pk_bf16_f32 v20, v20, v21\n\t"
    "v_cvt_pk_bf16_f32 v21, v22, v23\n\t"
    "ds_write_b64 v96, v[20:21] offset:160\n\t"
    "v_accvgpr_read_b32 v24, a16\n\t"
    "v_accvgpr_read_b32 v25, a17\n\t"
    "v_accvgpr_read_b32 v26, a18\n\t"
    "v_accvgpr_read_b32 v27, a19\n\t"
    "s_waitcnt vmcnt(5)\n\t"
    "v_pk_add_f32 v[24:25], v[24:25], v[148:149]\n\t"
    "v_pk_add_f32 v[26:27], v[26:27], v[150:151]\n\t"
    "v_cvt_pk_bf16_f32 v24, v24, v25\n\t"
    "v_cvt_pk_bf16_f32 v25, v26, v27\n\t"
    "ds_write_b64 v96, v[24:25] offset:6400\n\t"
    "v_accvgpr_read_b32 v28, a36\n\t"
    "v_accvgpr_read_b32 v29, a37\n\t"
    "v_accvgpr_read_b32 v30, a38\n\t"
    "v_accvgpr_read_b32 v31, a39\n\t"
    "s_waitcnt vmcnt(4)\n\t"
    "v_pk_add_f32 v[28:29], v[28:29], v[152:153]\n\t"
    "v_pk_add_f32 v[30:31], v[30:31], v[154:155]\n\t"
    "v_cvt_pk_bf16_f32 v28, v28, v29\n\t"
    "v_cvt_pk_bf16_f32 v29, v30, v31\n\t"
    "ds_write_b64 v96, v[28:29] offset:6432\n\t"
    "v_accvgpr_read_b32 v0, a56\n\t"
    "v_accvgpr_read_b32 v1, a57\n\t"
    "v_accvgpr_read_b32 v2, a58\n\t"
    "v_accvgpr_read_b32 v3, a59\n\t"
    "s_waitcnt vmcnt(3)\n\t"
    "v_pk_add_f32 v[0:1], v[0:1], v[156:157]\n\t"
    "v_pk_add_f32 v[2:3], v[2:3], v[158:159]\n\t"
    "v_cvt_pk_bf16_f32 v0, v0, v1\n\t"
    "v_cvt_pk_bf16_f32 v1, v2, v3\n\t"
    "ds_write_b64 v96, v[0:1] offset:6464\n\t"
    "v_accvgpr_read_b32 v4, a76\n\t"
    "v_accvgpr_read_b32 v5, a77\n\t"
    "v_accvgpr_read_b32 v6, a78\n\t"
    "v_accvgpr_read_b32 v7, a79\n\t"
    "s_waitcnt vmcnt(2)\n\t"
    "v_pk_add_f32 v[4:5], v[4:5], v[160:161]\n\t"
    "v_pk_add_f32 v[6:7], v[6:7], v[162:163]\n\t"
    "v_cvt_pk_bf16_f32 v4, v4, v5\n\t"
    "v_cvt_pk_bf16_f32 v5, v6, v7\n\t"
    "ds_write_b64 v96, v[4:5] offset:6496\n\t"
    "v_accvgpr_read_b32 v8, a96\n\t"
    "v_accvgpr_read_b32 v9, a97\n\t"
    "v_accvgpr_read_b32 v10, a98\n\t"
    "v_accvgpr_read_b32 v11, a99\n\t"
    "s_waitcnt vmcnt(1)\n\t"
    "v_pk_add_f32 v[8:9], v[8:9], v[164:165]\n\t"
    "v_pk_add_f32 v[10:11], v[10:11], v[166:167]\n\t"
    "v_cvt_pk_bf16_f32 v8, v8, v9\n\t"
    "v_cvt_pk_bf16_f32 v9, v10, v11\n\t"
    "ds_write_b64 v96, v[8:9] offset:6528\n\t"
    "v_accvgpr_read_b32 v12, a116\n\t"
    "v_accvgpr_read_b32 v13, a117\n\t"
    "v_accvgpr_read_b32 v14, a118\n\t"
    "v_accvgpr_read_b32 v15, a119\n\t"
    "s_waitcnt vmcnt(0)\n\t"
    "v_pk_add_f32 v[12:13], v[12:13], v[168:169]\n\t"
    "v_pk_add_f32 v[14:15], v[14:15], v[170:171]\n\t"
    "v_cvt_pk_bf16_f32 v12, v12, v13\n\t"
    "v_cvt_pk_bf16_f32 v13, v14, v15\n\t"
    "ds_write_b64 v96, v[12:13] offset:6560\n\t"
    "s_branch .Lo_stageda\n\t"
    ".Lo_rcva3:\n\t"
    "s_mov_b32 s40, 2048\n\t"
    "buffer_load_dwordx4 v[124:127], v214, s[16:19], s40 offen sc1\n\t"
    "s_mov_b32 s40, 7168\n\t"
    "buffer_load_dwordx4 v[128:131], v214, s[16:19], s40 offen sc1\n\t"
    "s_mov_b32 s40, 12288\n\t"
    "buffer_load_dwordx4 v[132:135], v214, s[16:19], s40 offen sc1\n\t"
    "s_mov_b32 s40, 17408\n\t"
    "buffer_load_dwordx4 v[136:139], v214, s[16:19], s40 offen sc1\n\t"
    "s_mov_b32 s40, 22528\n\t"
    "buffer_load_dwordx4 v[140:143], v214, s[16:19], s40 offen sc1\n\t"
    "s_mov_b32 s40, 27648\n\t"
    "buffer_load_dwordx4 v[144:147], v214, s[16:19], s40 offen sc1\n\t"
    "s_mov_b32 s40, 3072\n\t"
    "buffer_load_dwordx4 v[148:151], v214, s[16:19], s40 offen sc1\n\t"
    "s_mov_b32 s40, 8192\n\t"
    "buffer_load_dwordx4 v[152:155], v214, s[16:19], s40 offen sc1\n\t"
    "s_mov_b32 s40, 13312\n\t"
    "buffer_load_dwordx4 v[156:159], v214, s[16:19], s40 offen sc1\n\t"
    "s_mov_b32 s40, 18432\n\t"
    "buffer_load_dwordx4 v[160:163], v214, s[16:19], s40 offen sc1\n\t"
    "s_mov_b32 s40, 23552\n\t"
    "buffer_load_dwordx4 v[164:167], v214, s[16:19], s40 offen sc1\n\t"
    "s_mov_b32 s40, 28672\n\t"
    "buffer_load_dwordx4 v[168:171], v214, s[16:19], s40 offen sc1\n\t"
    "s_mov_b32 s40, 4096\n\t"
    "buffer_load_dwordx4 v[172:175], v214, s[16:19], s40 offen sc1\n\t"
    "s_mov_b32 s40, 9216\n\t"
    "buffer_load_dwordx4 v[176:179], v214, s[16:19], s40 offen sc1\n\t"
    "s_mov_b32 s40, 14336\n\t"
    "buffer_load_dwordx4 v[180:183], v214, s[16:19], s40 offen sc1\n\t"
    "s_mov_b32 s40, 19456\n\t"
    "buffer_load_dwordx4 v[184:187], v214, s[16:19], s40 offen sc1\n\t"
    "s_mov_b32 s40, 24576\n\t"
    "buffer_load_dwordx4 v[188:191], v214, s[16:19], s40 offen sc1\n\t"
    "s_mov_b32 s40, 29696\n\t"
    "buffer_load_dwordx4 v[192:195], v214, s[16:19], s40 offen sc1\n\t"
    "v_accvgpr_read_b32 v0, a8\n\t"
    "v_accvgpr_read_b32 v1, a9\n\t"
    "v_accvgpr_read_b32 v2, a10\n\t"
    "v_accvgpr_read_b32 v3, a11\n\t"
    "s_waitcnt vmcnt(17)\n\t"
    "v_pk_add_f32 v[0:1], v[0:1], v[124:125]\n\t"
    "v_pk_add_f32 v[2:3], v[2:3], v[126:127]\n\t"
    "v_cvt_pk_bf16_f32 v0, v0, v1\n\t"
    "v_cvt_pk_bf16_f32 v1, v2, v3\n\t"
    "ds_write_b64 v96, v[0:1] offset:0\n\t"
    "v_accvgpr_read_b32 v4, a28\n\t"
    "v_accvgpr_read_b32 v5, a29\n\t"
    "v_accvgpr_read_b32 v6, a30\n\t"
    "v_accvgpr_read_b32 v7, a31\n\t"
    "s_waitcnt vmcnt(16)\n\t"
    "v_pk_add_f32 v[4:5], v[4:5], v[128:129]\n\t"
    "v_pk_add_f32 v[6:7], v[6:7], v[130:131]\n\t"
    "v_cvt_pk_bf16_f32 v4, v4, v5\n\t"
    "v_cvt_pk_bf16_f32 v5, v6, v7\n\t"
    "ds_write_b64 v96, v[4:5] offset:32\n\t"
    "v_accvgpr_read_b32 v8, a48\n\t"
    "v_accvgpr_read_b32 v9, a49\n\t"
    "v_accvgpr_read_b32 v10, a50\n\t"
    "v_accvgpr_read_b32 v11, a51\n\t"
    "s_waitcnt vmcnt(15)\n\t"
    "v_pk_add_f32 v[8:9], v[8:9], v[132:133]\n\t"
    "v_pk_add_f32 v[10:11], v[10:11], v[134:135]\n\t"
    "v_cvt_pk_bf16_f32 v8, v8, v9\n\t"
    "v_cvt_pk_bf16_f32 v9, v10, v11\n\t"
    "ds_write_b64 v96, v[8:9] offset:64\n\t"
    "v_accvgpr_read_b32 v12, a68\n\t"
    "v_accvgpr_read_b32 v13, a69\n\t"
    "v_accvgpr_read_b32 v14, a70\n\t"
    "v_accvgpr_read_b32 v15, a71\n\t"
    "s_waitcnt vmcnt(14)\n\t"
    "v_pk_add_f32 v[12:13], v[12:13], v[136:137]\n\t"
    "v_pk_add_f32 v[14:15], v[14:15], v[138:139]\n\t"
    "v_cvt_pk_bf16_f32 v12, v12, v13\n\t"
    "v_cvt_pk_bf16_f32 v13, v14, v15\n\t"
    "ds_write_b64 v96, v[12:13] offset:96\n\t"
    "v_accvgpr_read_b32 v16, a88\n\t"
    "v_accvgpr_read_b32 v17, a89\n\t"
    "v_accvgpr_read_b32 v18, a90\n\t"
    "v_accvgpr_read_b32 v19, a91\n\t"
    "s_waitcnt vmcnt(13)\n\t"
    "v_pk_add_f32 v[16:17], v[16:17], v[140:141]\n\t"
    "v_pk_add_f32 v[18:19], v[18:19], v[142:143]\n\t"
    "v_cvt_pk_bf16_f32 v16, v16, v17\n\t"
    "v_cvt_pk_bf16_f32 v17, v18, v19\n\t"
    "ds_write_b64 v96, v[16:17] offset:128\n\t"
    "v_accvgpr_read_b32 v20, a108\n\t"
    "v_accvgpr_read_b32 v21, a109\n\t"
    "v_accvgpr_read_b32 v22, a110\n\t"
    "v_accvgpr_read_b32 v23, a111\n\t"
    "s_waitcnt vmcnt(12)\n\t"
    "v_pk_add_f32 v[20:21], v[20:21], v[144:145]\n\t"
    "v_pk_add_f32 v[22:23], v[22:23], v[146:147]\n\t"
    "v_cvt_pk_bf16_f32 v20, v20, v21\n\t"
    "v_cvt_pk_bf16_f32 v21, v22, v23\n\t"
    "ds_write_b64 v96, v[20:21] offset:160\n\t"
    "v_accvgpr_read_b32 v24, a12\n\t"
    "v_accvgpr_read_b32 v25, a13\n\t"
    "v_accvgpr_read_b32 v26, a14\n\t"
    "v_accvgpr_read_b32 v27, a15\n\t"
    "s_waitcnt vmcnt(11)\n\t"
    "v_pk_add_f32 v[24:25], v[24:25], v[148:149]\n\t"
    "v_pk_add_f32 v[26:27], v[26:27], v[150:151]\n\t"
    "v_cvt_pk_bf16_f32 v24, v24, v25\n\t"
    "v_cvt_pk_bf16_f32 v25, v26, v27\n\t"
    "ds_write_b64 v96, v[24:25] offset:6400\n\t"
    "v_accvgpr_read_b32 v28, a32\n\t"
    "v_accvgpr_read_b32 v29, a33\n\t"
    "v_accvgpr_read_b32 v30, a34\n\t"
    "v_accvgpr_read_b32 v31, a35\n\t"
    "s_waitcnt vmcnt(10)\n\t"
    "v_pk_add_f32 v[28:29], v[28:29], v[152:153]\n\t"
    "v_pk_add_f32 v[30:31], v[30:31], v[154:155]\n\t"
    "v_cvt_pk_bf16_f32 v28, v28, v29\n\t"
    "v_cvt_pk_bf16_f32 v29, v30, v31\n\t"
    "ds_write_b64 v96, v[28:29] offset:6432\n\t"
    "v_accvgpr_read_b32 v0, a52\n\t"
    "v_accvgpr_read_b32 v1, a53\n\t"
    "v_accvgpr_read_b32 v2, a54\n\t"
    "v_accvgpr_read_b32 v3, a55\n\t"
    "s_waitcnt vmcnt(9)\n\t"
    "v_pk_add_f32 v[0:1], v[0:1], v[156:157]\n\t"
    "v_pk_add_f32 v[2:3], v[2:3], v[158:159]\n\t"
    "v_cvt_pk_bf16_f32 v0, v0, v1\n\t"
    "v_cvt_pk_bf16_f32 v1, v2, v3\n\t"
    "ds_write_b64 v96, v[0:1] offset:6464\n\t"
    "v_accvgpr_read_b32 v4, a72\n\t"
    "v_accvgpr_read_b32 v5, a73\n\t"
    "v_accvgpr_read_b32 v6, a74\n\t"
    "v_accvgpr_read_b32 v7, a75\n\t"
    "s_waitcnt vmcnt(8)\n\t"
    "v_pk_add_f32 v[4:5], v[4:5], v[160:161]\n\t"
    "v_pk_add_f32 v[6:7], v[6:7], v[162:163]\n\t"
    "v_cvt_pk_bf16_f32 v4, v4, v5\n\t"
    "v_cvt_pk_bf16_f32 v5, v6, v7\n\t"
    "ds_write_b64 v96, v[4:5] offset:6496\n\t"
    "v_accvgpr_read_b32 v8, a92\n\t"
    "v_accvgpr_read_b32 v9, a93\n\t"
    "v_accvgpr_read_b32 v10, a94\n\t"
    "v_accvgpr_read_b32 v11, a95\n\t"
    "s_waitcnt vmcnt(7)\n\t"
    "v_pk_add_f32 v[8:9], v[8:9], v[164:165]\n\t"
    "v_pk_add_f32 v[10:11], v[10:11], v[166:167]\n\t"
    "v_cvt_pk_bf16_f32 v8, v8, v9\n\t"
    "v_cvt_pk_bf16_f32 v9, v10, v11\n\t"
    "ds_write_b64 v96, v[8:9] offset:6528\n\t"
    "v_accvgpr_read_b32 v12, a112\n\t"
    "v_accvgpr_read_b32 v13, a113\n\t"
    "v_accvgpr_read_b32 v14, a114\n\t"
    "v_accvgpr_read_b32 v15, a115\n\t"
    "s_waitcnt vmcnt(6)\n\t"
    "v_pk_add_f32 v[12:13], v[12:13], v[168:169]\n\t"
    "v_pk_add_f32 v[14:15], v[14:15], v[170:171]\n\t"
    "v_cvt_pk_bf16_f32 v12, v12, v13\n\t"
    "v_cvt_pk_bf16_f32 v13, v14, v15\n\t"
    "ds_write_b64 v96, v[12:13] offset:6560\n\t"
    "v_accvgpr_read_b32 v16, a16\n\t"
    "v_accvgpr_read_b32 v17, a17\n\t"
    "v_accvgpr_read_b32 v18, a18\n\t"
    "v_accvgpr_read_b32 v19, a19\n\t"
    "s_waitcnt vmcnt(5)\n\t"
    "v_pk_add_f32 v[16:17], v[16:17], v[172:173]\n\t"
    "v_pk_add_f32 v[18:19], v[18:19], v[174:175]\n\t"
    "v_cvt_pk_bf16_f32 v16, v16, v17\n\t"
    "v_cvt_pk_bf16_f32 v17, v18, v19\n\t"
    "ds_write_b64 v96, v[16:17] offset:12800\n\t"
    "v_accvgpr_read_b32 v20, a36\n\t"
    "v_accvgpr_read_b32 v21, a37\n\t"
    "v_accvgpr_read_b32 v22, a38\n\t"
    "v_accvgpr_read_b32 v23, a39\n\t"
    "s_waitcnt vmcnt(4)\n\t"
    "v_pk_add_f32 v[20:21], v[20:21], v[176:177]\n\t"
    "v_pk_add_f32 v[22:23], v[22:23], v[178:179]\n\t"
    "v_cvt_pk_bf16_f32 v20, v20, v21\n\t"
    "v_cvt_pk_bf16_f32 v21, v22, v23\n\t"
    "ds_write_b64 v96, v[20:21] offset:12832\n\t"
    "v_accvgpr_read_b32 v24, a56\n\t"
    "v_accvgpr_read_b32 v25, a57\n\t"
    "v_accvgpr_read_b32 v26, a58\n\t"
    "v_accvgpr_read_b32 v27, a59\n\t"
    "s_waitcnt vmcnt(3)\n\t"
    "v_pk_add_f32 v[24:25], v[24:25], v[180:181]\n\t"
    "v_pk_add_f32 v[26:27], v[26:27], v[182:183]\n\t"
    "v_cvt_pk_bf16_f32 v24, v24, v25\n\t"
    "v_cvt_pk_bf16_f32 v25, v26, v27\n\t"
    "ds_write_b64 v96, v[24:25] offset:12864\n\t"
    "v_accvgpr_read_b32 v28, a76\n\t"
    "v_accvgpr_read_b32 v29, a77\n\t"
    "v_accvgpr_read_b32 v30, a78\n\t"
    "v_accvgpr_read_b32 v31, a79\n\t"
    "s_waitcnt vmcnt(2)\n\t"
    "v_pk_add_f32 v[28:29], v[28:29], v[184:185]\n\t"
    "v_pk_add_f32 v[30:31], v[30:31], v[186:187]\n\t"
    "v_cvt_pk_bf16_f32 v28, v28, v29\n\t"
    "v_cvt_pk_bf16_f32 v29, v30, v31\n\t"
    "ds_write_b64 v96, v[28:29] offset:12896\n\t"
    "v_accvgpr_read_b32 v0, a96\n\t"
    "v_accvgpr_read_b32 v1, a97\n\t"
    "v_accvgpr_read_b32 v2, a98\n\t"
    "v_accvgpr_read_b32 v3, a99\n\t"
    "s_waitcnt vmcnt(1)\n\t"
    "v_pk_add_f32 v[0:1], v[0:1], v[188:189]\n\t"
    "v_pk_add_f32 v[2:3], v[2:3], v[190:191]\n\t"
    "v_cvt_pk_bf16_f32 v0, v0, v1\n\t"
    "v_cvt_pk_bf16_f32 v1, v2, v3\n\t"
    "ds_write_b64 v96, v[0:1] offset:12928\n\t"
    "v_accvgpr_read_b32 v4, a116\n\t"
    "v_accvgpr_read_b32 v5, a117\n\t"
    "v_accvgpr_read_b32 v6, a118\n\t"
    "v_accvgpr_read_b32 v7, a119\n\t"
    "s_waitcnt vmcnt(0)\n\t"
    "v_pk_add_f32 v[4:5], v[4:5], v[192:193]\n\t"
    "v_pk_add_f32 v[6:7], v[6:7], v[194:195]\n\t"
    "v_cvt_pk_bf16_f32 v4, v4, v5\n\t"
    "v_cvt_pk_bf16_f32 v5, v6, v7\n\t"
    "ds_write_b64 v96, v[4:5] offset:12960\n\t"
    ".Lo_stageda:\n\t"
    "s_waitcnt lgkmcnt(0)\n\t"
    "s_barrier\n\t"
    "ds_read_b128 v[124:127], v97\n\t"
    "ds_read_b128 v[128:131], v98\n\t"
    "ds_read_b128 v[132:135], v99\n\t"
    "ds_read_b128 v[136:139], v100\n\t"
    "ds_read_b128 v[140:143], v101\n\t"
    "ds_read_b128 v[144:147], v102\n\t"
    "ds_read_b128 v[148:151], v103\n\t"
    "ds_read_b128 v[152:155], v104\n\t"
    "s_waitcnt lgkmcnt(7)\n\t"
    "buffer_store_dwordx4 v[124:127], v106, s[20:23], 0 offen\n\t"
    "s_waitcnt lgkmcnt(6)\n\t"
    "buffer_store_dwordx4 v[128:131], v107, s[20:23], 0 offen\n\t"
    "s_waitcnt lgkmcnt(5)\n\t"
    "buffer_store_dwordx4 v[132:135], v108, s[20:23], 0 offen\n\t"
    "s_waitcnt lgkmcnt(4)\n\t"
    "buffer_store_dwordx4 v[136:139], v109, s[20:23], 0 offen\n\t"
    "s_waitcnt lgkmcnt(3)\n\t"
    "buffer_store_dwordx4 v[140:143], v110, s[20:23], 0 offen\n\t"
    "s_waitcnt lgkmcnt(2)\n\t"
    "buffer_store_dwordx4 v[144:147], v111, s[20:23], 0 offen\n\t"
    "s_waitcnt lgkmcnt(1)\n\t"
    "buffer_store_dwordx4 v[148:151], v112, s[20:23], 0 offen\n\t"
    "s_waitcnt lgkmcnt(0)\n\t"
    "buffer_store_dwordx4 v[152:155], v113, s[20:23], 0 offen\n\t"
    "s_branch .Lk1_end\n\t"
    ".Lk1_tyb:\n\t"
    "v_mbcnt_lo_u32_b32 v115, -1, 0\n\t"
    "v_mbcnt_hi_u32_b32 v115, -1, v115\n\t"
    "v_lshlrev_b32 v214, 4, v115\n\t"
    "v_add_u32 v116, 0, v115\n\t"
    "v_mul_u32_u24 v117, 4096, v116\n\t"
    "v_lshrrev_b32 v117, 16, v117\n\t"
    "v_mul_u32_u24 v118, 16, v117\n\t"
    "v_sub_u32 v118, v116, v118\n\t"
    "v_bfe_u32 v119, v117, 0, 3\n\t"
    "v_lshlrev_b32 v119, 1, v119\n\t"
    "v_xor_b32 v118, v118, v119\n\t"
    "v_mul_u32_u24 v119, 1280, v117\n\t"
    "v_lshl_add_u32 v118, v118, 4, v119\n\t"
    "v_subrev_u32 v207, 0, v118\n\t"
    "v_add_u32 v116, 64, v115\n\t"
    "v_mul_u32_u24 v117, 4096, v116\n\t"
    "v_lshrrev_b32 v117, 16, v117\n\t"
    "v_mul_u32_u24 v118, 16, v117\n\t"
    "v_sub_u32 v118, v116, v118\n\t"
    "v_bfe_u32 v119, v117, 0, 3\n\t"
    "v_lshlrev_b32 v119, 1, v119\n\t"
    "v_xor_b32 v118, v118, v119\n\t"
    "v_mul_u32_u24 v119, 1280, v117\n\t"
    "v_lshl_add_u32 v118, v118, 4, v119\n\t"
    "v_subrev_u32 v208, 1024, v118\n\t"
    "v_add_u32 v116, 128, v115\n\t"
    "v_mul_u32_u24 v117, 4096, v116\n\t"
    "v_lshrrev_b32 v117, 16, v117\n\t"
    "v_mul_u32_u24 v118, 16, v117\n\t"
    "v_sub_u32 v118, v116, v118\n\t"
    "v_bfe_u32 v119, v117, 0, 3\n\t"
    "v_lshlrev_b32 v119, 1, v119\n\t"
    "v_xor_b32 v118, v118, v119\n\t"
    "v_mul_u32_u24 v119, 1280, v117\n\t"
    "v_lshl_add_u32 v118, v118, 4, v119\n\t"
    "v_subrev_u32 v209, 2048, v118\n\t"
    "v_add_u32 v116, 192, v115\n\t"
    "v_mul_u32_u24 v117, 4096, v116\n\t"
    "v_lshrrev_b32 v117, 16, v117\n\t"
    "v_mul_u32_u24 v118, 16, v117\n\t"
    "v_sub_u32 v118, v116, v118\n\t"
    "v_bfe_u32 v119, v117, 0, 3\n\t"
    "v_lshlrev_b32 v119, 1, v119\n\t"
    "v_xor_b32 v118, v118, v119\n\t"
    "v_mul_u32_u24 v119, 1280, v117\n\t"
    "v_lshl_add_u32 v118, v118, 4, v119\n\t"
    "v_subrev_u32 v210, 3072, v118\n\t"
    "s_lshr_b32 s38, s7, 1\n\t"
    "s_and_b32 s39, s7, 1\n\t"
    "s_and_b32 s40, s78, 1\n\t"
    "s_mul_i32 s41, s40, 3932160\n\t"
    "s_bfe_u32 s42, s78, 0x10006\n\t"
    "s_mul_i32 s42, s42, 256\n\t"
    "s_add_u32 s42, s42, 768\n\t"
    "s_mov_b32 s20, s42\n\t"
    "s_add_u32 s41, s41, s42\n\t"
    "s_add_u32 s8, s94, s41\n\t"
    "s_addc_u32 s9, s95, 0\n\t"
    "s_sub_u32 s10, 3932160, s42\n\t"
    "s_lshl_b32 s41, s6, 4\n\t"
    "s_add_u32 s41, s41, s70\n\t"
    "s_lshl_b32 s41, s41, 1\n\t"
    "s_add_u32 s41, s41, s40\n\t"
    "s_mul_i32 s41, s41, 983040\n\t"
    "s_add_u32 s12, s68, s41\n\t"
    "s_addc_u32 s13, s69, 0\n\t"
    "s_mov_b32 s14, 983040\n\t"
    "s_mul_i32 s36, s7, 20480\n\t"
    "s_mul_i32 s37, s7, 5120\n\t"
    "s_mul_i32 s34, s7, 4096\n\t"
    "s_add_u32 s35, s37, 16384\n\t"
    "s_add_u32 s58, s34, 0\n\t"
    "s_add_u32 s59, s35, 0\n\t"
    "s_add_u32 m0, s59, 0\n\t"
    "s_nop 0\n\t"
    "buffer_load_dwordx4 v214, s[12:15], s37 offen sc1 lds\n\t"
    "buffer_load_dwordx4 v214, s[12:15], s37 offen offset:1024 sc1 lds\n\t"
    "buffer_load_dwordx4 v214, s[12:15], s37 offen offset:2048 sc1 lds\n\t"
    "buffer_load_dwordx4 v214, s[12:15], s37 offen offset:3072 sc1 lds\n\t"
    "s_add_u32 m0, s59, 4096\n\t"
    "s_add_u32 s40, s37, 4096\n\t"
    "s_nop 0\n\t"
    "buffer_load_dwordx4 v214, s[12:15], s40 offen sc1 lds\n\t"
    "s_add_u32 s37, s37, 20480\n\t"
    "s_add_u32 m0, s58, 0\n\t"
    "s_nop 0\n\t"
    "buffer_load_dwordx4 v207, s[8:11], s36 offen lds\n\t"
    "buffer_load_dwordx4 v208, s[8:11], s36 offen offset:1024 lds\n\t"
    "buffer_load_dwordx4 v209, s[8:11], s36 offen offset:2048 lds\n\t"
    "buffer_load_dwordx4 v210, s[8:11], s36 offen offset:3072 lds\n\t"
    "s_add_u32 s36, s36, 81920\n\t"
    "v_lshrrev_b32 v116, 4, v115\n\t"
    "v_bfe_u32 v117, v115, 2, 2\n\t"
    "v_and_b32 v118, 3, v115\n\t"
    "v_lshl_add_u32 v117, v116, 2, v117\n\t"
    "v_bfe_u32 v120, v117, 0, 3\n\t"
    "v_mul_u32_u24 v117, 256, v117\n\t"
    "v_lshl_add_u32 v117, v118, 3, v117\n\t"
    "s_mul_i32 s40, s39, 4\n\t"
    "s_add_u32 s41, s40, 0\n\t"
    "v_xor_b32 v119, s41, v120\n\t"
    "v_lshl_add_u32 v204, v119, 5, v117\n\t"
    "s_add_u32 s41, s40, 1\n\t"
    "v_xor_b32 v119, s41, v120\n\t"
    "v_lshl_add_u32 v205, v119, 5, v117\n\t"
    "s_add_u32 s41, s40, 2\n\t"
    "v_xor_b32 v119, s41, v120\n\t"
    "v_lshl_add_u32 v222, v119, 5, v117\n\t"
    "s_add_u32 s41, s40, 3\n\t"
    "v_xor_b32 v119, s41, v120\n\t"
    "v_lshl_add_u32 v223, v119, 5, v117\n\t"
    "s_mul_i32 s40, s38, 5120\n\t"
    "s_add_u32 s40, s40, 16384\n\t"
    "v_add_u32 v206, s40, v214\n\t"
    "s_nop 4\n\t"
    "s_mov_b32 s42, 0\n\t"
    "s_mov_b32 s43, 4096\n\t"
    "v_accvgpr_read_b32 v0, a0\n\t"
    "v_accvgpr_read_b32 v1, a1\n\t"
    "v_accvgpr_read_b32 v2, a2\n\t"
    "v_accvgpr_read_b32 v3, a3\n\t"
    "v_accvgpr_read_b32 v4, a20\n\t"
    "v_accvgpr_read_b32 v5, a21\n\t"
    "v_accvgpr_read_b32 v6, a22\n\t"
    "v_accvgpr_read_b32 v7, a23\n\t"
    "v_pk_mul_f32 v[0:1], v[0:1], v[144:145] op_sel_hi:[1,0]\n\t"
    "v_pk_mul_f32 v[2:3], v[2:3], v[144:145] op_sel_hi:[1,0]\n\t"
    "v_pk_mul_f32 v[4:5], v[4:5], v[144:145] op_sel_hi:[1,0]\n\t"
    "v_pk_mul_f32 v[6:7], v[6:7], v[144:145] op_sel_hi:[1,0]\n\t"
    "v_cvt_pk_bf16_f32 v0, v0, v1\n\t"
    "v_cvt_pk_bf16_f32 v1, v2, v3\n\t"
    "v_cvt_pk_bf16_f32 v2, v4, v5\n\t"
    "v_cvt_pk_bf16_f32 v3, v6, v7\n\t"
    "buffer_store_dwordx4 v[0:3], v252, s[24:27], s42 offen sc1\n\t"
    "s_add_u32 s58, s34, 36864\n\t"
    "s_add_u32 s59, s35, 36864\n\t"
    "s_add_u32 m0, s59, 0\n\t"
    "s_nop 0\n\t"
    "buffer_load_dwordx4 v214, s[12:15], s37 offen sc1 lds\n\t"
    "buffer_load_dwordx4 v214, s[12:15], s37 offen offset:1024 sc1 lds\n\t"
    "v_accvgpr_read_b32 v8, a4\n\t"
    "v_accvgpr_read_b32 v9, a5\n\t"
    "v_accvgpr_read_b32 v10, a6\n\t"
    "v_accvgpr_read_b32 v11, a7\n\t"
    "v_accvgpr_read_b32 v12, a24\n\t"
    "v_accvgpr_read_b32 v13, a25\n\t"
    "v_accvgpr_read_b32 v14, a26\n\t"
    "v_accvgpr_read_b32 v15, a27\n\t"
    "v_pk_mul_f32 v[8:9], v[8:9], v[146:147] op_sel_hi:[1,0]\n\t"
    "v_pk_mul_f32 v[10:11], v[10:11], v[146:147] op_sel_hi:[1,0]\n\t"
    "v_pk_mul_f32 v[12:13], v[12:13], v[146:147] op_sel_hi:[1,0]\n\t"
    "v_pk_mul_f32 v[14:15], v[14:15], v[146:147] op_sel_hi:[1,0]\n\t"
    "v_cvt_pk_bf16_f32 v8, v8, v9\n\t"
    "v_cvt_pk_bf16_f32 v9, v10, v11\n\t"
    "v_cvt_pk_bf16_f32 v10, v12, v13\n\t"
    "v_cvt_pk_bf16_f32 v11, v14, v15\n\t"
    "buffer_store_dwordx4 v[8:11], v252, s[24:27], s42 offen offset:1024 sc1\n\t"
    "buffer_load_dwordx4 v214, s[12:15], s37 offen offset:2048 sc1 lds\n\t"
    "buffer_load_dwordx4 v214, s[12:15], s37 offen offset:3072 sc1 lds\n\t"
    "v_accvgpr_read_b32 v16, a8\n\t"
    "v_accvgpr_read_b32 v17, a9\n\t"
    "v_accvgpr_read_b32 v18, a10\n\t"
    "v_accvgpr_read_b32 v19, a11\n\t"
    "v_accvgpr_read_b32 v20, a28\n\t"
    "v_accvgpr_read_b32 v21, a29\n\t"
    "v_accvgpr_read_b32 v22, a30\n\t"
    "v_accvgpr_read_b32 v23, a31\n\t"
    "v_pk_mul_f32 v[16:17], v[16:17], v[148:149] op_sel_hi:[1,0]\n\t"
    "v_pk_mul_f32 v[18:19], v[18:19], v[148:149] op_sel_hi:[1,0]\n\t"
    "v_pk_mul_f32 v[20:21], v[20:21], v[148:149] op_sel_hi:[1,0]\n\t"
    "v_pk_mul_f32 v[22:23], v[22:23], v[148:149] op_sel_hi:[1,0]\n\t"
    "v_cvt_pk_bf16_f32 v16, v16, v17\n\t"
    "v_cvt_pk_bf16_f32 v17, v18, v19\n\t"
    "v_cvt_pk_bf16_f32 v18, v20, v21\n\t"
    "v_cvt_pk_bf16_f32 v19, v22, v23\n\t"
    "buffer_store_dwordx4 v[16:19], v252, s[24:27], s42 offen offset:2048 sc1\n\t"
    "s_add_u32 m0, s59, 4096\n\t"
    "s_add_u32 s40, s37, 4096\n\t"
    "s_nop 0\n\t"
    "buffer_load_dwordx4 v214, s[12:15], s40 offen sc1 lds\n\t"
    "s_add_u32 s37, s37, 20480\n\t"
    "s_add_u32 m0, s58, 0\n\t"
    "s_nop 0\n\t"
    "buffer_load_dwordx4 v207, s[8:11], s36 offen lds\n\t"
    "v_accvgpr_read_b32 v24, a12\n\t"
    "v_accvgpr_read_b32 v25, a13\n\t"
    "v_accvgpr_read_b32 v26, a14\n\t"
    "v_accvgpr_read_b32 v27, a15\n\t"
    "v_accvgpr_read_b32 v28, a32\n\t"
    "v_accvgpr_read_b32 v29, a33\n\t"
    "v_accvgpr_read_b32 v30, a34\n\t"
    "v_accvgpr_read_b32 v31, a35\n\t"
    "v_pk_mul_f32 v[24:25], v[24:25], v[150:151] op_sel_hi:[1,0]\n\t"
    "v_pk_mul_f32 v[26:27], v[26:27], v[150:151] op_sel_hi:[1,0]\n\t"
    "v_pk_mul_f32 v[28:29], v[28:29], v[150:151] op_sel_hi:[1,0]\n\t"
    "v_pk_mul_f32 v[30:31], v[30:31], v[150:151] op_sel_hi:[1,0]\n\t"
    "v_cvt_pk_bf16_f32 v24, v24, v25\n\t"
    "v_cvt_pk_bf16_f32 v25, v26, v27\n\t"
    "v_cvt_pk_bf16_f32 v26, v28, v29\n\t"
    "v_cvt_pk_bf16_f32 v27, v30, v31\n\t"
    "buffer_store_dwordx4 v[24:27], v252, s[24:27], s42 offen offset:3072 sc1\n\t"
    "buffer_load_dwordx4 v208, s[8:11], s36 offen offset:1024 lds\n\t"
    "buffer_load_dwordx4 v209, s[8:11], s36 offen offset:2048 lds\n\t"
    "v_accvgpr_read_b32 v32, a16\n\t"
    "v_accvgpr_read_b32 v33, a17\n\t"
    "v_accvgpr_read_b32 v34, a18\n\t"
    "v_accvgpr_read_b32 v35, a19\n\t"
    "v_accvgpr_read_b32 v36, a36\n\t"
    "v_accvgpr_read_b32 v37, a37\n\t"
    "v_accvgpr_read_b32 v38, a38\n\t"
    "v_accvgpr_read_b32 v39, a39\n\t"
    "v_pk_mul_f32 v[32:33], v[32:33], v[152:153] op_sel_hi:[1,0]\n\t"
    "v_pk_mul_f32 v[34:35], v[34:35], v[152:153] op_sel_hi:[1,0]\n\t"
    "v_pk_mul_f32 v[36:37], v[36:37], v[152:153] op_sel_hi:[1,0]\n\t"
    "v_pk_mul_f32 v[38:39], v[38:39], v[152:153] op_sel_hi:[1,0]\n\t"
    "v_cvt_pk_bf16_f32 v32, v32, v33\n\t"
    "v_cvt_pk_bf16_f32 v33, v34, v35\n\t"
    "v_cvt_pk_bf16_f32 v34, v36, v37\n\t"
    "v_cvt_pk_bf16_f32 v35, v38, v39\n\t"
    "buffer_store_dwordx4 v[32:35], v252, s[24:27], s43 offen sc1\n\t"
    "buffer_load_dwordx4 v210, s[8:11], s36 offen offset:3072 lds\n\t"
    "s_add_u32 s36, s36, 81920\n\t"
    "s_add_u32 s58, s34, 73728\n\t"
    "s_add_u32 s59, s35, 73728\n\t"
    "s_add_u32 m0, s59, 0\n\t"
    "s_nop 0\n\t"
    "buffer_load_dwordx4 v214, s[12:15], s37 offen sc1 lds\n\t"
    "s_mov_b32 s42, 10240\n\t"
    "s_mov_b32 s43, 14336\n\t"
    "v_accvgpr_read_b32 v40, a40\n\t"
    "v_accvgpr_read_b32 v41, a41\n\t"
    "v_accvgpr_read_b32 v42, a42\n\t"
    "v_accvgpr_read_b32 v43, a43\n\t"
    "v_accvgpr_read_b32 v44, a60\n\t"
    "v_accvgpr_read_b32 v45, a61\n\t"
    "v_accvgpr_read_b32 v46, a62\n\t"
    "v_accvgpr_read_b32 v47, a63\n\t"
    "v_pk_mul_f32 v[40:41], v[40:41], v[144:145] op_sel_hi:[1,0]\n\t"
    "v_pk_mul_f32 v[42:43], v[42:43], v[144:145] op_sel_hi:[1,0]\n\t"
    "v_pk_mul_f32 v[44:45], v[44:45], v[144:145] op_sel_hi:[1,0]\n\t"
    "v_pk_mul_f32 v[46:47], v[46:47], v[144:145] op_sel_hi:[1,0]\n\t"
    "v_cvt_pk_bf16_f32 v40, v40, v41\n\t"
    "v_cvt_pk_bf16_f32 v41, v42, v43\n\t"
    "v_cvt_pk_bf16_f32 v42, v44, v45\n\t"
    "v_cvt_pk_bf16_f32 v43, v46, v47\n\t"
    "buffer_store_dwordx4 v[40:43], v252, s[24:27], s42 offen sc1\n\t"
    "buffer_load_dwordx4 v214, s[12:15], s37 offen offset:1024 sc1 lds\n\t"
    "buffer_load_dwordx4 v214, s[12:15], s37 offen offset:2048 sc1 lds\n\t"
    "v_accvgpr_read_b32 v48, a44\n\t"
    "v_accvgpr_read_b32 v49, a45\n\t"
    "v_accvgpr_read_b32 v50, a46\n\t"
    "v_accvgpr_read_b32 v51, a47\n\t"
    "v_accvgpr_read_b32 v52, a64\n\t"
    "v_accvgpr_read_b32 v53, a65\n\t"
    "v_accvgpr_read_b32 v54, a66\n\t"
    "v_accvgpr_read_b32 v55, a67\n\t"
    "v_pk_mul_f32 v[48:49], v[48:49], v[146:147] op_sel_hi:[1,0]\n\t"
    "v_pk_mul_f32 v[50:51], v[50:51], v[146:147] op_sel_hi:[1,0]\n\t"
    "v_pk_mul_f32 v[52:53], v[52:53], v[146:147] op_sel_hi:[1,0]\n\t"
    "v_pk_mul_f32 v[54:55], v[54:55], v[146:147] op_sel_hi:[1,0]\n\t"
    "v_cvt_pk_bf16_f32 v48, v48, v49\n\t"
    "v_cvt_pk_bf16_f32 v49, v50, v51\n\t"
    "v_cvt_pk_bf16_f32 v50, v52, v53\n\t"
    "v_cvt_pk_bf16_f32 v51, v54, v55\n\t"
    "buffer_store_dwordx4 v[48:51], v252, s[24:27], s42 offen offset:1024 sc1\n\t"
    "buffer_load_dwordx4 v214, s[12:15], s37 offen offset:3072 sc1 lds\n\t"
    "s_add_u32 m0, s59, 4096\n\t"
    "s_add_u32 s40, s37, 4096\n\t"
    "s_nop 0\n\t"
    "buffer_load_dwordx4 v214, s[12:15], s40 offen sc1 lds\n\t"
    "s_add_u32 s37, s37, 20480\n\t"
    "v_accvgpr_read_b32 v56, a48\n\t"
    "v_accvgpr_read_b32 v57, a49\n\t"
    "v_accvgpr_read_b32 v58, a50\n\t"
    "v_accvgpr_read_b32 v59, a51\n\t"
    "v_accvgpr_read_b32 v60, a68\n\t"
    "v_accvgpr_read_b32 v61, a69\n\t"
    "v_accvgpr_read_b32 v62, a70\n\t"
    "v_accvgpr_read_b32 v63, a71\n\t"
    "v_pk_mul_f32 v[56:57], v[56:57], v[148:149] op_sel_hi:[1,0]\n\t"
    "v_pk_mul_f32 v[58:59], v[58:59], v[148:149] op_sel_hi:[1,0]\n\t"
    "v_pk_mul_f32 v[60:61], v[60:61], v[148:149] op_sel_hi:[1,0]\n\t"
    "v_pk_mul_f32 v[62:63], v[62:63], v[148:149] op_sel_hi:[1,0]\n\t"
    "v_cvt_pk_bf16_f32 v56, v56, v57\n\t"
    "v_cvt_pk_bf16_f32 v57, v58, v59\n\t"
    "v_cvt_pk_bf16_f32 v58, v60, v61\n\t"
    "v_cvt_pk_bf16_f32 v59, v62, v63\n\t"
    "buffer_store_dwordx4 v[56:59], v252, s[24:27], s42 offen offset:2048 sc1\n\t"
    "s_add_u32 m0, s58, 0\n\t"
    "s_nop 0\n\t"
    "buffer_load_dwordx4 v207, s[8:11], s36 offen lds\n\t"
    "buffer_load_dwordx4 v208, s[8:11], s36 offen offset:1024 lds\n\t"
    "v_accvgpr_read_b32 v64, a52\n\t"
    "v_accvgpr_read_b32 v65, a53\n\t"
    "v_accvgpr_read_b32 v66, a54\n\t"
    "v_accvgpr_read_b32 v67, a55\n\t"
    "v_accvgpr_read_b32 v68, a72\n\t"
    "v_accvgpr_read_b32 v69, a73\n\t"
    "v_accvgpr_read_b32 v70, a74\n\t"
    "v_accvgpr_read_b32 v71, a75\n\t"
    "v_pk_mul_f32 v[64:65], v[64:65], v[150:151] op_sel_hi:[1,0]\n\t"
    "v_pk_mul_f32 v[66:67], v[66:67], v[150:151] op_sel_hi:[1,0]\n\t"
    "v_pk_mul_f32 v[68:69], v[68:69], v[150:151] op_sel_hi:[1,0]\n\t"
    "v_pk_mul_f32 v[70:71], v[70:71], v[150:151] op_sel_hi:[1,0]\n\t"
    "v_cvt_pk_bf16_f32 v64, v64, v65\n\t"
    "v_cvt_pk_bf16_f32 v65, v66, v67\n\t"
    "v_cvt_pk_bf16_f32 v66, v68, v69\n\t"
    "v_cvt_pk_bf16_f32 v67, v70, v71\n\t"
    "buffer_store_dwordx4 v[64:67], v252, s[24:27], s42 offen offset:3072 sc1\n\t"
    "buffer_load_dwordx4 v209, s[8:11], s36 offen offset:2048 lds\n\t"
    "buffer_load_dwordx4 v210, s[8:11], s36 offen offset:3072 lds\n\t"
    "s_add_u32 s36, s36, 81920\n\t"
    "v_accvgpr_read_b32 v72, a56\n\t"
    "v_accvgpr_read_b32 v73, a57\n\t"
    "v_accvgpr_read_b32 v74, a58\n\t"
    "v_accvgpr_read_b32 v75, a59\n\t"
    "v_accvgpr_read_b32 v76, a76\n\t"
    "v_accvgpr_read_b32 v77, a77\n\t"
    "v_accvgpr_read_b32 v78, a78\n\t"
    "v_accvgpr_read_b32 v79, a79\n\t"
    "v_pk_mul_f32 v[72:73], v[72:73], v[152:153] op_sel_hi:[1,0]\n\t"
    "v_pk_mul_f32 v[74:75], v[74:75], v[152:153] op_sel_hi:[1,0]\n\t"
    "v_pk_mul_f32 v[76:77], v[76:77], v[152:153] op_sel_hi:[1,0]\n\t"
    "v_pk_mul_f32 v[78:79], v[78:79], v[152:153] op_sel_hi:[1,0]\n\t"
    "v_cvt_pk_bf16_f32 v72, v72, v73\n\t"
    "v_cvt_pk_bf16_f32 v73, v74, v75\n\t"
    "v_cvt_pk_bf16_f32 v74, v76, v77\n\t"
    "v_cvt_pk_bf16_f32 v75, v78, v79\n\t"
    "buffer_store_dwordx4 v[72:75], v252, s[24:27], s43 offen sc1\n\t"
    "s_add_u32 s58, s34, 110592\n\t"
    "s_add_u32 s59, s35, 110592\n\t"
    "s_add_u32 m0, s59, 0\n\t"
    "s_nop 0\n\t"
    "buffer_load_dwordx4 v214, s[12:15], s37 offen sc1 lds\n\t"
    "buffer_load_dwordx4 v214, s[12:15], s37 offen offset:1024 sc1 lds\n\t"
    "s_mov_b32 s42, 20480\n\t"
    "s_mov_b32 s43, 24576\n\t"
    "v_accvgpr_read_b32 v80, a80\n\t"
    "v_accvgpr_read_b32 v81, a81\n\t"
    "v_accvgpr_read_b32 v82, a82\n\t"
    "v_accvgpr_read_b32 v83, a83\n\t"
    "v_accvgpr_read_b32 v84, a100\n\t"
    "v_accvgpr_read_b32 v85, a101\n\t"
    "v_accvgpr_read_b32 v86, a102\n\t"
    "v_accvgpr_read_b32 v87, a103\n\t"
    "v_pk_mul_f32 v[80:81], v[80:81], v[144:145] op_sel_hi:[1,0]\n\t"
    "v_pk_mul_f32 v[82:83], v[82:83], v[144:145] op_sel_hi:[1,0]\n\t"
    "v_pk_mul_f32 v[84:85], v[84:85], v[144:145] op_sel_hi:[1,0]\n\t"
    "v_pk_mul_f32 v[86:87], v[86:87], v[144:145] op_sel_hi:[1,0]\n\t"
    "v_cvt_pk_bf16_f32 v80, v80, v81\n\t"
    "v_cvt_pk_bf16_f32 v81, v82, v83\n\t"
    "v_cvt_pk_bf16_f32 v82, v84, v85\n\t"
    "v_cvt_pk_bf16_f32 v83, v86, v87\n\t"
    "buffer_store_dwordx4 v[80:83], v252, s[24:27], s42 offen sc1\n\t"
    "buffer_load_dwordx4 v214, s[12:15], s37 offen offset:2048 sc1 lds\n\t"
    "buffer_load_dwordx4 v214, s[12:15], s37 offen offset:3072 sc1 lds\n\t"
    "v_accvgpr_read_b32 v88, a84\n\t"
    "v_accvgpr_read_b32 v89, a85\n\t"
    "v_accvgpr_read_b32 v90, a86\n\t"
    "v_accvgpr_read_b32 v91, a87\n\t"
    "v_accvgpr_read_b32 v92, a104\n\t"
    "v_accvgpr_read_b32 v93, a105\n\t"
    "v_accvgpr_read_b32 v94, a106\n\t"
    "v_accvgpr_read_b32 v95, a107\n\t"
    "v_pk_mul_f32 v[88:89], v[88:89], v[146:147] op_sel_hi:[1,0]\n\t"
    "v_pk_mul_f32 v[90:91], v[90:91], v[146:147] op_sel_hi:[1,0]\n\t"
    "v_pk_mul_f32 v[92:93], v[92:93], v[146:147] op_sel_hi:[1,0]\n\t"
    "v_pk_mul_f32 v[94:95], v[94:95], v[146:147] op_sel_hi:[1,0]\n\t"
    "v_cvt_pk_bf16_f32 v88, v88, v89\n\t"
    "v_cvt_pk_bf16_f32 v89, v90, v91\n\t"
    "v_cvt_pk_bf16_f32 v90, v92, v93\n\t"
    "v_cvt_pk_bf16_f32 v91, v94, v95\n\t"
    "buffer_store_dwordx4 v[88:91], v252, s[24:27], s42 offen offset:1024 sc1\n\t"
    "s_add_u32 m0, s59, 4096\n\t"
    "s_add_u32 s40, s37, 4096\n\t"
    "s_nop 0\n\t"
    "buffer_load_dwordx4 v214, s[12:15], s40 offen sc1 lds\n\t"
    "s_add_u32 s37, s37, 20480\n\t"
    "v_accvgpr_read_b32 v0, a88\n\t"
    "v_accvgpr_read_b32 v1, a89\n\t"
    "v_accvgpr_read_b32 v2, a90\n\t"
    "v_accvgpr_read_b32 v3, a91\n\t"
    "v_accvgpr_read_b32 v4, a108\n\t"
    "v_accvgpr_read_b32 v5, a109\n\t"
    "v_accvgpr_read_b32 v6, a110\n\t"
    "v_accvgpr_read_b32 v7, a111\n\t"
    "v_pk_mul_f32 v[0:1], v[0:1], v[148:149] op_sel_hi:[1,0]\n\t"
    "v_pk_mul_f32 v[2:3], v[2:3], v[148:149] op_sel_hi:[1,0]\n\t"
    "v_pk_mul_f32 v[4:5], v[4:5], v[148:149] op_sel_hi:[1,0]\n\t"
    "v_pk_mul_f32 v[6:7], v[6:7], v[148:149] op_sel_hi:[1,0]\n\t"
    "v_cvt_pk_bf16_f32 v0, v0, v1\n\t"
    "v_cvt_pk_bf16_f32 v1, v2, v3\n\t"
    "v_cvt_pk_bf16_f32 v2, v4, v5\n\t"
    "v_cvt_pk_bf16_f32 v3, v6, v7\n\t"
    "buffer_store_dwordx4 v[0:3], v252, s[24:27], s42 offen offset:2048 sc1\n\t"
    "v_accvgpr_read_b32 v8, a92\n\t"
    "v_accvgpr_read_b32 v9, a93\n\t"
    "v_accvgpr_read_b32 v10, a94\n\t"
    "v_accvgpr_read_b32 v11, a95\n\t"
    "v_accvgpr_read_b32 v12, a112\n\t"
    "v_accvgpr_read_b32 v13, a113\n\t"
    "v_accvgpr_read_b32 v14, a114\n\t"
    "v_accvgpr_read_b32 v15, a115\n\t"
    "v_pk_mul_f32 v[8:9], v[8:9], v[150:151] op_sel_hi:[1,0]\n\t"
    "v_pk_mul_f32 v[10:11], v[10:11], v[150:151] op_sel_hi:[1,0]\n\t"
    "v_pk_mul_f32 v[12:13], v[12:13], v[150:151] op_sel_hi:[1,0]\n\t"
    "v_pk_mul_f32 v[14:15], v[14:15], v[150:151] op_sel_hi:[1,0]\n\t"
    "v_cvt_pk_bf16_f32 v8, v8, v9\n\t"
    "v_cvt_pk_bf16_f32 v9, v10, v11\n\t"
    "v_cvt_pk_bf16_f32 v10, v12, v13\n\t"
    "v_cvt_pk_bf16_f32 v11, v14, v15\n\t"
    "buffer_store_dwordx4 v[8:11], v252, s[24:27], s42 offen offset:3072 sc1\n\t"
    "v_accvgpr_read_b32 v16, a96\n\t"
    "v_accvgpr_read_b32 v17, a97\n\t"
    "v_accvgpr_read_b32 v18, a98\n\t"
    "v_accvgpr_read_b32 v19, a99\n\t"
    "v_accvgpr_read_b32 v20, a116\n\t"
    "v_accvgpr_read_b32 v21, a117\n\t"
    "v_accvgpr_read_b32 v22, a118\n\t"
    "v_accvgpr_read_b32 v23, a119\n\t"
    "v_pk_mul_f32 v[16:17], v[16:17], v[152:153] op_sel_hi:[1,0]\n\t"
    "v_pk_mul_f32 v[18:19], v[18:19], v[152:153] op_sel_hi:[1,0]\n\t"
    "v_pk_mul_f32 v[20:21], v[20:21], v[152:153] op_sel_hi:[1,0]\n\t"
    "v_pk_mul_f32 v[22:23], v[22:23], v[152:153] op_sel_hi:[1,0]\n\t"
    "v_cvt_pk_bf16_f32 v16, v16, v17\n\t"
    "v_cvt_pk_bf16_f32 v17, v18, v19\n\t"
    "v_cvt_pk_bf16_f32 v18, v20, v21\n\t"
    "v_cvt_pk_bf16_f32 v19, v22, v23\n\t"
    "buffer_store_dwordx4 v[16:19], v252, s[24:27], s43 offen sc1\n\t"
    "s_mov_b32 s42, 30720\n\t"
    "s_mov_b32 s43, 34816\n\t"
    "v_accvgpr_read_b32 v24, a120\n\t"
    "v_accvgpr_read_b32 v25, a121\n\t"
    "v_accvgpr_read_b32 v26, a122\n\t"
    "v_accvgpr_read_b32 v27, a123\n\t"
    "v_accvgpr_read_b32 v28, a140\n\t"
    "v_accvgpr_read_b32 v29, a141\n\t"
    "v_accvgpr_read_b32 v30, a142\n\t"
    "v_accvgpr_read_b32 v31, a143\n\t"
    "v_pk_mul_f32 v[24:25], v[24:25], v[144:145] op_sel_hi:[1,0]\n\t"
    "v_pk_mul_f32 v[26:27], v[26:27], v[144:145] op_sel_hi:[1,0]\n\t"
    "v_pk_mul_f32 v[28:29], v[28:29], v[144:145] op_sel_hi:[1,0]\n\t"
    "v_pk_mul_f32 v[30:31], v[30:31], v[144:145] op_sel_hi:[1,0]\n\t"
    "v_cvt_pk_bf16_f32 v24, v24, v25\n\t"
    "v_cvt_pk_bf16_f32 v25, v26, v27\n\t"
    "v_cvt_pk_bf16_f32 v26, v28, v29\n\t"
    "v_cvt_pk_bf16_f32 v27, v30, v31\n\t"
    "buffer_store_dwordx4 v[24:27], v252, s[24:27], s42 offen sc1\n\t"
    "v_accvgpr_read_b32 v32, a124\n\t"
    "v_accvgpr_read_b32 v33, a125\n\t"
    "v_accvgpr_read_b32 v34, a126\n\t"
    "v_accvgpr_read_b32 v35, a127\n\t"
    "v_accvgpr_read_b32 v36, a144\n\t"
    "v_accvgpr_read_b32 v37, a145\n\t"
    "v_accvgpr_read_b32 v38, a146\n\t"
    "v_accvgpr_read_b32 v39, a147\n\t"
    "v_pk_mul_f32 v[32:33], v[32:33], v[146:147] op_sel_hi:[1,0]\n\t"
    "v_pk_mul_f32 v[34:35], v[34:35], v[146:147] op_sel_hi:[1,0]\n\t"
    "v_pk_mul_f32 v[36:37], v[36:37], v[146:147] op_sel_hi:[1,0]\n\t"
    "v_pk_mul_f32 v[38:39], v[38:39], v[146:147] op_sel_hi:[1,0]\n\t"
    "v_cvt_pk_bf16_f32 v32, v32, v33\n\t"
    "v_cvt_pk_bf16_f32 v33, v34, v35\n\t"
    "v_cvt_pk_bf16_f32 v34, v36, v37\n\t"
    "v_cvt_pk_bf16_f32 v35, v38, v39\n\t"
    "buffer_store_dwordx4 v[32:35], v252, s[24:27], s42 offen offset:1024 sc1\n\t"
    "v_accvgpr_read_b32 v40, a128\n\t"
    "v_accvgpr_read_b32 v41, a129\n\t"
    "v_accvgpr_read_b32 v42, a130\n\t"
    "v_accvgpr_read_b32 v43, a131\n\t"
    "v_accvgpr_read_b32 v44, a148\n\t"
    "v_accvgpr_read_b32 v45, a149\n\t"
    "v_accvgpr_read_b32 v46, a150\n\t"
    "v_accvgpr_read_b32 v47, a151\n\t"
    "v_pk_mul_f32 v[40:41], v[40:41], v[148:149] op_sel_hi:[1,0]\n\t"
    "v_pk_mul_f32 v[42:43], v[42:43], v[148:149] op_sel_hi:[1,0]\n\t"
    "v_pk_mul_f32 v[44:45], v[44:45], v[148:149] op_sel_hi:[1,0]\n\t"
    "v_pk_mul_f32 v[46:47], v[46:47], v[148:149] op_sel_hi:[1,0]\n\t"
    "v_cvt_pk_bf16_f32 v40, v40, v41\n\t"
    "v_cvt_pk_bf16_f32 v41, v42, v43\n\t"
    "v_cvt_pk_bf16_f32 v42, v44, v45\n\t"
    "v_cvt_pk_bf16_f32 v43, v46, v47\n\t"
    "buffer_store_dwordx4 v[40:43], v252, s[24:27], s42 offen offset:2048 sc1\n\t"
    "v_accvgpr_read_b32 v48, a132\n\t"
    "v_accvgpr_read_b32 v49, a133\n\t"
    "v_accvgpr_read_b32 v50, a134\n\t"
    "v_accvgpr_read_b32 v51, a135\n\t"
    "v_accvgpr_read_b32 v52, a152\n\t"
    "v_accvgpr_read_b32 v53, a153\n\t"
    "v_accvgpr_read_b32 v54, a154\n\t"
    "v_accvgpr_read_b32 v55, a155\n\t"
    "v_pk_mul_f32 v[48:49], v[48:49], v[150:151] op_sel_hi:[1,0]\n\t"
    "v_pk_mul_f32 v[50:51], v[50:51], v[150:151] op_sel_hi:[1,0]\n\t"
    "v_pk_mul_f32 v[52:53], v[52:53], v[150:151] op_sel_hi:[1,0]\n\t"
    "v_pk_mul_f32 v[54:55], v[54:55], v[150:151] op_sel_hi:[1,0]\n\t"
    "v_cvt_pk_bf16_f32 v48, v48, v49\n\t"
    "v_cvt_pk_bf16_f32 v49, v50, v51\n\t"
    "v_cvt_pk_bf16_f32 v50, v52, v53\n\t"
    "v_cvt_pk_bf16_f32 v51, v54, v55\n\t"
    "buffer_store_dwordx4 v[48:51], v252, s[24:27], s42 offen offset:3072 sc1\n\t"
    "v_accvgpr_read_b32 v56, a136\n\t"
    "v_accvgpr_read_b32 v57, a137\n\t"
    "v_accvgpr_read_b32 v58, a138\n\t"
    "v_accvgpr_read_b32 v59, a139\n\t"
    "v_accvgpr_read_b32 v60, a156\n\t"
    "v_accvgpr_read_b32 v61, a157\n\t"
    "v_accvgpr_read_b32 v62, a158\n\t"
    "v_accvgpr_read_b32 v63, a159\n\t"
    "v_pk_mul_f32 v[56:57], v[56:57], v[152:153] op_sel_hi:[1,0]\n\t"
    "v_pk_mul_f32 v[58:59], v[58:59], v[152:153] op_sel_hi:[1,0]\n\t"
    "v_pk_mul_f32 v[60:61], v[60:61], v[152:153] op_sel_hi:[1,0]\n\t"
    "v_pk_mul_f32 v[62:63], v[62:63], v[152:153] op_sel_hi:[1,0]\n\t"
    "v_cvt_pk_bf16_f32 v56, v56, v57\n\t"
    "v_cvt_pk_bf16_f32 v57, v58, v59\n\t"
    "v_cvt_pk_bf16_f32 v58, v60, v61\n\t"
    "v_cvt_pk_bf16_f32 v59, v62, v63\n\t"
    "buffer_store_dwordx4 v[56:59], v252, s[24:27], s43 offen sc1\n\t"
    "s_mov_b32 s42, 40960\n\t"
    "s_mov_b32 s43, 45056\n\t"
    "v_accvgpr_read_b32 v64, a160\n\t"
    "v_accvgpr_read_b32 v65, a161\n\t"
    "v_accvgpr_read_b32 v66, a162\n\t"
    "v_accvgpr_read_b32 v67, a163\n\t"
    "v_accvgpr_read_b32 v68, a180\n\t"
    "v_accvgpr_read_b32 v69, a181\n\t"
    "v_accvgpr_read_b32 v70, a182\n\t"
    "v_accvgpr_read_b32 v71, a183\n\t"
    "v_pk_mul_f32 v[64:65], v[64:65], v[144:145] op_sel_hi:[1,0]\n\t"
    "v_pk_mul_f32 v[66:67], v[66:67], v[144:145] op_sel_hi:[1,0]\n\t"
    "v_pk_mul_f32 v[68:69], v[68:69], v[144:145] op_sel_hi:[1,0]\n\t"
    "v_pk_mul_f32 v[70:71], v[70:71], v[144:145] op_sel_hi:[1,0]\n\t"
    "v_cvt_pk_bf16_f32 v64, v64, v65\n\t"
    "v_cvt_pk_bf16_f32 v65, v66, v67\n\t"
    "v_cvt_pk_bf16_f32 v66, v68, v69\n\t"
    "v_cvt_pk_bf16_f32 v67, v70, v71\n\t"
    "buffer_store_dwordx4 v[64:67], v252, s[24:27], s42 offen sc1\n\t"
    "v_accvgpr_read_b32 v72, a164\n\t"
    "v_accvgpr_read_b32 v73, a165\n\t"
    "v_accvgpr_read_b32 v74, a166\n\t"
    "v_accvgpr_read_b32 v75, a167\n\t"
    "v_accvgpr_read_b32 v76, a184\n\t"
    "v_accvgpr_read_b32 v77, a185\n\t"
    "v_accvgpr_read_b32 v78, a186\n\t"
    "v_accvgpr_read_b32 v79, a187\n\t"
    "v_pk_mul_f32 v[72:73], v[72:73], v[146:147] op_sel_hi:[1,0]\n\t"
    "v_pk_mul_f32 v[74:75], v[74:75], v[146:147] op_sel_hi:[1,0]\n\t"
    "v_pk_mul_f32 v[76:77], v[76:77], v[146:147] op_sel_hi:[1,0]\n\t"
    "v_pk_mul_f32 v[78:79], v[78:79], v[146:147] op_sel_hi:[1,0]\n\t"
    "v_cvt_pk_bf16_f32 v72, v72, v73\n\t"
    "v_cvt_pk_bf16_f32 v73, v74, v75\n\t"
    "v_cvt_pk_bf16_f32 v74, v76, v77\n\t"
    "v_cvt_pk_bf16_f32 v75, v78, v79\n\t"
    "buffer_store_dwordx4 v[72:75], v252, s[24:27], s42 offen offset:1024 sc1\n\t"
    "v_accvgpr_read_b32 v80, a168\n\t"
    "v_accvgpr_read_b32 v81, a169\n\t"
    "v_accvgpr_read_b32 v82, a170\n\t"
    "v_accvgpr_read_b32 v83, a171\n\t"
    "v_accvgpr_read_b32 v84, a188\n\t"
    "v_accvgpr_read_b32 v85, a189\n\t"
    "v_accvgpr_read_b32 v86, a190\n\t"
    "v_accvgpr_read_b32 v87, a191\n\t"
    "v_pk_mul_f32 v[80:81], v[80:81], v[148:149] op_sel_hi:[1,0]\n\t"
    "v_pk_mul_f32 v[82:83], v[82:83], v[148:149] op_sel_hi:[1,0]\n\t"
    "v_pk_mul_f32 v[84:85], v[84:85], v[148:149] op_sel_hi:[1,0]\n\t"
    "v_pk_mul_f32 v[86:87], v[86:87], v[148:149] op_sel_hi:[1,0]\n\t"
    "v_cvt_pk_bf16_f32 v80, v80, v81\n\t"
    "v_cvt_pk_bf16_f32 v81, v82, v83\n\t"
    "v_cvt_pk_bf16_f32 v82, v84, v85\n\t"
    "v_cvt_pk_bf16_f32 v83, v86, v87\n\t"
    "buffer_store_dwordx4 v[80:83], v252, s[24:27], s42 offen offset:2048 sc1\n\t"
    "v_accvgpr_read_b32 v88, a172\n\t"
    "v_accvgpr_read_b32 v89, a173\n\t"
    "v_accvgpr_read_b32 v90, a174\n\t"
    "v_accvgpr_read_b32 v91, a175\n\t"
    "v_accvgpr_read_b32 v92, a192\n\t"
    "v_accvgpr_read_b32 v93, a193\n\t"
    "v_accvgpr_read_b32 v94, a194\n\t"
    "v_accvgpr_read_b32 v95, a195\n\t"
    "v_pk_mul_f32 v[88:89], v[88:89], v[150:151] op_sel_hi:[1,0]\n\t"
    "v_pk_mul_f32 v[90:91], v[90:91], v[150:151] op_sel_hi:[1,0]\n\t"
    "v_pk_mul_f32 v[92:93], v[92:93], v[150:151] op_sel_hi:[1,0]\n\t"
    "v_pk_mul_f32 v[94:95], v[94:95], v[150:151] op_sel_hi:[1,0]\n\t"
    "v_cvt_pk_bf16_f32 v88, v88, v89\n\t"
    "v_cvt_pk_bf16_f32 v89, v90, v91\n\t"
    "v_cvt_pk_bf16_f32 v90, v92, v93\n\t"
    "v_cvt_pk_bf16_f32 v91, v94, v95\n\t"
    "buffer_store_dwordx4 v[88:91], v252, s[24:27], s42 offen offset:3072 sc1\n\t"
    "v_accvgpr_read_b32 v0, a176\n\t"
    "v_accvgpr_read_b32 v1, a177\n\t"
    "v_accvgpr_read_b32 v2, a178\n\t"
    "v_accvgpr_read_b32 v3, a179\n\t"
    "v_accvgpr_read_b32 v4, a196\n\t"
    "v_accvgpr_read_b32 v5, a197\n\t"
    "v_accvgpr_read_b32 v6, a198\n\t"
    "v_accvgpr_read_b32 v7, a199\n\t"
    "v_pk_mul_f32 v[0:1], v[0:1], v[152:153] op_sel_hi:[1,0]\n\t"
    "v_pk_mul_f32 v[2:3], v[2:3], v[152:153] op_sel_hi:[1,0]\n\t"
    "v_pk_mul_f32 v[4:5], v[4:5], v[152:153] op_sel_hi:[1,0]\n\t"
    "v_pk_mul_f32 v[6:7], v[6:7], v[152:153] op_sel_hi:[1,0]\n\t"
    "v_cvt_pk_bf16_f32 v0, v0, v1\n\t"
    "v_cvt_pk_bf16_f32 v1, v2, v3\n\t"
    "v_cvt_pk_bf16_f32 v2, v4, v5\n\t"
    "v_cvt_pk_bf16_f32 v3, v6, v7\n\t"
    "buffer_store_dwordx4 v[0:3], v252, s[24:27], s43 offen sc1\n\t"
    "s_mov_b32 s42, 51200\n\t"
    "s_mov_b32 s43, 55296\n\t"
    "v_accvgpr_read_b32 v8, a200\n\t"
    "v_accvgpr_read_b32 v9, a201\n\t"
    "v_accvgpr_read_b32 v10, a202\n\t"
    "v_accvgpr_read_b32 v11, a203\n\t"
    "v_accvgpr_read_b32 v12, a220\n\t"
    "v_accvgpr_read_b32 v13, a221\n\t"
    "v_accvgpr_read_b32 v14, a222\n\t"
    "v_accvgpr_read_b32 v15, a223\n\t"
    "v_pk_mul_f32 v[8:9], v[8:9], v[144:145] op_sel_hi:[1,0]\n\t"
    "v_pk_mul_f32 v[10:11], v[10:11], v[144:145] op_sel_hi:[1,0]\n\t"
    "v_pk_mul_f32 v[12:13], v[12:13], v[144:145] op_sel_hi:[1,0]\n\t"
    "v_pk_mul_f32 v[14:15], v[14:15], v[144:145] op_sel_hi:[1,0]\n\t"
    "v_cvt_pk_bf16_f32 v8, v8, v9\n\t"
    "v_cvt_pk_bf16_f32 v9, v10, v11\n\t"
    "v_cvt_pk_bf16_f32 v10, v12, v13\n\t"
    "v_cvt_pk_bf16_f32 v11, v14, v15\n\t"
    "buffer_store_dwordx4 v[8:11], v252, s[24:27], s42 offen sc1\n\t"
    "v_accvgpr_read_b32 v16, a204\n\t"
    "v_accvgpr_read_b32 v17, a205\n\t"
    "v_accvgpr_read_b32 v18, a206\n\t"
    "v_accvgpr_read_b32 v19, a207\n\t"
    "v_accvgpr_read_b32 v20, a224\n\t"
    "v_accvgpr_read_b32 v21, a225\n\t"
    "v_accvgpr_read_b32 v22, a226\n\t"
    "v_accvgpr_read_b32 v23, a227\n\t"
    "v_pk_mul_f32 v[16:17], v[16:17], v[146:147] op_sel_hi:[1,0]\n\t"
    "v_pk_mul_f32 v[18:19], v[18:19], v[146:147] op_sel_hi:[1,0]\n\t"
    "v_pk_mul_f32 v[20:21], v[20:21], v[146:147] op_sel_hi:[1,0]\n\t"
    "v_pk_mul_f32 v[22:23], v[22:23], v[146:147] op_sel_hi:[1,0]\n\t"
    "v_cvt_pk_bf16_f32 v16, v16, v17\n\t"
    "v_cvt_pk_bf16_f32 v17, v18, v19\n\t"
    "v_cvt_pk_bf16_f32 v18, v20, v21\n\t"
    "v_cvt_pk_bf16_f32 v19, v22, v23\n\t"
    "buffer_store_dwordx4 v[16:19], v252, s[24:27], s42 offen offset:1024 sc1\n\t"
    "v_accvgpr_read_b32 v24, a208\n\t"
    "v_accvgpr_read_b32 v25, a209\n\t"
    "v_accvgpr_read_b32 v26, a210\n\t"
    "v_accvgpr_read_b32 v27, a211\n\t"
    "v_accvgpr_read_b32 v28, a228\n\t"
    "v_accvgpr_read_b32 v29, a229\n\t"
    "v_accvgpr_read_b32 v30, a230\n\t"
    "v_accvgpr_read_b32 v31, a231\n\t"
    "v_pk_mul_f32 v[24:25], v[24:25], v[148:149] op_sel_hi:[1,0]\n\t"
    "v_pk_mul_f32 v[26:27], v[26:27], v[148:149] op_sel_hi:[1,0]\n\t"
    "v_pk_mul_f32 v[28:29], v[28:29], v[148:149] op_sel_hi:[1,0]\n\t"
    "v_pk_mul_f32 v[30:31], v[30:31], v[148:149] op_sel_hi:[1,0]\n\t"
    "v_cvt_pk_bf16_f32 v24, v24, v25\n\t"
    "v_cvt_pk_bf16_f32 v25, v26, v27\n\t"
    "v_cvt_pk_bf16_f32 v26, v28, v29\n\t"
    "v_cvt_pk_bf16_f32 v27, v30, v31\n\t"
    "buffer_store_dwordx4 v[24:27], v252, s[24:27], s42 offen offset:2048 sc1\n\t"
    "v_accvgpr_read_b32 v32, a212\n\t"
    "v_accvgpr_read_b32 v33, a213\n\t"
    "v_accvgpr_read_b32 v34, a214\n\t"
    "v_accvgpr_read_b32 v35, a215\n\t"
    "v_accvgpr_read_b32 v36, a232\n\t"
    "v_accvgpr_read_b32 v37, a233\n\t"
    "v_accvgpr_read_b32 v38, a234\n\t"
    "v_accvgpr_read_b32 v39, a235\n\t"
    "v_pk_mul_f32 v[32:33], v[32:33], v[150:151] op_sel_hi:[1,0]\n\t"
    "v_pk_mul_f32 v[34:35], v[34:35], v[150:151] op_sel_hi:[1,0]\n\t"
    "v_pk_mul_f32 v[36:37], v[36:37], v[150:151] op_sel_hi:[1,0]\n\t"
    "v_pk_mul_f32 v[38:39], v[38:39], v[150:151] op_sel_hi:[1,0]\n\t"
    "v_cvt_pk_bf16_f32 v32, v32, v33\n\t"
    "v_cvt_pk_bf16_f32 v33, v34, v35\n\t"
    "v_cvt_pk_bf16_f32 v34, v36, v37\n\t"
    "v_cvt_pk_bf16_f32 v35, v38, v39\n\t"
    "buffer_store_dwordx4 v[32:35], v252, s[24:27], s42 offen offset:3072 sc1\n\t"
    "v_accvgpr_read_b32 v40, a216\n\t"
    "v_accvgpr_read_b32 v41, a217\n\t"
    "v_accvgpr_read_b32 v42, a218\n\t"
    "v_accvgpr_read_b32 v43, a219\n\t"
    "v_accvgpr_read_b32 v44, a236\n\t"
    "v_accvgpr_read_b32 v45, a237\n\t"
    "v_accvgpr_read_b32 v46, a238\n\t"
    "v_accvgpr_read_b32 v47, a239\n\t"
    "v_pk_mul_f32 v[40:41], v[40:41], v[152:153] op_sel_hi:[1,0]\n\t"
    "v_pk_mul_f32 v[42:43], v[42:43], v[152:153] op_sel_hi:[1,0]\n\t"
    "v_pk_mul_f32 v[44:45], v[44:45], v[152:153] op_sel_hi:[1,0]\n\t"
    "v_pk_mul_f32 v[46:47], v[46:47], v[152:153] op_sel_hi:[1,0]\n\t"
    "v_cvt_pk_bf16_f32 v40, v40, v41\n\t"
    "v_cvt_pk_bf16_f32 v41, v42, v43\n\t"
    "v_cvt_pk_bf16_f32 v42, v44, v45\n\t"
    "v_cvt_pk_bf16_f32 v43, v46, v47\n\t"
    "buffer_store_dwordx4 v[40:43], v252, s[24:27], s43 offen sc1\n\t"
    "s_mov_b32 s28, 0\n\t"
    "v_accvgpr_write_b32 a0, 0\n\t"
    "v_accvgpr_write_b32 a1, 0\n\t"
    "v_accvgpr_write_b32 a2, 0\n\t"
    "v_accvgpr_write_b32 a3, 0\n\t"
    "v_accvgpr_write_b32 a4, 0\n\t"
    "v_accvgpr_write_b32 a5, 0\n\t"
    "v_accvgpr_write_b32 a6, 0\n\t"
    "v_accvgpr_write_b32 a7, 0\n\t"
    "v_accvgpr_write_b32 a8, 0\n\t"
    "v_accvgpr_write_b32 a9, 0\n\t"
    "v_accvgpr_write_b32 a10, 0\n\t"
    "v_accvgpr_write_b32 a11, 0\n\t"
    "v_accvgpr_write_b32 a12, 0\n\t"
    "v_accvgpr_write_b32 a13, 0\n\t"
    "v_accvgpr_write_b32 a14, 0\n\t"
    "v_accvgpr_write_b32 a15, 0\n\t"
    "v_accvgpr_write_b32 a16, 0\n\t"
    "v_accvgpr_write_b32 a17, 0\n\t"
    "v_accvgpr_write_b32 a18, 0\n\t"
    "v_accvgpr_write_b32 a19, 0\n\t"
    "v_accvgpr_write_b32 a20, 0\n\t"
    "v_accvgpr_write_b32 a21, 0\n\t"
    "v_accvgpr_write_b32 a22, 0\n\t"
    "v_accvgpr_write_b32 a23, 0\n\t"
    "v_accvgpr_write_b32 a24, 0\n\t"
    "v_accvgpr_write_b32 a25, 0\n\t"
    "v_accvgpr_write_b32 a26, 0\n\t"
    "v_accvgpr_write_b32 a27, 0\n\t"
    "v_accvgpr_write_b32 a28, 0\n\t"
    "v_accvgpr_write_b32 a29, 0\n\t"
    "v_accvgpr_write_b32 a30, 0\n\t"
    "v_accvgpr_write_b32 a31, 0\n\t"
    "v_accvgpr_write_b32 a32, 0\n\t"
    "v_accvgpr_write_b32 a33, 0\n\t"
    "v_accvgpr_write_b32 a34, 0\n\t"
    "v_accvgpr_write_b32 a35, 0\n\t"
    "v_accvgpr_write_b32 a36, 0\n\t"
    "v_accvgpr_write_b32 a37, 0\n\t"
    "v_accvgpr_write_b32 a38, 0\n\t"
    "v_accvgpr_write_b32 a39, 0\n\t"
    "v_accvgpr_write_b32 a40, 0\n\t"
    "v_accvgpr_write_b32 a41, 0\n\t"
    "v_accvgpr_write_b32 a42, 0\n\t"
    "v_accvgpr_write_b32 a43, 0\n\t"
    "v_accvgpr_write_b32 a44, 0\n\t"
    "v_accvgpr_write_b32 a45, 0\n\t"
    "v_accvgpr_write_b32 a46, 0\n\t"
    "v_accvgpr_write_b32 a47, 0\n\t"
    "v_accvgpr_write_b32 a48, 0\n\t"
    "v_accvgpr_write_b32 a49, 0\n\t"
    "v_accvgpr_write_b32 a50, 0\n\t"
    "v_accvgpr_write_b32 a51, 0\n\t"
    "v_accvgpr_write_b32 a52, 0\n\t"
    "v_accvgpr_write_b32 a53, 0\n\t"
    "v_accvgpr_write_b32 a54, 0\n\t"
    "v_accvgpr_write_b32 a55, 0\n\t"
    "v_accvgpr_write_b32 a56, 0\n\t"
    "v_accvgpr_write_b32 a57, 0\n\t"
    "v_accvgpr_write_b32 a58, 0\n\t"
    "v_accvgpr_write_b32 a59, 0\n\t"
    "v_accvgpr_write_b32 a60, 0\n\t"
    "v_accvgpr_write_b32 a61, 0\n\t"
    "v_accvgpr_write_b32 a62, 0\n\t"
    "v_accvgpr_write_b32 a63, 0\n\t"
    "v_accvgpr_write_b32 a64, 0\n\t"
    "v_accvgpr_write_b32 a65, 0\n\t"
    "v_accvgpr_write_b32 a66, 0\n\t"
    "v_accvgpr_write_b32 a67, 0\n\t"
    "v_accvgpr_write_b32 a68, 0\n\t"
    "v_accvgpr_write_b32 a69, 0\n\t"
    "v_accvgpr_write_b32 a70, 0\n\t"
    "v_accvgpr_write_b32 a71, 0\n\t"
    "v_accvgpr_write_b32 a72, 0\n\t"
    "v_accvgpr_write_b32 a73, 0\n\t"
    "v_accvgpr_write_b32 a74, 0\n\t"
    "v_accvgpr_write_b32 a75, 0\n\t"
    "v_accvgpr_write_b32 a76, 0\n\t"
    "v_accvgpr_write_b32 a77, 0\n\t"
    "v_accvgpr_write_b32 a78, 0\n\t"
    "v_accvgpr_write_b32 a79, 0\n\t"
    "s_waitcnt vmcnt(53)\n\t"
    "s_barrier\n\t"
    "ds_read_b64_tr_b16 v[0:1], v204 offset:0\n\t"
    "ds_read_b64_tr_b16 v[2:3], v204 offset:4096\n\t"
    "ds_read_b128 v[56:59], v206 offset:0\n\t"
    "ds_read_b128 v[60:63], v206 offset:1024\n\t"
    "ds_read_b128 v[64:67], v206 offset:2048\n\t"
    "ds_read_b128 v[68:71], v206 offset:3072\n\t"
    "ds_read_b128 v[72:75], v206 offset:4096\n\t"
    "ds_read_b64_tr_b16 v[4:5], v205 offset:0\n\t"
    "ds_read_b64_tr_b16 v[6:7], v205 offset:4096\n\t"
    "ds_read_b64_tr_b16 v[8:9], v222 offset:0\n\t"
    "ds_read_b64_tr_b16 v[10:11], v222 offset:4096\n\t"
    "ds_read_b64_tr_b16 v[12:13], v223 offset:0\n\t"
    "ds_read_b64_tr_b16 v[14:15], v223 offset:4096\n\t"
    "s_waitcnt lgkmcnt(0)\n\t"
    "s_mov_b32 s44, 3\n\t"
    "s_getpc_b64 s[52:53]\n\t"
    ".Lo_pcb0:\n\t"
    "s_add_u32 s52, s52, .Lo_retb0-.Lo_pcb0\n\t"
    "s_addc_u32 s53, s53, 0\n\t"
    "s_branch .Lo_loopb\n\t"
    ".Lo_retb0:\n\t"
    "s_lshr_b32 s40, s78, 6\n\t"
    "s_add_u32 s40, s40, 4\n\t"
    "s_lshl_b64 exec, 1, s40\n\t"
    "global_store_dword v254, v255, s[60:61] sc1\n\t"
    "s_mov_b64 exec, -1\n\t"
    "s_mov_b32 s44, 14\n\t"
    "s_getpc_b64 s[52:53]\n\t"
    ".Lo_pcb1:\n\t"
    "s_add_u32 s52, s52, .Lo_retb1-.Lo_pcb1\n\t"
    "s_addc_u32 s53, s53, 0\n\t"
    "s_branch .Lo_loopb\n\t"
    ".Lo_retb1:\n\t"
    "global_load_dword v218, v254, s[60:61] sc1\n\t"
    "s_mov_b32 s44, 3\n\t"
    "s_getpc_b64 s[52:53]\n\t"
    ".Lo_pcb2:\n\t"
    "s_add_u32 s52, s52, .Lo_retb2-.Lo_pcb2\n\t"
    "s_addc_u32 s53, s53, 0\n\t"
    "s_branch .Lo_loopb\n\t"
    ".Lo_retb2:\n\t"
    "s_mov_b32 s41, 32768\n\t"
    ".Lo_pollb1:\n\t"
    "v_cmp_ne_u32 vcc, v255, v218\n\t"
    "s_and_b32 s40, vcc_lo, 0x30\n\t"
    "s_cbranch_scc0 .Lo_okb1 ; scc-ok\n\t"
    "s_sleep 1\n\t"
    "global_load_dword v218, v254, s[60:61] sc1\n\t"
    "s_sub_u32 s41, s41, 1\n\t"
    "s_waitcnt vmcnt(0)\n\t"
    "s_cbranch_scc0 .Lo_pollb1 ; scc-ok\n\t"
    ".Lo_okb1:\n\t"
    "s_mov_b32 s44, 9\n\t"
    "s_getpc_b64 s[52:53]\n\t"
    ".Lo_pcb3:\n\t"
    "s_add_u32 s52, s52, .Lo_retb3-.Lo_pcb3\n\t"
    "s_addc_u32 s53, s53, 0\n\t"
    "s_branch .Lo_loopb\n\t"
    ".Lo_retb3:\n\t"
    "global_load_dword v218, v254, s[60:61] sc1\n\t"
    "s_mov_b32 s44, 3\n\t"
    "s_getpc_b64 s[52:53]\n\t"
    ".Lo_pcb4:\n\t"
    "s_add_u32 s52, s52, .Lo_retb4-.Lo_pcb4\n\t"
    "s_addc_u32 s53, s53, 0\n\t"
    "s_branch .Lo_loopb\n\t"
    ".Lo_retb4:\n\t"
    "s_mov_b32 s41, 32768\n\t"
    ".Lo_pollb2:\n\t"
    "v_cmp_ne_u32 vcc, v255, v218\n\t"
    "s_and_b32 s40, vcc_lo, 0xc0\n\t"
    "s_cbranch_scc0 .Lo_okb2 ; scc-ok\n\t"
    "s_sleep 1\n\t"
    "global_load_dword v218, v254, s[60:61] sc1\n\t"
    "s_sub_u32 s41, s41, 1\n\t"
    "s_waitcnt vmcnt(0)\n\t"
    "s_cbranch_scc0 .Lo_pollb2 ; scc-ok\n\t"
    ".Lo_okb2:\n\t"
    "s_mov_b32 s44, 16\n\t"
    "s_getpc_b64 s[52:53]\n\t"
    ".Lo_pcb5:\n\t"
    "s_add_u32 s52, s52, .Lo_retb5-.Lo_pcb5\n\t"
    "s_addc_u32 s53, s53, 0\n\t"
    "s_branch .Lo_loopb\n\t"
    ".Lo_retb5:\n\t"
    "s_branch .Lo_doneb\n\t"
    ".Lo_loopb:\n\t"
    "v_mfma_f32_16x16x32_bf16 a[0:3], v[0:3], v[56:59], a[0:3]\n\t"
    "s_add_u32 s30, s28, 36864\n\t"
    "ds_read_b64_tr_b16 v[28:29], v204 offset:8192\n\t"
    "v_mfma_f32_16x16x32_bf16 a[4:7], v[0:3], v[60:63], a[4:7]\n\t"
    "s_add_u32 m0, s58, 0\n\t"
    "ds_read_b64_tr_b16 v[30:31], v204 offset:12288\n\t"
    "v_mfma_f32_16x16x32_bf16 a[8:11], v[0:3], v[64:67], a[8:11]\n\t"
    "buffer_load_dwordx4 v207, s[8:11], s36 offen lds\n\t"
    "v_mfma_f32_16x16x32_bf16 a[12:15], v[0:3], v[68:71], a[12:15]\n\t"
    "s_cmp_eq_u32 s30, 147456\n\t"
    "s_cselect_b32 s30, 0, s30\n\t"
    "ds_read_b128 v[76:79], v206 offset:10240\n\t"
    "v_mfma_f32_16x16x32_bf16 a[16:19], v[0:3], v[72:75], a[16:19]\n\t"
    "s_sub_u32 s31, s30, s28\n\t"
    "ds_read_b128 v[80:83], v206 offset:11264\n\t"
    "v_mfma_f32_16x16x32_bf16 a[20:23], v[4:7], v[56:59], a[20:23]\n\t"
    "ds_read_b128 v[84:87], v206 offset:12288\n\t"
    "v_mfma_f32_16x16x32_bf16 a[24:27], v[4:7], v[60:63], a[24:27]\n\t"
    "buffer_load_dwordx4 v208, s[8:11], s36 offen offset:1024 lds\n\t"
    "v_mfma_f32_16x16x32_bf16 a[28:31], v[4:7], v[64:67], a[28:31]\n\t"
    "ds_read_b128 v[88:91], v206 offset:13312\n\t"
    "v_mfma_f32_16x16x32_bf16 a[32:35], v[4:7], v[68:71], a[32:35]\n\t"
    "ds_read_b128 v[92:95], v206 offset:14336\n\t"
    "v_mfma_f32_16x16x32_bf16 a[36:39], v[4:7], v[72:75], a[36:39]\n\t"
    "ds_read_b64_tr_b16 v[32:33], v205 offset:8192\n\t"
    "v_mfma_f32_16x16x32_bf16 a[40:43], v[8:11], v[56:59], a[40:43]\n\t"
    "buffer_load_dwordx4 v209, s[8:11], s36 offen offset:2048 lds\n\t"
    "v_mfma_f32_16x16x32_bf16 a[44:47], v[8:11], v[60:63], a[44:47]\n\t"
    "ds_read_b64_tr_b16 v[34:35], v205 offset:12288\n\t"
    "v_mfma_f32_16x16x32_bf16 a[48:51], v[8:11], v[64:67], a[48:51]\n\t"
    "ds_read_b64_tr_b16 v[36:37], v222 offset:8192\n\t"
    "v_mfma_f32_16x16x32_bf16 a[52:55], v[8:11], v[68:71], a[52:55]\n\t"
    "ds_read_b64_tr_b16 v[38:39], v222 offset:12288\n\t"
    "v_mfma_f32_16x16x32_bf16 a[56:59], v[8:11], v[72:75], a[56:59]\n\t"
    "buffer_load_dwordx4 v210, s[8:11], s36 offen offset:3072 lds\n\t"
    "v_mfma_f32_16x16x32_bf16 a[60:63], v[12:15], v[56:59], a[60:63]\n\t"
    "s_add_u32 s36, s36, 81920\n\t"
    "ds_read_b64_tr_b16 v[40:41], v223 offset:8192\n\t"
    "v_mfma_f32_16x16x32_bf16 a[64:67], v[12:15], v[60:63], a[64:67]\n\t"
    "ds_read_b64_tr_b16 v[42:43], v223 offset:12288\n\t"
    "v_mfma_f32_16x16x32_bf16 a[68:71], v[12:15], v[64:67], a[68:71]\n\t"
    "v_mfma_f32_16x16x32_bf16 a[72:75], v[12:15], v[68:71], a[72:75]\n\t"
    "v_add_u32 v204, s31, v204\n\t"
    "v_add_u32 v205, s31, v205\n\t"
    "v_add_u32 v222, s31, v222\n\t"
    "v_add_u32 v223, s31, v223\n\t"
    "v_add_u32 v206, s31, v206\n\t"
    "v_mfma_f32_16x16x32_bf16 a[76:79], v[12:15], v[72:75], a[76:79]\n\t"
    "s_waitcnt vmcnt(18) lgkmcnt(0)\n\t"
    "s_barrier\n\t"
    "v_mfma_f32_16x16x32_bf16 a[0:3], v[28:31], v[76:79], a[0:3]\n\t"
    "s_add_u32 s58, s28, s34\n\t"
    "s_add_u32 s59, s28, s35\n\t"
    "s_mov_b32 s28, s30\n\t"
    "ds_read_b64_tr_b16 v[0:1], v204 offset:0\n\t"
    "v_mfma_f32_16x16x32_bf16 a[4:7], v[28:31], v[80:83], a[4:7]\n\t"
    "s_add_u32 m0, s59, 0\n\t"
    "ds_read_b64_tr_b16 v[2:3], v204 offset:4096\n\t"
    "v_mfma_f32_16x16x32_bf16 a[8:11], v[28:31], v[84:87], a[8:11]\n\t"
    "buffer_load_dwordx4 v214, s[12:15], s37 offen sc1 lds\n\t"
    "v_mfma_f32_16x16x32_bf16 a[12:15], v[28:31], v[88:91], a[12:15]\n\t"
    "ds_read_b128 v[56:59], v206 offset:0\n\t"
    "v_mfma_f32_16x16x32_bf16 a[16:19], v[28:31], v[92:95], a[16:19]\n\t"
    "ds_read_b128 v[60:63], v206 offset:1024\n\t"
    "v_mfma_f32_16x16x32_bf16 a[20:23], v[32:35], v[76:79], a[20:23]\n\t"
    "buffer_load_dwordx4 v214, s[12:15], s37 offen offset:1024 sc1 lds\n\t"
    "v_mfma_f32_16x16x32_bf16 a[24:27], v[32:35], v[80:83], a[24:27]\n\t"
    "ds_read_b128 v[64:67], v206 offset:2048\n\t"
    "v_mfma_f32_16x16x32_bf16 a[28:31], v[32:35], v[84:87], a[28:31]\n\t"
    "ds_read_b128 v[68:71], v206 offset:3072\n\t"
    "v_mfma_f32_16x16x32_bf16 a[32:35], v[32:35], v[88:91], a[32:35]\n\t"
    "buffer_load_dwordx4 v214, s[12:15], s37 offen offset:2048 sc1 lds\n\t"
    "v_mfma_f32_16x16x32_bf16 a[36:39], v[32:35], v[92:95], a[36:39]\n\t"
    "ds_read_b128 v[72:75], v206 offset:4096\n\t"
    "v_mfma_f32_16x16x32_bf16 a[40:43], v[36:39], v[76:79], a[40:43]\n\t"
    "ds_read_b64_tr_b16 v[4:5], v205 offset:0\n\t"
    "v_mfma_f32_16x16x32_bf16 a[44:47], v[36:39], v[80:83], a[44:47]\n\t"
    "ds_read_b64_tr_b16 v[6:7], v205 offset:4096\n\t"
    "v_mfma_f32_16x16x32_bf16 a[48:51], v[36:39], v[84:87], a[48:51]\n\t"
    "buffer_load_dwordx4 v214, s[12:15], s37 offen offset:3072 sc1 lds\n\t"
    "v_mfma_f32_16x16x32_bf16 a[52:55], v[36:39], v[88:91], a[52:55]\n\t"
    "ds_read_b64_tr_b16 v[8:9], v222 offset:0\n\t"
    "v_mfma_f32_16x16x32_bf16 a[56:59], v[36:39], v[92:95], a[56:59]\n\t"
    "s_add_u32 m0, s59, 4096\n\t"
    "s_add_u32 s40, s37, 4096\n\t"
    "ds_read_b64_tr_b16 v[10:11], v222 offset:4096\n\t"
    "v_mfma_f32_16x16x32_bf16 a[60:63], v[40:43], v[76:79], a[60:63]\n\t"
    "buffer_load_dwordx4 v214, s[12:15], s40 offen sc1 lds\n\t"
    "v_mfma_f32_16x16x32_bf16 a[64:67], v[40:43], v[80:83], a[64:67]\n\t"
    "s_add_u32 s37, s37, 20480\n\t"
    "ds_read_b64_tr_b16 v[12:13], v223 offset:0\n\t"
    "v_mfma_f32_16x16x32_bf16 a[68:71], v[40:43], v[84:87], a[68:71]\n\t"
    "ds_read_b64_tr_b16 v[14:15], v223 offset:4096\n\t"
    "v_mfma_f32_16x16x32_bf16 a[72:75], v[40:43], v[88:91], a[72:75]\n\t"
    "v_mfma_f32_16x16x32_bf16 a[76:79], v[40:43], v[92:95], a[76:79]\n\t"
    "s_waitcnt lgkmcnt(0)\n\t"
    "s_sub_u32 s44, s44, 1\n\t"
    "s_cmp_lg_u32 s44, 0\n\t"
    "s_cbranch_scc1 .Lo_loopb\n\t"
    "s_setpc_b64 s[52:53]\n\t"
    ".Lo_doneb:\n\t"
    "s_lshr_b32 s40, s78, 1\n\t"
    "s_lshl_b32 s40, s40, 2\n\t"
    "s_add_u32 s40, s40, s7\n\t"
    "s_mul_i32 s40, s40, 35840\n\t"
    "s_add_u32 s40, s40, 62914560\n\t"
    "s_add_u32 s16, s68, s40\n\t"
    "s_addc_u32 s17, s69, 0\n\t"
    "s_mov_b32 s18, 35840\n\t"
    "s_and_b32 s41, s78, 1\n\t"
    "s_lshl_b32 s41, s41, 1\n\t"
    "s_add_u32 s44, s41, s38\n\t"
    "s_cmp_eq_u32 s44, 1\n\t"
    "s_cbranch_scc1 .Lo_sndb1\n\t"
    "s_cmp_eq_u32 s44, 2\n\t"
    "s_cbranch_scc1 .Lo_sndb2\n\t"
    "s_cmp_eq_u32 s44, 3\n\t"
    "s_cbranch_scc1 .Lo_sndb3\n\t"
    "s_mov_b32 s40, 3072\n\t"
    "buffer_store_dwordx4 a[12:15], v214, s[16:19], s40 offen sc1\n\t"
    "s_mov_b32 s40, 4096\n\t"
    "buffer_store_dwordx4 a[16:19], v214, s[16:19], s40 offen sc1\n\t"
    "s_mov_b32 s40, 8192\n\t"
    "buffer_store_dwordx4 a[32:35], v214, s[16:19], s40 offen sc1\n\t"
    "s_mov_b32 s40, 9216\n\t"
    "buffer_store_dwordx4 a[36:39], v214, s[16:19], s40 offen sc1\n\t"
    "s_mov_b32 s40, 13312\n\t"
    "buffer_store_dwordx4 a[52:55], v214, s[16:19], s40 offen sc1\n\t"
    "s_mov_b32 s40, 14336\n\t"
    "buffer_store_dwordx4 a[56:59], v214, s[16:19], s40 offen sc1\n\t"
    "s_mov_b32 s40, 18432\n\t"
    "buffer_store_dwordx4 a[72:75], v214, s[16:19], s40 offen sc1\n\t"
    "s_mov_b32 s40, 19456\n\t"
    "buffer_store_dwordx4 a[76:79], v214, s[16:19], s40 offen sc1\n\t"
    "s_branch .Lo_sentb\n\t"
    ".Lo_sndb1:\n\t"
    "s_mov_b32 s40, 2048\n\t"
    "buffer_store_dwordx4 a[8:11], v214, s[16:19], s40 offen sc1\n\t"
    "s_mov_b32 s40, 3072\n\t"
    "buffer_store_dwordx4 a[12:15], v214, s[16:19], s40 offen sc1\n\t"
    "s_mov_b32 s40, 4096\n\t"
    "buffer_store_dwordx4 a[16:19], v214, s[16:19], s40 offen sc1\n\t"
    "s_mov_b32 s40, 7168\n\t"
    "buffer_store_dwordx4 a[28:31], v214, s[16:19], s40 offen sc1\n\t"
    "s_mov_b32 s40, 8192\n\t"
    "buffer_store_dwordx4 a[32:35], v214, s[16:19], s40 offen sc1\n\t"
    "s_mov_b32 s40, 9216\n\t"
    "buffer_store_dwordx4 a[36:39], v214, s[16:19], s40 offen sc1\n\t"
    "s_mov_b32 s40, 12288\n\t"
    "buffer_store_dwordx4 a[48:51], v214, s[16:19], s40 offen sc1\n\t"
    "s_mov_b32 s40, 13312\n\t"
    "buffer_store_dwordx4 a[52:55], v214, s[16:19], s40 offen sc1\n\t"
    "s_mov_b32 s40, 14336\n\t"
    "buffer_store_dwordx4 a[56:59], v214, s[16:19], s40 offen sc1\n\t"
    "s_mov_b32 s40, 17408\n\t"
    "buffer_store_dwordx4 a[68:71], v214, s[16:19], s40 offen sc1\n\t"
    "s_mov_b32 s40, 18432\n\t"
    "buffer_store_dwordx4 a[72:75], v214, s[16:19], s40 offen sc1\n\t"
    "s_mov_b32 s40, 19456\n\t"
    "buffer_store_dwordx4 a[76:79], v214, s[16:19], s40 offen sc1\n\t"
    "s_branch .Lo_sentb\n\t"
    ".Lo_sndb2:\n\t"
    "s_mov_b32 s40, 0\n\t"
    "buffer_store_dwordx4 a[0:3], v214, s[16:19], s40 offen sc1\n\t"
    "s_mov_b32 s40, 1024\n\t"
    "buffer_store_dwordx4 a[4:7], v214, s[16:19], s40 offen sc1\n\t"
    "s_mov_b32 s40, 2048\n\t"
    "buffer_store_dwordx4 a[8:11], v214, s[16:19], s40 offen sc1\n\t"
    "s_mov_b32 s40, 5120\n\t"
    "buffer_store_dwordx4 a[20:23], v214, s[16:19], s40 offen sc1\n\t"
    "s_mov_b32 s40, 6144\n\t"
    "buffer_store_dwordx4 a[24:27], v214, s[16:19], s40 offen sc1\n\t"
    "s_mov_b32 s40, 7168\n\t"
    "buffer_store_dwordx4 a[28:31], v214, s[16:19], s40 offen sc1\n\t"
    "s_mov_b32 s40, 10240\n\t"
    "buffer_store_dwordx4 a[40:43], v214, s[16:19], s40 offen sc1\n\t"
    "s_mov_b32 s40, 11264\n\t"
    "buffer_store_dwordx4 a[44:47], v214, s[16:19], s40 offen sc1\n\t"
    "s_mov_b32 s40, 12288\n\t"
    "buffer_store_dwordx4 a[48:51], v214, s[16:19], s40 offen sc1\n\t"
    "s_mov_b32 s40, 15360\n\t"
    "buffer_store_dwordx4 a[60:63], v214, s[16:19], s40 offen sc1\n\t"
    "s_mov_b32 s40, 16384\n\t"
    "buffer_store_dwordx4 a[64:67], v214, s[16:19], s40 offen sc1\n\t"
    "s_mov_b32 s40, 17408\n\t"
    "buffer_store_dwordx4 a[68:71], v214, s[16:19], s40 offen sc1\n\t"
    "s_branch .Lo_sentb\n\t"
    ".Lo_sndb3:\n\t"
    "s_mov_b32 s40, 0\n\t"
    "buffer_store_dwordx4 a[0:3], v214, s[16:19], s40 offen sc1\n\t"
    "s_mov_b32 s40, 1024\n\t"
    "buffer_store_dwordx4 a[4:7], v214, s[16:19], s40 offen sc1\n\t"
    "s_mov_b32 s40, 5120\n\t"
    "buffer_store_dwordx4 a[20:23], v214, s[16:19], s40 offen sc1\n\t"
    "s_mov_b32 s40, 6144\n\t"
    "buffer_store_dwordx4 a[24:27], v214, s[16:19], s40 offen sc1\n\t"
    "s_mov_b32 s40, 10240\n\t"
    "buffer_store_dwordx4 a[40:43], v214, s[16:19], s40 offen sc1\n\t"
    "s_mov_b32 s40, 11264\n\t"
    "buffer_store_dwordx4 a[44:47], v214, s[16:19], s40 offen sc1\n\t"
    "s_mov_b32 s40, 15360\n\t"
    "buffer_store_dwordx4 a[60:63], v214, s[16:19], s40 offen sc1\n\t"
    "s_mov_b32 s40, 16384\n\t"
    "buffer_store_dwordx4 a[64:67], v214, s[16:19], s40 offen sc1\n\t"
    ".Lo_sentb:\n\t"
    "s_mul_i32 s40, s70, 409600\n\t"
    "s_mul_i32 s41, s6, 1280\n\t"
    "s_add_u32 s40, s40, s41\n\t"
    "s_add_u32 s40, s40, s20\n\t"
    "s_add_u32 s20, s4, s40\n\t"
    "s_addc_u32 s21, s5, 0\n\t"
    "s_mov_b32 s22, 407296\n\t"
    "s_bitcmp0_b32 s78, 0\n\t"
    "s_cselect_b32 s40, 48, 32\n\t"
    "s_cselect_b32 s42, 0, 48\n\t"
    "s_mul_i32 s41, s40, s38\n\t"
    "v_lshrrev_b32 v115, 4, v214\n\t"
    "v_and_b32 v116, 15, v115\n\t"
    "v_add_u32 v116, s41, v116\n\t"
    "v_mul_u32_u24 v116, 272, v116\n\t"
    "v_lshrrev_b32 v117, 4, v115\n\t"
    "v_lshl_add_u32 v116, v117, 3, v116\n\t"
    "s_mul_i32 s41, s39, 128\n\t"
    "v_add_u32 v96, s41, v116\n\t"
    "s_lshl_b32 s41, s7, 6\n\t"
    "v_add_u32 v115, s41, v115\n\t"
    "v_add_u32 v116, 0, v115\n\t"
    "v_mul_u32_u24 v117, 4096, v116\n\t"
    "v_lshrrev_b32 v117, 16, v117\n\t"
    "v_mul_u32_u24 v118, 16, v117\n\t"
    "v_sub_u32 v118, v116, v118\n\t"
    "v_lshlrev_b32 v118, 4, v118\n\t"
    "v_mul_u32_u24 v119, 272, v117\n\t"
    "v_add_u32 v97, v119, v118\n\t"
    "v_cmp_le_u32 vcc, s40, v117\n\t"
    "v_add_u32 v117, s42, v117\n\t"
    "v_add_u32 v119, 32, v117\n\t"
    "v_cndmask_b32 v117, v117, v119, vcc\n\t"
    "v_mul_u32_u24 v117, 2560, v117\n\t"
    "v_add_u32 v106, v117, v118\n\t"
    "v_add_u32 v116, 256, v115\n\t"
    "v_mul_u32_u24 v117, 4096, v116\n\t"
    "v_lshrrev_b32 v117, 16, v117\n\t"
    "v_mul_u32_u24 v118, 16, v117\n\t"
    "v_sub_u32 v118, v116, v118\n\t"
    "v_lshlrev_b32 v118, 4, v118\n\t"
    "v_mul_u32_u24 v119, 272, v117\n\t"
    "v_add_u32 v98, v119, v118\n\t"
    "v_cmp_le_u32 vcc, s40, v117\n\t"
    "v_add_u32 v117, s42, v117\n\t"
    "v_add_u32 v119, 32, v117\n\t"
    "v_cndmask_b32 v117, v117, v119, vcc\n\t"
    "v_mul_u32_u24 v117, 2560, v117\n\t"
    "v_add_u32 v107, v117, v118\n\t"
    "v_add_u32 v116, 512, v115\n\t"
    "v_mul_u32_u24 v117, 4096, v116\n\t"
    "v_lshrrev_b32 v117, 16, v117\n\t"
    "v_mul_u32_u24 v118, 16, v117\n\t"
    "v_sub_u32 v118, v116, v118\n\t"
    "v_lshlrev_b32 v118, 4, v118\n\t"
    "v_mul_u32_u24 v119, 272, v117\n\t"
    "v_add_u32 v99, v119, v118\n\t"
    "v_cmp_le_u32 vcc, s40, v117\n\t"
    "v_add_u32 v117, s42, v117\n\t"
    "v_add_u32 v119, 32, v117\n\t"
    "v_cndmask_b32 v117, v117, v119, vcc\n\t"
    "v_mul_u32_u24 v117, 2560, v117\n\t"
    "v_add_u32 v108, v117, v118\n\t"
    "v_add_u32 v116, 768, v115\n\t"
    "v_mul_u32_u24 v117, 4096, v116\n\t"
    "v_lshrrev_b32 v117, 16, v117\n\t"
    "v_mul_u32_u24 v118, 16, v117\n\t"
    "v_sub_u32 v118, v116, v118\n\t"
    "v_lshlrev_b32 v118, 4, v118\n\t"
    "v_mul_u32_u24 v119, 272, v117\n\t"
    "v_add_u32 v100, v119, v118\n\t"
    "v_cmp_le_u32 vcc, s40, v117\n\t"
    "v_add_u32 v117, s42, v117\n\t"
    "v_add_u32 v119, 32, v117\n\t"
    "v_cndmask_b32 v117, v117, v119, vcc\n\t"
    "v_mul_u32_u24 v117, 2560, v117\n\t"
    "v_add_u32 v109, v117, v118\n\t"
    "v_add_u32 v116, 1024, v115\n\t"
    "v_mul_u32_u24 v117, 4096, v116\n\t"
    "v_lshrrev_b32 v117, 16, v117\n\t"
    "v_mul_u32_u24 v118, 16, v117\n\t"
    "v_sub_u32 v118, v116, v118\n\t"
    "v_lshlrev_b32 v118, 4, v118\n\t"
    "v_mul_u32_u24 v119, 272, v117\n\t"
    "v_add_u32 v101, v119, v118\n\t"
    "v_cmp_le_u32 vcc, s40, v117\n\t"
    "v_add_u32 v117, s42, v117\n\t"
    "v_add_u32 v119, 32, v117\n\t"
    "v_cndmask_b32 v117, v117, v119, vcc\n\t"
    "v_mul_u32_u24 v117, 2560, v117\n\t"
    "v_add_u32 v110, v117, v118\n\t"
    "s_waitcnt vmcnt(0) lgkmcnt(0)\n\t"
    "s_barrier\n\t"
    "s_mov_b64 exec, 0x400\n\t"
    "global_store_dword v254, v255, s[60:61] sc1\n\t"
    "s_mov_b64 exec, -1\n\t"
    "global_load_dword v218, v254, s[60:61] sc1\n\t"
    "s_waitcnt vmcnt(0)\n\t"
    "s_mov_b32 s41, 32768\n\t"
    ".Lo_pollbx:\n\t"
    "v_cmp_ne_u32 vcc, v255, v218\n\t"
    "s_and_b32 s40, vcc_lo, 0x100\n\t"
    "s_cbranch_scc0 .Lo_okbx ; scc-ok\n\t"
    "s_sleep 1\n\t"
    "global_load_dword v218, v254, s[60:61] sc1\n\t"
    "s_sub_u32 s41, s41, 1\n\t"
    "s_waitcnt vmcnt(0)\n\t"
    "s_cbranch_scc0 .Lo_pollbx ; scc-ok\n\t"
    ".Lo_okbx:\n\t"
    "s_cmp_eq_u32 s44, 1\n\t"
    "s_cbranch_scc1 .Lo_rcvb1\n\t"
    "s_cmp_eq_u32 s44, 2\n\t"
    "s_cbranch_scc1 .Lo_rcvb2\n\t"
    "s_cmp_eq_u32 s44, 3\n\t"
    "s_cbranch_scc1 .Lo_rcvb3\n\t"
    "s_mov_b32 s40, 0\n\t"
    "buffer_load_dwordx4 v[124:127], v214, s[16:19], s40 offen sc1\n\t"
    "s_mov_b32 s40, 5120\n\t"
    "buffer_load_dwordx4 v[128:131], v214, s[16:19], s40 offen sc1\n\t"
    "s_mov_b32 s40, 10240\n\t"
    "buffer_load_dwordx4 v[132:135], v214, s[16:19], s40 offen sc1\n\t"
    "s_mov_b32 s40, 15360\n\t"
    "buffer_load_dwordx4 v[136:139], v214, s[16:19], s40 offen sc1\n\t"
    "s_mov_b32 s40, 1024\n\t"
    "buffer_load_dwordx4 v[140:143], v214, s[16:19], s40 offen sc1\n\t"
    "s_mov_b32 s40, 6144\n\t"
    "buffer_load_dwordx4 v[144:147], v214, s[16:19], s40 offen sc1\n\t"
    "s_mov_b32 s40, 11264\n\t"
    "buffer_load_dwordx4 v[148:151], v214, s[16:19], s40 offen sc1\n\t"
    "s_mov_b32 s40, 16384\n\t"
    "buffer_load_dwordx4 v[152:155], v214, s[16:19], s40 offen sc1\n\t"
    "s_mov_b32 s40, 2048\n\t"
    "buffer_load_dwordx4 v[156:159], v214, s[16:19], s40 offen sc1\n\t"
    "s_mov_b32 s40, 7168\n\t"
    "buffer_load_dwordx4 v[160:163], v214, s[16:19], s40 offen sc1\n\t"
    "s_mov_b32 s40, 12288\n\t"
    "buffer_load_dwordx4 v[164:167], v214, s[16:19], s40 offen sc1\n\t"
    "s_mov_b32 s40, 17408\n\t"
    "buffer_load_dwordx4 v[168:171], v214, s[16:19], s40 offen sc1\n\t"
    "v_accvgpr_read_b32 v0, a0\n\t"
    "v_accvgpr_read_b32 v1, a1\n\t"
    "v_accvgpr_read_b32 v2, a2\n\t"
    "v_accvgpr_read_b32 v3, a3\n\t"
    "s_waitcnt vmcnt(11)\n\t"
    "v_pk_add_f32 v[0:1], v[0:1], v[124:125]\n\t"
    "v_pk_add_f32 v[2:3], v[2:3], v[126:127]\n\t"
    "v_cvt_pk_bf16_f32 v0, v0, v1\n\t"
    "v_cvt_pk_bf16_f32 v1, v2, v3\n\t"
    "ds_write_b64 v96, v[0:1] offset:0\n\t"
    "v_accvgpr_read_b32 v4, a20\n\t"
    "v_accvgpr_read_b32 v5, a21\n\t"
    "v_accvgpr_read_b32 v6, a22\n\t"
    "v_accvgpr_read_b32 v7, a23\n\t"
    "s_waitcnt vmcnt(10)\n\t"
    "v_pk_add_f32 v[4:5], v[4:5], v[128:129]\n\t"
    "v_pk_add_f32 v[6:7], v[6:7], v[130:131]\n\t"
    "v_cvt_pk_bf16_f32 v4, v4, v5\n\t"
    "v_cvt_pk_bf16_f32 v5, v6, v7\n\t"
    "ds_write_b64 v96, v[4:5] offset:32\n\t"
    "v_accvgpr_read_b32 v8, a40\n\t"
    "v_accvgpr_read_b32 v9, a41\n\t"
    "v_accvgpr_read_b32 v10, a42\n\t"
    "v_accvgpr_read_b32 v11, a43\n\t"
    "s_waitcnt vmcnt(9)\n\t"
    "v_pk_add_f32 v[8:9], v[8:9], v[132:133]\n\t"
    "v_pk_add_f32 v[10:11], v[10:11], v[134:135]\n\t"
    "v_cvt_pk_bf16_f32 v8, v8, v9\n\t"
    "v_cvt_pk_bf16_f32 v9, v10, v11\n\t"
    "ds_write_b64 v96, v[8:9] offset:64\n\t"
    "v_accvgpr_read_b32 v12, a60\n\t"
    "v_accvgpr_read_b32 v13, a61\n\t"
    "v_accvgpr_read_b32 v14, a62\n\t"
    "v_accvgpr_read_b32 v15, a63\n\t"
    "s_waitcnt vmcnt(8)\n\t"
    "v_pk_add_f32 v[12:13], v[12:13], v[136:137]\n\t"
    "v_pk_add_f32 v[14:15], v[14:15], v[138:139]\n\t"
    "v_cvt_pk_bf16_f32 v12, v12, v13\n\t"
    "v_cvt_pk_bf16_f32 v13, v14, v15\n\t"
    "ds_write_b64 v96, v[12:13] offset:96\n\t"
    "v_accvgpr_read_b32 v16, a4\n\t"
    "v_accvgpr_read_b32 v17, a5\n\t"
    "v_accvgpr_read_b32 v18, a6\n\t"
    "v_accvgpr_read_b32 v19, a7\n\t"
    "s_waitcnt vmcnt(7)\n\t"
    "v_pk_add_f32 v[16:17], v[16:17], v[140:141]\n\t"
    "v_pk_add_f32 v[18:19], v[18:19], v[142:143]\n\t"
    "v_cvt_pk_bf16_f32 v16, v16, v17\n\t"
    "v_cvt_pk_bf16_f32 v17, v18, v19\n\t"
    "ds_write_b64 v96, v[16:17] offset:4352\n\t"
    "v_accvgpr_read_b32 v20, a24\n\t"
    "v_accvgpr_read_b32 v21, a25\n\t"
    "v_accvgpr_read_b32 v22, a26\n\t"
    "v_accvgpr_read_b32 v23, a27\n\t"
    "s_waitcnt vmcnt(6)\n\t"
    "v_pk_add_f32 v[20:21], v[20:21], v[144:145]\n\t"
    "v_pk_add_f32 v[22:23], v[22:23], v[146:147]\n\t"
    "v_cvt_pk_bf16_f32 v20, v20, v21\n\t"
    "v_cvt_pk_bf16_f32 v21, v22, v23\n\t"
    "ds_write_b64 v96, v[20:21] offset:4384\n\t"
    "v_accvgpr_read_b32 v24, a44\n\t"
    "v_accvgpr_read_b32 v25, a45\n\t"
    "v_accvgpr_read_b32 v26, a46\n\t"
    "v_accvgpr_read_b32 v27, a47\n\t"
    "s_waitcnt vmcnt(5)\n\t"
    "v_pk_add_f32 v[24:25], v[24:25], v[148:149]\n\t"
    "v_pk_add_f32 v[26:27], v[26:27], v[150:151]\n\t"
    "v_cvt_pk_bf16_f32 v24, v24, v25\n\t"
    "v_cvt_pk_bf16_f32 v25, v26, v27\n\t"
    "ds_write_b64 v96, v[24:25] offset:4416\n\t"
    "v_accvgpr_read_b32 v28, a64\n\t"
    "v_accvgpr_read_b32 v29, a65\n\t"
    "v_accvgpr_read_b32 v30, a66\n\t"
    "v_accvgpr_read_b32 v31, a67\n\t"
    "s_waitcnt vmcnt(4)\n\t"
    "v_pk_add_f32 v[28:29], v[28:29], v[152:153]\n\t"
    "v_pk_add_f32 v[30:31], v[30:31], v[154:155]\n\t"
    "v_cvt_pk_bf16_f32 v28, v28, v29\n\t"
    "v_cvt_pk_bf16_f32 v29, v30, v31\n\t"
    "ds_write_b64 v96, v[28:29] offset:4448\n\t"
    "v_accvgpr_read_b32 v0, a8\n\t"
    "v_accvgpr_read_b32 v1, a9\n\t"
    "v_accvgpr_read_b32 v2, a10\n\t"
    "v_accvgpr_read_b32 v3, a11\n\t"
    "s_waitcnt vmcnt(3)\n\t"
    "v_pk_add_f32 v[0:1], v[0:1], v[156:157]\n\t"
    "v_pk_add_f32 v[2:3], v[2:3], v[158:159]\n\t"
    "v_cvt_pk_bf16_f32 v0, v0, v1\n\t"
    "v_cvt_pk_bf16_f32 v1, v2, v3\n\t"
    "ds_write_b64 v96, v[0:1] offset:8704\n\t"
    "v_accvgpr_read_b32 v4, a28\n\t"
    "v_accvgpr_read_b32 v5, a29\n\t"
    "v_accvgpr_read_b32 v6, a30\n\t"
    "v_accvgpr_read_b32 v7, a31\n\t"
    "s_waitcnt vmcnt(2)\n\t"
    "v_pk_add_f32 v[4:5], v[4:5], v[160:161]\n\t"
    "v_pk_add_f32 v[6:7], v[6:7], v[162:163]\n\t"
    "v_cvt_pk_bf16_f32 v4, v4, v5\n\t"
    "v_cvt_pk_bf16_f32 v5, v6, v7\n\t"
    "ds_write_b64 v96, v[4:5] offset:8736\n\t"
    "v_accvgpr_read_b32 v8, a48\n\t"
    "v_accvgpr_read_b32 v9, a49\n\t"
    "v_accvgpr_read_b32 v10, a50\n\t"
    "v_accvgpr_read_b32 v11, a51\n\t"
    "s_waitcnt vmcnt(1)\n\t"
    "v_pk_add_f32 v[8:9], v[8:9], v[164:165]\n\t"
    "v_pk_add_f32 v[10:11], v[10:11], v[166:167]\n\t"
    "v_cvt_pk_bf16_f32 v8, v8, v9\n\t"
    "v_cvt_pk_bf16_f32 v9, v10, v11\n\t"
    "ds_write_b64 v96, v[8:9] offset:8768\n\t"
    "v_accvgpr_read_b32 v12, a68\n\t"
    "v_accvgpr_read_b32 v13, a69\n\t"
    "v_accvgpr_read_b32 v14, a70\n\t"
    "v_accvgpr_read_b32 v15, a71\n\t"
    "s_waitcnt vmcnt(0)\n\t"
    "v_pk_add_f32 v[12:13], v[12:13], v[168:169]\n\t"
    "v_pk_add_f32 v[14:15], v[14:15], v[170:171]\n\t"
    "v_cvt_pk_bf16_f32 v12, v12, v13\n\t"
    "v_cvt_pk_bf16_f32 v13, v14, v15\n\t"
    "ds_write_b64 v96, v[12:13] offset:8800\n\t"
    "s_branch .Lo_stagedb\n\t"
    ".Lo_rcvb1:\n\t"
    "s_mov_b32 s40, 0\n\t"
    "buffer_load_dwordx4 v[124:127], v214, s[16:19], s40 offen sc1\n\t"
    "s_mov_b32 s40, 5120\n\t"
    "buffer_load_dwordx4 v[128:131], v214, s[16:19], s40 offen sc1\n\t"
    "s_mov_b32 s40, 10240\n\t"
    "buffer_load_dwordx4 v[132:135], v214, s[16:19], s40 offen sc1\n\t"
    "s_mov_b32 s40, 15360\n\t"
    "buffer_load_dwordx4 v[136:139], v214, s[16:19], s40 offen sc1\n\t"
    "s_mov_b32 s40, 1024\n\t"
    "buffer_load_dwordx4 v[140:143], v214, s[16:19], s40 offen sc1\n\t"
    "s_mov_b32 s40, 6144\n\t"
    "buffer_load_dwordx4 v[144:147], v214, s[16:19], s40 offen sc1\n\t"
    "s_mov_b32 s40, 11264\n\t"
    "buffer_load_dwordx4 v[148:151], v214, s[16:19], s40 offen sc1\n\t"
    "s_mov_b32 s40, 16384\n\t"
    "buffer_load_dwordx4 v[152:155], v214, s[16:19], s40 offen sc1\n\t"
    "v_accvgpr_read_b32 v0, a0\n\t"
    "v_accvgpr_read_b32 v1, a1\n\t"
    "v_accvgpr_read_b32 v2, a2\n\t"
    "v_accvgpr_read_b32 v3, a3\n\t"
    "s_waitcnt vmcnt(7)\n\t"
    "v_pk_add_f32 v[0:1], v[0:1], v[124:125]\n\t"
    "v_pk_add_f32 v[2:3], v[2:3], v[126:127]\n\t"
    "v_cvt_pk_bf16_f32 v0, v0, v1\n\t"
    "v_cvt_pk_bf16_f32 v1, v2, v3\n\t"
    "ds_write_b64 v96, v[0:1] offset:0\n\t"
    "v_accvgpr_read_b32 v4, a20\n\t"
    "v_accvgpr_read_b32 v5, a21\n\t"
    "v_accvgpr_read_b32 v6, a22\n\t"
    "v_accvgpr_read_b32 v7, a23\n\t"
    "s_waitcnt vmcnt(6)\n\t"
    "v_pk_add_f32 v[4:5], v[4:5], v[128:129]\n\t"
    "v_pk_add_f32 v[6:7], v[6:7], v[130:131]\n\t"
    "v_cvt_pk_bf16_f32 v4, v4, v5\n\t"
    "v_cvt_pk_bf16_f32 v5, v6, v7\n\t"
    "ds_write_b64 v96, v[4:5] offset:32\n\t"
    "v_accvgpr_read_b32 v8, a40\n\t"
    "v_accvgpr_read_b32 v9, a41\n\t"
    "v_accvgpr_read_b32 v10, a42\n\t"
    "v_accvgpr_read_b32 v11, a43\n\t"
    "s_waitcnt vmcnt(5)\n\t"
    "v_pk_add_f32 v[8:9], v[8:9], v[132:133]\n\t"
    "v_pk_add_f32 v[10:11], v[10:11], v[134:135]\n\t"
    "v_cvt_pk_bf16_f32 v8, v8, v9\n\t"
    "v_cvt_pk_bf16_f32 v9, v10, v11\n\t"
    "ds_write_b64 v96, v[8:9] offset:64\n\t"
    "v_accvgpr_read_b32 v12, a60\n\t"
    "v_accvgpr_read_b32 v13, a61\n\t"
    "v_accvgpr_read_b32 v14, a62\n\t"
    "v_accvgpr_read_b32 v15, a63\n\t"
    "s_waitcnt vmcnt(4)\n\t"
    "v_pk_add_f32 v[12:13], v[12:13], v[136:137]\n\t"
    "v_pk_add_f32 v[14:15], v[14:15], v[138:139]\n\t"
    "v_cvt_pk_bf16_f32 v12, v12, v13\n\t"
    "v_cvt_pk_bf16_f32 v13, v14, v15\n\t"
    "ds_write_b64 v96, v[12:13] offset:96\n\t"
    "v_accvgpr_read_b32 v16, a4\n\t"
    "v_accvgpr_read_b32 v17, a5\n\t"
    "v_accvgpr_read_b32 v18, a6\n\t"
    "v_accvgpr_read_b32 v19, a7\n\t"
    "s_waitcnt vmcnt(3)\n\t"
    "v_pk_add_f32 v[16:17], v[16:17], v[140:141]\n\t"
    "v_pk_add_f32 v[18:19], v[18:19], v[142:143]\n\t"
    "v_cvt_pk_bf16_f32 v16, v16, v17\n\t"
    "v_cvt_pk_bf16_f32 v17, v18, v19\n\t"
    "ds_write_b64 v96, v[16:17] offset:4352\n\t"
    "v_accvgpr_read_b32 v20, a24\n\t"
    "v_accvgpr_read_b32 v21, a25\n\t"
    "v_accvgpr_read_b32 v22, a26\n\t"
    "v_accvgpr_read_b32 v23, a27\n\t"
    "s_waitcnt vmcnt(2)\n\t"
    "v_pk_add_f32 v[20:21], v[20:21], v[144:145]\n\t"
    "v_pk_add_f32 v[22:23], v[22:23], v[146:147]\n\t"
    "v_cvt_pk_bf16_f32 v20, v20, v21\n\t"
    "v_cvt_pk_bf16_f32 v21, v22, v23\n\t"
    "ds_write_b64 v96, v[20:21] offset:4384\n\t"
    "v_accvgpr_read_b32 v24, a44\n\t"
    "v_accvgpr_read_b32 v25, a45\n\t"
    "v_accvgpr_read_b32 v26, a46\n\t"
    "v_accvgpr_read_b32 v27, a47\n\t"
    "s_waitcnt vmcnt(1)\n\t"
    "v_pk_add_f32 v[24:25], v[24:25], v[148:149]\n\t"
    "v_pk_add_f32 v[26:27], v[26:27], v[150:151]\n\t"
    "v_cvt_pk_bf16_f32 v24, v24, v25\n\t"
    "v_cvt_pk_bf16_f32 v25, v26, v27\n\t"
    "ds_write_b64 v96, v[24:25] offset:4416\n\t"
    "v_accvgpr_read_b32 v28, a64\n\t"
    "v_accvgpr_read_b32 v29, a65\n\t"
    "v_accvgpr_read_b32 v30, a66\n\t"
    "v_accvgpr_read_b32 v31, a67\n\t"
    "s_waitcnt vmcnt(0)\n\t"
    "v_pk_add_f32 v[28:29], v[28:29], v[152:153]\n\t"
    "v_pk_add_f32 v[30:31], v[30:31], v[154:155]\n\t"
    "v_cvt_pk_bf16_f32 v28, v28, v29\n\t"
    "v_cvt_pk_bf16_f32 v29, v30, v31\n\t"
    "ds_write_b64 v96, v[28:29] offset:4448\n\t"
    "s_branch .Lo_stagedb\n\t"
    ".Lo_rcvb2:\n\t"
    "s_mov_b32 s40, 3072\n\t"
    "buffer_load_dwordx4 v[124:127], v214, s[16:19], s40 offen sc1\n\t"
    "s_mov_b32 s40, 8192\n\t"
    "buffer_load_dwordx4 v[128:131], v214, s[16:19], s40 offen sc1\n\t"
    "s_mov_b32 s40, 13312\n\t"
    "buffer_load_dwordx4 v[132:135], v214, s[16:19], s40 offen sc1\n\t"
    "s_mov_b32 s40, 18432\n\t"
    "buffer_load_dwordx4 v[136:139], v214, s[16:19], s40 offen sc1\n\t"
    "s_mov_b32 s40, 4096\n\t"
    "buffer_load_dwordx4 v[140:143], v214, s[16:19], s40 offen sc1\n\t"
    "s_mov_b32 s40, 9216\n\t"
    "buffer_load_dwordx4 v[144:147], v214, s[16:19], s40 offen sc1\n\t"
    "s_mov_b32 s40, 14336\n\t"
    "buffer_load_dwordx4 v[148:151], v214, s[16:19], s40 offen sc1\n\t"
    "s_mov_b32 s40, 19456\n\t"
    "buffer_load_dwordx4 v[152:155], v214, s[16:19], s40 offen sc1\n\t"
    "v_accvgpr_read_b32 v0, a12\n\t"
    "v_accvgpr_read_b32 v1, a13\n\t"
    "v_accvgpr_read_b32 v2, a14\n\t"
    "v_accvgpr_read_b32 v3, a15\n\t"
    "s_waitcnt vmcnt(7)\n\t"
    "v_pk_add_f32 v[0:1], v[0:1], v[124:125]\n\t"
    "v_pk_add_f32 v[2:3], v[2:3], v[126:127]\n\t"
    "v_cvt_pk_bf16_f32 v0, v0, v1\n\t"
    "v_cvt_pk_bf16_f32 v1, v2, v3\n\t"
    "ds_write_b64 v96, v[0:1] offset:0\n\t"
    "v_accvgpr_read_b32 v4, a32\n\t"
    "v_accvgpr_read_b32 v5, a33\n\t"
    "v_accvgpr_read_b32 v6, a34\n\t"
    "v_accvgpr_read_b32 v7, a35\n\t"
    "s_waitcnt vmcnt(6)\n\t"
    "v_pk_add_f32 v[4:5], v[4:5], v[128:129]\n\t"
    "v_pk_add_f32 v[6:7], v[6:7], v[130:131]\n\t"
    "v_cvt_pk_bf16_f32 v4, v4, v5\n\t"
    "v_cvt_pk_bf16_f32 v5, v6, v7\n\t"
    "ds_write_b64 v96, v[4:5] offset:32\n\t"
    "v_accvgpr_read_b32 v8, a52\n\t"
    "v_accvgpr_read_b32 v9, a53\n\t"
    "v_accvgpr_read_b32 v10, a54\n\t"
    "v_accvgpr_read_b32 v11, a55\n\t"
    "s_waitcnt vmcnt(5)\n\t"
    "v_pk_add_f32 v[8:9], v[8:9], v[132:133]\n\t"
    "v_pk_add_f32 v[10:11], v[10:11], v[134:135]\n\t"
    "v_cvt_pk_bf16_f32 v8, v8, v9\n\t"
    "v_cvt_pk_bf16_f32 v9, v10, v11\n\t"
    "ds_write_b64 v96, v[8:9] offset:64\n\t"
    "v_accvgpr_read_b32 v12, a72\n\t"
    "v_accvgpr_read_b32 v13, a73\n\t"
    "v_accvgpr_read_b32 v14, a74\n\t"
    "v_accvgpr_read_b32 v15, a75\n\t"
    "s_waitcnt vmcnt(4)\n\t"
    "v_pk_add_f32 v[12:13], v[12:13], v[136:137]\n\t"
    "v_pk_add_f32 v[14:15], v[14:15], v[138:139]\n\t"
    "v_cvt_pk_bf16_f32 v12, v12, v13\n\t"
    "v_cvt_pk_bf16_f32 v13, v14, v15\n\t"
    "ds_write_b64 v96, v[12:13] offset:96\n\t"
    "v_accvgpr_read_b32 v16, a16\n\t"
    "v_accvgpr_read_b32 v17, a17\n\t"
    "v_accvgpr_read_b32 v18, a18\n\t"
    "v_accvgpr_read_b32 v19, a19\n\t"
    "s_waitcnt vmcnt(3)\n\t"
    "v_pk_add_f32 v[16:17], v[16:17], v[140:141]\n\t"
    "v_pk_add_f32 v[18:19], v[18:19], v[142:143]\n\t"
    "v_cvt_pk_bf16_f32 v16, v16, v17\n\t"
    "v_cvt_pk_bf16_f32 v17, v18, v19\n\t"
    "ds_write_b64 v96, v[16:17] offset:4352\n\t"
    "v_accvgpr_read_b32 v20, a36\n\t"
    "v_accvgpr_read_b32 v21, a37\n\t"
    "v_accvgpr_read_b32 v22, a38\n\t"
    "v_accvgpr_read_b32 v23, a39\n\t"
    "s_waitcnt vmcnt(2)\n\t"
    "v_pk_add_f32 v[20:21], v[20:21], v[144:145]\n\t"
    "v_pk_add_f32 v[22:23], v[22:23], v[146:147]\n\t"
    "v_cvt_pk_bf16_f32 v20, v20, v21\n\t"
    "v_cvt_pk_bf16_f32 v21, v22, v23\n\t"
    "ds_write_b64 v96, v[20:21] offset:4384\n\t"
    "v_accvgpr_read_b32 v24, a56\n\t"
    "v_accvgpr_read_b32 v25, a57\n\t"
    "v_accvgpr_read_b32 v26, a58\n\t"
    "v_accvgpr_read_b32 v27, a59\n\t"
    "s_waitcnt vmcnt(1)\n\t"
    "v_pk_add_f32 v[24:25], v[24:25], v[148:149]\n\t"
    "v_pk_add_f32 v[26:27], v[26:27], v[150:151]\n\t"
    "v_cvt_pk_bf16_f32 v24, v24, v25\n\t"
    "v_cvt_pk_bf16_f32 v25, v26, v27\n\t"
    "ds_write_b64 v96, v[24:25] offset:4416\n\t"
    "v_accvgpr_read_b32 v28, a76\n\t"
    "v_accvgpr_read_b32 v29, a77\n\t"
    "v_accvgpr_read_b32 v30, a78\n\t"
    "v_accvgpr_read_b32 v31, a79\n\t"
    "s_waitcnt vmcnt(0)\n\t"
    "v_pk_add_f32 v[28:29], v[28:29], v[152:153]\n\t"
    "v_pk_add_f32 v[30:31], v[30:31], v[154:155]\n\t"
    "v_cvt_pk_bf16_f32 v28, v28, v29\n\t"
    "v_cvt_pk_bf16_f32 v29, v30, v31\n\t"
    "ds_write_b64 v96, v[28:29] offset:4448\n\t"
    "s_branch .Lo_stagedb\n\t"
    ".Lo_rcvb3:\n\t"
    "s_mov_b32 s40, 2048\n\t"
    "buffer_load_dwordx4 v[124:127], v214, s[16:19], s40 offen sc1\n\t"
    "s_mov_b32 s40, 7168\n\t"
    "buffer_load_dwordx4 v[128:131], v214, s[16:19], s40 offen sc1\n\t"
    "s_mov_b32 s40, 12288\n\t"
    "buffer_load_dwordx4 v[132:135], v214, s[16:19], s40 offen sc1\n\t"
    "s_mov_b32 s40, 17408\n\t"
    "buffer_load_dwordx4 v[136:139], v214, s[16:19], s40 offen sc1\n\t"
    "s_mov_b32 s40, 3072\n\t"
    "buffer_load_dwordx4 v[140:143], v214, s[16:19], s40 offen sc1\n\t"
    "s_mov_b32 s40, 8192\n\t"
    "buffer_load_dwordx4 v[144:147], v214, s[16:19], s40 offen sc1\n\t"
    "s_mov_b32 s40, 13312\n\t"
    "buffer_load_dwordx4 v[148:151], v214, s[16:19], s40 offen sc1\n\t"
    "s_mov_b32 s40, 18432\n\t"
    "buffer_load_dwordx4 v[152:155], v214, s[16:19], s40 offen sc1\n\t"
    "s_mov_b32 s40, 4096\n\t"
    "buffer_load_dwordx4 v[156:159], v214, s[16:19], s40 offen sc1\n\t"
    "s_mov_b32 s40, 9216\n\t"
    "buffer_load_dwordx4 v[160:163], v214, s[16:19], s40 offen sc1\n\t"
    "s_mov_b32 s40, 14336\n\t"
    "buffer_load_dwordx4 v[164:167], v214, s[16:19], s40 offen sc1\n\t"
    "s_mov_b32 s40, 19456\n\t"
    "buffer_load_dwordx4 v[168:171], v214, s[16:19], s40 offen sc1\n\t"
    "v_accvgpr_read_b32 v0, a8\n\t"
    "v_accvgpr_read_b32 v1, a9\n\t"
    "v_accvgpr_read_b32 v2, a10\n\t"
    "v_accvgpr_read_b32 v3, a11\n\t"
    "s_waitcnt vmcnt(11)\n\t"
    "v_pk_add_f32 v[0:1], v[0:1], v[124:125]\n\t"
    "v_pk_add_f32 v[2:3], v[2:3], v[126:127]\n\t"
    "v_cvt_pk_bf16_f32 v0, v0, v1\n\t"
    "v_cvt_pk_bf16_f32 v1, v2, v3\n\t"
    "ds_write_b64 v96, v[0:1] offset:0\n\t"
    "v_accvgpr_read_b32 v4, a28\n\t"
    "v_accvgpr_read_b32 v5, a29\n\t"
    "v_accvgpr_read_b32 v6, a30\n\t"
    "v_accvgpr_read_b32 v7, a31\n\t"
    "s_waitcnt vmcnt(10)\n\t"
    "v_pk_add_f32 v[4:5], v[4:5], v[128:129]\n\t"
    "v_pk_add_f32 v[6:7], v[6:7], v[130:131]\n\t"
    "v_cvt_pk_bf16_f32 v4, v4, v5\n\t"
    "v_cvt_pk_bf16_f32 v5, v6, v7\n\t"
    "ds_write_b64 v96, v[4:5] offset:32\n\t"
    "v_accvgpr_read_b32 v8, a48\n\t"
    "v_accvgpr_read_b32 v9, a49\n\t"
    "v_accvgpr_read_b32 v10, a50\n\t"
    "v_accvgpr_read_b32 v11, a51\n\t"
    "s_waitcnt vmcnt(9)\n\t"
    "v_pk_add_f32 v[8:9], v[8:9], v[132:133]\n\t"
    "v_pk_add_f32 v[10:11], v[10:11], v[134:135]\n\t"
    "v_cvt_pk_bf16_f32 v8, v8, v9\n\t"
    "v_cvt_pk_bf16_f32 v9, v10, v11\n\t"
    "ds_write_b64 v96, v[8:9] offset:64\n\t"
    "v_accvgpr_read_b32 v12, a68\n\t"
    "v_accvgpr_read_b32 v13, a69\n\t"
    "v_accvgpr_read_b32 v14, a70\n\t"
    "v_accvgpr_read_b32 v15, a71\n\t"
    "s_waitcnt vmcnt(8)\n\t"
    "v_pk_add_f32 v[12:13], v[12:13], v[136:137]\n\t"
    "v_pk_add_f32 v[14:15], v[14:15], v[138:139]\n\t"
    "v_cvt_pk_bf16_f32 v12, v12, v13\n\t"
    "v_cvt_pk_bf16_f32 v13, v14, v15\n\t"
    "ds_write_b64 v96, v[12:13] offset:96\n\t"
    "v_accvgpr_read_b32 v16, a12\n\t"
    "v_accvgpr_read_b32 v17, a13\n\t"
    "v_accvgpr_read_b32 v18, a14\n\t"
    "v_accvgpr_read_b32 v19, a15\n\t"
    "s_waitcnt vmcnt(7)\n\t"
    "v_pk_add_f32 v[16:17], v[16:17], v[140:141]\n\t"
    "v_pk_add_f32 v[18:19], v[18:19], v[142:143]\n\t"
    "v_cvt_pk_bf16_f32 v16, v16, v17\n\t"
    "v_cvt_pk_bf16_f32 v17, v18, v19\n\t"
    "ds_write_b64 v96, v[16:17] offset:4352\n\t"
    "v_accvgpr_read_b32 v20, a32\n\t"
    "v_accvgpr_read_b32 v21, a33\n\t"
    "v_accvgpr_read_b32 v22, a34\n\t"
    "v_accvgpr_read_b32 v23, a35\n\t"
    "s_waitcnt vmcnt(6)\n\t"
    "v_pk_add_f32 v[20:21], v[20:21], v[144:145]\n\t"
    "v_pk_add_f32 v[22:23], v[22:23], v[146:147]\n\t"
    "v_cvt_pk_bf16_f32 v20, v20, v21\n\t"
    "v_cvt_pk_bf16_f32 v21, v22, v23\n\t"
    "ds_write_b64 v96, v[20:21] offset:4384\n\t"
    "v_accvgpr_read_b32 v24, a52\n\t"
    "v_accvgpr_read_b32 v25, a53\n\t"
    "v_accvgpr_read_b32 v26, a54\n\t"
    "v_accvgpr_read_b32 v27, a55\n\t"
    "s_waitcnt vmcnt(5)\n\t"
    "v_pk_add_f32 v[24:25], v[24:25], v[148:149]\n\t"
    "v_pk_add_f32 v[26:27], v[26:27], v[150:151]\n\t"
    "v_cvt_pk_bf16_f32 v24, v24, v25\n\t"
    "v_cvt_pk_bf16_f32 v25, v26, v27\n\t"
    "ds_write_b64 v96, v[24:25] offset:4416\n\t"
    "v_accvgpr_read_b32 v28, a72\n\t"
    "v_accvgpr_read_b32 v29, a73\n\t"
    "v_accvgpr_read_b32 v30, a74\n\t"
    "v_accvgpr_read_b32 v31, a75\n\t"
    "s_waitcnt vmcnt(4)\n\t"
    "v_pk_add_f32 v[28:29], v[28:29], v[152:153]\n\t"
    "v_pk_add_f32 v[30:31], v[30:31], v[154:155]\n\t"
    "v_cvt_pk_bf16_f32 v28, v28, v29\n\t"
    "v_cvt_pk_bf16_f32 v29, v30, v31\n\t"
    "ds_write_b64 v96, v[28:29] offset:4448\n\t"
    "v_accvgpr_read_b32 v0, a16\n\t"
    "v_accvgpr_read_b32 v1, a17\n\t"
    "v_accvgpr_read_b32 v2, a18\n\t"
    "v_accvgpr_read_b32 v3, a19\n\t"
    "s_waitcnt vmcnt(3)\n\t"
    "v_pk_add_f32 v[0:1], v[0:1], v[156:157]\n\t"
    "v_pk_add_f32 v[2:3], v[2:3], v[158:159]\n\t"
    "v_cvt_pk_bf16_f32 v0, v0, v1\n\t"
    "v_cvt_pk_bf16_f32 v1, v2, v3\n\t"
    "ds_write_b64 v96, v[0:1] offset:8704\n\t"
    "v_accvgpr_read_b32 v4, a36\n\t"
    "v_accvgpr_read_b32 v5, a37\n\t"
    "v_accvgpr_read_b32 v6, a38\n\t"
    "v_accvgpr_read_b32 v7, a39\n\t"
    "s_waitcnt vmcnt(2)\n\t"
    "v_pk_add_f32 v[4:5], v[4:5], v[160:161]\n\t"
    "v_pk_add_f32 v[6:7], v[6:7], v[162:163]\n\t"
    "v_cvt_pk_bf16_f32 v4, v4, v5\n\t"
    "v_cvt_pk_bf16_f32 v5, v6, v7\n\t"
    "ds_write_b64 v96, v[4:5] offset:8736\n\t"
    "v_accvgpr_read_b32 v8, a56\n\t"
    "v_accvgpr_read_b32 v9, a57\n\t"
    "v_accvgpr_read_b32 v10, a58\n\t"
    "v_accvgpr_read_b32 v11, a59\n\t"
    "s_waitcnt vmcnt(1)\n\t"
    "v_pk_add_f32 v[8:9], v[8:9], v[164:165]\n\t"
    "v_pk_add_f32 v[10:11], v[10:11], v[166:167]\n\t"
    "v_cvt_pk_bf16_f32 v8, v8, v9\n\t"
    "v_cvt_pk_bf16_f32 v9, v10, v11\n\t"
    "ds_write_b64 v96, v[8:9] offset:8768\n\t"
    "v_accvgpr_read_b32 v12, a76\n\t"
    "v_accvgpr_read_b32 v13, a77\n\t"
    "v_accvgpr_read_b32 v14, a78\n\t"
    "v_accvgpr_read_b32 v15, a79\n\t"
    "s_waitcnt vmcnt(0)\n\t"
    "v_pk_add_f32 v[12:13], v[12:13], v[168:169]\n\t"
    "v_pk_add_f32 v[14:15], v[14:15], v[170:171]\n\t"
    "v_cvt_pk_bf16_f32 v12, v12, v13\n\t"
    "v_cvt_pk_bf16_f32 v13, v14, v15\n\t"
    "ds_write_b64 v96, v[12:13] offset:8800\n\t"
    ".Lo_stagedb:\n\t"
    "s_waitcnt lgkmcnt(0)\n\t"
    "s_barrier\n\t"
    "ds_read_b128 v[124:127], v97\n\t"
    "ds_read_b128 v[128:131], v98\n\t"
    "ds_read_b128 v[132:135], v99\n\t"
    "ds_read_b128 v[136:139], v100\n\t"
    "ds_read_b128 v[140:143], v101\n\t"
    "s_waitcnt lgkmcnt(4)\n\t"
    "buffer_store_dwordx4 v[124:127], v106, s[20:23], 0 offen\n\t"
    "s_waitcnt lgkmcnt(3)\n\t"
    "buffer_store_dwordx4 v[128:131], v107, s[20:23], 0 offen\n\t"
    "s_waitcnt lgkmcnt(2)\n\t"
    "buffer_store_dwordx4 v[132:135], v108, s[20:23], 0 offen\n\t"
    "s_waitcnt lgkmcnt(1)\n\t"
    "buffer_store_dwordx4 v[136:139], v109, s[20:23], 0 offen\n\t"
    "s_waitcnt lgkmcnt(0)\n\t"
    "buffer_store_dwordx4 v[140:143], v110, s[20:23], 0 offen\n\t"
    "s_branch .Lk1_end\n\t"
    ".Lk1_stub_s0:\n\t"
    "s_swappc_b64 s[52:53], s[56:57]\n\t"
    "s_branch .Lk1_cont_s0\n\t"
    ".Lk1_stub_sa:\n\t"
    "s_swappc_b64 s[52:53], s[54:55]\n\t"
    "s_branch .Lk1_cont_sa\n\t"
    ".Lk1_stub_sb:\n\t"
    "s_swappc_b64 s[52:53], s[56:57]\n\t"
    "s_branch .Lk1_cont_sb\n\t"
    ".Lk1_stub_s11:\n\t"
    "s_swappc_b64 s[52:53], s[54:55]\n\t"
    "s_branch .Lk1_cont_s11\n\t"
    ".Lk1_stub_s12:\n\t"
    "s_swappc_b64 s[52:53], s[56:57]\n\t"
    "s_branch .Lk1_cont_s12\n\t"
    ".Lk1_stub_s13:\n\t"
    "s_swappc_b64 s[52:53], s[54:55]\n\t"
    "s_branch .Lk1_cont_s13\n\t"
    ".Lk1_stub_s14q:\n\t"
    "s_swappc_b64 s[52:53], s[56:57]\n\t"
    "s_branch .Lk1_cont_s14q\n\t"
    ".Lk1_stub_s14e:\n\t"
    "s_swappc_b64 s[52:53], s[56:57]\n\t"
    "s_branch .Lk1_cont_s14e\n\t"
    ".Lk1_slow0:\n\t"
    "s_nop 7\n\t"
    "s_nop 7\n\t"
    "v_max3_f32 v225, v104, v105, v106\n\t"
    "v_max3_f32 v225, v225, v107, v124\n\t"
    "v_max3_f32 v225, v225, v125, v126\n\t"
    "v_max_f32 v225, v225, v127\n\t"
    "v_max3_f32 v226, v108, v109, v110\n\t"
    "v_max3_f32 v226, v226, v111, v128\n\t"
    "v_max3_f32 v226, v226, v129, v130\n\t"
    "v_max_f32 v226, v226, v131\n\t"
    "v_max3_f32 v227, v112, v113, v114\n\t"
    "v_max3_f32 v227, v227, v115, v132\n\t"
    "v_max3_f32 v227, v227, v133, v134\n\t"
    "v_max_f32 v227, v227, v135\n\t"
    "v_max3_f32 v228, v116, v117, v118\n\t"
    "v_max3_f32 v228, v228, v119, v136\n\t"
    "v_max3_f32 v228, v228, v137, v138\n\t"
    "v_max_f32 v228, v228, v139\n\t"
    "v_max3_f32 v229, v120, v121, v122\n\t"
    "v_max3_f32 v229, v229, v123, v140\n\t"
    "v_max3_f32 v229, v229, v141, v142\n\t"
    "v_max_f32 v229, v229, v143\n\t"
    "v_mov_b32 v230, v225\n\t"
    "v_mov_b32 v231, v226\n\t"
    "v_mov_b32 v232, v227\n\t"
    "v_mov_b32 v233, v228\n\t"
    "v_mov_b32 v234, v229\n\t"
    "s_nop 0\n\t"
    "v_permlane32_swap_b32 v225, v230\n\t"
    "v_permlane32_swap_b32 v226, v231\n\t"
    "v_permlane32_swap_b32 v227, v232\n\t"
    "v_permlane32_swap_b32 v228, v233\n\t"
    "v_permlane32_swap_b32 v229, v234\n\t"
    "v_max_f32 v225, v225, v230\n\t"
    "v_max_f32 v226, v226, v231\n\t"
    "v_max_f32 v227, v227, v232\n\t"
    "v_max_f32 v228, v228, v233\n\t"
    "v_max_f32 v229, v229, v234\n\t"
    "v_mov_b32 v230, v225\n\t"
    "v_mov_b32 v231, v226\n\t"
    "v_mov_b32 v232, v227\n\t"
    "v_mov_b32 v233, v228\n\t"
    "v_mov_b32 v234, v229\n\t"
    "s_nop 0\n\t"
    "v_permlane16_swap_b32 v225, v230\n\t"
    "v_permlane16_swap_b32 v226, v231\n\t"
    "v_permlane16_swap_b32 v227, v232\n\t"
    "v_permlane16_swap_b32 v228, v233\n\t"
    "v_permlane16_swap_b32 v229, v234\n\t"
    "v_max_f32 v225, v225, v230\n\t"
    "v_max_f32 v226, v226, v231\n\t"
    "v_max_f32 v227, v227, v232\n\t"
    "v_max_f32 v228, v228, v233\n\t"
    "v_max_f32 v229, v229, v234\n\t"
    "v_max_f32 v225, 0, v225\n\t"
    "v_max_f32 v226, 0, v226\n\t"
    "v_max_f32 v227, 0, v227\n\t"
    "v_max_f32 v228, 0, v228\n\t"
    "v_max_f32 v229, 0, v229\n\t"
    "v_sub_f32 v144, v144, v225\n\t"
    "v_sub_f32 v145, v145, v225\n\t"
    "v_sub_f32 v146, v146, v225\n\t"
    "v_sub_f32 v147, v147, v225\n\t"
    "v_sub_f32 v104, v104, v225\n\t"
    "v_sub_f32 v105, v105, v225\n\t"
    "v_sub_f32 v106, v106, v225\n\t"
    "v_sub_f32 v107, v107, v225\n\t"
    "v_sub_f32 v124, v124, v225\n\t"
    "v_sub_f32 v125, v125, v225\n\t"
    "v_sub_f32 v126, v126, v225\n\t"
    "v_sub_f32 v127, v127, v225\n\t"
    "v_sub_f32 v148, v148, v226\n\t"
    "v_sub_f32 v149, v149, v226\n\t"
    "v_sub_f32 v150, v150, v226\n\t"
    "v_sub_f32 v151, v151, v226\n\t"
    "v_sub_f32 v108, v108, v226\n\t"
    "v_sub_f32 v109, v109, v226\n\t"
    "v_sub_f32 v110, v110, v226\n\t"
    "v_sub_f32 v111, v111, v226\n\t"
    "v_sub_f32 v128, v128, v226\n\t"
    "v_sub_f32 v129, v129, v226\n\t"
    "v_sub_f32 v130, v130, v226\n\t"
    "v_sub_f32 v131, v131, v226\n\t"
    "v_sub_f32 v152, v152, v227\n\t"
    "v_sub_f32 v153, v153, v227\n\t"
    "v_sub_f32 v154, v154, v227\n\t"
    "v_sub_f32 v155, v155, v227\n\t"
    "v_sub_f32 v112, v112, v227\n\t"
    "v_sub_f32 v113, v113, v227\n\t"
    "v_sub_f32 v114, v114, v227\n\t"
    "v_sub_f32 v115, v115, v227\n\t"
    "v_sub_f32 v132, v132, v227\n\t"
    "v_sub_f32 v133, v133, v227\n\t"
    "v_sub_f32 v134, v134, v227\n\t"
    "v_sub_f32 v135, v135, v227\n\t"
    "v_sub_f32 v156, v156, v228\n\t"
    "v_sub_f32 v157, v157, v228\n\t"
    "v_sub_f32 v158, v158, v228\n\t"
    "v_sub_f32 v159, v159, v228\n\t"
    "v_sub_f32 v116, v116, v228\n\t"
    "v_sub_f32 v117, v117, v228\n\t"
    "v_sub_f32 v118, v118, v228\n\t"
    "v_sub_f32 v119, v119, v228\n\t"
    "v_sub_f32 v136, v136, v228\n\t"
    "v_sub_f32 v137, v137, v228\n\t"
    "v_sub_f32 v138, v138, v228\n\t"
    "v_sub_f32 v139, v139, v228\n\t"
    "v_sub_f32 v160, v160, v229\n\t"
    "v_sub_f32 v161, v161, v229\n\t"
    "v_sub_f32 v162, v162, v229\n\t"
    "v_sub_f32 v163, v163, v229\n\t"
    "v_sub_f32 v120, v120, v229\n\t"
    "v_sub_f32 v121, v121, v229\n\t"
    "v_sub_f32 v122, v122, v229\n\t"
    "v_sub_f32 v123, v123, v229\n\t"
    "v_sub_f32 v140, v140, v229\n\t"
    "v_sub_f32 v141, v141, v229\n\t"
    "v_sub_f32 v142, v142, v229\n\t"
    "v_sub_f32 v143, v143, v229\n\t"
    "v_exp_f32 v225, -v225\n\t"
    "v_exp_f32 v226, -v226\n\t"
    "v_exp_f32 v227, -v227\n\t"
    "v_exp_f32 v228, -v228\n\t"
    "v_exp_f32 v229, -v229\n\t"
    "s_nop 0\n\t"
    "v_mul_f32 v220, v220, v225\n\t"
    "v_mul_f32 v221, v221, v226\n\t"
    "v_mul_f32 v222, v222, v227\n\t"
    "v_mul_f32 v223, v223, v228\n\t"
    "v_mul_f32 v224, v224, v229\n\t"
    "v_accvgpr_read_b32 v230, a0\n\t"
    "v_accvgpr_read_b32 v231, a1\n\t"
    "v_accvgpr_read_b32 v232, a2\n\t"
    "v_accvgpr_read_b32 v233, a3\n\t"
    "v_mul_f32 v230, v230, v225\n\t"
    "v_mul_f32 v231, v231, v225\n\t"
    "v_mul_f32 v232, v232, v225\n\t"
    "v_mul_f32 v233, v233, v225\n\t"
    "v_accvgpr_write_b32 a0, v230\n\t"
    "v_accvgpr_write_b32 a1, v231\n\t"
    "v_accvgpr_write_b32 a2, v232\n\t"
    "v_accvgpr_write_b32 a3, v233\n\t"
    "v_accvgpr_read_b32 v230, a4\n\t"
    "v_accvgpr_read_b32 v231, a5\n\t"
    "v_accvgpr_read_b32 v232, a6\n\t"
    "v_accvgpr_read_b32 v233, a7\n\t"
    "v_mul_f32 v230, v230, v226\n\t"
    "v_mul_f32 v231, v231, v226\n\t"
    "v_mul_f32 v232, v232, v226\n\t"
    "v_mul_f32 v233, v233, v226\n\t"
    "v_accvgpr_write_b32 a4, v230\n\t"
    "v_accvgpr_write_b32 a5, v231\n\t"
    "v_accvgpr_write_b32 a6, v232\n\t"
    "v_accvgpr_write_b32 a7, v233\n\t"
    "v_accvgpr_read_b32 v230, a8\n\t"
    "v_accvgpr_read_b32 v231, a9\n\t"
    "v_accvgpr_read_b32 v232, a10\n\t"
    "v_accvgpr_read_b32 v233, a11\n\t"
    "v_mul_f32 v230, v230, v227\n\t"
    "v_mul_f32 v231, v231, v227\n\t"
    "v_mul_f32 v232, v232, v227\n\t"
    "v_mul_f32 v233, v233, v227\n\t"
    "v_accvgpr_write_b32 a8, v230\n\t"
    "v_accvgpr_write_b32 a9, v231\n\t"
    "v_accvgpr_write_b32 a10, v232\n\t"
    "v_accvgpr_write_b32 a11, v233\n\t"
    "v_accvgpr_read_b32 v230, a12\n\t"
    "v_accvgpr_read_b32 v231, a13\n\t"
    "v_accvgpr_read_b32 v232, a14\n\t"
    "v_accvgpr_read_b32 v233, a15\n\t"
    "v_mul_f32 v230, v230, v228\n\t"
    "v_mul_f32 v231, v231, v228\n\t"
    "v_mul_f32 v232, v232, v228\n\t"
    "v_mul_f32 v233, v233, v228\n\t"
    "v_accvgpr_write_b32 a12, v230\n\t"
    "v_accvgpr_write_b32 a13, v231\n\t"
    "v_accvgpr_write_b32 a14, v232\n\t"
    "v_accvgpr_write_b32 a15, v233\n\t"
    "v_accvgpr_read_b32 v230, a16\n\t"
    "v_accvgpr_read_b32 v231, a17\n\t"
    "v_accvgpr_read_b32 v232, a18\n\t"
    "v_accvgpr_read_b32 v233, a19\n\t"
    "v_mul_f32 v230, v230, v229\n\t"
    "v_mul_f32 v231, v231, v229\n\t"
    "v_mul_f32 v232, v232, v229\n\t"
    "v_mul_f32 v233, v233, v229\n\t"
    "v_accvgpr_write_b32 a16, v230\n\t"
    "v_accvgpr_write_b32 a17, v231\n\t"
    "v_accvgpr_write_b32 a18, v232\n\t"
    "v_accvgpr_write_b32 a19, v233\n\t"
    "v_accvgpr_read_b32 v230, a20\n\t"
    "v_accvgpr_read_b32 v231, a21\n\t"
    "v_accvgpr_read_b32 v232, a22\n\t"
    "v_accvgpr_read_b32 v233, a23\n\t"
    "v_mul_f32 v230, v230, v225\n\t"
    "v_mul_f32 v231, v231, v225\n\t"
    "v_mul_f32 v232, v232, v225\n\t"
    "v_mul_f32 v233, v233, v225\n\t"
    "v_accvgpr_write_b32 a20, v230\n\t"
    "v_accvgpr_write_b32 a21, v231\n\t"
    "v_accvgpr_write_b32 a22, v232\n\t"
    "v_accvgpr_write_b32 a23, v233\n\t"
    "v_accvgpr_read_b32 v230, a24\n\t"
    "v_accvgpr_read_b32 v231, a25\n\t"
    "v_accvgpr_read_b32 v232, a26\n\t"
    "v_accvgpr_read_b32 v233, a27\n\t"
    "v_mul_f32 v230, v230, v226\n\t"
    "v_mul_f32 v231, v231, v226\n\t"
    "v_mul_f32 v232, v232, v226\n\t"
    "v_mul_f32 v233, v233, v226\n\t"
    "v_accvgpr_write_b32 a24, v230\n\t"
    "v_accvgpr_write_b32 a25, v231\n\t"
    "v_accvgpr_write_b32 a26, v232\n\t"
    "v_accvgpr_write_b32 a27, v233\n\t"
    "v_accvgpr_read_b32 v230, a28\n\t"
    "v_accvgpr_read_b32 v231, a29\n\t"
    "v_accvgpr_read_b32 v232, a30\n\t"
    "v_accvgpr_read_b32 v233, a31\n\t"
    "v_mul_f32 v230, v230, v227\n\t"
    "v_mul_f32 v231, v231, v227\n\t"
    "v_mul_f32 v232, v232, v227\n\t"
    "v_mul_f32 v233, v233, v227\n\t"
    "v_accvgpr_write_b32 a28, v230\n\t"
    "v_accvgpr_write_b32 a29, v231\n\t"
    "v_accvgpr_write_b32 a30, v232\n\t"
    "v_accvgpr_write_b32 a31, v233\n\t"
    "v_accvgpr_read_b32 v230, a32\n\t"
    "v_accvgpr_read_b32 v231, a33\n\t"
    "v_accvgpr_read_b32 v232, a34\n\t"
    "v_accvgpr_read_b32 v233, a35\n\t"
    "v_mul_f32 v230, v230, v228\n\t"
    "v_mul_f32 v231, v231, v228\n\t"
    "v_mul_f32 v232, v232, v228\n\t"
    "v_mul_f32 v233, v233, v228\n\t"
    "v_accvgpr_write_b32 a32, v230\n\t"
    "v_accvgpr_write_b32 a33, v231\n\t"
    "v_accvgpr_write_b32 a34, v232\n\t"
    "v_accvgpr_write_b32 a35, v233\n\t"
    "v_accvgpr_read_b32 v230, a36\n\t"
    "v_accvgpr_read_b32 v231, a37\n\t"
    "v_accvgpr_read_b32 v232, a38\n\t"
    "v_accvgpr_read_b32 v233, a39\n\t"
    "v_mul_f32 v230, v230, v229\n\t"
    "v_mul_f32 v231, v231, v229\n\t"
    "v_mul_f32 v232, v232, v229\n\t"
    "v_mul_f32 v233, v233, v229\n\t"
    "v_accvgpr_write_b32 a36, v230\n\t"
    "v_accvgpr_write_b32 a37, v231\n\t"
    "v_accvgpr_write_b32 a38, v232\n\t"
    "v_accvgpr_write_b32 a39, v233\n\t"
    "v_accvgpr_read_b32 v230, a40\n\t"
    "v_accvgpr_read_b32 v231, a41\n\t"
    "v_accvgpr_read_b32 v232, a42\n\t"
    "v_accvgpr_read_b32 v233, a43\n\t"
    "v_mul_f32 v230, v230, v225\n\t"
    "v_mul_f32 v231, v231, v225\n\t"
    "v_mul_f32 v232, v232, v225\n\t"
    "v_mul_f32 v233, v233, v225\n\t"
    "v_accvgpr_write_b32 a40, v230\n\t"
    "v_accvgpr_write_b32 a41, v231\n\t"
    "v_accvgpr_write_b32 a42, v232\n\t"
    "v_accvgpr_write_b32 a43, v233\n\t"
    "v_accvgpr_read_b32 v230, a44\n\t"
    "v_accvgpr_read_b32 v231, a45\n\t"
    "v_accvgpr_read_b32 v232, a46\n\t"
    "v_accvgpr_read_b32 v233, a47\n\t"
    "v_mul_f32 v230, v230, v226\n\t"
    "v_mul_f32 v231, v231, v226\n\t"
    "v_mul_f32 v232, v232, v226\n\t"
    "v_mul_f32 v233, v233, v226\n\t"
    "v_accvgpr_write_b32 a44, v230\n\t"
    "v_accvgpr_write_b32 a45, v231\n\t"
    "v_accvgpr_write_b32 a46, v232\n\t"
    "v_accvgpr_write_b32 a47, v233\n\t"
    "v_accvgpr_read_b32 v230, a48\n\t"
    "v_accvgpr_read_b32 v231, a49\n\t"
    "v_accvgpr_read_b32 v232, a50\n\t"
    "v_accvgpr_read_b32 v233, a51\n\t"
    "v_mul_f32 v230, v230, v227\n\t"
    "v_mul_f32 v231, v231, v227\n\t"
    "v_mul_f32 v232, v232, v227\n\t"
    "v_mul_f32 v233, v233, v227\n\t"
    "v_accvgpr_write_b32 a48, v230\n\t"
    "v_accvgpr_write_b32 a49, v231\n\t"
    "v_accvgpr_write_b32 a50, v232\n\t"
    "v_accvgpr_write_b32 a51, v233\n\t"
    "v_accvgpr_read_b32 v230, a52\n\t"
    "v_accvgpr_read_b32 v231, a53\n\t"
    "v_accvgpr_read_b32 v232, a54\n\t"
    "v_accvgpr_read_b32 v233, a55\n\t"
    "v_mul_f32 v230, v230, v228\n\t"
    "v_mul_f32 v231, v231, v228\n\t"
    "v_mul_f32 v232, v232, v228\n\t"
    "v_mul_f32 v233, v233, v228\n\t"
    "v_accvgpr_write_b32 a52, v230\n\t"
    "v_accvgpr_write_b32 a53, v231\n\t"
    "v_accvgpr_write_b32 a54, v232\n\t"
    "v_accvgpr_write_b32 a55, v233\n\t"
    "v_accvgpr_read_b32 v230, a56\n\t"
    "v_accvgpr_read_b32 v231, a57\n\t"
    "v_accvgpr_read_b32 v232, a58\n\t"
    "v_accvgpr_read_b32 v233, a59\n\t"
    "v_mul_f32 v230, v230, v229\n\t"
    "v_mul_f32 v231, v231, v229\n\t"
    "v_mul_f32 v232, v232, v229\n\t"
    "v_mul_f32 v233, v233, v229\n\t"
    "v_accvgpr_write_b32 a56, v230\n\t"
    "v_accvgpr_write_b32 a57, v231\n\t"
    "v_accvgpr_write_b32 a58, v232\n\t"
    "v_accvgpr_write_b32 a59, v233\n\t"
    "v_accvgpr_read_b32 v230, a60\n\t"
    "v_accvgpr_read_b32 v231, a61\n\t"
    "v_accvgpr_read_b32 v232, a62\n\t"
    "v_accvgpr_read_b32 v233, a63\n\t"
    "v_mul_f32 v230, v230, v225\n\t"
    "v_mul_f32 v231, v231, v225\n\t"
    "v_mul_f32 v232, v232, v225\n\t"
    "v_mul_f32 v233, v233, v225\n\t"
    "v_accvgpr_write_b32 a60, v230\n\t"
    "v_accvgpr_write_b32 a61, v231\n\t"
    "v_accvgpr_write_b32 a62, v232\n\t"
    "v_accvgpr_write_b32 a63, v233\n\t"
    "v_accvgpr_read_b32 v230, a64\n\t"
    "v_accvgpr_read_b32 v231, a65\n\t"
    "v_accvgpr_read_b32 v232, a66\n\t"
    "v_accvgpr_read_b32 v233, a67\n\t"
    "v_mul_f32 v230, v230, v226\n\t"
    "v_mul_f32 v231, v231, v226\n\t"
    "v_mul_f32 v232, v232, v226\n\t"
    "v_mul_f32 v233, v233, v226\n\t"
    "v_accvgpr_write_b32 a64, v230\n\t"
    "v_accvgpr_write_b32 a65, v231\n\t"
    "v_accvgpr_write_b32 a66, v232\n\t"
    "v_accvgpr_write_b32 a67, v233\n\t"
    "v_accvgpr_read_b32 v230, a68\n\t"
    "v_accvgpr_read_b32 v231, a69\n\t"
    "v_accvgpr_read_b32 v232, a70\n\t"
    "v_accvgpr_read_b32 v233, a71\n\t"
    "v_mul_f32 v230, v230, v227\n\t"
    "v_mul_f32 v231, v231, v227\n\t"
    "v_mul_f32 v232, v232, v227\n\t"
    "v_mul_f32 v233, v233, v227\n\t"
    "v_accvgpr_write_b32 a68, v230\n\t"
    "v_accvgpr_write_b32 a69, v231\n\t"
    "v_accvgpr_write_b32 a70, v232\n\t"
    "v_accvgpr_write_b32 a71, v233\n\t"
    "v_accvgpr_read_b32 v230, a72\n\t"
    "v_accvgpr_read_b32 v231, a73\n\t"
    "v_accvgpr_read_b32 v232, a74\n\t"
    "v_accvgpr_read_b32 v233, a75\n\t"
    "v_mul_f32 v230, v230, v228\n\t"
    "v_mul_f32 v231, v231, v228\n\t"
    "v_mul_f32 v232, v232, v228\n\t"
    "v_mul_f32 v233, v233, v228\n\t"
    "v_accvgpr_write_b32 a72, v230\n\t"
    "v_accvgpr_write_b32 a73, v231\n\t"
    "v_accvgpr_write_b32 a74, v232\n\t"
    "v_accvgpr_write_b32 a75, v233\n\t"
    "v_accvgpr_read_b32 v230, a76\n\t"
    "v_accvgpr_read_b32 v231, a77\n\t"
    "v_accvgpr_read_b32 v232, a78\n\t"
    "v_accvgpr_read_b32 v233, a79\n\t"
    "v_mul_f32 v230, v230, v229\n\t"
    "v_mul_f32 v231, v231, v229\n\t"
    "v_mul_f32 v232, v232, v229\n\t"
    "v_mul_f32 v233, v233, v229\n\t"
    "v_accvgpr_write_b32 a76, v230\n\t"
    "v_accvgpr_write_b32 a77, v231\n\t"
    "v_accvgpr_write_b32 a78, v232\n\t"
    "v_accvgpr_write_b32 a79, v233\n\t"
    "v_accvgpr_read_b32 v230, a80\n\t"
    "v_accvgpr_read_b32 v231, a81\n\t"
    "v_accvgpr_read_b32 v232, a82\n\t"
    "v_accvgpr_read_b32 v233, a83\n\t"
    "v_mul_f32 v230, v230, v225\n\t"
    "v_mul_f32 v231, v231, v225\n\t"
    "v_mul_f32 v232, v232, v225\n\t"
    "v_mul_f32 v233, v233, v225\n\t"
    "v_accvgpr_write_b32 a80, v230\n\t"
    "v_accvgpr_write_b32 a81, v231\n\t"
    "v_accvgpr_write_b32 a82, v232\n\t"
    "v_accvgpr_write_b32 a83, v233\n\t"
    "v_accvgpr_read_b32 v230, a84\n\t"
    "v_accvgpr_read_b32 v231, a85\n\t"
    "v_accvgpr_read_b32 v232, a86\n\t"
    "v_accvgpr_read_b32 v233, a87\n\t"
    "v_mul_f32 v230, v230, v226\n\t"
    "v_mul_f32 v231, v231, v226\n\t"
    "v_mul_f32 v232, v232, v226\n\t"
    "v_mul_f32 v233, v233, v226\n\t"
    "v_accvgpr_write_b32 a84, v230\n\t"
    "v_accvgpr_write_b32 a85, v231\n\t"
    "v_accvgpr_write_b32 a86, v232\n\t"
    "v_accvgpr_write_b32 a87, v233\n\t"
    "v_accvgpr_read_b32 v230, a88\n\t"
    "v_accvgpr_read_b32 v231, a89\n\t"
    "v_accvgpr_read_b32 v232, a90\n\t"
    "v_accvgpr_read_b32 v233, a91\n\t"
    "v_mul_f32 v230, v230, v227\n\t"
    "v_mul_f32 v231, v231, v227\n\t"
    "v_mul_f32 v232, v232, v227\n\t"
    "v_mul_f32 v233, v233, v227\n\t"
    "v_accvgpr_write_b32 a88, v230\n\t"
    "v_accvgpr_write_b32 a89, v231\n\t"
    "v_accvgpr_write_b32 a90, v232\n\t"
    "v_accvgpr_write_b32 a91, v233\n\t"
    "v_accvgpr_read_b32 v230, a92\n\t"
    "v_accvgpr_read_b32 v231, a93\n\t"
    "v_accvgpr_read_b32 v232, a94\n\t"
    "v_accvgpr_read_b32 v233, a95\n\t"
    "v_mul_f32 v230, v230, v228\n\t"
    "v_mul_f32 v231, v231, v228\n\t"
    "v_mul_f32 v232, v232, v228\n\t"
    "v_mul_f32 v233, v233, v228\n\t"
    "v_accvgpr_write_b32 a92, v230\n\t"
    "v_accvgpr_write_b32 a93, v231\n\t"
    "v_accvgpr_write_b32 a94, v232\n\t"
    "v_accvgpr_write_b32 a95, v233\n\t"
    "v_accvgpr_read_b32 v230, a96\n\t"
    "v_accvgpr_read_b32 v231, a97\n\t"
    "v_accvgpr_read_b32 v232, a98\n\t"
    "v_accvgpr_read_b32 v233, a99\n\t"
    "v_mul_f32 v230, v230, v229\n\t"
    "v_mul_f32 v231, v231, v229\n\t"
    "v_mul_f32 v232, v232, v229\n\t"
    "v_mul_f32 v233, v233, v229\n\t"
    "v_accvgpr_write_b32 a96, v230\n\t"
    "v_accvgpr_write_b32 a97, v231\n\t"
    "v_accvgpr_write_b32 a98, v232\n\t"
    "v_accvgpr_write_b32 a99, v233\n\t"
    "v_accvgpr_read_b32 v230, a100\n\t"
    "v_accvgpr_read_b32 v231, a101\n\t"
    "v_accvgpr_read_b32 v232, a102\n\t"
    "v_accvgpr_read_b32 v233, a103\n\t"
    "v_mul_f32 v230, v230, v225\n\t"
    "v_mul_f32 v231, v231, v225\n\t"
    "v_mul_f32 v232, v232, v225\n\t"
    "v_mul_f32 v233, v233, v225\n\t"
    "v_accvgpr_write_b32 a100, v230\n\t"
    "v_accvgpr_write_b32 a101, v231\n\t"
    "v_accvgpr_write_b32 a102, v232\n\t"
    "v_accvgpr_write_b32 a103, v233\n\t"
    "v_accvgpr_read_b32 v230, a104\n\t"
    "v_accvgpr_read_b32 v231, a105\n\t"
    "v_accvgpr_read_b32 v232, a106\n\t"
    "v_accvgpr_read_b32 v233, a107\n\t"
    "v_mul_f32 v230, v230, v226\n\t"
    "v_mul_f32 v231, v231, v226\n\t"
    "v_mul_f32 v232, v232, v226\n\t"
    "v_mul_f32 v233, v233, v226\n\t"
    "v_accvgpr_write_b32 a104, v230\n\t"
    "v_accvgpr_write_b32 a105, v231\n\t"
    "v_accvgpr_write_b32 a106, v232\n\t"
    "v_accvgpr_write_b32 a107, v233\n\t"
    "v_accvgpr_read_b32 v230, a108\n\t"
    "v_accvgpr_read_b32 v231, a109\n\t"
    "v_accvgpr_read_b32 v232, a110\n\t"
    "v_accvgpr_read_b32 v233, a111\n\t"
    "v_mul_f32 v230, v230, v227\n\t"
    "v_mul_f32 v231, v231, v227\n\t"
    "v_mul_f32 v232, v232, v227\n\t"
    "v_mul_f32 v233, v233, v227\n\t"
    "v_accvgpr_write_b32 a108, v230\n\t"
    "v_accvgpr_write_b32 a109, v231\n\t"
    "v_accvgpr_write_b32 a110, v232\n\t"
    "v_accvgpr_write_b32 a111, v233\n\t"
    "v_accvgpr_read_b32 v230, a112\n\t"
    "v_accvgpr_read_b32 v231, a113\n\t"
    "v_accvgpr_read_b32 v232, a114\n\t"
    "v_accvgpr_read_b32 v233, a115\n\t"
    "v_mul_f32 v230, v230, v228\n\t"
    "v_mul_f32 v231, v231, v228\n\t"
    "v_mul_f32 v232, v232, v228\n\t"
    "v_mul_f32 v233, v233, v228\n\t"
    "v_accvgpr_write_b32 a112, v230\n\t"
    "v_accvgpr_write_b32 a113, v231\n\t"
    "v_accvgpr_write_b32 a114, v232\n\t"
    "v_accvgpr_write_b32 a115, v233\n\t"
    "v_accvgpr_read_b32 v230, a116\n\t"
    "v_accvgpr_read_b32 v231, a117\n\t"
    "v_accvgpr_read_b32 v232, a118\n\t"
    "v_accvgpr_read_b32 v233, a119\n\t"
    "v_mul_f32 v230, v230, v229\n\t"
    "v_mul_f32 v231, v231, v229\n\t"
    "v_mul_f32 v232, v232, v229\n\t"
    "v_mul_f32 v233, v233, v229\n\t"
    "v_accvgpr_write_b32 a116, v230\n\t"
    "v_accvgpr_write_b32 a117, v231\n\t"
    "v_accvgpr_write_b32 a118, v232\n\t"
    "v_accvgpr_write_b32 a119, v233\n\t"
    "v_accvgpr_read_b32 v230, a120\n\t"
    "v_accvgpr_read_b32 v231, a121\n\t"
    "v_accvgpr_read_b32 v232, a122\n\t"
    "v_accvgpr_read_b32 v233, a123\n\t"
    "v_mul_f32 v230, v230, v225\n\t"
    "v_mul_f32 v231, v231, v225\n\t"
    "v_mul_f32 v232, v232, v225\n\t"
    "v_mul_f32 v233, v233, v225\n\t"
    "v_accvgpr_write_b32 a120, v230\n\t"
    "v_accvgpr_write_b32 a121, v231\n\t"
    "v_accvgpr_write_b32 a122, v232\n\t"
    "v_accvgpr_write_b32 a123, v233\n\t"
    "v_accvgpr_read_b32 v230, a124\n\t"
    "v_accvgpr_read_b32 v231, a125\n\t"
    "v_accvgpr_read_b32 v232, a126\n\t"
    "v_accvgpr_read_b32 v233, a127\n\t"
    "v_mul_f32 v230, v230, v226\n\t"
    "v_mul_f32 v231, v231, v226\n\t"
    "v_mul_f32 v232, v232, v226\n\t"
    "v_mul_f32 v233, v233, v226\n\t"
    "v_accvgpr_write_b32 a124, v230\n\t"
    "v_accvgpr_write_b32 a125, v231\n\t"
    "v_accvgpr_write_b32 a126, v232\n\t"
    "v_accvgpr_write_b32 a127, v233\n\t"
    "v_accvgpr_read_b32 v230, a128\n\t"
    "v_accvgpr_read_b32 v231, a129\n\t"
    "v_accvgpr_read_b32 v232, a130\n\t"
    "v_accvgpr_read_b32 v233, a131\n\t"
    "v_mul_f32 v230, v230, v227\n\t"
    "v_mul_f32 v231, v231, v227\n\t"
    "v_mul_f32 v232, v232, v227\n\t"
    "v_mul_f32 v233, v233, v227\n\t"
    "v_accvgpr_write_b32 a128, v230\n\t"
    "v_accvgpr_write_b32 a129, v231\n\t"
    "v_accvgpr_write_b32 a130, v232\n\t"
    "v_accvgpr_write_b32 a131, v233\n\t"
    "v_accvgpr_read_b32 v230, a132\n\t"
    "v_accvgpr_read_b32 v231, a133\n\t"
    "v_accvgpr_read_b32 v232, a134\n\t"
    "v_accvgpr_read_b32 v233, a135\n\t"
    "v_mul_f32 v230, v230, v228\n\t"
    "v_mul_f32 v231, v231, v228\n\t"
    "v_mul_f32 v232, v232, v228\n\t"
    "v_mul_f32 v233, v233, v228\n\t"
    "v_accvgpr_write_b32 a132, v230\n\t"
    "v_accvgpr_write_b32 a133, v231\n\t"
    "v_accvgpr_write_b32 a134, v232\n\t"
    "v_accvgpr_write_b32 a135, v233\n\t"
    "v_accvgpr_read_b32 v230, a136\n\t"
    "v_accvgpr_read_b32 v231, a137\n\t"
    "v_accvgpr_read_b32 v232, a138\n\t"
    "v_accvgpr_read_b32 v233, a139\n\t"
    "v_mul_f32 v230, v230, v229\n\t"
    "v_mul_f32 v231, v231, v229\n\t"
    "v_mul_f32 v232, v232, v229\n\t"
    "v_mul_f32 v233, v233, v229\n\t"
    "v_accvgpr_write_b32 a136, v230\n\t"
    "v_accvgpr_write_b32 a137, v231\n\t"
    "v_accvgpr_write_b32 a138, v232\n\t"
    "v_accvgpr_write_b32 a139, v233\n\t"
    "v_accvgpr_read_b32 v230, a140\n\t"
    "v_accvgpr_read_b32 v231, a141\n\t"
    "v_accvgpr_read_b32 v232, a142\n\t"
    "v_accvgpr_read_b32 v233, a143\n\t"
    "v_mul_f32 v230, v230, v225\n\t"
    "v_mul_f32 v231, v231, v225\n\t"
    "v_mul_f32 v232, v232, v225\n\t"
    "v_mul_f32 v233, v233, v225\n\t"
    "v_accvgpr_write_b32 a140, v230\n\t"
    "v_accvgpr_write_b32 a141, v231\n\t"
    "v_accvgpr_write_b32 a142, v232\n\t"
    "v_accvgpr_write_b32 a143, v233\n\t"
    "v_accvgpr_read_b32 v230, a144\n\t"
    "v_accvgpr_read_b32 v231, a145\n\t"
    "v_accvgpr_read_b32 v232, a146\n\t"
    "v_accvgpr_read_b32 v233, a147\n\t"
    "v_mul_f32 v230, v230, v226\n\t"
    "v_mul_f32 v231, v231, v226\n\t"
    "v_mul_f32 v232, v232, v226\n\t"
    "v_mul_f32 v233, v233, v226\n\t"
    "v_accvgpr_write_b32 a144, v230\n\t"
    "v_accvgpr_write_b32 a145, v231\n\t"
    "v_accvgpr_write_b32 a146, v232\n\t"
    "v_accvgpr_write_b32 a147, v233\n\t"
    "v_accvgpr_read_b32 v230, a148\n\t"
    "v_accvgpr_read_b32 v231, a149\n\t"
    "v_accvgpr_read_b32 v232, a150\n\t"
    "v_accvgpr_read_b32 v233, a151\n\t"
    "v_mul_f32 v230, v230, v227\n\t"
    "v_mul_f32 v231, v231, v227\n\t"
    "v_mul_f32 v232, v232, v227\n\t"
    "v_mul_f32 v233, v233, v227\n\t"
    "v_accvgpr_write_b32 a148, v230\n\t"
    "v_accvgpr_write_b32 a149, v231\n\t"
    "v_accvgpr_write_b32 a150, v232\n\t"
    "v_accvgpr_write_b32 a151, v233\n\t"
    "v_accvgpr_read_b32 v230, a152\n\t"
    "v_accvgpr_read_b32 v231, a153\n\t"
    "v_accvgpr_read_b32 v232, a154\n\t"
    "v_accvgpr_read_b32 v233, a155\n\t"
    "v_mul_f32 v230, v230, v228\n\t"
    "v_mul_f32 v231, v231, v228\n\t"
    "v_mul_f32 v232, v232, v228\n\t"
    "v_mul_f32 v233, v233, v228\n\t"
    "v_accvgpr_write_b32 a152, v230\n\t"
    "v_accvgpr_write_b32 a153, v231\n\t"
    "v_accvgpr_write_b32 a154, v232\n\t"
    "v_accvgpr_write_b32 a155, v233\n\t"
    "v_accvgpr_read_b32 v230, a156\n\t"
    "v_accvgpr_read_b32 v231, a157\n\t"
    "v_accvgpr_read_b32 v232, a158\n\t"
    "v_accvgpr_read_b32 v233, a159\n\t"
    "v_mul_f32 v230, v230, v229\n\t"
    "v_mul_f32 v231, v231, v229\n\t"
    "v_mul_f32 v232, v232, v229\n\t"
    "v_mul_f32 v233, v233, v229\n\t"
    "v_accvgpr_write_b32 a156, v230\n\t"
    "v_accvgpr_write_b32 a157, v231\n\t"
    "v_accvgpr_write_b32 a158, v232\n\t"
    "v_accvgpr_write_b32 a159, v233\n\t"
    "v_accvgpr_read_b32 v230, a160\n\t"
    "v_accvgpr_read_b32 v231, a161\n\t"
    "v_accvgpr_read_b32 v232, a162\n\t"
    "v_accvgpr_read_b32 v233, a163\n\t"
    "v_mul_f32 v230, v230, v225\n\t"
    "v_mul_f32 v231, v231, v225\n\t"
    "v_mul_f32 v232, v232, v225\n\t"
    "v_mul_f32 v233, v233, v225\n\t"
    "v_accvgpr_write_b32 a160, v230\n\t"
    "v_accvgpr_write_b32 a161, v231\n\t"
    "v_accvgpr_write_b32 a162, v232\n\t"
    "v_accvgpr_write_b32 a163, v233\n\t"
    "v_accvgpr_read_b32 v230, a164\n\t"
    "v_accvgpr_read_b32 v231, a165\n\t"
    "v_accvgpr_read_b32 v232, a166\n\t"
    "v_accvgpr_read_b32 v233, a167\n\t"
    "v_mul_f32 v230, v230, v226\n\t"
    "v_mul_f32 v231, v231, v226\n\t"
    "v_mul_f32 v232, v232, v226\n\t"
    "v_mul_f32 v233, v233, v226\n\t"
    "v_accvgpr_write_b32 a164, v230\n\t"
    "v_accvgpr_write_b32 a165, v231\n\t"
    "v_accvgpr_write_b32 a166, v232\n\t"
    "v_accvgpr_write_b32 a167, v233\n\t"
    "v_accvgpr_read_b32 v230, a168\n\t"
    "v_accvgpr_read_b32 v231, a169\n\t"
    "v_accvgpr_read_b32 v232, a170\n\t"
    "v_accvgpr_read_b32 v233, a171\n\t"
    "v_mul_f32 v230, v230, v227\n\t"
    "v_mul_f32 v231, v231, v227\n\t"
    "v_mul_f32 v232, v232, v227\n\t"
    "v_mul_f32 v233, v233, v227\n\t"
    "v_accvgpr_write_b32 a168, v230\n\t"
    "v_accvgpr_write_b32 a169, v231\n\t"
    "v_accvgpr_write_b32 a170, v232\n\t"
    "v_accvgpr_write_b32 a171, v233\n\t"
    "v_accvgpr_read_b32 v230, a172\n\t"
    "v_accvgpr_read_b32 v231, a173\n\t"
    "v_accvgpr_read_b32 v232, a174\n\t"
    "v_accvgpr_read_b32 v233, a175\n\t"
    "v_mul_f32 v230, v230, v228\n\t"
    "v_mul_f32 v231, v231, v228\n\t"
    "v_mul_f32 v232, v232, v228\n\t"
    "v_mul_f32 v233, v233, v228\n\t"
    "v_accvgpr_write_b32 a172, v230\n\t"
    "v_accvgpr_write_b32 a173, v231\n\t"
    "v_accvgpr_write_b32 a174, v232\n\t"
    "v_accvgpr_write_b32 a175, v233\n\t"
    "v_accvgpr_read_b32 v230, a176\n\t"
    "v_accvgpr_read_b32 v231, a177\n\t"
    "v_accvgpr_read_b32 v232, a178\n\t"
    "v_accvgpr_read_b32 v233, a179\n\t"
    "v_mul_f32 v230, v230, v229\n\t"
    "v_mul_f32 v231, v231, v229\n\t"
    "v_mul_f32 v232, v232, v229\n\t"
    "v_mul_f32 v233, v233, v229\n\t"
    "v_accvgpr_write_b32 a176, v230\n\t"
    "v_accvgpr_write_b32 a177, v231\n\t"
    "v_accvgpr_write_b32 a178, v232\n\t"
    "v_accvgpr_write_b32 a179, v233\n\t"
    "v_accvgpr_read_b32 v230, a180\n\t"
    "v_accvgpr_read_b32 v231, a181\n\t"
    "v_accvgpr_read_b32 v232, a182\n\t"
    "v_accvgpr_read_b32 v233, a183\n\t"
    "v_mul_f32 v230, v230, v225\n\t"
    "v_mul_f32 v231, v231, v225\n\t"
    "v_mul_f32 v232, v232, v225\n\t"
    "v_mul_f32 v233, v233, v225\n\t"
    "v_accvgpr_write_b32 a180, v230\n\t"
    "v_accvgpr_write_b32 a181, v231\n\t"
    "v_accvgpr_write_b32 a182, v232\n\t"
    "v_accvgpr_write_b32 a183, v233\n\t"
    "v_accvgpr_read_b32 v230, a184\n\t"
    "v_accvgpr_read_b32 v231, a185\n\t"
    "v_accvgpr_read_b32 v232, a186\n\t"
    "v_accvgpr_read_b32 v233, a187\n\t"
    "v_mul_f32 v230, v230, v226\n\t"
    "v_mul_f32 v231, v231, v226\n\t"
    "v_mul_f32 v232, v232, v226\n\t"
    "v_mul_f32 v233, v233, v226\n\t"
    "v_accvgpr_write_b32 a184, v230\n\t"
    "v_accvgpr_write_b32 a185, v231\n\t"
    "v_accvgpr_write_b32 a186, v232\n\t"
    "v_accvgpr_write_b32 a187, v233\n\t"
    "v_accvgpr_read_b32 v230, a188\n\t"
    "v_accvgpr_read_b32 v231, a189\n\t"
    "v_accvgpr_read_b32 v232, a190\n\t"
    "v_accvgpr_read_b32 v233, a191\n\t"
    "v_mul_f32 v230, v230, v227\n\t"
    "v_mul_f32 v231, v231, v227\n\t"
    "v_mul_f32 v232, v232, v227\n\t"
    "v_mul_f32 v233, v233, v227\n\t"
    "v_accvgpr_write_b32 a188, v230\n\t"
    "v_accvgpr_write_b32 a189, v231\n\t"
    "v_accvgpr_write_b32 a190, v232\n\t"
    "v_accvgpr_write_b32 a191, v233\n\t"
    "v_accvgpr_read_b32 v230, a192\n\t"
    "v_accvgpr_read_b32 v231, a193\n\t"
    "v_accvgpr_read_b32 v232, a194\n\t"
    "v_accvgpr_read_b32 v233, a195\n\t"
    "v_mul_f32 v230, v230, v228\n\t"
    "v_mul_f32 v231, v231, v228\n\t"
    "v_mul_f32 v232, v232, v228\n\t"
    "v_mul_f32 v233, v233, v228\n\t"
    "v_accvgpr_write_b32 a192, v230\n\t"
    "v_accvgpr_write_b32 a193, v231\n\t"
    "v_accvgpr_write_b32 a194, v232\n\t"
    "v_accvgpr_write_b32 a195, v233\n\t"
    "v_accvgpr_read_b32 v230, a196\n\t"
    "v_accvgpr_read_b32 v231, a197\n\t"
    "v_accvgpr_read_b32 v232, a198\n\t"
    "v_accvgpr_read_b32 v233, a199\n\t"
    "v_mul_f32 v230, v230, v229\n\t"
    "v_mul_f32 v231, v231, v229\n\t"
    "v_mul_f32 v232, v232, v229\n\t"
    "v_mul_f32 v233, v233, v229\n\t"
    "v_accvgpr_write_b32 a196, v230\n\t"
    "v_accvgpr_write_b32 a197, v231\n\t"
    "v_accvgpr_write_b32 a198, v232\n\t"
    "v_accvgpr_write_b32 a199, v233\n\t"
    "v_accvgpr_read_b32 v230, a200\n\t"
    "v_accvgpr_read_b32 v231, a201\n\t"
    "v_accvgpr_read_b32 v232, a202\n\t"
    "v_accvgpr_read_b32 v233, a203\n\t"
    "v_mul_f32 v230, v230, v225\n\t"
    "v_mul_f32 v231, v231, v225\n\t"
    "v_mul_f32 v232, v232, v225\n\t"
    "v_mul_f32 v233, v233, v225\n\t"
    "v_accvgpr_write_b32 a200, v230\n\t"
    "v_accvgpr_write_b32 a201, v231\n\t"
    "v_accvgpr_write_b32 a202, v232\n\t"
    "v_accvgpr_write_b32 a203, v233\n\t"
    "v_accvgpr_read_b32 v230, a204\n\t"
    "v_accvgpr_read_b32 v231, a205\n\t"
    "v_accvgpr_read_b32 v232, a206\n\t"
    "v_accvgpr_read_b32 v233, a207\n\t"
    "v_mul_f32 v230, v230, v226\n\t"
    "v_mul_f32 v231, v231, v226\n\t"
    "v_mul_f32 v232, v232, v226\n\t"
    "v_mul_f32 v233, v233, v226\n\t"
    "v_accvgpr_write_b32 a204, v230\n\t"
    "v_accvgpr_write_b32 a205, v231\n\t"
    "v_accvgpr_write_b32 a206, v232\n\t"
    "v_accvgpr_write_b32 a207, v233\n\t"
    "v_accvgpr_read_b32 v230, a208\n\t"
    "v_accvgpr_read_b32 v231, a209\n\t"
    "v_accvgpr_read_b32 v232, a210\n\t"
    "v_accvgpr_read_b32 v233, a211\n\t"
    "v_mul_f32 v230, v230, v227\n\t"
    "v_mul_f32 v231, v231, v227\n\t"
    "v_mul_f32 v232, v232, v227\n\t"
    "v_mul_f32 v233, v233, v227\n\t"
    "v_accvgpr_write_b32 a208, v230\n\t"
    "v_accvgpr_write_b32 a209, v231\n\t"
    "v_accvgpr_write_b32 a210, v232\n\t"
    "v_accvgpr_write_b32 a211, v233\n\t"
    "v_accvgpr_read_b32 v230, a212\n\t"
    "v_accvgpr_read_b32 v231, a213\n\t"
    "v_accvgpr_read_b32 v232, a214\n\t"
    "v_accvgpr_read_b32 v233, a215\n\t"
    "v_mul_f32 v230, v230, v228\n\t"
    "v_mul_f32 v231, v231, v228\n\t"
    "v_mul_f32 v232, v232, v228\n\t"
    "v_mul_f32 v233, v233, v228\n\t"
    "v_accvgpr_write_b32 a212, v230\n\t"
    "v_accvgpr_write_b32 a213, v231\n\t"
    "v_accvgpr_write_b32 a214, v232\n\t"
    "v_accvgpr_write_b32 a215, v233\n\t"
    "v_accvgpr_read_b32 v230, a216\n\t"
    "v_accvgpr_read_b32 v231, a217\n\t"
    "v_accvgpr_read_b32 v232, a218\n\t"
    "v_accvgpr_read_b32 v233, a219\n\t"
    "v_mul_f32 v230, v230, v229\n\t"
    "v_mul_f32 v231, v231, v229\n\t"
    "v_mul_f32 v232, v232, v229\n\t"
    "v_mul_f32 v233, v233, v229\n\t"
    "v_accvgpr_write_b32 a216, v230\n\t"
    "v_accvgpr_write_b32 a217, v231\n\t"
    "v_accvgpr_write_b32 a218, v232\n\t"
    "v_accvgpr_write_b32 a219, v233\n\t"
    "v_accvgpr_read_b32 v230, a220\n\t"
    "v_accvgpr_read_b32 v231, a221\n\t"
    "v_accvgpr_read_b32 v232, a222\n\t"
    "v_accvgpr_read_b32 v233, a223\n\t"
    "v_mul_f32 v230, v230, v225\n\t"
    "v_mul_f32 v231, v231, v225\n\t"
    "v_mul_f32 v232, v232, v225\n\t"
    "v_mul_f32 v233, v233, v225\n\t"
    "v_accvgpr_write_b32 a220, v230\n\t"
    "v_accvgpr_write_b32 a221, v231\n\t"
    "v_accvgpr_write_b32 a222, v232\n\t"
    "v_accvgpr_write_b32 a223, v233\n\t"
    "v_accvgpr_read_b32 v230, a224\n\t"
    "v_accvgpr_read_b32 v231, a225\n\t"
    "v_accvgpr_read_b32 v232, a226\n\t"
    "v_accvgpr_read_b32 v233, a227\n\t"
    "v_mul_f32 v230, v230, v226\n\t"
    "v_mul_f32 v231, v231, v226\n\t"
    "v_mul_f32 v232, v232, v226\n\t"
    "v_mul_f32 v233, v233, v226\n\t"
    "v_accvgpr_write_b32 a224, v230\n\t"
    "v_accvgpr_write_b32 a225, v231\n\t"
    "v_accvgpr_write_b32 a226, v232\n\t"
    "v_accvgpr_write_b32 a227, v233\n\t"
    "v_accvgpr_read_b32 v230, a228\n\t"
    "v_accvgpr_read_b32 v231, a229\n\t"
    "v_accvgpr_read_b32 v232, a230\n\t"
    "v_accvgpr_read_b32 v233, a231\n\t"
    "v_mul_f32 v230, v230, v227\n\t"
    "v_mul_f32 v231, v231, v227\n\t"
    "v_mul_f32 v232, v232, v227\n\t"
    "v_mul_f32 v233, v233, v227\n\t"
    "v_accvgpr_write_b32 a228, v230\n\t"
    "v_accvgpr_write_b32 a229, v231\n\t"
    "v_accvgpr_write_b32 a230, v232\n\t"
    "v_accvgpr_write_b32 a231, v233\n\t"
    "v_accvgpr_read_b32 v230, a232\n\t"
    "v_accvgpr_read_b32 v231, a233\n\t"
    "v_accvgpr_read_b32 v232, a234\n\t"
    "v_accvgpr_read_b32 v233, a235\n\t"
    "v_mul_f32 v230, v230, v228\n\t"
    "v_mul_f32 v231, v231, v228\n\t"
    "v_mul_f32 v232, v232, v228\n\t"
    "v_mul_f32 v233, v233, v228\n\t"
    "v_accvgpr_write_b32 a232, v230\n\t"
    "v_accvgpr_write_b32 a233, v231\n\t"
    "v_accvgpr_write_b32 a234, v232\n\t"
    "v_accvgpr_write_b32 a235, v233\n\t"
    "v_accvgpr_read_b32 v230, a236\n\t"
    "v_accvgpr_read_b32 v231, a237\n\t"
    "v_accvgpr_read_b32 v232, a238\n\t"
    "v_accvgpr_read_b32 v233, a239\n\t"
    "v_mul_f32 v230, v230, v229\n\t"
    "v_mul_f32 v231, v231, v229\n\t"
    "v_mul_f32 v232, v232, v229\n\t"
    "v_mul_f32 v233, v233, v229\n\t"
    "v_accvgpr_write_b32 a236, v230\n\t"
    "v_accvgpr_write_b32 a237, v231\n\t"
    "v_accvgpr_write_b32 a238, v232\n\t"
    "v_accvgpr_write_b32 a239, v233\n\t"
    "v_exp_f32 v230, v104\n\t"
    "v_exp_f32 v231, v105\n\t"
    "v_exp_f32 v232, v106\n\t"
    "v_exp_f32 v233, v107\n\t"
    "v_add_f32 v225, v230, v231\n\t"
    "v_cvt_pk_bf16_f32 v164, v230, v231\n\t"
    "v_add_f32 v225, v232, v225\n\t"
    "v_add_f32 v225, v233, v225\n\t"
    "v_cvt_pk_bf16_f32 v165, v232, v233\n\t"
    "v_exp_f32 v230, v124\n\t"
    "v_exp_f32 v231, v125\n\t"
    "v_exp_f32 v232, v126\n\t"
    "v_exp_f32 v233, v127\n\t"
    "v_add_f32 v225, v230, v225\n\t"
    "v_add_f32 v225, v231, v225\n\t"
    "v_cvt_pk_bf16_f32 v166, v230, v231\n\t"
    "v_add_f32 v225, v232, v225\n\t"
    "v_add_f32 v225, v233, v225\n\t"
    "v_cvt_pk_bf16_f32 v167, v232, v233\n\t"
    "v_exp_f32 v230, v108\n\t"
    "v_exp_f32 v231, v109\n\t"
    "v_exp_f32 v232, v110\n\t"
    "v_exp_f32 v233, v111\n\t"
    "v_add_f32 v226, v230, v231\n\t"
    "v_cvt_pk_bf16_f32 v168, v230, v231\n\t"
    "v_add_f32 v226, v232, v226\n\t"
    "v_add_f32 v226, v233, v226\n\t"
    "v_cvt_pk_bf16_f32 v169, v232, v233\n\t"
    "v_exp_f32 v230, v128\n\t"
    "v_exp_f32 v231, v129\n\t"
    "v_exp_f32 v232, v130\n\t"
    "v_exp_f32 v233, v131\n\t"
    "v_add_f32 v226, v230, v226\n\t"
    "v_add_f32 v226, v231, v226\n\t"
    "v_cvt_pk_bf16_f32 v170, v230, v231\n\t"
    "v_add_f32 v226, v232, v226\n\t"
    "v_add_f32 v226, v233, v226\n\t"
    "v_cvt_pk_bf16_f32 v171, v232, v233\n\t"
    "v_exp_f32 v230, v112\n\t"
    "v_exp_f32 v231, v113\n\t"
    "v_exp_f32 v232, v114\n\t"
    "v_exp_f32 v233, v115\n\t"
    "v_add_f32 v227, v230, v231\n\t"
    "v_cvt_pk_bf16_f32 v172, v230, v231\n\t"
    "v_add_f32 v227, v232, v227\n\t"
    "v_add_f32 v227, v233, v227\n\t"
    "v_cvt_pk_bf16_f32 v173, v232, v233\n\t"
    "v_exp_f32 v230, v132\n\t"
    "v_exp_f32 v231, v133\n\t"
    "v_exp_f32 v232, v134\n\t"
    "v_exp_f32 v233, v135\n\t"
    "v_add_f32 v227, v230, v227\n\t"
    "v_add_f32 v227, v231, v227\n\t"
    "v_cvt_pk_bf16_f32 v174, v230, v231\n\t"
    "v_add_f32 v227, v232, v227\n\t"
    "v_add_f32 v227, v233, v227\n\t"
    "v_cvt_pk_bf16_f32 v175, v232, v233\n\t"
    "v_exp_f32 v230, v116\n\t"
    "v_exp_f32 v231, v117\n\t"
    "v_exp_f32 v232, v118\n\t"
    "v_exp_f32 v233, v119\n\t"
    "v_add_f32 v228, v230, v231\n\t"
    "v_cvt_pk_bf16_f32 v176, v230, v231\n\t"
    "v_add_f32 v228, v232, v228\n\t"
    "v_add_f32 v228, v233, v228\n\t"
    "v_cvt_pk_bf16_f32 v177, v232, v233\n\t"
    "v_exp_f32 v230, v136\n\t"
    "v_exp_f32 v231, v137\n\t"
    "v_exp_f32 v232, v138\n\t"
    "v_exp_f32 v233, v139\n\t"
    "v_add_f32 v228, v230, v228\n\t"
    "v_add_f32 v228, v231, v228\n\t"
    "v_cvt_pk_bf16_f32 v178, v230, v231\n\t"
    "v_add_f32 v228, v232, v228\n\t"
    "v_add_f32 v228, v233, v228\n\t"
    "v_cvt_pk_bf16_f32 v179, v232, v233\n\t"
    "v_exp_f32 v230, v120\n\t"
    "v_exp_f32 v231, v121\n\t"
    "v_exp_f32 v232, v122\n\t"
    "v_exp_f32 v233, v123\n\t"
    "v_add_f32 v229, v230, v231\n\t"
    "v_cvt_pk_bf16_f32 v180, v230, v231\n\t"
    "v_add_f32 v229, v232, v229\n\t"
    "v_add_f32 v229, v233, v229\n\t"
    "v_cvt_pk_bf16_f32 v181, v232, v233\n\t"
    "v_exp_f32 v230, v140\n\t"
    "v_exp_f32 v231, v141\n\t"
    "v_exp_f32 v232, v142\n\t"
    "v_exp_f32 v233, v143\n\t"
    "v_add_f32 v229, v230, v229\n\t"
    "v_add_f32 v229, v231, v229\n\t"
    "v_cvt_pk_bf16_f32 v182, v230, v231\n\t"
    "v_add_f32 v229, v232, v229\n\t"
    "v_add_f32 v229, v233, v229\n\t"
    "v_cvt_pk_bf16_f32 v183, v232, v233\n\t"
    "s_nop 1\n\t"
    "s_nop 1\n\t"
    "s_setpc_b64 s[52:53]\n\t"
    ".Lk1_slow1:\n\t"
    "s_nop 7\n\t"
    "s_nop 7\n\t"
    "v_max3_f32 v225, v104, v105, v106\n\t"
    "v_max3_f32 v225, v225, v107, v124\n\t"
    "v_max3_f32 v225, v225, v125, v126\n\t"
    "v_max_f32 v225, v225, v127\n\t"
    "v_max3_f32 v226, v108, v109, v110\n\t"
    "v_max3_f32 v226, v226, v111, v128\n\t"
    "v_max3_f32 v226, v226, v129, v130\n\t"
    "v_max_f32 v226, v226, v131\n\t"
    "v_max3_f32 v227, v112, v113, v114\n\t"
    "v_max3_f32 v227, v227, v115, v132\n\t"
    "v_max3_f32 v227, v227, v133, v134\n\t"
    "v_max_f32 v227, v227, v135\n\t"
    "v_max3_f32 v228, v116, v117, v118\n\t"
    "v_max3_f32 v228, v228, v119, v136\n\t"
    "v_max3_f32 v228, v228, v137, v138\n\t"
    "v_max_f32 v228, v228, v139\n\t"
    "v_max3_f32 v229, v120, v121, v122\n\t"
    "v_max3_f32 v229, v229, v123, v140\n\t"
    "v_max3_f32 v229, v229, v141, v142\n\t"
    "v_max_f32 v229, v229, v143\n\t"
    "v_mov_b32 v230, v225\n\t"
    "v_mov_b32 v231, v226\n\t"
    "v_mov_b32 v232, v227\n\t"
    "v_mov_b32 v233, v228\n\t"
    "v_mov_b32 v234, v229\n\t"
    "s_nop 0\n\t"
    "v_permlane32_swap_b32 v225, v230\n\t"
    "v_permlane32_swap_b32 v226, v231\n\t"
    "v_permlane32_swap_b32 v227, v232\n\t"
    "v_permlane32_swap_b32 v228, v233\n\t"
    "v_permlane32_swap_b32 v229, v234\n\t"
    "v_max_f32 v225, v225, v230\n\t"
    "v_max_f32 v226, v226, v231\n\t"
    "v_max_f32 v227, v227, v232\n\t"
    "v_max_f32 v228, v228, v233\n\t"
    "v_max_f32 v229, v229, v234\n\t"
    "v_mov_b32 v230, v225\n\t"
    "v_mov_b32 v231, v226\n\t"
    "v_mov_b32 v232, v227\n\t"
    "v_mov_b32 v233, v228\n\t"
    "v_mov_b32 v234, v229\n\t"
    "s_nop 0\n\t"
    "v_permlane16_swap_b32 v225, v230\n\t"
    "v_permlane16_swap_b32 v226, v231\n\t"
    "v_permlane16_swap_b32 v227, v232\n\t"
    "v_permlane16_swap_b32 v228, v233\n\t"
    "v_permlane16_swap_b32 v229, v234\n\t"
    "v_max_f32 v225, v225, v230\n\t"
    "v_max_f32 v226, v226, v231\n\t"
    "v_max_f32 v227, v227, v232\n\t"
    "v_max_f32 v228, v228, v233\n\t"
    "v_max_f32 v229, v229, v234\n\t"
    "v_max_f32 v225, 0, v225\n\t"
    "v_max_f32 v226, 0, v226\n\t"
    "v_max_f32 v227, 0, v227\n\t"
    "v_max_f32 v228, 0, v228\n\t"
    "v_max_f32 v229, 0, v229\n\t"
    "v_sub_f32 v144, v144, v225\n\t"
    "v_sub_f32 v145, v145, v225\n\t"
    "v_sub_f32 v146, v146, v225\n\t"
    "v_sub_f32 v147, v147, v225\n\t"
    "v_sub_f32 v104, v104, v225\n\t"
    "v_sub_f32 v105, v105, v225\n\t"
    "v_sub_f32 v106, v106, v225\n\t"
    "v_sub_f32 v107, v107, v225\n\t"
    "v_sub_f32 v124, v124, v225\n\t"
    "v_sub_f32 v125, v125, v225\n\t"
    "v_sub_f32 v126, v126, v225\n\t"
    "v_sub_f32 v127, v127, v225\n\t"
    "v_sub_f32 v148, v148, v226\n\t"
    "v_sub_f32 v149, v149, v226\n\t"
    "v_sub_f32 v150, v150, v226\n\t"
    "v_sub_f32 v151, v151, v226\n\t"
    "v_sub_f32 v108, v108, v226\n\t"
    "v_sub_f32 v109, v109, v226\n\t"
    "v_sub_f32 v110, v110, v226\n\t"
    "v_sub_f32 v111, v111, v226\n\t"
    "v_sub_f32 v128, v128, v226\n\t"
    "v_sub_f32 v129, v129, v226\n\t"
    "v_sub_f32 v130, v130, v226\n\t"
    "v_sub_f32 v131, v131, v226\n\t"
    "v_sub_f32 v152, v152, v227\n\t"
    "v_sub_f32 v153, v153, v227\n\t"
    "v_sub_f32 v154, v154, v227\n\t"
    "v_sub_f32 v155, v155, v227\n\t"
    "v_sub_f32 v112, v112, v227\n\t"
    "v_sub_f32 v113, v113, v227\n\t"
    "v_sub_f32 v114, v114, v227\n\t"
    "v_sub_f32 v115, v115, v227\n\t"
    "v_sub_f32 v132, v132, v227\n\t"
    "v_sub_f32 v133, v133, v227\n\t"
    "v_sub_f32 v134, v134, v227\n\t"
    "v_sub_f32 v135, v135, v227\n\t"
    "v_sub_f32 v156, v156, v228\n\t"
    "v_sub_f32 v157, v157, v228\n\t"
    "v_sub_f32 v158, v158, v228\n\t"
    "v_sub_f32 v159, v159, v228\n\t"
    "v_sub_f32 v116, v116, v228\n\t"
    "v_sub_f32 v117, v117, v228\n\t"
    "v_sub_f32 v118, v118, v228\n\t"
    "v_sub_f32 v119, v119, v228\n\t"
    "v_sub_f32 v136, v136, v228\n\t"
    "v_sub_f32 v137, v137, v228\n\t"
    "v_sub_f32 v138, v138, v228\n\t"
    "v_sub_f32 v139, v139, v228\n\t"
    "v_sub_f32 v160, v160, v229\n\t"
    "v_sub_f32 v161, v161, v229\n\t"
    "v_sub_f32 v162, v162, v229\n\t"
    "v_sub_f32 v163, v163, v229\n\t"
    "v_sub_f32 v120, v120, v229\n\t"
    "v_sub_f32 v121, v121, v229\n\t"
    "v_sub_f32 v122, v122, v229\n\t"
    "v_sub_f32 v123, v123, v229\n\t"
    "v_sub_f32 v140, v140, v229\n\t"
    "v_sub_f32 v141, v141, v229\n\t"
    "v_sub_f32 v142, v142, v229\n\t"
    "v_sub_f32 v143, v143, v229\n\t"
    "v_exp_f32 v225, -v225\n\t"
    "v_exp_f32 v226, -v226\n\t"
    "v_exp_f32 v227, -v227\n\t"
    "v_exp_f32 v228, -v228\n\t"
    "v_exp_f32 v229, -v229\n\t"
    "s_nop 0\n\t"
    "v_mul_f32 v220, v220, v225\n\t"
    "v_mul_f32 v221, v221, v226\n\t"
    "v_mul_f32 v222, v222, v227\n\t"
    "v_mul_f32 v223, v223, v228\n\t"
    "v_mul_f32 v224, v224, v229\n\t"
    "v_accvgpr_read_b32 v230, a0\n\t"
    "v_accvgpr_read_b32 v231, a1\n\t"
    "v_accvgpr_read_b32 v232, a2\n\t"
    "v_accvgpr_read_b32 v233, a3\n\t"
    "v_mul_f32 v230, v230, v225\n\t"
    "v_mul_f32 v231, v231, v225\n\t"
    "v_mul_f32 v232, v232, v225\n\t"
    "v_mul_f32 v233, v233, v225\n\t"
    "v_accvgpr_write_b32 a0, v230\n\t"
    "v_accvgpr_write_b32 a1, v231\n\t"
    "v_accvgpr_write_b32 a2, v232\n\t"
    "v_accvgpr_write_b32 a3, v233\n\t"
    "v_accvgpr_read_b32 v230, a4\n\t"
    "v_accvgpr_read_b32 v231, a5\n\t"
    "v_accvgpr_read_b32 v232, a6\n\t"
    "v_accvgpr_read_b32 v233, a7\n\t"
    "v_mul_f32 v230, v230, v226\n\t"
    "v_mul_f32 v231, v231, v226\n\t"
    "v_mul_f32 v232, v232, v226\n\t"
    "v_mul_f32 v233, v233, v226\n\t"
    "v_accvgpr_write_b32 a4, v230\n\t"
    "v_accvgpr_write_b32 a5, v231\n\t"
    "v_accvgpr_write_b32 a6, v232\n\t"
    "v_accvgpr_write_b32 a7, v233\n\t"
    "v_accvgpr_read_b32 v230, a8\n\t"
    "v_accvgpr_read_b32 v231, a9\n\t"
    "v_accvgpr_read_b32 v232, a10\n\t"
    "v_accvgpr_read_b32 v233, a11\n\t"
    "v_mul_f32 v230, v230, v227\n\t"
    "v_mul_f32 v231, v231, v227\n\t"
    "v_mul_f32 v232, v232, v227\n\t"
    "v_mul_f32 v233, v233, v227\n\t"
    "v_accvgpr_write_b32 a8, v230\n\t"
    "v_accvgpr_write_b32 a9, v231\n\t"
    "v_accvgpr_write_b32 a10, v232\n\t"
    "v_accvgpr_write_b32 a11, v233\n\t"
    "v_accvgpr_read_b32 v230, a12\n\t"
    "v_accvgpr_read_b32 v231, a13\n\t"
    "v_accvgpr_read_b32 v232, a14\n\t"
    "v_accvgpr_read_b32 v233, a15\n\t"
    "v_mul_f32 v230, v230, v228\n\t"
    "v_mul_f32 v231, v231, v228\n\t"
    "v_mul_f32 v232, v232, v228\n\t"
    "v_mul_f32 v233, v233, v228\n\t"
    "v_accvgpr_write_b32 a12, v230\n\t"
    "v_accvgpr_write_b32 a13, v231\n\t"
    "v_accvgpr_write_b32 a14, v232\n\t"
    "v_accvgpr_write_b32 a15, v233\n\t"
    "v_accvgpr_read_b32 v230, a16\n\t"
    "v_accvgpr_read_b32 v231, a17\n\t"
    "v_accvgpr_read_b32 v232, a18\n\t"
    "v_accvgpr_read_b32 v233, a19\n\t"
    "v_mul_f32 v230, v230, v229\n\t"
    "v_mul_f32 v231, v231, v229\n\t"
    "v_mul_f32 v232, v232, v229\n\t"
    "v_mul_f32 v233, v233, v229\n\t"
    "v_accvgpr_write_b32 a16, v230\n\t"
    "v_accvgpr_write_b32 a17, v231\n\t"
    "v_accvgpr_write_b32 a18, v232\n\t"
    "v_accvgpr_write_b32 a19, v233\n\t"
    "v_accvgpr_read_b32 v230, a20\n\t"
    "v_accvgpr_read_b32 v231, a21\n\t"
    "v_accvgpr_read_b32 v232, a22\n\t"
    "v_accvgpr_read_b32 v233, a23\n\t"
    "v_mul_f32 v230, v230, v225\n\t"
    "v_mul_f32 v231, v231, v225\n\t"
    "v_mul_f32 v232, v232, v225\n\t"
    "v_mul_f32 v233, v233, v225\n\t"
    "v_accvgpr_write_b32 a20, v230\n\t"
    "v_accvgpr_write_b32 a21, v231\n\t"
    "v_accvgpr_write_b32 a22, v232\n\t"
    "v_accvgpr_write_b32 a23, v233\n\t"
    "v_accvgpr_read_b32 v230, a24\n\t"
    "v_accvgpr_read_b32 v231, a25\n\t"
    "v_accvgpr_read_b32 v232, a26\n\t"
    "v_accvgpr_read_b32 v233, a27\n\t"
    "v_mul_f32 v230, v230, v226\n\t"
    "v_mul_f32 v231, v231, v226\n\t"
    "v_mul_f32 v232, v232, v226\n\t"
    "v_mul_f32 v233, v233, v226\n\t"
    "v_accvgpr_write_b32 a24, v230\n\t"
    "v_accvgpr_write_b32 a25, v231\n\t"
    "v_accvgpr_write_b32 a26, v232\n\t"
    "v_accvgpr_write_b32 a27, v233\n\t"
    "v_accvgpr_read_b32 v230, a28\n\t"
    "v_accvgpr_read_b32 v231, a29\n\t"
    "v_accvgpr_read_b32 v232, a30\n\t"
    "v_accvgpr_read_b32 v233, a31\n\t"
    "v_mul_f32 v230, v230, v227\n\t"
    "v_mul_f32 v231, v231, v227\n\t"
    "v_mul_f32 v232, v232, v227\n\t"
    "v_mul_f32 v233, v233, v227\n\t"
    "v_accvgpr_write_b32 a28, v230\n\t"
    "v_accvgpr_write_b32 a29, v231\n\t"
    "v_accvgpr_write_b32 a30, v232\n\t"
    "v_accvgpr_write_b32 a31, v233\n\t"
    "v_accvgpr_read_b32 v230, a32\n\t"
    "v_accvgpr_read_b32 v231, a33\n\t"
    "v_accvgpr_read_b32 v232, a34\n\t"
    "v_accvgpr_read_b32 v233, a35\n\t"
    "v_mul_f32 v230, v230, v228\n\t"
    "v_mul_f32 v231, v231, v228\n\t"
    "v_mul_f32 v232, v232, v228\n\t"
    "v_mul_f32 v233, v233, v228\n\t"
    "v_accvgpr_write_b32 a32, v230\n\t"
    "v_accvgpr_write_b32 a33, v231\n\t"
    "v_accvgpr_write_b32 a34, v232\n\t"
    "v_accvgpr_write_b32 a35, v233\n\t"
    "v_accvgpr_read_b32 v230, a36\n\t"
    "v_accvgpr_read_b32 v231, a37\n\t"
    "v_accvgpr_read_b32 v232, a38\n\t"
    "v_accvgpr_read_b32 v233, a39\n\t"
    "v_mul_f32 v230, v230, v229\n\t"
    "v_mul_f32 v231, v231, v229\n\t"
    "v_mul_f32 v232, v232, v229\n\t"
    "v_mul_f32 v233, v233, v229\n\t"
    "v_accvgpr_write_b32 a36, v230\n\t"
    "v_accvgpr_write_b32 a37, v231\n\t"
    "v_accvgpr_write_b32 a38, v232\n\t"
    "v_accvgpr_write_b32 a39, v233\n\t"
    "v_accvgpr_read_b32 v230, a40\n\t"
    "v_accvgpr_read_b32 v231, a41\n\t"
    "v_accvgpr_read_b32 v232, a42\n\t"
    "v_accvgpr_read_b32 v233, a43\n\t"
    "v_mul_f32 v230, v230, v225\n\t"
    "v_mul_f32 v231, v231, v225\n\t"
    "v_mul_f32 v232, v232, v225\n\t"
    "v_mul_f32 v233, v233, v225\n\t"
    "v_accvgpr_write_b32 a40, v230\n\t"
    "v_accvgpr_write_b32 a41, v231\n\t"
    "v_accvgpr_write_b32 a42, v232\n\t"
    "v_accvgpr_write_b32 a43, v233\n\t"
    "v_accvgpr_read_b32 v230, a44\n\t"
    "v_accvgpr_read_b32 v231, a45\n\t"
    "v_accvgpr_read_b32 v232, a46\n\t"
    "v_accvgpr_read_b32 v233, a47\n\t"
    "v_mul_f32 v230, v230, v226\n\t"
    "v_mul_f32 v231, v231, v226\n\t"
    "v_mul_f32 v232, v232, v226\n\t"
    "v_mul_f32 v233, v233, v226\n\t"
    "v_accvgpr_write_b32 a44, v230\n\t"
    "v_accvgpr_write_b32 a45, v231\n\t"
    "v_accvgpr_write_b32 a46, v232\n\t"
    "v_accvgpr_write_b32 a47, v233\n\t"
    "v_accvgpr_read_b32 v230, a48\n\t"
    "v_accvgpr_read_b32 v231, a49\n\t"
    "v_accvgpr_read_b32 v232, a50\n\t"
    "v_accvgpr_read_b32 v233, a51\n\t"
    "v_mul_f32 v230, v230, v227\n\t"
    "v_mul_f32 v231, v231, v227\n\t"
    "v_mul_f32 v232, v232, v227\n\t"
    "v_mul_f32 v233, v233, v227\n\t"
    "v_accvgpr_write_b32 a48, v230\n\t"
    "v_accvgpr_write_b32 a49, v231\n\t"
    "v_accvgpr_write_b32 a50, v232\n\t"
    "v_accvgpr_write_b32 a51, v233\n\t"
    "v_accvgpr_read_b32 v230, a52\n\t"
    "v_accvgpr_read_b32 v231, a53\n\t"
    "v_accvgpr_read_b32 v232, a54\n\t"
    "v_accvgpr_read_b32 v233, a55\n\t"
    "v_mul_f32 v230, v230, v228\n\t"
    "v_mul_f32 v231, v231, v228\n\t"
    "v_mul_f32 v232, v232, v228\n\t"
    "v_mul_f32 v233, v233, v228\n\t"
    "v_accvgpr_write_b32 a52, v230\n\t"
    "v_accvgpr_write_b32 a53, v231\n\t"
    "v_accvgpr_write_b32 a54, v232\n\t"
    "v_accvgpr_write_b32 a55, v233\n\t"
    "v_accvgpr_read_b32 v230, a56\n\t"
    "v_accvgpr_read_b32 v231, a57\n\t"
    "v_accvgpr_read_b32 v232, a58\n\t"
    "v_accvgpr_read_b32 v233, a59\n\t"
    "v_mul_f32 v230, v230, v229\n\t"
    "v_mul_f32 v231, v231, v229\n\t"
    "v_mul_f32 v232, v232, v229\n\t"
    "v_mul_f32 v233, v233, v229\n\t"
    "v_accvgpr_write_b32 a56, v230\n\t"
    "v_accvgpr_write_b32 a57, v231\n\t"
    "v_accvgpr_write_b32 a58, v232\n\t"
    "v_accvgpr_write_b32 a59, v233\n\t"
    "v_accvgpr_read_b32 v230, a60\n\t"
    "v_accvgpr_read_b32 v231, a61\n\t"
    "v_accvgpr_read_b32 v232, a62\n\t"
    "v_accvgpr_read_b32 v233, a63\n\t"
    "v_mul_f32 v230, v230, v225\n\t"
    "v_mul_f32 v231, v231, v225\n\t"
    "v_mul_f32 v232, v232, v225\n\t"
    "v_mul_f32 v233, v233, v225\n\t"
    "v_accvgpr_write_b32 a60, v230\n\t"
    "v_accvgpr_write_b32 a61, v231\n\t"
    "v_accvgpr_write_b32 a62, v232\n\t"
    "v_accvgpr_write_b32 a63, v233\n\t"
    "v_accvgpr_read_b32 v230, a64\n\t"
    "v_accvgpr_read_b32 v231, a65\n\t"
    "v_accvgpr_read_b32 v232, a66\n\t"
    "v_accvgpr_read_b32 v233, a67\n\t"
    "v_mul_f32 v230, v230, v226\n\t"
    "v_mul_f32 v231, v231, v226\n\t"
    "v_mul_f32 v232, v232, v226\n\t"
    "v_mul_f32 v233, v233, v226\n\t"
    "v_accvgpr_write_b32 a64, v230\n\t"
    "v_accvgpr_write_b32 a65, v231\n\t"
    "v_accvgpr_write_b32 a66, v232\n\t"
    "v_accvgpr_write_b32 a67, v233\n\t"
    "v_accvgpr_read_b32 v230, a68\n\t"
    "v_accvgpr_read_b32 v231, a69\n\t"
    "v_accvgpr_read_b32 v232, a70\n\t"
    "v_accvgpr_read_b32 v233, a71\n\t"
    "v_mul_f32 v230, v230, v227\n\t"
    "v_mul_f32 v231, v231, v227\n\t"
    "v_mul_f32 v232, v232, v227\n\t"
    "v_mul_f32 v233, v233, v227\n\t"
    "v_accvgpr_write_b32 a68, v230\n\t"
    "v_accvgpr_write_b32 a69, v231\n\t"
    "v_accvgpr_write_b32 a70, v232\n\t"
    "v_accvgpr_write_b32 a71, v233\n\t"
    "v_accvgpr_read_b32 v230, a72\n\t"
    "v_accvgpr_read_b32 v231, a73\n\t"
    "v_accvgpr_read_b32 v232, a74\n\t"
    "v_accvgpr_read_b32 v233, a75\n\t"
    "v_mul_f32 v230, v230, v228\n\t"
    "v_mul_f32 v231, v231, v228\n\t"
    "v_mul_f32 v232, v232, v228\n\t"
    "v_mul_f32 v233, v233, v228\n\t"
    "v_accvgpr_write_b32 a72, v230\n\t"
    "v_accvgpr_write_b32 a73, v231\n\t"
    "v_accvgpr_write_b32 a74, v232\n\t"
    "v_accvgpr_write_b32 a75, v233\n\t"
    "v_accvgpr_read_b32 v230, a76\n\t"
    "v_accvgpr_read_b32 v231, a77\n\t"
    "v_accvgpr_read_b32 v232, a78\n\t"
    "v_accvgpr_read_b32 v233, a79\n\t"
    "v_mul_f32 v230, v230, v229\n\t"
    "v_mul_f32 v231, v231, v229\n\t"
    "v_mul_f32 v232, v232, v229\n\t"
    "v_mul_f32 v233, v233, v229\n\t"
    "v_accvgpr_write_b32 a76, v230\n\t"
    "v_accvgpr_write_b32 a77, v231\n\t"
    "v_accvgpr_write_b32 a78, v232\n\t"
    "v_accvgpr_write_b32 a79, v233\n\t"
    "v_accvgpr_read_b32 v230, a80\n\t"
    "v_accvgpr_read_b32 v231, a81\n\t"
    "v_accvgpr_read_b32 v232, a82\n\t"
    "v_accvgpr_read_b32 v233, a83\n\t"
    "v_mul_f32 v230, v230, v225\n\t"
    "v_mul_f32 v231, v231, v225\n\t"
    "v_mul_f32 v232, v232, v225\n\t"
    "v_mul_f32 v233, v233, v225\n\t"
    "v_accvgpr_write_b32 a80, v230\n\t"
    "v_accvgpr_write_b32 a81, v231\n\t"
    "v_accvgpr_write_b32 a82, v232\n\t"
    "v_accvgpr_write_b32 a83, v233\n\t"
    "v_accvgpr_read_b32 v230, a84\n\t"
    "v_accvgpr_read_b32 v231, a85\n\t"
    "v_accvgpr_read_b32 v232, a86\n\t"
    "v_accvgpr_read_b32 v233, a87\n\t"
    "v_mul_f32 v230, v230, v226\n\t"
    "v_mul_f32 v231, v231, v226\n\t"
    "v_mul_f32 v232, v232, v226\n\t"
    "v_mul_f32 v233, v233, v226\n\t"
    "v_accvgpr_write_b32 a84, v230\n\t"
    "v_accvgpr_write_b32 a85, v231\n\t"
    "v_accvgpr_write_b32 a86, v232\n\t"
    "v_accvgpr_write_b32 a87, v233\n\t"
    "v_accvgpr_read_b32 v230, a88\n\t"
    "v_accvgpr_read_b32 v231, a89\n\t"
    "v_accvgpr_read_b32 v232, a90\n\t"
    "v_accvgpr_read_b32 v233, a91\n\t"
    "v_mul_f32 v230, v230, v227\n\t"
    "v_mul_f32 v231, v231, v227\n\t"
    "v_mul_f32 v232, v232, v227\n\t"
    "v_mul_f32 v233, v233, v227\n\t"
    "v_accvgpr_write_b32 a88, v230\n\t"
    "v_accvgpr_write_b32 a89, v231\n\t"
    "v_accvgpr_write_b32 a90, v232\n\t"
    "v_accvgpr_write_b32 a91, v233\n\t"
    "v_accvgpr_read_b32 v230, a92\n\t"
    "v_accvgpr_read_b32 v231, a93\n\t"
    "v_accvgpr_read_b32 v232, a94\n\t"
    "v_accvgpr_read_b32 v233, a95\n\t"
    "v_mul_f32 v230, v230, v228\n\t"
    "v_mul_f32 v231, v231, v228\n\t"
    "v_mul_f32 v232, v232, v228\n\t"
    "v_mul_f32 v233, v233, v228\n\t"
    "v_accvgpr_write_b32 a92, v230\n\t"
    "v_accvgpr_write_b32 a93, v231\n\t"
    "v_accvgpr_write_b32 a94, v232\n\t"
    "v_accvgpr_write_b32 a95, v233\n\t"
    "v_accvgpr_read_b32 v230, a96\n\t"
    "v_accvgpr_read_b32 v231, a97\n\t"
    "v_accvgpr_read_b32 v232, a98\n\t"
    "v_accvgpr_read_b32 v233, a99\n\t"
    "v_mul_f32 v230, v230, v229\n\t"
    "v_mul_f32 v231, v231, v229\n\t"
    "v_mul_f32 v232, v232, v229\n\t"
    "v_mul_f32 v233, v233, v229\n\t"
    "v_accvgpr_write_b32 a96, v230\n\t"
    "v_accvgpr_write_b32 a97, v231\n\t"
    "v_accvgpr_write_b32 a98, v232\n\t"
    "v_accvgpr_write_b32 a99, v233\n\t"
    "v_accvgpr_read_b32 v230, a100\n\t"
    "v_accvgpr_read_b32 v231, a101\n\t"
    "v_accvgpr_read_b32 v232, a102\n\t"
    "v_accvgpr_read_b32 v233, a103\n\t"
    "v_mul_f32 v230, v230, v225\n\t"
    "v_mul_f32 v231, v231, v225\n\t"
    "v_mul_f32 v232, v232, v225\n\t"
    "v_mul_f32 v233, v233, v225\n\t"
    "v_accvgpr_write_b32 a100, v230\n\t"
    "v_accvgpr_write_b32 a101, v231\n\t"
    "v_accvgpr_write_b32 a102, v232\n\t"
    "v_accvgpr_write_b32 a103, v233\n\t"
    "v_accvgpr_read_b32 v230, a104\n\t"
    "v_accvgpr_read_b32 v231, a105\n\t"
    "v_accvgpr_read_b32 v232, a106\n\t"
    "v_accvgpr_read_b32 v233, a107\n\t"
    "v_mul_f32 v230, v230, v226\n\t"
    "v_mul_f32 v231, v231, v226\n\t"
    "v_mul_f32 v232, v232, v226\n\t"
    "v_mul_f32 v233, v233, v226\n\t"
    "v_accvgpr_write_b32 a104, v230\n\t"
    "v_accvgpr_write_b32 a105, v231\n\t"
    "v_accvgpr_write_b32 a106, v232\n\t"
    "v_accvgpr_write_b32 a107, v233\n\t"
    "v_accvgpr_read_b32 v230, a108\n\t"
    "v_accvgpr_read_b32 v231, a109\n\t"
    "v_accvgpr_read_b32 v232, a110\n\t"
    "v_accvgpr_read_b32 v233, a111\n\t"
    "v_mul_f32 v230, v230, v227\n\t"
    "v_mul_f32 v231, v231, v227\n\t"
    "v_mul_f32 v232, v232, v227\n\t"
    "v_mul_f32 v233, v233, v227\n\t"
    "v_accvgpr_write_b32 a108, v230\n\t"
    "v_accvgpr_write_b32 a109, v231\n\t"
    "v_accvgpr_write_b32 a110, v232\n\t"
    "v_accvgpr_write_b32 a111, v233\n\t"
    "v_accvgpr_read_b32 v230, a112\n\t"
    "v_accvgpr_read_b32 v231, a113\n\t"
    "v_accvgpr_read_b32 v232, a114\n\t"
    "v_accvgpr_read_b32 v233, a115\n\t"
    "v_mul_f32 v230, v230, v228\n\t"
    "v_mul_f32 v231, v231, v228\n\t"
    "v_mul_f32 v232, v232, v228\n\t"
    "v_mul_f32 v233, v233, v228\n\t"
    "v_accvgpr_write_b32 a112, v230\n\t"
    "v_accvgpr_write_b32 a113, v231\n\t"
    "v_accvgpr_write_b32 a114, v232\n\t"
    "v_accvgpr_write_b32 a115, v233\n\t"
    "v_accvgpr_read_b32 v230, a116\n\t"
    "v_accvgpr_read_b32 v231, a117\n\t"
    "v_accvgpr_read_b32 v232, a118\n\t"
    "v_accvgpr_read_b32 v233, a119\n\t"
    "v_mul_f32 v230, v230, v229\n\t"
    "v_mul_f32 v231, v231, v229\n\t"
    "v_mul_f32 v232, v232, v229\n\t"
    "v_mul_f32 v233, v233, v229\n\t"
    "v_accvgpr_write_b32 a116, v230\n\t"
    "v_accvgpr_write_b32 a117, v231\n\t"
    "v_accvgpr_write_b32 a118, v232\n\t"
    "v_accvgpr_write_b32 a119, v233\n\t"
    "v_accvgpr_read_b32 v230, a120\n\t"
    "v_accvgpr_read_b32 v231, a121\n\t"
    "v_accvgpr_read_b32 v232, a122\n\t"
    "v_accvgpr_read_b32 v233, a123\n\t"
    "v_mul_f32 v230, v230, v225\n\t"
    "v_mul_f32 v231, v231, v225\n\t"
    "v_mul_f32 v232, v232, v225\n\t"
    "v_mul_f32 v233, v233, v225\n\t"
    "v_accvgpr_write_b32 a120, v230\n\t"
    "v_accvgpr_write_b32 a121, v231\n\t"
    "v_accvgpr_write_b32 a122, v232\n\t"
    "v_accvgpr_write_b32 a123, v233\n\t"
    "v_accvgpr_read_b32 v230, a124\n\t"
    "v_accvgpr_read_b32 v231, a125\n\t"
    "v_accvgpr_read_b32 v232, a126\n\t"
    "v_accvgpr_read_b32 v233, a127\n\t"
    "v_mul_f32 v230, v230, v226\n\t"
    "v_mul_f32 v231, v231, v226\n\t"
    "v_mul_f32 v232, v232, v226\n\t"
    "v_mul_f32 v233, v233, v226\n\t"
    "v_accvgpr_write_b32 a124, v230\n\t"
    "v_accvgpr_write_b32 a125, v231\n\t"
    "v_accvgpr_write_b32 a126, v232\n\t"
    "v_accvgpr_write_b32 a127, v233\n\t"
    "v_accvgpr_read_b32 v230, a128\n\t"
    "v_accvgpr_read_b32 v231, a129\n\t"
    "v_accvgpr_read_b32 v232, a130\n\t"
    "v_accvgpr_read_b32 v233, a131\n\t"
    "v_mul_f32 v230, v230, v227\n\t"
    "v_mul_f32 v231, v231, v227\n\t"
    "v_mul_f32 v232, v232, v227\n\t"
    "v_mul_f32 v233, v233, v227\n\t"
    "v_accvgpr_write_b32 a128, v230\n\t"
    "v_accvgpr_write_b32 a129, v231\n\t"
    "v_accvgpr_write_b32 a130, v232\n\t"
    "v_accvgpr_write_b32 a131, v233\n\t"
    "v_accvgpr_read_b32 v230, a132\n\t"
    "v_accvgpr_read_b32 v231, a133\n\t"
    "v_accvgpr_read_b32 v232, a134\n\t"
    "v_accvgpr_read_b32 v233, a135\n\t"
    "v_mul_f32 v230, v230, v228\n\t"
    "v_mul_f32 v231, v231, v228\n\t"
    "v_mul_f32 v232, v232, v228\n\t"
    "v_mul_f32 v233, v233, v228\n\t"
    "v_accvgpr_write_b32 a132, v230\n\t"
    "v_accvgpr_write_b32 a133, v231\n\t"
    "v_accvgpr_write_b32 a134, v232\n\t"
    "v_accvgpr_write_b32 a135, v233\n\t"
    "v_accvgpr_read_b32 v230, a136\n\t"
    "v_accvgpr_read_b32 v231, a137\n\t"
    "v_accvgpr_read_b32 v232, a138\n\t"
    "v_accvgpr_read_b32 v233, a139\n\t"
    "v_mul_f32 v230, v230, v229\n\t"
    "v_mul_f32 v231, v231, v229\n\t"
    "v_mul_f32 v232, v232, v229\n\t"
    "v_mul_f32 v233, v233, v229\n\t"
    "v_accvgpr_write_b32 a136, v230\n\t"
    "v_accvgpr_write_b32 a137, v231\n\t"
    "v_accvgpr_write_b32 a138, v232\n\t"
    "v_accvgpr_write_b32 a139, v233\n\t"
    "v_accvgpr_read_b32 v230, a140\n\t"
    "v_accvgpr_read_b32 v231, a141\n\t"
    "v_accvgpr_read_b32 v232, a142\n\t"
    "v_accvgpr_read_b32 v233, a143\n\t"
    "v_mul_f32 v230, v230, v225\n\t"
    "v_mul_f32 v231, v231, v225\n\t"
    "v_mul_f32 v232, v232, v225\n\t"
    "v_mul_f32 v233, v233, v225\n\t"
    "v_accvgpr_write_b32 a140, v230\n\t"
    "v_accvgpr_write_b32 a141, v231\n\t"
    "v_accvgpr_write_b32 a142, v232\n\t"
    "v_accvgpr_write_b32 a143, v233\n\t"
    "v_accvgpr_read_b32 v230, a144\n\t"
    "v_accvgpr_read_b32 v231, a145\n\t"
    "v_accvgpr_read_b32 v232, a146\n\t"
    "v_accvgpr_read_b32 v233, a147\n\t"
    "v_mul_f32 v230, v230, v226\n\t"
    "v_mul_f32 v231, v231, v226\n\t"
    "v_mul_f32 v232, v232, v226\n\t"
    "v_mul_f32 v233, v233, v226\n\t"
    "v_accvgpr_write_b32 a144, v230\n\t"
    "v_accvgpr_write_b32 a145, v231\n\t"
    "v_accvgpr_write_b32 a146, v232\n\t"
    "v_accvgpr_write_b32 a147, v233\n\t"
    "v_accvgpr_read_b32 v230, a148\n\t"
    "v_accvgpr_read_b32 v231, a149\n\t"
    "v_accvgpr_read_b32 v232, a150\n\t"
    "v_accvgpr_read_b32 v233, a151\n\t"
    "v_mul_f32 v230, v230, v227\n\t"
    "v_mul_f32 v231, v231, v227\n\t"
    "v_mul_f32 v232, v232, v227\n\t"
    "v_mul_f32 v233, v233, v227\n\t"
    "v_accvgpr_write_b32 a148, v230\n\t"
    "v_accvgpr_write_b32 a149, v231\n\t"
    "v_accvgpr_write_b32 a150, v232\n\t"
    "v_accvgpr_write_b32 a151, v233\n\t"
    "v_accvgpr_read_b32 v230, a152\n\t"
    "v_accvgpr_read_b32 v231, a153\n\t"
    "v_accvgpr_read_b32 v232, a154\n\t"
    "v_accvgpr_read_b32 v233, a155\n\t"
    "v_mul_f32 v230, v230, v228\n\t"
    "v_mul_f32 v231, v231, v228\n\t"
    "v_mul_f32 v232, v232, v228\n\t"
    "v_mul_f32 v233, v233, v228\n\t"
    "v_accvgpr_write_b32 a152, v230\n\t"
    "v_accvgpr_write_b32 a153, v231\n\t"
    "v_accvgpr_write_b32 a154, v232\n\t"
    "v_accvgpr_write_b32 a155, v233\n\t"
    "v_accvgpr_read_b32 v230, a156\n\t"
    "v_accvgpr_read_b32 v231, a157\n\t"
    "v_accvgpr_read_b32 v232, a158\n\t"
    "v_accvgpr_read_b32 v233, a159\n\t"
    "v_mul_f32 v230, v230, v229\n\t"
    "v_mul_f32 v231, v231, v229\n\t"
    "v_mul_f32 v232, v232, v229\n\t"
    "v_mul_f32 v233, v233, v229\n\t"
    "v_accvgpr_write_b32 a156, v230\n\t"
    "v_accvgpr_write_b32 a157, v231\n\t"
    "v_accvgpr_write_b32 a158, v232\n\t"
    "v_accvgpr_write_b32 a159, v233\n\t"
    "v_accvgpr_read_b32 v230, a160\n\t"
    "v_accvgpr_read_b32 v231, a161\n\t"
    "v_accvgpr_read_b32 v232, a162\n\t"
    "v_accvgpr_read_b32 v233, a163\n\t"
    "v_mul_f32 v230, v230, v225\n\t"
    "v_mul_f32 v231, v231, v225\n\t"
    "v_mul_f32 v232, v232, v225\n\t"
    "v_mul_f32 v233, v233, v225\n\t"
    "v_accvgpr_write_b32 a160, v230\n\t"
    "v_accvgpr_write_b32 a161, v231\n\t"
    "v_accvgpr_write_b32 a162, v232\n\t"
    "v_accvgpr_write_b32 a163, v233\n\t"
    "v_accvgpr_read_b32 v230, a164\n\t"
    "v_accvgpr_read_b32 v231, a165\n\t"
    "v_accvgpr_read_b32 v232, a166\n\t"
    "v_accvgpr_read_b32 v233, a167\n\t"
    "v_mul_f32 v230, v230, v226\n\t"
    "v_mul_f32 v231, v231, v226\n\t"
    "v_mul_f32 v232, v232, v226\n\t"
    "v_mul_f32 v233, v233, v226\n\t"
    "v_accvgpr_write_b32 a164, v230\n\t"
    "v_accvgpr_write_b32 a165, v231\n\t"
    "v_accvgpr_write_b32 a166, v232\n\t"
    "v_accvgpr_write_b32 a167, v233\n\t"
    "v_accvgpr_read_b32 v230, a168\n\t"
    "v_accvgpr_read_b32 v231, a169\n\t"
    "v_accvgpr_read_b32 v232, a170\n\t"
    "v_accvgpr_read_b32 v233, a171\n\t"
    "v_mul_f32 v230, v230, v227\n\t"
    "v_mul_f32 v231, v231, v227\n\t"
    "v_mul_f32 v232, v232, v227\n\t"
    "v_mul_f32 v233, v233, v227\n\t"
    "v_accvgpr_write_b32 a168, v230\n\t"
    "v_accvgpr_write_b32 a169, v231\n\t"
    "v_accvgpr_write_b32 a170, v232\n\t"
    "v_accvgpr_write_b32 a171, v233\n\t"
    "v_accvgpr_read_b32 v230, a172\n\t"
    "v_accvgpr_read_b32 v231, a173\n\t"
    "v_accvgpr_read_b32 v232, a174\n\t"
    "v_accvgpr_read_b32 v233, a175\n\t"
    "v_mul_f32 v230, v230, v228\n\t"
    "v_mul_f32 v231, v231, v228\n\t"
    "v_mul_f32 v232, v232, v228\n\t"
    "v_mul_f32 v233, v233, v228\n\t"
    "v_accvgpr_write_b32 a172, v230\n\t"
    "v_accvgpr_write_b32 a173, v231\n\t"
    "v_accvgpr_write_b32 a174, v232\n\t"
    "v_accvgpr_write_b32 a175, v233\n\t"
    "v_accvgpr_read_b32 v230, a176\n\t"
    "v_accvgpr_read_b32 v231, a177\n\t"
    "v_accvgpr_read_b32 v232, a178\n\t"
    "v_accvgpr_read_b32 v233, a179\n\t"
    "v_mul_f32 v230, v230, v229\n\t"
    "v_mul_f32 v231, v231, v229\n\t"
    "v_mul_f32 v232, v232, v229\n\t"
    "v_mul_f32 v233, v233, v229\n\t"
    "v_accvgpr_write_b32 a176, v230\n\t"
    "v_accvgpr_write_b32 a177, v231\n\t"
    "v_accvgpr_write_b32 a178, v232\n\t"
    "v_accvgpr_write_b32 a179, v233\n\t"
    "v_accvgpr_read_b32 v230, a180\n\t"
    "v_accvgpr_read_b32 v231, a181\n\t"
    "v_accvgpr_read_b32 v232, a182\n\t"
    "v_accvgpr_read_b32 v233, a183\n\t"
    "v_mul_f32 v230, v230, v225\n\t"
    "v_mul_f32 v231, v231, v225\n\t"
    "v_mul_f32 v232, v232, v225\n\t"
    "v_mul_f32 v233, v233, v225\n\t"
    "v_accvgpr_write_b32 a180, v230\n\t"
    "v_accvgpr_write_b32 a181, v231\n\t"
    "v_accvgpr_write_b32 a182, v232\n\t"
    "v_accvgpr_write_b32 a183, v233\n\t"
    "v_accvgpr_read_b32 v230, a184\n\t"
    "v_accvgpr_read_b32 v231, a185\n\t"
    "v_accvgpr_read_b32 v232, a186\n\t"
    "v_accvgpr_read_b32 v233, a187\n\t"
    "v_mul_f32 v230, v230, v226\n\t"
    "v_mul_f32 v231, v231, v226\n\t"
    "v_mul_f32 v232, v232, v226\n\t"
    "v_mul_f32 v233, v233, v226\n\t"
    "v_accvgpr_write_b32 a184, v230\n\t"
    "v_accvgpr_write_b32 a185, v231\n\t"
    "v_accvgpr_write_b32 a186, v232\n\t"
    "v_accvgpr_write_b32 a187, v233\n\t"
    "v_accvgpr_read_b32 v230, a188\n\t"
    "v_accvgpr_read_b32 v231, a189\n\t"
    "v_accvgpr_read_b32 v232, a190\n\t"
    "v_accvgpr_read_b32 v233, a191\n\t"
    "v_mul_f32 v230, v230, v227\n\t"
    "v_mul_f32 v231, v231, v227\n\t"
    "v_mul_f32 v232, v232, v227\n\t"
    "v_mul_f32 v233, v233, v227\n\t"
    "v_accvgpr_write_b32 a188, v230\n\t"
    "v_accvgpr_write_b32 a189, v231\n\t"
    "v_accvgpr_write_b32 a190, v232\n\t"
    "v_accvgpr_write_b32 a191, v233\n\t"
    "v_accvgpr_read_b32 v230, a192\n\t"
    "v_accvgpr_read_b32 v231, a193\n\t"
    "v_accvgpr_read_b32 v232, a194\n\t"
    "v_accvgpr_read_b32 v233, a195\n\t"
    "v_mul_f32 v230, v230, v228\n\t"
    "v_mul_f32 v231, v231, v228\n\t"
    "v_mul_f32 v232, v232, v228\n\t"
    "v_mul_f32 v233, v233, v228\n\t"
    "v_accvgpr_write_b32 a192, v230\n\t"
    "v_accvgpr_write_b32 a193, v231\n\t"
    "v_accvgpr_write_b32 a194, v232\n\t"
    "v_accvgpr_write_b32 a195, v233\n\t"
    "v_accvgpr_read_b32 v230, a196\n\t"
    "v_accvgpr_read_b32 v231, a197\n\t"
    "v_accvgpr_read_b32 v232, a198\n\t"
    "v_accvgpr_read_b32 v233, a199\n\t"
    "v_mul_f32 v230, v230, v229\n\t"
    "v_mul_f32 v231, v231, v229\n\t"
    "v_mul_f32 v232, v232, v229\n\t"
    "v_mul_f32 v233, v233, v229\n\t"
    "v_accvgpr_write_b32 a196, v230\n\t"
    "v_accvgpr_write_b32 a197, v231\n\t"
    "v_accvgpr_write_b32 a198, v232\n\t"
    "v_accvgpr_write_b32 a199, v233\n\t"
    "v_accvgpr_read_b32 v230, a200\n\t"
    "v_accvgpr_read_b32 v231, a201\n\t"
    "v_accvgpr_read_b32 v232, a202\n\t"
    "v_accvgpr_read_b32 v233, a203\n\t"
    "v_mul_f32 v230, v230, v225\n\t"
    "v_mul_f32 v231, v231, v225\n\t"
    "v_mul_f32 v232, v232, v225\n\t"
    "v_mul_f32 v233, v233, v225\n\t"
    "v_accvgpr_write_b32 a200, v230\n\t"
    "v_accvgpr_write_b32 a201, v231\n\t"
    "v_accvgpr_write_b32 a202, v232\n\t"
    "v_accvgpr_write_b32 a203, v233\n\t"
    "v_accvgpr_read_b32 v230, a204\n\t"
    "v_accvgpr_read_b32 v231, a205\n\t"
    "v_accvgpr_read_b32 v232, a206\n\t"
    "v_accvgpr_read_b32 v233, a207\n\t"
    "v_mul_f32 v230, v230, v226\n\t"
    "v_mul_f32 v231, v231, v226\n\t"
    "v_mul_f32 v232, v232, v226\n\t"
    "v_mul_f32 v233, v233, v226\n\t"
    "v_accvgpr_write_b32 a204, v230\n\t"
    "v_accvgpr_write_b32 a205, v231\n\t"
    "v_accvgpr_write_b32 a206, v232\n\t"
    "v_accvgpr_write_b32 a207, v233\n\t"
    "v_accvgpr_read_b32 v230, a208\n\t"
    "v_accvgpr_read_b32 v231, a209\n\t"
    "v_accvgpr_read_b32 v232, a210\n\t"
    "v_accvgpr_read_b32 v233, a211\n\t"
    "v_mul_f32 v230, v230, v227\n\t"
    "v_mul_f32 v231, v231, v227\n\t"
    "v_mul_f32 v232, v232, v227\n\t"
    "v_mul_f32 v233, v233, v227\n\t"
    "v_accvgpr_write_b32 a208, v230\n\t"
    "v_accvgpr_write_b32 a209, v231\n\t"
    "v_accvgpr_write_b32 a210, v232\n\t"
    "v_accvgpr_write_b32 a211, v233\n\t"
    "v_accvgpr_read_b32 v230, a212\n\t"
    "v_accvgpr_read_b32 v231, a213\n\t"
    "v_accvgpr_read_b32 v232, a214\n\t"
    "v_accvgpr_read_b32 v233, a215\n\t"
    "v_mul_f32 v230, v230, v228\n\t"
    "v_mul_f32 v231, v231, v228\n\t"
    "v_mul_f32 v232, v232, v228\n\t"
    "v_mul_f32 v233, v233, v228\n\t"
    "v_accvgpr_write_b32 a212, v230\n\t"
    "v_accvgpr_write_b32 a213, v231\n\t"
    "v_accvgpr_write_b32 a214, v232\n\t"
    "v_accvgpr_write_b32 a215, v233\n\t"
    "v_accvgpr_read_b32 v230, a216\n\t"
    "v_accvgpr_read_b32 v231, a217\n\t"
    "v_accvgpr_read_b32 v232, a218\n\t"
    "v_accvgpr_read_b32 v233, a219\n\t"
    "v_mul_f32 v230, v230, v229\n\t"
    "v_mul_f32 v231, v231, v229\n\t"
    "v_mul_f32 v232, v232, v229\n\t"
    "v_mul_f32 v233, v233, v229\n\t"
    "v_accvgpr_write_b32 a216, v230\n\t"
    "v_accvgpr_write_b32 a217, v231\n\t"
    "v_accvgpr_write_b32 a218, v232\n\t"
    "v_accvgpr_write_b32 a219, v233\n\t"
    "v_accvgpr_read_b32 v230, a220\n\t"
    "v_accvgpr_read_b32 v231, a221\n\t"
    "v_accvgpr_read_b32 v232, a222\n\t"
    "v_accvgpr_read_b32 v233, a223\n\t"
    "v_mul_f32 v230, v230, v225\n\t"
    "v_mul_f32 v231, v231, v225\n\t"
    "v_mul_f32 v232, v232, v225\n\t"
    "v_mul_f32 v233, v233, v225\n\t"
    "v_accvgpr_write_b32 a220, v230\n\t"
    "v_accvgpr_write_b32 a221, v231\n\t"
    "v_accvgpr_write_b32 a222, v232\n\t"
    "v_accvgpr_write_b32 a223, v233\n\t"
    "v_accvgpr_read_b32 v230, a224\n\t"
    "v_accvgpr_read_b32 v231, a225\n\t"
    "v_accvgpr_read_b32 v232, a226\n\t"
    "v_accvgpr_read_b32 v233, a227\n\t"
    "v_mul_f32 v230, v230, v226\n\t"
    "v_mul_f32 v231, v231, v226\n\t"
    "v_mul_f32 v232, v232, v226\n\t"
    "v_mul_f32 v233, v233, v226\n\t"
    "v_accvgpr_write_b32 a224, v230\n\t"
    "v_accvgpr_write_b32 a225, v231\n\t"
    "v_accvgpr_write_b32 a226, v232\n\t"
    "v_accvgpr_write_b32 a227, v233\n\t"
    "v_accvgpr_read_b32 v230, a228\n\t"
    "v_accvgpr_read_b32 v231, a229\n\t"
    "v_accvgpr_read_b32 v232, a230\n\t"
    "v_accvgpr_read_b32 v233, a231\n\t"
    "v_mul_f32 v230, v230, v227\n\t"
    "v_mul_f32 v231, v231, v227\n\t"
    "v_mul_f32 v232, v232, v227\n\t"
    "v_mul_f32 v233, v233, v227\n\t"
    "v_accvgpr_write_b32 a228, v230\n\t"
    "v_accvgpr_write_b32 a229, v231\n\t"
    "v_accvgpr_write_b32 a230, v232\n\t"
    "v_accvgpr_write_b32 a231, v233\n\t"
    "v_accvgpr_read_b32 v230, a232\n\t"
    "v_accvgpr_read_b32 v231, a233\n\t"
    "v_accvgpr_read_b32 v232, a234\n\t"
    "v_accvgpr_read_b32 v233, a235\n\t"
    "v_mul_f32 v230, v230, v228\n\t"
    "v_mul_f32 v231, v231, v228\n\t"
    "v_mul_f32 v232, v232, v228\n\t"
    "v_mul_f32 v233, v233, v228\n\t"
    "v_accvgpr_write_b32 a232, v230\n\t"
    "v_accvgpr_write_b32 a233, v231\n\t"
    "v_accvgpr_write_b32 a234, v232\n\t"
    "v_accvgpr_write_b32 a235, v233\n\t"
    "v_accvgpr_read_b32 v230, a236\n\t"
    "v_accvgpr_read_b32 v231, a237\n\t"
    "v_accvgpr_read_b32 v232, a238\n\t"
    "v_accvgpr_read_b32 v233, a239\n\t"
    "v_mul_f32 v230, v230, v229\n\t"
    "v_mul_f32 v231, v231, v229\n\t"
    "v_mul_f32 v232, v232, v229\n\t"
    "v_mul_f32 v233, v233, v229\n\t"
    "v_accvgpr_write_b32 a236, v230\n\t"
    "v_accvgpr_write_b32 a237, v231\n\t"
    "v_accvgpr_write_b32 a238, v232\n\t"
    "v_accvgpr_write_b32 a239, v233\n\t"
    "v_exp_f32 v230, v104\n\t"
    "v_exp_f32 v231, v105\n\t"
    "v_exp_f32 v232, v106\n\t"
    "v_exp_f32 v233, v107\n\t"
    "v_add_f32 v225, v230, v231\n\t"
    "v_cvt_pk_bf16_f32 v184, v230, v231\n\t"
    "v_add_f32 v225, v232, v225\n\t"
    "v_add_f32 v225, v233, v225\n\t"
    "v_cvt_pk_bf16_f32 v185, v232, v233\n\t"
    "v_exp_f32 v230, v124\n\t"
    "v_exp_f32 v231, v125\n\t"
    "v_exp_f32 v232, v126\n\t"
    "v_exp_f32 v233, v127\n\t"
    "v_add_f32 v225, v230, v225\n\t"
    "v_add_f32 v225, v231, v225\n\t"
    "v_cvt_pk_bf16_f32 v186, v230, v231\n\t"
    "v_add_f32 v225, v232, v225\n\t"
    "v_add_f32 v225, v233, v225\n\t"
    "v_cvt_pk_bf16_f32 v187, v232, v233\n\t"
    "v_exp_f32 v230, v108\n\t"
    "v_exp_f32 v231, v109\n\t"
    "v_exp_f32 v232, v110\n\t"
    "v_exp_f32 v233, v111\n\t"
    "v_add_f32 v226, v230, v231\n\t"
    "v_cvt_pk_bf16_f32 v188, v230, v231\n\t"
    "v_add_f32 v226, v232, v226\n\t"
    "v_add_f32 v226, v233, v226\n\t"
    "v_cvt_pk_bf16_f32 v189, v232, v233\n\t"
    "v_exp_f32 v230, v128\n\t"
    "v_exp_f32 v231, v129\n\t"
    "v_exp_f32 v232, v130\n\t"
    "v_exp_f32 v233, v131\n\t"
    "v_add_f32 v226, v230, v226\n\t"
    "v_add_f32 v226, v231, v226\n\t"
    "v_cvt_pk_bf16_f32 v190, v230, v231\n\t"
    "v_add_f32 v226, v232, v226\n\t"
    "v_add_f32 v226, v233, v226\n\t"
    "v_cvt_pk_bf16_f32 v191, v232, v233\n\t"
    "v_exp_f32 v230, v112\n\t"
    "v_exp_f32 v231, v113\n\t"
    "v_exp_f32 v232, v114\n\t"
    "v_exp_f32 v233, v115\n\t"
    "v_add_f32 v227, v230, v231\n\t"
    "v_cvt_pk_bf16_f32 v192, v230, v231\n\t"
    "v_add_f32 v227, v232, v227\n\t"
    "v_add_f32 v227, v233, v227\n\t"
    "v_cvt_pk_bf16_f32 v193, v232, v233\n\t"
    "v_exp_f32 v230, v132\n\t"
    "v_exp_f32 v231, v133\n\t"
    "v_exp_f32 v232, v134\n\t"
    "v_exp_f32 v233, v135\n\t"
    "v_add_f32 v227, v230, v227\n\t"
    "v_add_f32 v227, v231, v227\n\t"
    "v_cvt_pk_bf16_f32 v194, v230, v231\n\t"
    "v_add_f32 v227, v232, v227\n\t"
    "v_add_f32 v227, v233, v227\n\t"
    "v_cvt_pk_bf16_f32 v195, v232, v233\n\t"
    "v_exp_f32 v230, v116\n\t"
    "v_exp_f32 v231, v117\n\t"
    "v_exp_f32 v232, v118\n\t"
    "v_exp_f32 v233, v119\n\t"
    "v_add_f32 v228, v230, v231\n\t"
    "v_cvt_pk_bf16_f32 v196, v230, v231\n\t"
    "v_add_f32 v228, v232, v228\n\t"
    "v_add_f32 v228, v233, v228\n\t"
    "v_cvt_pk_bf16_f32 v197, v232, v233\n\t"
    "v_exp_f32 v230, v136\n\t"
    "v_exp_f32 v231, v137\n\t"
    "v_exp_f32 v232, v138\n\t"
    "v_exp_f32 v233, v139\n\t"
    "v_add_f32 v228, v230, v228\n\t"
    "v_add_f32 v228, v231, v228\n\t"
    "v_cvt_pk_bf16_f32 v198, v230, v231\n\t"
    "v_add_f32 v228, v232, v228\n\t"
    "v_add_f32 v228, v233, v228\n\t"
    "v_cvt_pk_bf16_f32 v199, v232, v233\n\t"
    "v_exp_f32 v230, v120\n\t"
    "v_exp_f32 v231, v121\n\t"
    "v_exp_f32 v232, v122\n\t"
    "v_exp_f32 v233, v123\n\t"
    "v_add_f32 v229, v230, v231\n\t"
    "v_cvt_pk_bf16_f32 v200, v230, v231\n\t"
    "v_add_f32 v229, v232, v229\n\t"
    "v_add_f32 v229, v233, v229\n\t"
    "v_cvt_pk_bf16_f32 v201, v232, v233\n\t"
    "v_exp_f32 v230, v140\n\t"
    "v_exp_f32 v231, v141\n\t"
    "v_exp_f32 v232, v142\n\t"
    "v_exp_f32 v233, v143\n\t"
    "v_add_f32 v229, v230, v229\n\t"
    "v_add_f32 v229, v231, v229\n\t"
    "v_cvt_pk_bf16_f32 v202, v230, v231\n\t"
    "v_add_f32 v229, v232, v229\n\t"
    "v_add_f32 v229, v233, v229\n\t"
    "v_cvt_pk_bf16_f32 v203, v232, v233\n\t"
    "s_nop 1\n\t"
    "s_nop 1\n\t"
    "s_setpc_b64 s[52:53]\n\t"
    ".Lk1_end:\n\t"
    : "+v"(tid)
    : "s"(__builtin_amdgcn_kernarg_segment_ptr()), "s"(blockIdx.x), "s"((unsigned)(size_t)lds)
    : "v1", "v2", "v3", "v4", "v5", "v6", "v7", "v8", "v9", "v10", "v11", "v12", "v13", "v14", "v15", "v16", "v17", "v18", "v19", "v20", "v21", "v22", "v23", "v24", "v25", "v26", "v27", "v28", "v29", "v30", "v31", "v32", "v33", "v34", "v35", "v36", "v37", "v38", "v39", "v40", "v41", "v42", "v43", "v44", "v45", "v46", "v47", "v48", "v49", "v50", "v51", "v52", "v53", "v54", "v55", "v56", "v57", "v58", "v59", "v60", "v61", "v62", "v63", "v64", "v65", "v66", "v67", "v68", "v69", "v70", "v71", "v72", "v73", "v74", "v75", "v76", "v77", "v78", "v79", "v80", "v81", "v82", "v83", "v84", "v85", "v86", "v87", "v88", "v89", "v90", "v91", "v92", "v93", "v94", "v95", "v96", "v97", "v98", "v99", "v100", "v101", "v102", "v103", "v104", "v105", "v106", "v107", "v108", "v109", "v110", "v111", "v112", "v113", "v114", "v115", "v116", "v117", "v118", "v119", "v120", "v121", "v122", "v123", "v124", "v125", "v126", "v127", "v128", "v129", "v130", "v131", "v132", "v133", "v134", "v135", "v136", "v137", "v138", "v139", "v140", "v141", "v142", "v143", "v144", "v145", "v146", "v147", "v148", "v149", "v150", "v151", "v152", "v153", "v154", "v155", "v156", "v157", "v158", "v159", "v160", "v161", "v162", "v163", "v164", "v165", "v166", "v167", "v168", "v169", "v170", "v171", "v172", "v173", "v174", "v175", "v176", "v177", "v178", "v179", "v180", "v181", "v182", "v183", "v184", "v185", "v186", "v187", "v188", "v189", "v190", "v191", "v192", "v193", "v194", "v195", "v196", "v197", "v198", "v199", "v200", "v201", "v202", "v203", "v204", "v205", "v206", "v207", "v208", "v209", "v210", "v211", "v212", "v213", "v214", "v215", "v216", "v217", "v218", "v219", "v220", "v221", "v222", "v223", "v224", "v225", "v226", "v227", "v228", "v229", "v230", "v231", "v232", "v233", "v234", "v235", "v236", "v237", "v238", "v239", "v240", "v241", "v242", "v243", "v244", "v245", "v246", "v247", "v248", "v249", "v250", "v251", "v252", "v253", "v254", "v255", "a0", "a1", "a2", "a3", "a4", "a5", "a6", "a7", "a8", "a9", "a10", "a11", "a12", "a13", "a14", "a15", "a16", "a17", "a18", "a19", "a20", "a21", "a22", "a23", "a24", "a25", "a26", "a27", "a28", "a29", "a30", "a31", "a32", "a33", "a34", "a35", "a36", "a37", "a38", "a39", "a40", "a41", "a42", "a43", "a44", "a45", "a46", "a47", "a48", "a49", "a50", "a51", "a52", "a53", "a54", "a55", "a56", "a57", "a58", "a59", "a60", "a61", "a62", "a63", "a64", "a65", "a66", "a67", "a68", "a69", "a70", "a71", "a72", "a73", "a74", "a75", "a76", "a77", "a78", "a79", "a80", "a81", "a82", "a83", "a84", "a85", "a86", "a87", "a88", "a89", "a90", "a91", "a92", "a93", "a94", "a95", "a96", "a97", "a98", "a99", "a100", "a101", "a102", "a103", "a104", "a105", "a106", "a107", "a108", "a109", "a110", "a111", "a112", "a113", "a114", "a115", "a116", "a117", "a118", "a119", "a120", "a121", "a122", "a123", "a124", "a125", "a126", "a127", "a128", "a129", "a130", "a131", "a132", "a133", "a134", "a135", "a136", "a137", "a138", "a139", "a140", "a141", "a142", "a143", "a144", "a145", "a146", "a147", "a148", "a149", "a150", "a151", "a152", "a153", "a154", "a155", "a156", "a157", "a158", "a159", "a160", "a161", "a162", "a163", "a164", "a165", "a166", "a167", "a168", "a169", "a170", "a171", "a172", "a173", "a174", "a175", "a176", "a177", "a178", "a179", "a180", "a181", "a182", "a183", "a184", "a185", "a186", "a187", "a188", "a189", "a190", "a191", "a192", "a193", "a194", "a195", "a196", "a197", "a198", "a199", "a200", "a201", "a202", "a203", "a204", "a205", "a206", "a207", "a208", "a209", "a210", "a211", "a212", "a213", "a214", "a215", "a216", "a217", "a218", "a219", "a220", "a221", "a222", "a223", "a224", "a225", "a226", "a227", "a228", "a229", "a230", "a231", "a232", "a233", "a234", "a235", "a236", "a237", "a238", "a239", "a240", "a241", "a242", "a243", "a244", "a245", "a246", "a247", "a248", "a249", "a250", "a251", "a252", "a253", "a254", "a255", "s4", "s5", "s6", "s7", "s8", "s9", "s10", "s11", "s12", "s13", "s14", "s15", "s16", "s17", "s18", "s19", "s20", "s21", "s22", "s23", "s24", "s25", "s26", "s27", "s28", "s29", "s30", "s31", "s34", "s35", "s36", "s37", "s38", "s39", "s40", "s41", "s42", "s43", "s44", "s45", "s46", "s47", "s48", "s49", "s50", "s51", "s52", "s53", "s54", "s55", "s56", "s57", "s58", "s59", "s60", "s61", "s62", "s63", "s64", "s65", "s66", "s67", "s68", "s69", "s70", "s71", "s72", "s73", "s74", "s75", "s76", "s77", "s78", "s79", "s80", "s81", "s82", "s83", "s84", "s85", "s86", "s87", "s88", "s89", "s90", "s91", "s92", "s93", "s94", "s95", "s96", "s97", "s98", "s99", "vcc", "scc", "memory");
}

extern "C" __global__ __launch_bounds__(256)
void init_flags(unsigned* flags, const char* x0, const char* x1, const char* wq0, const char* wq1) {
  const unsigned t = blockIdx.x * 256u + threadIdx.x;
  if (t < 8192u) flags[t] = 0u;
}

constexpr size_t FLAG_BYTES = 1048576;

extern "C" void kernel_launch(void* const* d_in, const int* in_sizes, int n_in, void* d_out, int out_size, void* d_ws, size_t ws_size,
                              hipStream_t stream) {
  const bf16* const* in = (const bf16* const*)d_in;
  fused_block<<<256, 256, 0, stream>>>(in[0], in[1], in[2], in[3], in[4], in[5], in[6], in[7], in[8], in[9], (char*)d_ws + FLAG_BYTES, (bf16*)d_out);
}
```
